```python
import torch

def _softmax(x, axis=-1):
    return torch.softmax(x, dim=axis)
def _gelu(x):
    return torch.nn.functional.gelu(x, approximate="tanh")
def _silu(x):
    return torch.nn.functional.silu(x)
def _sigmoid(x):
    return torch.sigmoid(x)
def _layernorm(x, axis=-1, eps=1e-5):
    m = x.mean(dim=axis, keepdim=True)
    v = x.var(dim=axis, keepdim=True, unbiased=False)
    return (x - m) / torch.sqrt(v + eps)
def _scatter_add(tgt, idx, src, axis=0):
    return tgt.clone().index_add_(axis, idx, src)
def _segment_sum(src, seg, n):
    out = torch.zeros((n,) + src.shape[1:], dtype=src.dtype)
    return out.index_add_(0, seg, src)
def _pad(x, pw):
    flat = tuple(v for pair in reversed(pw) for v in pair)
    return torch.nn.functional.pad(x, flat)

kernel_name = 'parallel_sum__compressed_kv_cla__mixer_channel_mlp__prompt'

def setup_inputs():

    _w = (lambda a: (lambda t: t.to(torch.bfloat16) if t.is_floating_point() else t)(torch.as_tensor(a)))
    def _A_setup():
        B, H, Sq, Sk, D, M = 1, 32, 2048, 2048, 128, 640
        Dc = 4 * D
        return {
            "x": _w((lambda *s: torch.randn(*s, dtype=torch.bfloat16) * 0.05)(B, Sq, M)),
            "c_kv": _w((lambda *s: torch.randn(*s, dtype=torch.bfloat16) * 0.4)(B, Sk, Dc)),
            "w_q": _w((lambda *s: torch.randn(*s, dtype=torch.bfloat16) * 3.2)(M, H, D) * (M ** -0.5)),
            "w_uk": _w((lambda *s: torch.randn(*s, dtype=torch.bfloat16) * 3.2)(Dc, H, D) * (Dc ** -0.5)),
            "w_uv": _w((lambda *s: torch.randn(*s, dtype=torch.bfloat16) * 0.4)(Dc, H, D) * (Dc ** -0.5)),
            "w_o": _w((lambda *s: torch.randn(*s, dtype=torch.bfloat16) * 0.4)(H, D, M) * ((H * D) ** -0.5)),
        }
    _A_in = _A_setup()

    def _B_setup():
        B, S, D, F = 1, 2048, 640, 1728
        return {
            "x": _w((lambda *s: torch.randn(*s, dtype=torch.bfloat16) * 0.05)(B, S, D)),
            "w1": _w((lambda *s: torch.randn(*s, dtype=torch.bfloat16) * 0.8)(D, F) * (D ** -0.5)),
            "b1": _w((lambda *s: torch.randn(*s, dtype=torch.bfloat16) * 0.4)(F)),
            "w2": _w((lambda *s: torch.randn(*s, dtype=torch.bfloat16) * 0.05)(F, D) * (F ** -0.5)),
            "b2": _w((lambda *s: torch.randn(*s, dtype=torch.bfloat16) * 0.05)(D)),
        }
    _B_in = _B_setup()

    return {
        "A_x": _A_in["x"],
        "A_c_kv": _A_in["c_kv"],
        "A_w_q": _A_in["w_q"],
        "A_w_uk": _A_in["w_uk"],
        "A_w_uv": _A_in["w_uv"],
        "A_w_o": _A_in["w_o"],
        "B_x": _B_in["x"],
        "B_w1": _B_in["w1"],
        "B_b1": _B_in["b1"],
        "B_w2": _B_in["w2"],
        "B_b2": _B_in["b2"],
    }

def reference(A_x, A_c_kv, A_w_q, A_w_uk, A_w_uv, A_w_o, B_x, B_w1, B_b1, B_w2, B_b2):
    def _A_ref(x, c_kv, w_q, w_uk, w_uv, w_o):
        B, Sq, M = x.shape[0], x.shape[1], x.shape[2]
        Sk = c_kv.shape[1]
        H, D = w_q.shape[1], w_q.shape[2]
        q = torch.einsum("bqm,mhd->bhqd", x, w_q)
        k = torch.einsum("bkc,chd->bhkd", c_kv, w_uk)
        v = torch.einsum("bkc,chd->bhkd", c_kv, w_uv)
        scores = torch.einsum("bhqd,bhkd->bhqk", q, k) * (D ** -0.5)
        off = Sk - Sq
        qi = torch.arange(Sq)[:, None]
        kj = torch.arange(Sk)[None, :]
        cmask = kj <= (qi + off)
        scores = torch.where(cmask[None, None], scores, -1e9)
        p = _softmax(scores, axis=-1)
        ctx = torch.einsum("bhqk,bhkd->bhqd", p, v)
        return torch.einsum("bhqd,hdm->bqm", ctx, w_o)

    def _B_ref(x, w1, b1, w2, b2):
        y = _layernorm(x, axis=-1)
        h = _gelu(torch.einsum("bsd,df->bsf", y, w1) + b1)
        return torch.einsum("bsf,fd->bsd", h, w2) + b2

    x = A_x
    xn = _layernorm(x, axis=-1)
    return x + _A_ref(xn, A_c_kv, A_w_q, A_w_uk, A_w_uv, A_w_o) + _B_ref(xn, B_w1, B_b1, B_w2, B_b2)
```

```cpp
#include <hip/hip_runtime.h>
#include <hip/hip_bf16.h>
typedef unsigned short u16;
__device__ __forceinline__ float bf2f(u16 b){ return __uint_as_float(((unsigned)b)<<16); }
__device__ __forceinline__ u16 f2bf(float f){ unsigned u=__float_as_uint(f); u += 0x7FFF + ((u>>16)&1); return (u16)(u>>16); }
__device__ __forceinline__ float rbf(float f){ return bf2f(f2bf(f)); }

constexpr int S=2048, M=640, H=32, D=128, DC=512, F=1728;

__global__ void ln_kernel(const u16* x, u16* xn, u16* y){
  int row = blockIdx.x; int lane = threadIdx.x;
  float v[10];
  float s=0;
  for(int i=0;i<10;i++){ v[i]=bf2f(x[row*M + lane + 64*i]); s+=v[i]; }
  for(int o=32;o>0;o>>=1) s+=__shfl_xor(s,o);
  float m = s/M;
  float q=0; for(int i=0;i<10;i++){ float d=v[i]-m; q+=d*d; }
  for(int o=32;o>0;o>>=1) q+=__shfl_xor(q,o);
  float var=q/M;
  float mb=rbf(m), vb=rbf(var);
  float den = rbf(sqrtf(rbf(vb+1e-5f)));
  s=0;
  for(int i=0;i<10;i++){ v[i]=rbf(rbf(v[i]-mb)/den); xn[row*M+lane+64*i]=f2bf(v[i]); s+=v[i]; }
  for(int o=32;o>0;o>>=1) s+=__shfl_xor(s,o);
  m=s/M;
  q=0; for(int i=0;i<10;i++){ float d=v[i]-m; q+=d*d; }
  for(int o=32;o>0;o>>=1) q+=__shfl_xor(q,o);
  var=q/M;
  mb=rbf(m); vb=rbf(var);
  den = rbf(sqrtf(rbf(vb+1e-5f)));
  for(int i=0;i<10;i++){ float t=rbf(rbf(v[i]-mb)/den); y[row*M+lane+64*i]=f2bf(t); }
}

template<int MODE>
__global__ void gemm_naive(const u16* A, const u16* B, void* C, const u16* bias, int Mm, int N, int K){
  __shared__ float As[16][65], Bs[16][65];
  int tx=threadIdx.x&15, ty=threadIdx.x>>4;
  int m0=blockIdx.y*64, n0=blockIdx.x*64;
  float acc[4][4]={};
  for(int k0=0;k0<K;k0+=16){
    for(int i=threadIdx.x;i<1024;i+=256){ int r=i>>4,c=i&15; As[c][r]=bf2f(A[(size_t)(m0+r)*K+k0+c]); }
    for(int i=threadIdx.x;i<1024;i+=256){ int r=i>>6,c=i&63; Bs[r][c]=bf2f(B[(size_t)(k0+r)*N+n0+c]); }
    __syncthreads();
    for(int k=0;k<16;k++){ float a[4],b[4]; for(int i=0;i<4;i++){a[i]=As[k][ty*4+i]; b[i]=Bs[k][tx*4+i];}
      for(int i=0;i<4;i++)for(int j=0;j<4;j++) acc[i][j]+=a[i]*b[j]; }
    __syncthreads();
  }
  for(int i=0;i<4;i++)for(int j=0;j<4;j++){ int r=m0+ty*4+i,c=n0+tx*4+j; float v=acc[i][j];
    if(MODE==0) ((u16*)C)[(size_t)r*N+c]=f2bf(v);
    if(MODE==1){ float t=v+bf2f(bias[c]); float u=0.7978845608f*(t+0.044715f*t*t*t); float g=0.5f*t*(1.f+tanhf(u)); ((u16*)C)[(size_t)r*N+c]=f2bf(g);}
    if(MODE==2) ((float*)C)[(size_t)r*N+c]=v;
    if(MODE==3) ((float*)C)[(size_t)r*N+c]+=v;
  }
}
__global__ void attn_naive(const u16* Q,const u16* Kk,const u16* V,u16* O){
  __shared__ float p[S]; __shared__ float qs[D]; __shared__ float red[128];
  int q=blockIdx.x,h=blockIdx.y,t=threadIdx.x;
  qs[t]=bf2f(Q[(size_t)q*H*D+h*D+t]); __syncthreads();
  float mx=-1e30f;
  for(int k=t;k<=q;k+=128){ float s=0; const u16* kr=Kk+(size_t)k*H*D+h*D; for(int d=0;d<D;d++) s+=qs[d]*bf2f(kr[d]); s*=0.08838834764f; p[k]=s; mx=fmaxf(mx,s);}
  red[t]=mx; __syncthreads(); for(int o=64;o>0;o>>=1){ if(t<o) red[t]=fmaxf(red[t],red[t+o]); __syncthreads(); } mx=red[0]; __syncthreads();
  float sum=0; for(int k=t;k<=q;k+=128){ float e=__expf(p[k]-mx); p[k]=e; sum+=e; }
  red[t]=sum; __syncthreads(); for(int o=64;o>0;o>>=1){ if(t<o) red[t]+=red[t+o]; __syncthreads(); } sum=red[0];
  float acc=0; for(int k=0;k<=q;k++) acc+=p[k]*bf2f(V[(size_t)k*H*D+h*D+t]);
  O[(size_t)q*H*D+h*D+t]=f2bf(acc/sum);
}
__global__ void fin(const u16* x,const float* acc,const u16* b2,u16* out){
  int i=blockIdx.x*256+threadIdx.x; out[i]=f2bf(bf2f(x[i])+acc[i]+bf2f(b2[i%M]));
}
extern "C" void kernel_launch(void* const* d_in, const int* in_sizes, int n_in, void* d_out, int out_size, void* d_ws, size_t ws_size, hipStream_t stream){
  const u16* x=(const u16*)d_in[0]; const u16* ckv=(const u16*)d_in[1]; const u16* wq=(const u16*)d_in[2]; const u16* wuk=(const u16*)d_in[3];
  const u16* wuv=(const u16*)d_in[4]; const u16* wo=(const u16*)d_in[5]; const u16* w1=(const u16*)d_in[7]; const u16* b1=(const u16*)d_in[8];
  const u16* w2=(const u16*)d_in[9]; const u16* b2=(const u16*)d_in[10];
  char* ws=(char*)d_ws; size_t o=0;
  u16* xn=(u16*)(ws+o); o+=S*M*2; u16* y=(u16*)(ws+o); o+=S*M*2;
  u16* Q=(u16*)(ws+o); o+=(size_t)S*H*D*2; u16* K=(u16*)(ws+o); o+=(size_t)S*H*D*2; u16* V=(u16*)(ws+o); o+=(size_t)S*H*D*2;
  u16* C=(u16*)(ws+o); o+=(size_t)S*H*D*2; u16* hh=(u16*)(ws+o); o+=(size_t)S*F*2; float* acc=(float*)(ws+o);
  ln_kernel<<<S,64,0,stream>>>(x,xn,y);
  gemm_naive<0><<<dim3(H*D/64,S/64),256,0,stream>>>(xn,wq,Q,nullptr,S,H*D,M);
  gemm_naive<0><<<dim3(H*D/64,S/64),256,0,stream>>>(ckv,wuk,K,nullptr,S,H*D,DC);
  gemm_naive<0><<<dim3(H*D/64,S/64),256,0,stream>>>(ckv,wuv,V,nullptr,S,H*D,DC);
  gemm_naive<1><<<dim3(F/64,S/64),256,0,stream>>>(y,w1,hh,b1,S,F,M);
  attn_naive<<<dim3(S,H),128,0,stream>>>(Q,K,V,C);
  gemm_naive<2><<<dim3(M/64,S/64),256,0,stream>>>(C,wo,acc,nullptr,S,M,H*D);
  gemm_naive<3><<<dim3(M/64,S/64),256,0,stream>>>(hh,w2,acc,nullptr,S,M,F);
  fin<<<S*M/256,256,0,stream>>>(x,acc,b2,(u16*)d_out);
}
```

```cpp
#include <hip/hip_runtime.h>
typedef unsigned short u16;

__global__ __launch_bounds__(256, 1) void mega_kernel(const u16* x, const u16* ckv, const u16* wq, const u16* wuk, const u16* wuv, const u16* wo, const u16* w1, const u16* b1, const u16* w2, const u16* b2, u16* out, char* ws) {
  __shared__ __attribute__((aligned(16))) char lds[163840];
  asm volatile("" :: "v"(lds) : "memory");
  asm volatile(
".macro M4 a:vararg\nv_mfma_f32_16x16x32_bf16 \\a\n.endm\n.macro M8 a:vararg\nv_mfma_f32_32x32x16_bf16 \\a\n.endm\n.macro AR a:vararg\nv_accvgpr_read_b32 \\a\n"
".endm\n.macro AW a:vararg\nv_accvgpr_write_b32 \\a\n.endm\n.macro CV a:vararg\nv_cvt_pk_bf16_f32 \\a\n.endm\n.macro DR a:vararg\n"
"ds_read_b128 \\a\n.endm\n.macro DT a:vararg\nds_read_b64_tr_b16 \\a\n.endm\n.macro BL a:vararg\nbuffer_load_dwordx4 \\a\n.endm\n"
".macro BS a:vararg\nbuffer_store_dwordx4 \\a\n.endm\n.macro EX a:vararg\nv_exp_f32 \\a\n.endm\n.macro VA a:vararg\nv_add_f32 \\a\n"
".endm\n.macro SA a:vararg\ns_add_u32 \\a\n.endm\n.macro SW a:vararg\ns_waitcnt \\a\n.endm\n.macro PM a:vararg\n"
"v_pk_mul_f32 \\a\n.endm\n.macro PF a:vararg\nv_pk_fma_f32 \\a\n.endm\n.macro CM a:vararg\nv_cndmask_b32 \\a\n.endm\n"
".macro CG a:vararg\nv_cmp_ge_i32 \\a\n.endm\n.macro VS a:vararg\nv_sub_f32 \\a\n.endm\n.macro VU a:vararg\nv_add_u32 \\a\n"
".endm\n.macro VL a:vararg\nv_lshlrev_b32 \\a\n.endm\n.macro DW a:vararg\nds_write_b128 \\a\n.endm\n.macro VN a:vararg\n"
"v_and_b32 \\a\n.endm\n.macro VM a:vararg\nv_mul_f32 \\a\n.endm\n.macro SM a:vararg\ns_mov_b32 \\a\n.endm\n"
".macro RC a:vararg\nv_rcp_f32 \\a\n.endm\ns_getpc_b64 s[56:57]\nMEGA_PC:\ns_and_b32 s57,s57,0xffff\nSM s58,MEGA_END-MEGA_PC\nSM s59,0x20000\n"
"VL v240,7,%0\nbuffer_load_dword v241,v240,s[56:59],0 offen\ns_lshr_b32 s34,%1,3\ns_lshl_b32 s34,s34,12\nSA s34,s34,32768\nbuffer_load_dword v242,v240,s[56:59],s34 offen\ns_mov_b64 s[94:95],%2\nSM s88,%1\n"
"s_load_dwordx2 s[92:93],s[94:95],0x58\ns_load_dwordx8 s[40:47],s[94:95],0x0\ns_load_dwordx8 s[48:55],s[94:95],0x20\nVN v200,63,%0\nv_lshrrev_b32 v170,6,%0\ns_nop 0\nv_readfirstlane_b32 s89,v170\nSW lgkmcnt(0)\n"
"SA s71,s92,63963136\ns_addc_u32 s72,s93,0\nSM s96,s71\nSM s97,s72\ns_load_dwordx2 s[90:91],s[96:97],0x0\ns_and_b32 s71,s88,7\ns_lshr_b32 s72,s88,3\ns_and_b32 s63,s72,7\n"
"s_lshr_b32 s72,s72,3\ns_lshl_b32 s71,s71,2\nSA s62,s71,s72\ns_lshr_b32 s60,s89,1\ns_and_b32 s61,s89,1\ns_cmp_eq_u32 s60,0\ns_cselect_b32 s84,s46,s48\ns_cselect_b32 s85,s47,s49\n"
"SM s86,s42\nSM s87,s43\nSM s76,s40\nSM s77,s41\ns_lshl_b32 s69,s89,12\ns_lshl_b32 s70,s89,13\nSA s70,s70,65536\nv_lshrrev_b32 v170,3,v200\n"
"v_mul_u32_u24 v172,1024,v170\nVN v171,7,v200\nv_lshrrev_b32 v170,4,v200\nVU v173,0,v170\nv_xor_b32 v173,v173,v171\nv_lshl_add_u32 v173,v173,4,v172\nVU v152,0,v173\nVU v154,14336,v173\n"
"VU v173,4,v170\nv_xor_b32 v173,v173,v171\nv_lshl_add_u32 v173,v173,4,v172\nVU v153,7168,v173\nVU v155,21504,v173\nv_lshrrev_b32 v170,4,v200\nVN v171,15,v200\nv_mul_u32_u24 v172,8192,v170\n"
"v_or_b32 v173,0,v170\nVL v173,1,v173\nv_xor_b32 v173,v173,v171\nv_lshl_add_u32 v173,v173,4,v172\nVU v148,0,v173\nv_or_b32 v173,0,v170\nVL v173,1,v173\nv_xor_b32 v173,v173,v171\n"
"v_lshl_add_u32 v173,v173,4,v172\nVU v149,31744,v173\nv_or_b32 v173,4,v170\nVL v173,1,v173\nv_xor_b32 v173,v173,v171\nv_lshl_add_u32 v173,v173,4,v172\nVU v150,63488,v173\nv_or_b32 v173,4,v170\n"
"VL v173,1,v173\nv_xor_b32 v173,v173,v171\nv_lshl_add_u32 v173,v173,4,v172\nVU v151,95232,v173\ns_mul_i32 s64,s61,131072\ns_mul_i32 s65,s89,65536\ns_lshl_b32 s71,s62,8\ns_sub_u32 s72,4194304,s71\n"
"SA s36,s84,s71\ns_addc_u32 s37,s85,0\ns_and_b32 s37,s37,0xffff\nSM s38,s72\nSM s39,0x20000\ns_mul_i32 s71,s63,262144\nSA s40,s86,s71\ns_addc_u32 s41,s87,0\n"
"s_and_b32 s41,s41,0xffff\nSM s42,262144\nSM s43,0x20000\nSA m0,s69,0\ns_nop 0\nBL v148,s[36:39],s64 offen offset:0 lds\nBL v149,s[36:39],s64 offen offset:1024 lds\nBL v150,s[36:39],s64 offen offset:2048 lds\n"
"BL v151,s[36:39],s64 offen offset:3072 lds\nSA s64,s64,262144\nSA m0,s70,0\ns_nop 0\nBL v152,s[40:43],s65 offen offset:0 sc1 lds\nBL v153,s[40:43],s65 offen offset:1024 sc1 lds\nBL v154,s[40:43],s65 offen offset:2048 sc1 lds\nBL v155,s[40:43],s65 offen offset:3072 sc1 lds\n"
"SA m0,s70,4096\nSA s72,s65,32768\nBL v152,s[40:43],s72 offen offset:0 sc1 lds\nBL v153,s[40:43],s72 offen offset:1024 sc1 lds\nBL v154,s[40:43],s72 offen offset:2048 sc1 lds\nBL v155,s[40:43],s72 offen offset:3072 sc1 lds\nSA s65,s65,128\nv_lshrrev_b32 v235,5,v200\n"
"v_mul_u32_u24 v234,1280,v235\nVN v236,31,v200\nv_lshl_add_u32 v234,v236,3,v234\nVL v235,3,v235\ns_lshl_b32 s71,s88,2\nSA s71,s71,s89\ns_mul_i32 s78,s71,2560\ns_lshl_b32 s79,s71,4\n"
"SA s79,s79,2621440\nSM s44,s76\ns_and_b32 s45,s77,0xffff\nSM s46,2621440\nSM s47,0x20000\nbuffer_load_dwordx2 v[204:205],v234,s[44:47],s78 offen offset:0\nbuffer_load_dwordx2 v[206:207],v234,s[44:47],s78 offen offset:256\nbuffer_load_dwordx2 v[208:209],v234,s[44:47],s78 offen offset:512\n"
"buffer_load_dwordx2 v[210:211],v234,s[44:47],s78 offen offset:768\nbuffer_load_dwordx2 v[212:213],v234,s[44:47],s78 offen offset:1024\nSA m0,s69,16384\ns_nop 0\nBL v148,s[36:39],s64 offen offset:0 lds\nBL v149,s[36:39],s64 offen offset:1024 lds\nBL v150,s[36:39],s64 offen offset:2048 lds\nBL v151,s[36:39],s64 offen offset:3072 lds\n"
"SA s64,s64,262144\ns_lshl_b32 s73,s62,6\ns_lshl_b32 s71,s63,3\nSA s73,s73,s71\ns_lshl_b32 s71,s60,2\nSA s73,s73,s71\ns_lshl_b32 s80,s73,13\nVN v170,15,v200\n"
"v_lshrrev_b32 v171,4,v200\nv_lshrrev_b32 v172,1,v170\ns_lshl_b32 s71,s60,14\nSA s71,s71,65536\nVU v173,0,v171\nv_xor_b32 v173,v173,v172\nVL v173,4,v173\nv_lshl_add_u32 v173,v170,7,v173\n"
"VU v146,s71,v173\nv_mov_b32 v144,v146\nVU v173,4,v171\nv_xor_b32 v173,v173,v172\nVL v173,4,v173\nv_lshl_add_u32 v173,v170,7,v173\nVU v147,s71,v173\nv_mov_b32 v145,v147\n"
"v_lshrrev_b32 v174,4,v200\nv_bfe_u32 v175,v200,2,2\nv_lshl_add_u32 v170,v174,3,v175\nVN v171,3,v200\nVL v171,3,v171\nVN v174,1,v174\nv_lshl_or_b32 v172,v174,2,v175\nv_lshrrev_b32 v175,1,v175\n"
"v_lshl_or_b32 v173,v174,1,v175\ns_lshl_b32 s71,s61,13\nv_lshl_add_u32 v176,v170,8,v171\nv_xor_b32 v177,0,v172\nv_lshl_add_u32 v177,v177,5,v176\nVU v136,s71,v177\nv_xor_b32 v177,1,v172\nv_lshl_add_u32 v177,v177,5,v176\n"
"VU v137,s71,v177\nv_xor_b32 v177,2,v172\nv_lshl_add_u32 v177,v177,5,v176\nVU v138,s71,v177\nv_xor_b32 v177,3,v172\nv_lshl_add_u32 v177,v177,5,v176\nVU v139,s71,v177\nv_xor_b32 v177,4,v172\n"
"v_lshl_add_u32 v177,v177,5,v176\nVU v140,s71,v177\nv_xor_b32 v177,5,v172\nv_lshl_add_u32 v177,v177,5,v176\nVU v141,s71,v177\nv_xor_b32 v177,6,v172\nv_lshl_add_u32 v177,v177,5,v176\nVU v142,s71,v177\n"
"v_xor_b32 v177,7,v172\nv_lshl_add_u32 v177,v177,5,v176\nVU v143,s71,v177\ns_lshl_b32 s71,s61,2\nSA s72,s71,0\nv_xor_b32 v177,s72,v172\nv_lshl_add_u32 v164,v177,5,v176\nSA s72,s71,1\n"
"v_xor_b32 v177,s72,v172\nv_lshl_add_u32 v165,v177,5,v176\nSA s72,s71,2\nv_xor_b32 v177,s72,v172\nv_lshl_add_u32 v166,v177,5,v176\nSA s72,s71,3\nv_xor_b32 v177,s72,v172\nv_lshl_add_u32 v167,v177,5,v176\n"
"v_lshl_add_u32 v176,v170,7,v171\nVU v176,8192,v176\ns_lshl_b32 s71,s61,1\nSA s72,s71,0\nv_xor_b32 v177,s72,v173\nv_lshl_add_u32 v168,v177,5,v176\nSA s72,s71,1\nv_xor_b32 v177,s72,v173\n"
"v_lshl_add_u32 v169,v177,5,v176\ns_load_dwordx2 s[84:85],s[94:95],0x10\ns_load_dwordx2 s[86:87],s[94:95],0x30\ns_and_b32 s73,s89,1\ns_lshl_b32 s74,s73,2\nv_lshrrev_b32 v170,4,v200\nVN v171,15,v200\nv_mul_u32_u24 v172,8192,v170\n"
"v_or_b32 v173,s74,v170\nVL v173,1,v173\nv_xor_b32 v173,v173,v171\nv_lshl_add_u32 v173,v173,4,v172\nVU v156,0,v173\nv_or_b32 v173,s74,v170\nVL v173,1,v173\nv_xor_b32 v173,v173,v171\n"
"v_lshl_add_u32 v173,v173,4,v172\nVU v157,31744,v173\nv_bfe_u32 v170,v200,4,1\ns_lshl_b32 s74,s73,1\nv_or_b32 v170,s74,v170\nVL v170,1,v170\nVN v171,7,v200\nv_xor_b32 v170,v170,v171\n"
"v_lshrrev_b32 v171,3,v200\nv_mul_u32_u24 v171,3456,v171\nv_lshl_add_u32 v158,v170,4,v171\ns_lshl_b32 s81,s89,11\ns_lshl_b32 s82,s89,10\nSA s82,s82,8192\ns_mul_i32 s66,s89,65536\ns_mul_i32 s67,s89,27648\n"
"v_lshrrev_b32 v170,3,v200\nv_mul_u32_u24 v172,1280,v170\nVN v171,7,v200\nv_lshrrev_b32 v170,4,v200\nVU v173,0,v170\nv_xor_b32 v173,v173,v171\nv_lshl_add_u32 v173,v173,4,v172\nVU v160,0,v173\n"
"VU v162,18432,v173\nVU v173,4,v170\nv_xor_b32 v173,v173,v171\nv_lshl_add_u32 v173,v173,4,v172\nVU v161,9216,v173\nVU v163,27648,v173\ns_sub_u32 s71,15,s63\ns_cmp_eq_u32 s60,0\n"
"s_cselect_b32 s71,s71,s63\ns_lshl_b32 s71,s71,7\ns_lshl_b32 s72,s61,6\nSA s71,s71,s72\ns_mul_i32 s68,s71,1280\nSW lgkmcnt(0)\nSA s90,s90,1\ns_addc_u32 s91,s91,0\n"
"s_lshl_b32 s71,s62,8\ns_sub_u32 s72,5242880,s71\nSA s48,s84,s71\ns_addc_u32 s49,s85,0\ns_and_b32 s49,s49,0xffff\nSM s50,s72\nSM s51,0x20000\ns_lshl_b32 s71,s62,7\n"
"s_sub_u32 s72,2211840,s71\nSA s52,s86,s71\ns_addc_u32 s53,s87,0\ns_and_b32 s53,s53,0xffff\nSM s54,s72\nSM s55,0x20000\ns_cmp_ge_u32 s62,27\ns_cselect_b32 s54,0,s54\n"
"SM s71,0\nSA s56,s92,s71\ns_addc_u32 s57,s93,0\ns_and_b32 s57,s57,0xffff\nSM s58,2621440\nSM s59,0x20000\nSA s96,s92,s71\ns_addc_u32 s97,s93,0\n"
"s_and_b32 s97,s97,0xffff\nSM s98,5242880\nSM s99,0x20000\ns_lshl_b32 s71,s89,6\nVU v170,s71,v200\ns_lshl_b32 s72,s63,3\nv_lshrrev_b32 v171,1,v170\nVU v171,s72,v171\n"
"v_mul_u32_u24 v171,8192,v171\nVN v172,1,v170\nv_lshl_add_u32 v171,v172,7,v171\nv_cmp_gt_u32 vcc,16,v170\nv_mov_b32 v173,0x7f000000\ns_nop 0\nCM v250,v173,v171,vcc\nv_cmp_gt_u32 vcc,8,v170\n"
"VU v171,s72,v170\nv_mul_u32_u24 v171,3456,v171\ns_nop 0\nCM v251,v173,v171,vcc\ns_mul_i32 s72,s88,228\nVU v171,s72,v170\nv_cmp_gt_u32 vcc,228,v170\nVL v171,7,v171\n"
"s_nop 0\nCM v202,v173,v171,vcc\nv_subrev_u32 v171,5242880,v202\nv_cmp_gt_u32 vcc,2211840,v171\ns_nop 1\nCM v203,v173,v171,vcc\nSM s76,0\nSM s77,0\n"
"SW vmcnt(9)\ns_barrier\nDT v[40:41],v136 offset:0\nDT v[42:43],v136 offset:1024\nDR v[8:11],v144 offset:0\nDR v[12:15],v144 offset:2048\nDR v[16:19],v144 offset:4096\nDR v[20:23],v144 offset:6144\n"
"DR v[24:27],v144 offset:8192\nDR v[28:31],v144 offset:10240\nDR v[32:35],v144 offset:12288\nDR v[36:39],v144 offset:14336\nDT v[44:45],v137 offset:0\nDT v[46:47],v137 offset:1024\nDT v[48:49],v138 offset:0\nDT v[50:51],v138 offset:1024\n"
"DT v[52:53],v139 offset:0\nDT v[54:55],v139 offset:1024\nDT v[56:57],v140 offset:0\nDT v[58:59],v140 offset:1024\nDT v[60:61],v141 offset:0\nDT v[62:63],v141 offset:1024\nDT v[64:65],v142 offset:0\nDT v[66:67],v142 offset:1024\n"
"DT v[68:69],v143 offset:0\nDT v[70:71],v143 offset:1024\nSW vmcnt(0) lgkmcnt(0)\ns_barrier\ns_cmp_eq_u32 s61,0\ns_cbranch_scc0 P1_V\nM4 a[0:3],v[40:43],v[8:11],0\nDT v[104:105],v136 offset:16384\n"
"M4 a[4:7],v[40:43],v[12:15],0\nDT v[106:107],v136 offset:17408\nSA m0,s70,32768\nVL v214,16,v204\nVN v215,0xffff0000,v204\nM4 a[8:11],v[40:43],v[16:19],0\nVL v216,16,v205\nVN v217,0xffff0000,v205\n"
"M4 a[12:15],v[40:43],v[20:23],0\nDR v[72:75],v145 offset:0\nBL v152,s[40:43],s65 offen offset:0 sc1 lds\nVL v218,16,v206\nM4 a[16:19],v[40:43],v[24:27],0\nVN v219,0xffff0000,v206\nVL v220,16,v207\nM4 a[20:23],v[40:43],v[28:31],0\n"
"DR v[76:79],v145 offset:2048\nVN v221,0xffff0000,v207\nVL v222,16,v208\nM4 a[24:27],v[40:43],v[32:35],0\nBL v153,s[40:43],s65 offen offset:1024 sc1 lds\nVN v223,0xffff0000,v208\nM4 a[28:31],v[40:43],v[36:39],0\nDR v[80:83],v145 offset:4096\n"
"VL v224,16,v209\nVN v225,0xffff0000,v209\nM4 a[32:35],v[44:47],v[8:11],0\nBL v154,s[40:43],s65 offen offset:2048 sc1 lds\nVL v226,16,v210\nM4 a[36:39],v[44:47],v[12:15],0\nDR v[84:87],v145 offset:6144\nVN v227,0xffff0000,v210\n"
"VL v228,16,v211\nM4 a[40:43],v[44:47],v[16:19],0\nVN v229,0xffff0000,v211\nVL v230,16,v212\nM4 a[44:47],v[44:47],v[20:23],0\nDR v[88:91],v145 offset:8192\nBL v155,s[40:43],s65 offen offset:3072 sc1 lds\nVN v231,0xffff0000,v212\n"
"M4 a[48:51],v[44:47],v[24:27],0\nVL v232,16,v213\nVN v233,0xffff0000,v213\nM4 a[52:55],v[44:47],v[28:31],0\nDR v[92:95],v145 offset:10240\nSA m0,s69,32768\nVA v236,v214,v215\nM4 a[56:59],v[44:47],v[32:35],0\n"
"VA v236,v236,v216\nVA v236,v236,v217\nM4 a[60:63],v[44:47],v[36:39],0\nDR v[96:99],v145 offset:12288\nVA v236,v236,v218\nVA v236,v236,v219\nM4 a[64:67],v[48:51],v[8:11],0\nBL v148,s[36:39],s64 offen offset:0 lds\n"
"VA v236,v236,v220\nM4 a[68:71],v[48:51],v[12:15],0\nDR v[100:103],v145 offset:14336\nVA v236,v236,v221\nVA v236,v236,v222\nM4 a[72:75],v[48:51],v[16:19],0\nBL v149,s[36:39],s64 offen offset:1024 lds\nVA v236,v236,v223\n"
"VA v236,v236,v224\nM4 a[76:79],v[48:51],v[20:23],0\nDT v[108:109],v137 offset:16384\nVA v236,v236,v225\nM4 a[80:83],v[48:51],v[24:27],0\nVA v236,v236,v226\nVA v236,v236,v227\nM4 a[84:87],v[48:51],v[28:31],0\n"
"DT v[110:111],v137 offset:17408\nBL v150,s[36:39],s64 offen offset:2048 lds\nVA v236,v236,v228\nM4 a[88:91],v[48:51],v[32:35],0\nVA v236,v236,v229\nVA v236,v236,v230\nM4 a[92:95],v[48:51],v[36:39],0\nDT v[112:113],v138 offset:16384\n"
"BL v151,s[36:39],s64 offen offset:3072 lds\nVA v236,v236,v231\nVA v236,v236,v232\nM4 a[96:99],v[52:55],v[8:11],0\nDT v[114:115],v138 offset:17408\nVA v236,v236,v233\nM4 a[100:103],v[52:55],v[12:15],0\ns_nop 0\n"
"v_add_f32_dpp v236,v236,v236 row_ror:8 row_mask:0xf bank_mask:0xf\ns_nop 1\nv_add_f32_dpp v236,v236,v236 row_ror:4 row_mask:0xf bank_mask:0xf\nM4 a[104:107],v[52:55],v[16:19],0\nDT v[116:117],v139 offset:16384\nSA s64,s64,262144\nv_add_f32_dpp v236,v236,v236 row_ror:2 row_mask:0xf bank_mask:0xf\nM4 a[108:111],v[52:55],v[20:23],0\n"
"s_nop 0\nv_add_f32_dpp v236,v236,v236 row_ror:1 row_mask:0xf bank_mask:0xf\nv_mov_b32 v237,v236\nM4 a[112:115],v[52:55],v[24:27],0\nDT v[118:119],v139 offset:17408\nSA m0,s70,36864\nv_permlane16_swap_b32 v236,v237\nVA v236,v236,v237\n"
"M4 a[116:119],v[52:55],v[28:31],0\nVM v236,0x3acccccd,v236\nM4 a[120:123],v[52:55],v[32:35],0\nDT v[120:121],v140 offset:16384\nVS v238,v214,v236\nVM v239,v238,v238\nM4 a[124:127],v[52:55],v[36:39],0\nSA s72,s65,32768\n"
"VS v238,v215,v236\nM4 a[128:131],v[56:59],v[8:11],0\nDT v[122:123],v140 offset:17408\nv_fmac_f32 v239,v238,v238\nVS v238,v216,v236\nM4 a[132:135],v[56:59],v[12:15],0\nBL v152,s[40:43],s72 offen offset:0 sc1 lds\nv_fmac_f32 v239,v238,v238\n"
"VS v238,v217,v236\nM4 a[136:139],v[56:59],v[16:19],0\nDT v[124:125],v141 offset:16384\nv_fmac_f32 v239,v238,v238\nM4 a[140:143],v[56:59],v[20:23],0\nVS v238,v218,v236\nv_fmac_f32 v239,v238,v238\nM4 a[144:147],v[56:59],v[24:27],0\n"
"DT v[126:127],v141 offset:17408\nBL v153,s[40:43],s72 offen offset:1024 sc1 lds\nVS v238,v219,v236\nv_fmac_f32 v239,v238,v238\nM4 a[148:151],v[56:59],v[28:31],0\nVS v238,v220,v236\nM4 a[152:155],v[56:59],v[32:35],0\nDT v[128:129],v142 offset:16384\n"
"BL v154,s[40:43],s72 offen offset:2048 sc1 lds\nv_fmac_f32 v239,v238,v238\nVS v238,v221,v236\nM4 a[156:159],v[56:59],v[36:39],0\nv_fmac_f32 v239,v238,v238\nM4 a[160:163],v[60:63],v[8:11],0\nDT v[130:131],v142 offset:17408\nVS v238,v222,v236\n"
"v_fmac_f32 v239,v238,v238\nM4 a[164:167],v[60:63],v[12:15],0\nBL v155,s[40:43],s72 offen offset:3072 sc1 lds\nVS v238,v223,v236\nv_fmac_f32 v239,v238,v238\nM4 a[168:171],v[60:63],v[16:19],0\nDT v[132:133],v143 offset:16384\nVS v238,v224,v236\n"
"M4 a[172:175],v[60:63],v[20:23],0\nSA s65,s65,128\nv_fmac_f32 v239,v238,v238\nVS v238,v225,v236\nM4 a[176:179],v[60:63],v[24:27],0\nDT v[134:135],v143 offset:17408\nv_fmac_f32 v239,v238,v238\nM4 a[180:183],v[60:63],v[28:31],0\n"
"VS v238,v226,v236\nv_fmac_f32 v239,v238,v238\nM4 a[184:187],v[60:63],v[32:35],0\nSA m0,s69,49152\nVS v238,v227,v236\nv_fmac_f32 v239,v238,v238\nM4 a[188:191],v[60:63],v[36:39],0\nVS v238,v228,v236\n"
"M4 a[192:195],v[64:67],v[8:11],0\nBL v148,s[36:39],s64 offen offset:0 lds\nv_fmac_f32 v239,v238,v238\nVS v238,v229,v236\nM4 a[196:199],v[64:67],v[12:15],0\nv_fmac_f32 v239,v238,v238\nVS v238,v230,v236\nM4 a[200:203],v[64:67],v[16:19],0\n"
"v_fmac_f32 v239,v238,v238\nM4 a[204:207],v[64:67],v[20:23],0\nBL v149,s[36:39],s64 offen offset:1024 lds\nVS v238,v231,v236\nv_fmac_f32 v239,v238,v238\nM4 a[208:211],v[64:67],v[24:27],0\nVS v238,v232,v236\nM4 a[212:215],v[64:67],v[28:31],0\n"
"BL v150,s[36:39],s64 offen offset:2048 lds\nv_fmac_f32 v239,v238,v238\nVS v238,v233,v236\nM4 a[216:219],v[64:67],v[32:35],0\nv_fmac_f32 v239,v238,v238\ns_nop 1\nv_add_f32_dpp v239,v239,v239 row_ror:8 row_mask:0xf bank_mask:0xf\nM4 a[220:223],v[64:67],v[36:39],0\n"
"s_nop 0\nv_add_f32_dpp v239,v239,v239 row_ror:4 row_mask:0xf bank_mask:0xf\nM4 a[224:227],v[68:71],v[8:11],0\nBL v151,s[36:39],s64 offen offset:3072 lds\nv_add_f32_dpp v239,v239,v239 row_ror:2 row_mask:0xf bank_mask:0xf\ns_nop 1\nv_add_f32_dpp v239,v239,v239 row_ror:1 row_mask:0xf bank_mask:0xf\nM4 a[228:231],v[68:71],v[12:15],0\n"
"v_mov_b32 v237,v239\nM4 a[232:235],v[68:71],v[16:19],0\nSA s64,s64,262144\nv_permlane16_swap_b32 v239,v237\nVA v239,v239,v237\nM4 a[236:239],v[68:71],v[20:23],0\nVM v239,0x3acccccd,v239\nCV v237,v236,v236\n"
"M4 a[240:243],v[68:71],v[24:27],0\nVL v236,16,v237\nM4 a[244:247],v[68:71],v[28:31],0\nCV v237,v239,v239\nVL v239,16,v237\nM4 a[248:251],v[68:71],v[32:35],0\nVA v239,0x3727c5ac,v239\nM4 a[252:255],v[68:71],v[36:39],0\n"
"SW vmcnt(4) lgkmcnt(0)\ns_barrier\nM4 a[0:3],v[104:107],v[72:75],a[0:3]\nVU v144,32768,v146\nM4 a[4:7],v[104:107],v[76:79],a[4:7]\nVU v145,32768,v147\nCV v237,v239,v239\nVL v239,16,v237\n"
"M4 a[8:11],v[104:107],v[80:83],a[8:11]\nDT v[40:41],v136 offset:32768\nv_sqrt_f32 v239,v239\ns_nop 0\nCV v237,v239,v239\nM4 a[12:15],v[104:107],v[84:87],a[12:15]\nDT v[42:43],v136 offset:33792\nSA m0,s70,65536\n"
"VL v239,16,v237\nRC v239,v239\nM4 a[16:19],v[104:107],v[88:91],a[16:19]\nVS v214,v214,v236\nM4 a[20:23],v[104:107],v[92:95],a[20:23]\nDR v[8:11],v144 offset:0\nBL v152,s[40:43],s65 offen offset:0 sc1 lds\nVS v215,v215,v236\n"
"VS v216,v216,v236\nM4 a[24:27],v[104:107],v[96:99],a[24:27]\nVS v217,v217,v236\nVS v218,v218,v236\nM4 a[28:31],v[104:107],v[100:103],a[28:31]\nDR v[12:15],v144 offset:2048\nBL v153,s[40:43],s65 offen offset:1024 sc1 lds\nVS v219,v219,v236\n"
"M4 a[32:35],v[108:111],v[72:75],a[32:35]\nVS v220,v220,v236\nVS v221,v221,v236\nM4 a[36:39],v[108:111],v[76:79],a[36:39]\nDR v[16:19],v144 offset:4096\nBL v154,s[40:43],s65 offen offset:2048 sc1 lds\nVS v222,v222,v236\nVS v223,v223,v236\n"
"M4 a[40:43],v[108:111],v[80:83],a[40:43]\nVS v224,v224,v236\nM4 a[44:47],v[108:111],v[84:87],a[44:47]\nDR v[20:23],v144 offset:6144\nBL v155,s[40:43],s65 offen offset:3072 sc1 lds\nVS v225,v225,v236\nVS v226,v226,v236\nM4 a[48:51],v[108:111],v[88:91],a[48:51]\n"
"VS v227,v227,v236\nVS v228,v228,v236\nM4 a[52:55],v[108:111],v[92:95],a[52:55]\nDR v[24:27],v144 offset:8192\nSA m0,s69,0\nVS v229,v229,v236\nM4 a[56:59],v[108:111],v[96:99],a[56:59]\nDR v[28:31],v144 offset:10240\n"
"VS v230,v230,v236\nVS v231,v231,v236\nM4 a[60:63],v[108:111],v[100:103],a[60:63]\nBL v148,s[36:39],s64 offen offset:0 lds\nVS v232,v232,v236\nVS v233,v233,v236\nM4 a[64:67],v[112:115],v[72:75],a[64:67]\nDR v[32:35],v144 offset:12288\n"
"CV v204,v214,v215\nM4 a[68:71],v[112:115],v[76:79],a[68:71]\nBL v149,s[36:39],s64 offen offset:1024 lds\nCV v205,v216,v217\nCV v206,v218,v219\nM4 a[72:75],v[112:115],v[80:83],a[72:75]\nDR v[36:39],v144 offset:14336\nCV v207,v220,v221\n"
"CV v208,v222,v223\nM4 a[76:79],v[112:115],v[84:87],a[76:79]\nBL v150,s[36:39],s64 offen offset:2048 lds\nCV v209,v224,v225\nM4 a[80:83],v[112:115],v[88:91],a[80:83]\nDT v[44:45],v137 offset:32768\nCV v210,v226,v227\nCV v211,v228,v229\n"
"M4 a[84:87],v[112:115],v[92:95],a[84:87]\nBL v151,s[36:39],s64 offen offset:3072 lds\nCV v212,v230,v231\nCV v213,v232,v233\nM4 a[88:91],v[112:115],v[96:99],a[88:91]\nDT v[46:47],v137 offset:33792\nVL v214,16,v204\nM4 a[92:95],v[112:115],v[100:103],a[92:95]\n"
"SA s64,s64,262144\nVN v215,0xffff0000,v204\nVL v216,16,v205\nM4 a[96:99],v[116:119],v[72:75],a[96:99]\nDT v[48:49],v138 offset:32768\nVN v217,0xffff0000,v205\nVL v218,16,v206\nM4 a[100:103],v[116:119],v[76:79],a[100:103]\n"
"DT v[50:51],v138 offset:33792\nSA m0,s70,69632\nVN v219,0xffff0000,v206\nM4 a[104:107],v[116:119],v[80:83],a[104:107]\nVL v220,16,v207\nVN v221,0xffff0000,v207\nM4 a[108:111],v[116:119],v[84:87],a[108:111]\nDT v[52:53],v139 offset:32768\n"
"SA s72,s65,32768\nVL v222,16,v208\nVN v223,0xffff0000,v208\nM4 a[112:115],v[116:119],v[88:91],a[112:115]\nVL v224,16,v209\nM4 a[116:119],v[116:119],v[92:95],a[116:119]\nDT v[54:55],v139 offset:33792\nBL v152,s[40:43],s72 offen offset:0 sc1 lds\n"
"VN v225,0xffff0000,v209\nVL v226,16,v210\nM4 a[120:123],v[116:119],v[96:99],a[120:123]\nVN v227,0xffff0000,v210\nVL v228,16,v211\nM4 a[124:127],v[116:119],v[100:103],a[124:127]\nDT v[56:57],v140 offset:32768\nBL v153,s[40:43],s72 offen offset:1024 sc1 lds\n"
"VN v229,0xffff0000,v211\nM4 a[128:131],v[120:123],v[72:75],a[128:131]\nVL v230,16,v212\nVN v231,0xffff0000,v212\nM4 a[132:135],v[120:123],v[76:79],a[132:135]\nDT v[58:59],v140 offset:33792\nBL v154,s[40:43],s72 offen offset:2048 sc1 lds\nVL v232,16,v213\n"
"VN v233,0xffff0000,v213\nM4 a[136:139],v[120:123],v[80:83],a[136:139]\nVM v214,v214,v239\nVM v215,v215,v239\nM4 a[140:143],v[120:123],v[84:87],a[140:143]\nDT v[60:61],v141 offset:32768\nBL v155,s[40:43],s72 offen offset:3072 sc1 lds\nVM v216,v216,v239\n"
"M4 a[144:147],v[120:123],v[88:91],a[144:147]\nDT v[62:63],v141 offset:33792\nVM v217,v217,v239\nVM v218,v218,v239\nM4 a[148:151],v[120:123],v[92:95],a[148:151]\nSA s65,s65,128\nVM v219,v219,v239\nVM v220,v220,v239\n"
"M4 a[152:155],v[120:123],v[96:99],a[152:155]\nDT v[64:65],v142 offset:32768\nVM v221,v221,v239\nM4 a[156:159],v[120:123],v[100:103],a[156:159]\nSA m0,s69,16384\nVM v222,v222,v239\nVM v223,v223,v239\nM4 a[160:163],v[124:127],v[72:75],a[160:163]\n"
"DT v[66:67],v142 offset:33792\nVM v224,v224,v239\nVM v225,v225,v239\nM4 a[164:167],v[124:127],v[76:79],a[164:167]\nBL v148,s[36:39],s64 offen offset:0 lds\nVM v226,v226,v239\nM4 a[168:171],v[124:127],v[80:83],a[168:171]\nDT v[68:69],v143 offset:32768\n"
"VM v227,v227,v239\nVM v228,v228,v239\nM4 a[172:175],v[124:127],v[84:87],a[172:175]\nBL v149,s[36:39],s64 offen offset:1024 lds\nVM v229,v229,v239\nVM v230,v230,v239\nM4 a[176:179],v[124:127],v[88:91],a[176:179]\nDT v[70:71],v143 offset:33792\n"
"VM v231,v231,v239\nM4 a[180:183],v[124:127],v[92:95],a[180:183]\nBL v150,s[36:39],s64 offen offset:2048 lds\nVM v232,v232,v239\nVM v233,v233,v239\nM4 a[184:187],v[124:127],v[96:99],a[184:187]\nCV v204,v214,v215\nCV v205,v216,v217\n"
"M4 a[188:191],v[124:127],v[100:103],a[188:191]\nBL v151,s[36:39],s64 offen offset:3072 lds\nCV v206,v218,v219\nM4 a[192:195],v[128:131],v[72:75],a[192:195]\nCV v207,v220,v221\nCV v208,v222,v223\nM4 a[196:199],v[128:131],v[76:79],a[196:199]\nSA s64,s64,262144\n"
"CV v209,v224,v225\nCV v210,v226,v227\nM4 a[200:203],v[128:131],v[80:83],a[200:203]\nCV v211,v228,v229\nM4 a[204:207],v[128:131],v[84:87],a[204:207]\nSA m0,s70,0\nCV v212,v230,v231\nCV v213,v232,v233\n"
"M4 a[208:211],v[128:131],v[88:91],a[208:211]\nbuffer_store_dwordx2 v[204:205],v234,s[96:99],s78 offen offset:0 sc1\nbuffer_store_dwordx2 v[206:207],v234,s[96:99],s78 offen offset:256 sc1\nM4 a[212:215],v[128:131],v[92:95],a[212:215]\nBL v152,s[40:43],s65 offen offset:0 sc1 lds\nbuffer_store_dwordx2 v[208:209],v234,s[96:99],s78 offen offset:512 sc1\nM4 a[216:219],v[128:131],v[96:99],a[216:219]\nbuffer_store_dwordx2 v[210:211],v234,s[96:99],s78 offen offset:768 sc1\n"
"buffer_store_dwordx2 v[212:213],v234,s[96:99],s78 offen offset:1024 sc1\nM4 a[220:223],v[128:131],v[100:103],a[220:223]\nBL v153,s[40:43],s65 offen offset:1024 sc1 lds\nVL v214,16,v204\nVN v215,0xffff0000,v204\nM4 a[224:227],v[132:135],v[72:75],a[224:227]\nVL v216,16,v205\nM4 a[228:231],v[132:135],v[76:79],a[228:231]\n"
"BL v154,s[40:43],s65 offen offset:2048 sc1 lds\nVN v217,0xffff0000,v205\nVL v218,16,v206\nM4 a[232:235],v[132:135],v[80:83],a[232:235]\nVN v219,0xffff0000,v206\nVL v220,16,v207\nM4 a[236:239],v[132:135],v[84:87],a[236:239]\nBL v155,s[40:43],s65 offen offset:3072 sc1 lds\n"
"VN v221,0xffff0000,v207\nM4 a[240:243],v[132:135],v[88:91],a[240:243]\nVL v222,16,v208\nbuffer_load_dword v244,v250,s[48:51],s76 offen\nM4 a[244:247],v[132:135],v[92:95],a[244:247]\nbuffer_load_dword v245,v251,s[52:55],s77 offen\nSA s76,s76,524288\nM4 a[248:251],v[132:135],v[96:99],a[248:251]\n"
"SA s77,s77,221184\nM4 a[252:255],v[132:135],v[100:103],a[252:255]\nSW vmcnt(27) lgkmcnt(0)\ns_barrier\nM4 a[0:3],v[40:43],v[8:11],a[0:3]\nDT v[104:105],v136 offset:49152\nM4 a[4:7],v[40:43],v[12:15],a[4:7]\nDT v[106:107],v136 offset:50176\n"
"SA m0,s69,32768\nVN v223,0xffff0000,v208\nVL v224,16,v209\nM4 a[8:11],v[40:43],v[16:19],a[8:11]\nVN v225,0xffff0000,v209\nVL v226,16,v210\nM4 a[12:15],v[40:43],v[20:23],a[12:15]\nDR v[72:75],v145 offset:0\n"
"VN v227,0xffff0000,v210\nVL v228,16,v211\nM4 a[16:19],v[40:43],v[24:27],a[16:19]\nVN v229,0xffff0000,v211\nM4 a[20:23],v[40:43],v[28:31],a[20:23]\nDR v[76:79],v145 offset:2048\nBL v148,s[36:39],s64 offen offset:0 lds\nVL v230,16,v212\n"
"VN v231,0xffff0000,v212\nM4 a[24:27],v[40:43],v[32:35],a[24:27]\nVL v232,16,v213\nVN v233,0xffff0000,v213\nM4 a[28:31],v[40:43],v[36:39],a[28:31]\nDR v[80:83],v145 offset:4096\nVA v236,v214,v215\nM4 a[32:35],v[44:47],v[8:11],a[32:35]\n"
"VA v236,v236,v216\nVA v236,v236,v217\nM4 a[36:39],v[44:47],v[12:15],a[36:39]\nDR v[84:87],v145 offset:6144\nVA v236,v236,v218\nVA v236,v236,v219\nM4 a[40:43],v[44:47],v[16:19],a[40:43]\nBL v149,s[36:39],s64 offen offset:1024 lds\n"
"VA v236,v236,v220\nM4 a[44:47],v[44:47],v[20:23],a[44:47]\nDR v[88:91],v145 offset:8192\nVA v236,v236,v221\nVA v236,v236,v222\nM4 a[48:51],v[44:47],v[24:27],a[48:51]\nVA v236,v236,v223\nVA v236,v236,v224\n"
"M4 a[52:55],v[44:47],v[28:31],a[52:55]\nDR v[92:95],v145 offset:10240\nVA v236,v236,v225\nM4 a[56:59],v[44:47],v[32:35],a[56:59]\nBL v150,s[36:39],s64 offen offset:2048 lds\nVA v236,v236,v226\nVA v236,v236,v227\nM4 a[60:63],v[44:47],v[36:39],a[60:63]\n"
"DR v[96:99],v145 offset:12288\nVA v236,v236,v228\nVA v236,v236,v229\nM4 a[64:67],v[48:51],v[8:11],a[64:67]\nVA v236,v236,v230\nM4 a[68:71],v[48:51],v[12:15],a[68:71]\nDR v[100:103],v145 offset:14336\nVA v236,v236,v231\n"
"VA v236,v236,v232\nM4 a[72:75],v[48:51],v[16:19],a[72:75]\nVA v236,v236,v233\ns_nop 1\nv_add_f32_dpp v236,v236,v236 row_ror:8 row_mask:0xf bank_mask:0xf\nM4 a[76:79],v[48:51],v[20:23],a[76:79]\nDT v[108:109],v137 offset:49152\nBL v151,s[36:39],s64 offen offset:3072 lds\n"
"v_add_f32_dpp v236,v236,v236 row_ror:4 row_mask:0xf bank_mask:0xf\nM4 a[80:83],v[48:51],v[24:27],a[80:83]\ns_nop 0\nv_add_f32_dpp v236,v236,v236 row_ror:2 row_mask:0xf bank_mask:0xf\ns_nop 1\nv_add_f32_dpp v236,v236,v236 row_ror:1 row_mask:0xf bank_mask:0xf\nM4 a[84:87],v[48:51],v[28:31],a[84:87]\nDT v[110:111],v137 offset:50176\n"
"v_mov_b32 v237,v236\ns_nop 1\nv_permlane16_swap_b32 v236,v237\nM4 a[88:91],v[48:51],v[32:35],a[88:91]\nVA v236,v236,v237\nM4 a[92:95],v[48:51],v[36:39],a[92:95]\nDT v[112:113],v138 offset:49152\nVM v236,0x3acccccd,v236\n"
"VS v238,v214,v236\nM4 a[96:99],v[52:55],v[8:11],a[96:99]\nDT v[114:115],v138 offset:50176\nSA s64,s64,262144\nVM v239,v238,v238\nVS v238,v215,v236\nM4 a[100:103],v[52:55],v[12:15],a[100:103]\nv_fmac_f32 v239,v238,v238\n"
"M4 a[104:107],v[52:55],v[16:19],a[104:107]\nDT v[116:117],v139 offset:49152\nVS v238,v216,v236\nv_fmac_f32 v239,v238,v238\nM4 a[108:111],v[52:55],v[20:23],a[108:111]\nVS v238,v217,v236\nv_fmac_f32 v239,v238,v238\nM4 a[112:115],v[52:55],v[24:27],a[112:115]\n"
"DT v[118:119],v139 offset:50176\nSA m0,s70,4096\nVS v238,v218,v236\nM4 a[116:119],v[52:55],v[28:31],a[116:119]\nv_fmac_f32 v239,v238,v238\nVS v238,v219,v236\nM4 a[120:123],v[52:55],v[32:35],a[120:123]\nDT v[120:121],v140 offset:49152\n"
"v_fmac_f32 v239,v238,v238\nVS v238,v220,v236\nM4 a[124:127],v[52:55],v[36:39],a[124:127]\nv_fmac_f32 v239,v238,v238\nM4 a[128:131],v[56:59],v[8:11],a[128:131]\nDT v[122:123],v140 offset:50176\nVS v238,v221,v236\nv_fmac_f32 v239,v238,v238\n"
"M4 a[132:135],v[56:59],v[12:15],a[132:135]\nSA s72,s65,32768\nVS v238,v222,v236\nv_fmac_f32 v239,v238,v238\nM4 a[136:139],v[56:59],v[16:19],a[136:139]\nDT v[124:125],v141 offset:49152\nVS v238,v223,v236\nv_fmac_f32 v239,v238,v238\n"
"M4 a[140:143],v[56:59],v[20:23],a[140:143]\nVS v238,v224,v236\nM4 a[144:147],v[56:59],v[24:27],a[144:147]\nDT v[126:127],v141 offset:50176\nv_fmac_f32 v239,v238,v238\nVS v238,v225,v236\nM4 a[148:151],v[56:59],v[28:31],a[148:151]\nBL v152,s[40:43],s72 offen offset:0 sc1 lds\n"
"v_fmac_f32 v239,v238,v238\nVS v238,v226,v236\nM4 a[152:155],v[56:59],v[32:35],a[152:155]\nDT v[128:129],v142 offset:49152\nv_fmac_f32 v239,v238,v238\nM4 a[156:159],v[56:59],v[36:39],a[156:159]\nVS v238,v227,v236\nv_fmac_f32 v239,v238,v238\n"
"M4 a[160:163],v[60:63],v[8:11],a[160:163]\nDT v[130:131],v142 offset:50176\nVS v238,v228,v236\nv_fmac_f32 v239,v238,v238\nM4 a[164:167],v[60:63],v[12:15],a[164:167]\nVS v238,v229,v236\nM4 a[168:171],v[60:63],v[16:19],a[168:171]\nDT v[132:133],v143 offset:49152\n"
"BL v153,s[40:43],s72 offen offset:1024 sc1 lds\nv_fmac_f32 v239,v238,v238\nVS v238,v230,v236\nM4 a[172:175],v[60:63],v[20:23],a[172:175]\nv_fmac_f32 v239,v238,v238\nVS v238,v231,v236\nM4 a[176:179],v[60:63],v[24:27],a[176:179]\nDT v[134:135],v143 offset:50176\n"
"v_fmac_f32 v239,v238,v238\nM4 a[180:183],v[60:63],v[28:31],a[180:183]\nVS v238,v232,v236\nv_fmac_f32 v239,v238,v238\nM4 a[184:187],v[60:63],v[32:35],a[184:187]\nVS v238,v233,v236\nv_fmac_f32 v239,v238,v238\nM4 a[188:191],v[60:63],v[36:39],a[188:191]\n"
"BL v154,s[40:43],s72 offen offset:2048 sc1 lds\nv_add_f32_dpp v239,v239,v239 row_ror:8 row_mask:0xf bank_mask:0xf\nM4 a[192:195],v[64:67],v[8:11],a[192:195]\ns_nop 0\nv_add_f32_dpp v239,v239,v239 row_ror:4 row_mask:0xf bank_mask:0xf\ns_nop 1\nv_add_f32_dpp v239,v239,v239 row_ror:2 row_mask:0xf bank_mask:0xf\nM4 a[196:199],v[64:67],v[12:15],a[196:199]\n"
"s_nop 0\nv_add_f32_dpp v239,v239,v239 row_ror:1 row_mask:0xf bank_mask:0xf\nv_mov_b32 v237,v239\nM4 a[200:203],v[64:67],v[16:19],a[200:203]\ns_nop 0\nv_permlane16_swap_b32 v239,v237\nM4 a[204:207],v[64:67],v[20:23],a[204:207]\nBL v155,s[40:43],s72 offen offset:3072 sc1 lds\n"
"VA v239,v239,v237\nVM v239,0x3acccccd,v239\nM4 a[208:211],v[64:67],v[24:27],a[208:211]\nCV v237,v236,v236\nVL v236,16,v237\nM4 a[212:215],v[64:67],v[28:31],a[212:215]\nCV v237,v239,v239\nM4 a[216:219],v[64:67],v[32:35],a[216:219]\n"
"VL v239,16,v237\nVA v239,0x3727c5ac,v239\nM4 a[220:223],v[64:67],v[36:39],a[220:223]\nCV v237,v239,v239\nVL v239,16,v237\nM4 a[224:227],v[68:71],v[8:11],a[224:227]\nSA s65,s65,128\nv_sqrt_f32 v239,v239\n"
"M4 a[228:231],v[68:71],v[12:15],a[228:231]\nCV v237,v239,v239\nVL v239,16,v237\nM4 a[232:235],v[68:71],v[16:19],a[232:235]\nRC v239,v239\ns_nop 0\nv_mov_b32 v240,v239\nM4 a[236:239],v[68:71],v[20:23],a[236:239]\n"
"VM v241,v236,v239\nM4 a[240:243],v[68:71],v[24:27],a[240:243]\nbuffer_store_dwordx2 v[240:241],v235,s[96:99],s79 offen sc1\nbuffer_load_dword v242,v250,s[48:51],s76 offen\nM4 a[244:247],v[68:71],v[28:31],a[244:247]\nbuffer_load_dword v243,v251,s[52:55],s77 offen\nSA s76,s76,524288\nM4 a[248:251],v[68:71],v[32:35],a[248:251]\n"
"SA s77,s77,221184\nM4 a[252:255],v[68:71],v[36:39],a[252:255]\nSW vmcnt(26) lgkmcnt(0)\ns_barrier\nM4 a[0:3],v[104:107],v[72:75],a[0:3]\nVU v144,65536,v146\nM4 a[4:7],v[104:107],v[76:79],a[4:7]\nVU v145,65536,v147\n"
"buffer_load_dword v244,v250,s[48:51],s76 offen\nM4 a[8:11],v[104:107],v[80:83],a[8:11]\nDT v[40:41],v136 offset:0\nM4 a[12:15],v[104:107],v[84:87],a[12:15]\nDT v[42:43],v136 offset:1024\nSA m0,s69,49152\nM4 a[16:19],v[104:107],v[88:91],a[16:19]\nM4 a[20:23],v[104:107],v[92:95],a[20:23]\n"
"DR v[8:11],v144 offset:0\nM4 a[24:27],v[104:107],v[96:99],a[24:27]\nM4 a[28:31],v[104:107],v[100:103],a[28:31]\nDR v[12:15],v144 offset:2048\nM4 a[32:35],v[108:111],v[72:75],a[32:35]\nBL v148,s[36:39],s64 offen offset:0 lds\nM4 a[36:39],v[108:111],v[76:79],a[36:39]\nDR v[16:19],v144 offset:4096\n"
"M4 a[40:43],v[108:111],v[80:83],a[40:43]\nM4 a[44:47],v[108:111],v[84:87],a[44:47]\nDR v[20:23],v144 offset:6144\nM4 a[48:51],v[108:111],v[88:91],a[48:51]\nM4 a[52:55],v[108:111],v[92:95],a[52:55]\nDR v[24:27],v144 offset:8192\nBL v149,s[36:39],s64 offen offset:1024 lds\nM4 a[56:59],v[108:111],v[96:99],a[56:59]\n"
"DR v[28:31],v144 offset:10240\nM4 a[60:63],v[108:111],v[100:103],a[60:63]\nM4 a[64:67],v[112:115],v[72:75],a[64:67]\nDR v[32:35],v144 offset:12288\nbuffer_load_dword v245,v251,s[52:55],s77 offen\nM4 a[68:71],v[112:115],v[76:79],a[68:71]\nM4 a[72:75],v[112:115],v[80:83],a[72:75]\nDR v[36:39],v144 offset:14336\n"
"BL v150,s[36:39],s64 offen offset:2048 lds\nM4 a[76:79],v[112:115],v[84:87],a[76:79]\nM4 a[80:83],v[112:115],v[88:91],a[80:83]\nDT v[44:45],v137 offset:0\nM4 a[84:87],v[112:115],v[92:95],a[84:87]\nM4 a[88:91],v[112:115],v[96:99],a[88:91]\nDT v[46:47],v137 offset:1024\nM4 a[92:95],v[112:115],v[100:103],a[92:95]\n"
"M4 a[96:99],v[116:119],v[72:75],a[96:99]\nDT v[48:49],v138 offset:0\nBL v151,s[36:39],s64 offen offset:3072 lds\nM4 a[100:103],v[116:119],v[76:79],a[100:103]\nDT v[50:51],v138 offset:1024\nM4 a[104:107],v[116:119],v[80:83],a[104:107]\nM4 a[108:111],v[116:119],v[84:87],a[108:111]\nDT v[52:53],v139 offset:0\n"
"M4 a[112:115],v[116:119],v[88:91],a[112:115]\nM4 a[116:119],v[116:119],v[92:95],a[116:119]\nDT v[54:55],v139 offset:1024\nSA s64,s64,262144\nM4 a[120:123],v[116:119],v[96:99],a[120:123]\nM4 a[124:127],v[116:119],v[100:103],a[124:127]\nDT v[56:57],v140 offset:0\nM4 a[128:131],v[120:123],v[72:75],a[128:131]\n"
"SA s76,s76,524288\nM4 a[132:135],v[120:123],v[76:79],a[132:135]\nDT v[58:59],v140 offset:1024\nM4 a[136:139],v[120:123],v[80:83],a[136:139]\nSA m0,s70,32768\nM4 a[140:143],v[120:123],v[84:87],a[140:143]\nDT v[60:61],v141 offset:0\nM4 a[144:147],v[120:123],v[88:91],a[144:147]\n"
"DT v[62:63],v141 offset:1024\nM4 a[148:151],v[120:123],v[92:95],a[148:151]\nM4 a[152:155],v[120:123],v[96:99],a[152:155]\nDT v[64:65],v142 offset:0\nM4 a[156:159],v[120:123],v[100:103],a[156:159]\nBL v152,s[40:43],s65 offen offset:0 sc1 lds\nM4 a[160:163],v[124:127],v[72:75],a[160:163]\nDT v[66:67],v142 offset:1024\n"
"M4 a[164:167],v[124:127],v[76:79],a[164:167]\nM4 a[168:171],v[124:127],v[80:83],a[168:171]\nDT v[68:69],v143 offset:0\nM4 a[172:175],v[124:127],v[84:87],a[172:175]\nM4 a[176:179],v[124:127],v[88:91],a[176:179]\nDT v[70:71],v143 offset:1024\nM4 a[180:183],v[124:127],v[92:95],a[180:183]\nBL v153,s[40:43],s65 offen offset:1024 sc1 lds\n"
"M4 a[184:187],v[124:127],v[96:99],a[184:187]\nM4 a[188:191],v[124:127],v[100:103],a[188:191]\nSA s77,s77,221184\nM4 a[192:195],v[128:131],v[72:75],a[192:195]\nM4 a[196:199],v[128:131],v[76:79],a[196:199]\nM4 a[200:203],v[128:131],v[80:83],a[200:203]\nBL v154,s[40:43],s65 offen offset:2048 sc1 lds\nM4 a[204:207],v[128:131],v[84:87],a[204:207]\n"
"M4 a[208:211],v[128:131],v[88:91],a[208:211]\nM4 a[212:215],v[128:131],v[92:95],a[212:215]\nM4 a[216:219],v[128:131],v[96:99],a[216:219]\nM4 a[220:223],v[128:131],v[100:103],a[220:223]\nBL v155,s[40:43],s65 offen offset:3072 sc1 lds\nM4 a[224:227],v[132:135],v[72:75],a[224:227]\nM4 a[228:231],v[132:135],v[76:79],a[228:231]\nM4 a[232:235],v[132:135],v[80:83],a[232:235]\n"
"M4 a[236:239],v[132:135],v[84:87],a[236:239]\nM4 a[240:243],v[132:135],v[88:91],a[240:243]\nM4 a[244:247],v[132:135],v[92:95],a[244:247]\nM4 a[248:251],v[132:135],v[96:99],a[248:251]\nM4 a[252:255],v[132:135],v[100:103],a[252:255]\nSW vmcnt(32) lgkmcnt(0)\ns_barrier\nM4 a[0:3],v[40:43],v[8:11],a[0:3]\n"
"DT v[104:105],v136 offset:16384\nM4 a[4:7],v[40:43],v[12:15],a[4:7]\nDT v[106:107],v136 offset:17408\nSA m0,s69,0\nbuffer_load_dword v242,v250,s[48:51],s76 offen\nM4 a[8:11],v[40:43],v[16:19],a[8:11]\nM4 a[12:15],v[40:43],v[20:23],a[12:15]\nDR v[72:75],v145 offset:0\n"
"M4 a[16:19],v[40:43],v[24:27],a[16:19]\nM4 a[20:23],v[40:43],v[28:31],a[20:23]\nDR v[76:79],v145 offset:2048\nBL v148,s[36:39],s64 offen offset:0 lds\nM4 a[24:27],v[40:43],v[32:35],a[24:27]\nM4 a[28:31],v[40:43],v[36:39],a[28:31]\nDR v[80:83],v145 offset:4096\nM4 a[32:35],v[44:47],v[8:11],a[32:35]\n"
"M4 a[36:39],v[44:47],v[12:15],a[36:39]\nDR v[84:87],v145 offset:6144\nM4 a[40:43],v[44:47],v[16:19],a[40:43]\nBL v149,s[36:39],s64 offen offset:1024 lds\nM4 a[44:47],v[44:47],v[20:23],a[44:47]\nDR v[88:91],v145 offset:8192\nM4 a[48:51],v[44:47],v[24:27],a[48:51]\nM4 a[52:55],v[44:47],v[28:31],a[52:55]\n"
"DR v[92:95],v145 offset:10240\nM4 a[56:59],v[44:47],v[32:35],a[56:59]\nBL v150,s[36:39],s64 offen offset:2048 lds\nM4 a[60:63],v[44:47],v[36:39],a[60:63]\nDR v[96:99],v145 offset:12288\nM4 a[64:67],v[48:51],v[8:11],a[64:67]\nbuffer_load_dword v243,v251,s[52:55],s77 offen\nM4 a[68:71],v[48:51],v[12:15],a[68:71]\n"
"DR v[100:103],v145 offset:14336\nM4 a[72:75],v[48:51],v[16:19],a[72:75]\nM4 a[76:79],v[48:51],v[20:23],a[76:79]\nDT v[108:109],v137 offset:16384\nBL v151,s[36:39],s64 offen offset:3072 lds\nM4 a[80:83],v[48:51],v[24:27],a[80:83]\nM4 a[84:87],v[48:51],v[28:31],a[84:87]\nDT v[110:111],v137 offset:17408\n"
"M4 a[88:91],v[48:51],v[32:35],a[88:91]\nM4 a[92:95],v[48:51],v[36:39],a[92:95]\nDT v[112:113],v138 offset:16384\nM4 a[96:99],v[52:55],v[8:11],a[96:99]\nDT v[114:115],v138 offset:17408\nSA s64,s64,262144\nM4 a[100:103],v[52:55],v[12:15],a[100:103]\nM4 a[104:107],v[52:55],v[16:19],a[104:107]\n"
"DT v[116:117],v139 offset:16384\nM4 a[108:111],v[52:55],v[20:23],a[108:111]\nM4 a[112:115],v[52:55],v[24:27],a[112:115]\nDT v[118:119],v139 offset:17408\nSA m0,s70,36864\nM4 a[116:119],v[52:55],v[28:31],a[116:119]\nM4 a[120:123],v[52:55],v[32:35],a[120:123]\nDT v[120:121],v140 offset:16384\n"
"M4 a[124:127],v[52:55],v[36:39],a[124:127]\nM4 a[128:131],v[56:59],v[8:11],a[128:131]\nDT v[122:123],v140 offset:17408\nSA s76,s76,524288\nM4 a[132:135],v[56:59],v[12:15],a[132:135]\nSA s72,s65,32768\nM4 a[136:139],v[56:59],v[16:19],a[136:139]\nDT v[124:125],v141 offset:16384\n"
"M4 a[140:143],v[56:59],v[20:23],a[140:143]\nM4 a[144:147],v[56:59],v[24:27],a[144:147]\nDT v[126:127],v141 offset:17408\nM4 a[148:151],v[56:59],v[28:31],a[148:151]\nBL v152,s[40:43],s72 offen offset:0 sc1 lds\nM4 a[152:155],v[56:59],v[32:35],a[152:155]\nDT v[128:129],v142 offset:16384\nM4 a[156:159],v[56:59],v[36:39],a[156:159]\n"
"M4 a[160:163],v[60:63],v[8:11],a[160:163]\nDT v[130:131],v142 offset:17408\nM4 a[164:167],v[60:63],v[12:15],a[164:167]\nM4 a[168:171],v[60:63],v[16:19],a[168:171]\nDT v[132:133],v143 offset:16384\nBL v153,s[40:43],s72 offen offset:1024 sc1 lds\nM4 a[172:175],v[60:63],v[20:23],a[172:175]\nM4 a[176:179],v[60:63],v[24:27],a[176:179]\n"
"DT v[134:135],v143 offset:17408\nM4 a[180:183],v[60:63],v[28:31],a[180:183]\nM4 a[184:187],v[60:63],v[32:35],a[184:187]\nM4 a[188:191],v[60:63],v[36:39],a[188:191]\nBL v154,s[40:43],s72 offen offset:2048 sc1 lds\nSA s77,s77,221184\nM4 a[192:195],v[64:67],v[8:11],a[192:195]\nM4 a[196:199],v[64:67],v[12:15],a[196:199]\n"
"M4 a[200:203],v[64:67],v[16:19],a[200:203]\nM4 a[204:207],v[64:67],v[20:23],a[204:207]\nBL v155,s[40:43],s72 offen offset:3072 sc1 lds\nM4 a[208:211],v[64:67],v[24:27],a[208:211]\nM4 a[212:215],v[64:67],v[28:31],a[212:215]\nM4 a[216:219],v[64:67],v[32:35],a[216:219]\nM4 a[220:223],v[64:67],v[36:39],a[220:223]\nM4 a[224:227],v[68:71],v[8:11],a[224:227]\n"
"SA s65,s65,128\nM4 a[228:231],v[68:71],v[12:15],a[228:231]\nM4 a[232:235],v[68:71],v[16:19],a[232:235]\nM4 a[236:239],v[68:71],v[20:23],a[236:239]\nM4 a[240:243],v[68:71],v[24:27],a[240:243]\nM4 a[244:247],v[68:71],v[28:31],a[244:247]\nM4 a[248:251],v[68:71],v[32:35],a[248:251]\nM4 a[252:255],v[68:71],v[36:39],a[252:255]\n"
"SW vmcnt(23) lgkmcnt(0)\ns_barrier\nSW vmcnt(22)\nM4 a[0:3],v[104:107],v[72:75],a[0:3]\nVU v144,0,v146\nM4 a[4:7],v[104:107],v[76:79],a[4:7]\nVU v145,0,v147\nbuffer_load_dword v244,v250,s[48:51],s76 offen\n"
"M4 a[8:11],v[104:107],v[80:83],a[8:11]\nDT v[40:41],v136 offset:32768\nM4 a[12:15],v[104:107],v[84:87],a[12:15]\nDT v[42:43],v136 offset:33792\nSA m0,s69,16384\nM4 a[16:19],v[104:107],v[88:91],a[16:19]\nM4 a[20:23],v[104:107],v[92:95],a[20:23]\nDR v[8:11],v144 offset:0\n"
"M4 a[24:27],v[104:107],v[96:99],a[24:27]\nM4 a[28:31],v[104:107],v[100:103],a[28:31]\nDR v[12:15],v144 offset:2048\nM4 a[32:35],v[108:111],v[72:75],a[32:35]\nBL v148,s[36:39],s64 offen offset:0 lds\nM4 a[36:39],v[108:111],v[76:79],a[36:39]\nDR v[16:19],v144 offset:4096\nM4 a[40:43],v[108:111],v[80:83],a[40:43]\n"
"M4 a[44:47],v[108:111],v[84:87],a[44:47]\nDR v[20:23],v144 offset:6144\nM4 a[48:51],v[108:111],v[88:91],a[48:51]\nM4 a[52:55],v[108:111],v[92:95],a[52:55]\nDR v[24:27],v144 offset:8192\nBL v149,s[36:39],s64 offen offset:1024 lds\nM4 a[56:59],v[108:111],v[96:99],a[56:59]\nDR v[28:31],v144 offset:10240\n"
"M4 a[60:63],v[108:111],v[100:103],a[60:63]\nM4 a[64:67],v[112:115],v[72:75],a[64:67]\nDR v[32:35],v144 offset:12288\nbuffer_load_dword v245,v251,s[52:55],s77 offen\nM4 a[68:71],v[112:115],v[76:79],a[68:71]\nM4 a[72:75],v[112:115],v[80:83],a[72:75]\nDR v[36:39],v144 offset:14336\nBL v150,s[36:39],s64 offen offset:2048 lds\n"
"M4 a[76:79],v[112:115],v[84:87],a[76:79]\nM4 a[80:83],v[112:115],v[88:91],a[80:83]\nDT v[44:45],v137 offset:32768\nM4 a[84:87],v[112:115],v[92:95],a[84:87]\nM4 a[88:91],v[112:115],v[96:99],a[88:91]\nDT v[46:47],v137 offset:33792\nM4 a[92:95],v[112:115],v[100:103],a[92:95]\nM4 a[96:99],v[116:119],v[72:75],a[96:99]\n"
"DT v[48:49],v138 offset:32768\nBL v151,s[36:39],s64 offen offset:3072 lds\nM4 a[100:103],v[116:119],v[76:79],a[100:103]\nDT v[50:51],v138 offset:33792\nM4 a[104:107],v[116:119],v[80:83],a[104:107]\nM4 a[108:111],v[116:119],v[84:87],a[108:111]\nDT v[52:53],v139 offset:32768\nM4 a[112:115],v[116:119],v[88:91],a[112:115]\n"
"M4 a[116:119],v[116:119],v[92:95],a[116:119]\nDT v[54:55],v139 offset:33792\nSA s64,s64,262144\nM4 a[120:123],v[116:119],v[96:99],a[120:123]\nM4 a[124:127],v[116:119],v[100:103],a[124:127]\nDT v[56:57],v140 offset:32768\nM4 a[128:131],v[120:123],v[72:75],a[128:131]\nSA s76,s76,524288\n"
"M4 a[132:135],v[120:123],v[76:79],a[132:135]\nDT v[58:59],v140 offset:33792\nM4 a[136:139],v[120:123],v[80:83],a[136:139]\nSA m0,s70,65536\nM4 a[140:143],v[120:123],v[84:87],a[140:143]\nDT v[60:61],v141 offset:32768\nM4 a[144:147],v[120:123],v[88:91],a[144:147]\nDT v[62:63],v141 offset:33792\n"
"M4 a[148:151],v[120:123],v[92:95],a[148:151]\nM4 a[152:155],v[120:123],v[96:99],a[152:155]\nDT v[64:65],v142 offset:32768\nM4 a[156:159],v[120:123],v[100:103],a[156:159]\nBL v152,s[40:43],s65 offen offset:0 sc1 lds\nM4 a[160:163],v[124:127],v[72:75],a[160:163]\nDT v[66:67],v142 offset:33792\nM4 a[164:167],v[124:127],v[76:79],a[164:167]\n"
"M4 a[168:171],v[124:127],v[80:83],a[168:171]\nDT v[68:69],v143 offset:32768\nM4 a[172:175],v[124:127],v[84:87],a[172:175]\nM4 a[176:179],v[124:127],v[88:91],a[176:179]\nDT v[70:71],v143 offset:33792\nM4 a[180:183],v[124:127],v[92:95],a[180:183]\nBL v153,s[40:43],s65 offen offset:1024 sc1 lds\nM4 a[184:187],v[124:127],v[96:99],a[184:187]\n"
"M4 a[188:191],v[124:127],v[100:103],a[188:191]\nSA s77,s77,221184\nM4 a[192:195],v[128:131],v[72:75],a[192:195]\nM4 a[196:199],v[128:131],v[76:79],a[196:199]\nM4 a[200:203],v[128:131],v[80:83],a[200:203]\nBL v154,s[40:43],s65 offen offset:2048 sc1 lds\nM4 a[204:207],v[128:131],v[84:87],a[204:207]\nM4 a[208:211],v[128:131],v[88:91],a[208:211]\n"
"M4 a[212:215],v[128:131],v[92:95],a[212:215]\nM4 a[216:219],v[128:131],v[96:99],a[216:219]\nM4 a[220:223],v[128:131],v[100:103],a[220:223]\nBL v155,s[40:43],s65 offen offset:3072 sc1 lds\nM4 a[224:227],v[132:135],v[72:75],a[224:227]\nM4 a[228:231],v[132:135],v[76:79],a[228:231]\nM4 a[232:235],v[132:135],v[80:83],a[232:235]\nM4 a[236:239],v[132:135],v[84:87],a[236:239]\n"
"M4 a[240:243],v[132:135],v[88:91],a[240:243]\nM4 a[244:247],v[132:135],v[92:95],a[244:247]\nM4 a[248:251],v[132:135],v[96:99],a[248:251]\nM4 a[252:255],v[132:135],v[100:103],a[252:255]\nSW vmcnt(24) lgkmcnt(0)\ns_barrier\ns_lshl_b32 s71,s88,3\nSA s71,s71,63967232\n"
"s_cmp_eq_u32 s89,0\ns_cbranch_scc0 P1_lnsK\nv_mov_b32 v170,s90\nv_mov_b32 v171,s91\nv_mov_b32 v172,0\nVU v172,s71,v172\nv_mov_b32 v173,s93\nv_add_co_u32 v172,vcc,s92,v172\n"
"s_nop 1\nv_addc_co_u32 v173,vcc,0,v173,vcc\ns_mov_b64 exec,1\nglobal_store_dwordx2 v[172:173],v[170:171],off sc1\ns_mov_b64 exec,-1\nP1_lnsK:\nM4 a[0:3],v[40:43],v[8:11],a[0:3]\nDT v[104:105],v136 offset:49152\n"
"M4 a[4:7],v[40:43],v[12:15],a[4:7]\nDT v[106:107],v136 offset:50176\nSA m0,s69,32768\nbuffer_load_dword v242,v250,s[48:51],s76 offen\nM4 a[8:11],v[40:43],v[16:19],a[8:11]\nM4 a[12:15],v[40:43],v[20:23],a[12:15]\nDR v[72:75],v145 offset:0\nM4 a[16:19],v[40:43],v[24:27],a[16:19]\n"
"M4 a[20:23],v[40:43],v[28:31],a[20:23]\nDR v[76:79],v145 offset:2048\nBL v148,s[36:39],s64 offen offset:0 lds\nM4 a[24:27],v[40:43],v[32:35],a[24:27]\nM4 a[28:31],v[40:43],v[36:39],a[28:31]\nDR v[80:83],v145 offset:4096\nM4 a[32:35],v[44:47],v[8:11],a[32:35]\nM4 a[36:39],v[44:47],v[12:15],a[36:39]\n"
"DR v[84:87],v145 offset:6144\nM4 a[40:43],v[44:47],v[16:19],a[40:43]\nBL v149,s[36:39],s64 offen offset:1024 lds\nM4 a[44:47],v[44:47],v[20:23],a[44:47]\nDR v[88:91],v145 offset:8192\nM4 a[48:51],v[44:47],v[24:27],a[48:51]\nM4 a[52:55],v[44:47],v[28:31],a[52:55]\nDR v[92:95],v145 offset:10240\n"
"M4 a[56:59],v[44:47],v[32:35],a[56:59]\nBL v150,s[36:39],s64 offen offset:2048 lds\nM4 a[60:63],v[44:47],v[36:39],a[60:63]\nDR v[96:99],v145 offset:12288\nM4 a[64:67],v[48:51],v[8:11],a[64:67]\nbuffer_load_dword v243,v251,s[52:55],s77 offen\nM4 a[68:71],v[48:51],v[12:15],a[68:71]\nDR v[100:103],v145 offset:14336\n"
"M4 a[72:75],v[48:51],v[16:19],a[72:75]\nM4 a[76:79],v[48:51],v[20:23],a[76:79]\nDT v[108:109],v137 offset:49152\nBL v151,s[36:39],s64 offen offset:3072 lds\nM4 a[80:83],v[48:51],v[24:27],a[80:83]\nM4 a[84:87],v[48:51],v[28:31],a[84:87]\nDT v[110:111],v137 offset:50176\nM4 a[88:91],v[48:51],v[32:35],a[88:91]\n"
"M4 a[92:95],v[48:51],v[36:39],a[92:95]\nDT v[112:113],v138 offset:49152\nM4 a[96:99],v[52:55],v[8:11],a[96:99]\nDT v[114:115],v138 offset:50176\nSA s64,s64,262144\nM4 a[100:103],v[52:55],v[12:15],a[100:103]\nM4 a[104:107],v[52:55],v[16:19],a[104:107]\nDT v[116:117],v139 offset:49152\n"
"M4 a[108:111],v[52:55],v[20:23],a[108:111]\nM4 a[112:115],v[52:55],v[24:27],a[112:115]\nDT v[118:119],v139 offset:50176\nSA m0,s70,69632\nM4 a[116:119],v[52:55],v[28:31],a[116:119]\nM4 a[120:123],v[52:55],v[32:35],a[120:123]\nDT v[120:121],v140 offset:49152\nM4 a[124:127],v[52:55],v[36:39],a[124:127]\n"
"M4 a[128:131],v[56:59],v[8:11],a[128:131]\nDT v[122:123],v140 offset:50176\nSA s76,s76,524288\nM4 a[132:135],v[56:59],v[12:15],a[132:135]\nSA s72,s65,32768\nM4 a[136:139],v[56:59],v[16:19],a[136:139]\nDT v[124:125],v141 offset:49152\nM4 a[140:143],v[56:59],v[20:23],a[140:143]\n"
"M4 a[144:147],v[56:59],v[24:27],a[144:147]\nDT v[126:127],v141 offset:50176\nM4 a[148:151],v[56:59],v[28:31],a[148:151]\nBL v152,s[40:43],s72 offen offset:0 sc1 lds\nM4 a[152:155],v[56:59],v[32:35],a[152:155]\nDT v[128:129],v142 offset:49152\nM4 a[156:159],v[56:59],v[36:39],a[156:159]\nM4 a[160:163],v[60:63],v[8:11],a[160:163]\n"
"DT v[130:131],v142 offset:50176\nM4 a[164:167],v[60:63],v[12:15],a[164:167]\nM4 a[168:171],v[60:63],v[16:19],a[168:171]\nDT v[132:133],v143 offset:49152\nBL v153,s[40:43],s72 offen offset:1024 sc1 lds\nM4 a[172:175],v[60:63],v[20:23],a[172:175]\nM4 a[176:179],v[60:63],v[24:27],a[176:179]\nDT v[134:135],v143 offset:50176\n"
"M4 a[180:183],v[60:63],v[28:31],a[180:183]\nM4 a[184:187],v[60:63],v[32:35],a[184:187]\nM4 a[188:191],v[60:63],v[36:39],a[188:191]\nBL v154,s[40:43],s72 offen offset:2048 sc1 lds\nSA s77,s77,221184\nM4 a[192:195],v[64:67],v[8:11],a[192:195]\nM4 a[196:199],v[64:67],v[12:15],a[196:199]\nM4 a[200:203],v[64:67],v[16:19],a[200:203]\n"
"M4 a[204:207],v[64:67],v[20:23],a[204:207]\nBL v155,s[40:43],s72 offen offset:3072 sc1 lds\nM4 a[208:211],v[64:67],v[24:27],a[208:211]\nM4 a[212:215],v[64:67],v[28:31],a[212:215]\nM4 a[216:219],v[64:67],v[32:35],a[216:219]\nM4 a[220:223],v[64:67],v[36:39],a[220:223]\nM4 a[224:227],v[68:71],v[8:11],a[224:227]\nSA s65,s65,128\n"
"M4 a[228:231],v[68:71],v[12:15],a[228:231]\nM4 a[232:235],v[68:71],v[16:19],a[232:235]\nM4 a[236:239],v[68:71],v[20:23],a[236:239]\nM4 a[240:243],v[68:71],v[24:27],a[240:243]\nM4 a[244:247],v[68:71],v[28:31],a[244:247]\nM4 a[248:251],v[68:71],v[32:35],a[248:251]\nM4 a[252:255],v[68:71],v[36:39],a[252:255]\nSW vmcnt(20) lgkmcnt(0)\n"
"s_barrier\nM4 a[0:3],v[104:107],v[72:75],a[0:3]\nVU v144,32768,v146\nM4 a[4:7],v[104:107],v[76:79],a[4:7]\nVU v145,32768,v147\nbuffer_load_dword v244,v250,s[48:51],s76 offen\nM4 a[8:11],v[104:107],v[80:83],a[8:11]\nDT v[40:41],v136 offset:0\n"
"M4 a[12:15],v[104:107],v[84:87],a[12:15]\nDT v[42:43],v136 offset:1024\nSA m0,s69,49152\nM4 a[16:19],v[104:107],v[88:91],a[16:19]\nM4 a[20:23],v[104:107],v[92:95],a[20:23]\nDR v[8:11],v144 offset:0\nM4 a[24:27],v[104:107],v[96:99],a[24:27]\nM4 a[28:31],v[104:107],v[100:103],a[28:31]\n"
"DR v[12:15],v144 offset:2048\nM4 a[32:35],v[108:111],v[72:75],a[32:35]\nBL v148,s[36:39],s64 offen offset:0 lds\nM4 a[36:39],v[108:111],v[76:79],a[36:39]\nDR v[16:19],v144 offset:4096\nM4 a[40:43],v[108:111],v[80:83],a[40:43]\nM4 a[44:47],v[108:111],v[84:87],a[44:47]\nDR v[20:23],v144 offset:6144\n"
"M4 a[48:51],v[108:111],v[88:91],a[48:51]\nM4 a[52:55],v[108:111],v[92:95],a[52:55]\nDR v[24:27],v144 offset:8192\nBL v149,s[36:39],s64 offen offset:1024 lds\nM4 a[56:59],v[108:111],v[96:99],a[56:59]\nDR v[28:31],v144 offset:10240\nM4 a[60:63],v[108:111],v[100:103],a[60:63]\nM4 a[64:67],v[112:115],v[72:75],a[64:67]\n"
"DR v[32:35],v144 offset:12288\nbuffer_load_dword v245,v251,s[52:55],s77 offen\nM4 a[68:71],v[112:115],v[76:79],a[68:71]\nM4 a[72:75],v[112:115],v[80:83],a[72:75]\nDR v[36:39],v144 offset:14336\nBL v150,s[36:39],s64 offen offset:2048 lds\nM4 a[76:79],v[112:115],v[84:87],a[76:79]\nM4 a[80:83],v[112:115],v[88:91],a[80:83]\n"
"DT v[44:45],v137 offset:0\nM4 a[84:87],v[112:115],v[92:95],a[84:87]\nM4 a[88:91],v[112:115],v[96:99],a[88:91]\nDT v[46:47],v137 offset:1024\nM4 a[92:95],v[112:115],v[100:103],a[92:95]\nM4 a[96:99],v[116:119],v[72:75],a[96:99]\nDT v[48:49],v138 offset:0\nBL v151,s[36:39],s64 offen offset:3072 lds\n"
"M4 a[100:103],v[116:119],v[76:79],a[100:103]\nDT v[50:51],v138 offset:1024\nM4 a[104:107],v[116:119],v[80:83],a[104:107]\nM4 a[108:111],v[116:119],v[84:87],a[108:111]\nDT v[52:53],v139 offset:0\nM4 a[112:115],v[116:119],v[88:91],a[112:115]\nM4 a[116:119],v[116:119],v[92:95],a[116:119]\nDT v[54:55],v139 offset:1024\n"
"SA s64,s64,262144\nM4 a[120:123],v[116:119],v[96:99],a[120:123]\nM4 a[124:127],v[116:119],v[100:103],a[124:127]\nDT v[56:57],v140 offset:0\nM4 a[128:131],v[120:123],v[72:75],a[128:131]\nSA s76,s76,524288\nM4 a[132:135],v[120:123],v[76:79],a[132:135]\nDT v[58:59],v140 offset:1024\n"
"M4 a[136:139],v[120:123],v[80:83],a[136:139]\nSA m0,s70,0\nM4 a[140:143],v[120:123],v[84:87],a[140:143]\nDT v[60:61],v141 offset:0\nM4 a[144:147],v[120:123],v[88:91],a[144:147]\nDT v[62:63],v141 offset:1024\nM4 a[148:151],v[120:123],v[92:95],a[148:151]\nM4 a[152:155],v[120:123],v[96:99],a[152:155]\n"
"DT v[64:65],v142 offset:0\nM4 a[156:159],v[120:123],v[100:103],a[156:159]\nBL v152,s[40:43],s65 offen offset:0 sc1 lds\nM4 a[160:163],v[124:127],v[72:75],a[160:163]\nDT v[66:67],v142 offset:1024\nM4 a[164:167],v[124:127],v[76:79],a[164:167]\nM4 a[168:171],v[124:127],v[80:83],a[168:171]\nDT v[68:69],v143 offset:0\n"
"M4 a[172:175],v[124:127],v[84:87],a[172:175]\nM4 a[176:179],v[124:127],v[88:91],a[176:179]\nDT v[70:71],v143 offset:1024\nM4 a[180:183],v[124:127],v[92:95],a[180:183]\nBL v153,s[40:43],s65 offen offset:1024 sc1 lds\nM4 a[184:187],v[124:127],v[96:99],a[184:187]\nM4 a[188:191],v[124:127],v[100:103],a[188:191]\nSA s77,s77,221184\n"
"M4 a[192:195],v[128:131],v[72:75],a[192:195]\nM4 a[196:199],v[128:131],v[76:79],a[196:199]\nM4 a[200:203],v[128:131],v[80:83],a[200:203]\nBL v154,s[40:43],s65 offen offset:2048 sc1 lds\nM4 a[204:207],v[128:131],v[84:87],a[204:207]\nM4 a[208:211],v[128:131],v[88:91],a[208:211]\nM4 a[212:215],v[128:131],v[92:95],a[212:215]\nM4 a[216:219],v[128:131],v[96:99],a[216:219]\n"
"M4 a[220:223],v[128:131],v[100:103],a[220:223]\nBL v155,s[40:43],s65 offen offset:3072 sc1 lds\nM4 a[224:227],v[132:135],v[72:75],a[224:227]\nM4 a[228:231],v[132:135],v[76:79],a[228:231]\nM4 a[232:235],v[132:135],v[80:83],a[232:235]\nM4 a[236:239],v[132:135],v[84:87],a[236:239]\nM4 a[240:243],v[132:135],v[88:91],a[240:243]\nM4 a[244:247],v[132:135],v[92:95],a[244:247]\n"
"M4 a[248:251],v[132:135],v[96:99],a[248:251]\nM4 a[252:255],v[132:135],v[100:103],a[252:255]\nSW vmcnt(24) lgkmcnt(0)\ns_barrier\nM4 a[0:3],v[40:43],v[8:11],a[0:3]\nDT v[104:105],v136 offset:16384\nM4 a[4:7],v[40:43],v[12:15],a[4:7]\nDT v[106:107],v136 offset:17408\n"
"SA m0,s69,0\nbuffer_load_dword v242,v250,s[48:51],s76 offen\nM4 a[8:11],v[40:43],v[16:19],a[8:11]\nM4 a[12:15],v[40:43],v[20:23],a[12:15]\nDR v[72:75],v145 offset:0\nM4 a[16:19],v[40:43],v[24:27],a[16:19]\nM4 a[20:23],v[40:43],v[28:31],a[20:23]\nDR v[76:79],v145 offset:2048\n"
"BL v148,s[36:39],s64 offen offset:0 lds\nM4 a[24:27],v[40:43],v[32:35],a[24:27]\nM4 a[28:31],v[40:43],v[36:39],a[28:31]\nDR v[80:83],v145 offset:4096\nM4 a[32:35],v[44:47],v[8:11],a[32:35]\nM4 a[36:39],v[44:47],v[12:15],a[36:39]\nDR v[84:87],v145 offset:6144\nM4 a[40:43],v[44:47],v[16:19],a[40:43]\n"
"BL v149,s[36:39],s64 offen offset:1024 lds\nM4 a[44:47],v[44:47],v[20:23],a[44:47]\nDR v[88:91],v145 offset:8192\nM4 a[48:51],v[44:47],v[24:27],a[48:51]\nM4 a[52:55],v[44:47],v[28:31],a[52:55]\nDR v[92:95],v145 offset:10240\nM4 a[56:59],v[44:47],v[32:35],a[56:59]\nBL v150,s[36:39],s64 offen offset:2048 lds\n"
"M4 a[60:63],v[44:47],v[36:39],a[60:63]\nDR v[96:99],v145 offset:12288\nM4 a[64:67],v[48:51],v[8:11],a[64:67]\nbuffer_load_dword v243,v251,s[52:55],s77 offen\nM4 a[68:71],v[48:51],v[12:15],a[68:71]\nDR v[100:103],v145 offset:14336\nM4 a[72:75],v[48:51],v[16:19],a[72:75]\nM4 a[76:79],v[48:51],v[20:23],a[76:79]\n"
"DT v[108:109],v137 offset:16384\nBL v151,s[36:39],s64 offen offset:3072 lds\nM4 a[80:83],v[48:51],v[24:27],a[80:83]\nM4 a[84:87],v[48:51],v[28:31],a[84:87]\nDT v[110:111],v137 offset:17408\nM4 a[88:91],v[48:51],v[32:35],a[88:91]\nM4 a[92:95],v[48:51],v[36:39],a[92:95]\nDT v[112:113],v138 offset:16384\n"
"M4 a[96:99],v[52:55],v[8:11],a[96:99]\nDT v[114:115],v138 offset:17408\nSA s64,s64,262144\nM4 a[100:103],v[52:55],v[12:15],a[100:103]\nM4 a[104:107],v[52:55],v[16:19],a[104:107]\nDT v[116:117],v139 offset:16384\nM4 a[108:111],v[52:55],v[20:23],a[108:111]\nM4 a[112:115],v[52:55],v[24:27],a[112:115]\n"
"DT v[118:119],v139 offset:17408\nSA m0,s70,4096\nM4 a[116:119],v[52:55],v[28:31],a[116:119]\nM4 a[120:123],v[52:55],v[32:35],a[120:123]\nDT v[120:121],v140 offset:16384\nM4 a[124:127],v[52:55],v[36:39],a[124:127]\nM4 a[128:131],v[56:59],v[8:11],a[128:131]\nDT v[122:123],v140 offset:17408\n"
"SA s76,s76,524288\nM4 a[132:135],v[56:59],v[12:15],a[132:135]\nSA s72,s65,32768\nM4 a[136:139],v[56:59],v[16:19],a[136:139]\nDT v[124:125],v141 offset:16384\nM4 a[140:143],v[56:59],v[20:23],a[140:143]\nM4 a[144:147],v[56:59],v[24:27],a[144:147]\nDT v[126:127],v141 offset:17408\n"
"M4 a[148:151],v[56:59],v[28:31],a[148:151]\nBL v152,s[40:43],s72 offen offset:0 sc1 lds\nM4 a[152:155],v[56:59],v[32:35],a[152:155]\nDT v[128:129],v142 offset:16384\nM4 a[156:159],v[56:59],v[36:39],a[156:159]\nM4 a[160:163],v[60:63],v[8:11],a[160:163]\nDT v[130:131],v142 offset:17408\nM4 a[164:167],v[60:63],v[12:15],a[164:167]\n"
"M4 a[168:171],v[60:63],v[16:19],a[168:171]\nDT v[132:133],v143 offset:16384\nBL v153,s[40:43],s72 offen offset:1024 sc1 lds\nM4 a[172:175],v[60:63],v[20:23],a[172:175]\nM4 a[176:179],v[60:63],v[24:27],a[176:179]\nDT v[134:135],v143 offset:17408\nM4 a[180:183],v[60:63],v[28:31],a[180:183]\nM4 a[184:187],v[60:63],v[32:35],a[184:187]\n"
"M4 a[188:191],v[60:63],v[36:39],a[188:191]\nBL v154,s[40:43],s72 offen offset:2048 sc1 lds\nSA s77,s77,221184\nM4 a[192:195],v[64:67],v[8:11],a[192:195]\nM4 a[196:199],v[64:67],v[12:15],a[196:199]\nM4 a[200:203],v[64:67],v[16:19],a[200:203]\nM4 a[204:207],v[64:67],v[20:23],a[204:207]\nBL v155,s[40:43],s72 offen offset:3072 sc1 lds\n"
"M4 a[208:211],v[64:67],v[24:27],a[208:211]\nM4 a[212:215],v[64:67],v[28:31],a[212:215]\nM4 a[216:219],v[64:67],v[32:35],a[216:219]\nM4 a[220:223],v[64:67],v[36:39],a[220:223]\nM4 a[224:227],v[68:71],v[8:11],a[224:227]\nSA s65,s65,128\nM4 a[228:231],v[68:71],v[12:15],a[228:231]\nM4 a[232:235],v[68:71],v[16:19],a[232:235]\n"
"M4 a[236:239],v[68:71],v[20:23],a[236:239]\nM4 a[240:243],v[68:71],v[24:27],a[240:243]\nM4 a[244:247],v[68:71],v[28:31],a[244:247]\nM4 a[248:251],v[68:71],v[32:35],a[248:251]\nM4 a[252:255],v[68:71],v[36:39],a[252:255]\nSW vmcnt(20) lgkmcnt(0)\ns_barrier\nM4 a[0:3],v[104:107],v[72:75],a[0:3]\n"
"VU v144,65536,v146\nM4 a[4:7],v[104:107],v[76:79],a[4:7]\nVU v145,65536,v147\nbuffer_load_dword v244,v250,s[48:51],s76 offen\nM4 a[8:11],v[104:107],v[80:83],a[8:11]\nDT v[40:41],v136 offset:32768\nM4 a[12:15],v[104:107],v[84:87],a[12:15]\nDT v[42:43],v136 offset:33792\n"
"SA m0,s69,16384\nM4 a[16:19],v[104:107],v[88:91],a[16:19]\nM4 a[20:23],v[104:107],v[92:95],a[20:23]\nDR v[8:11],v144 offset:0\nM4 a[24:27],v[104:107],v[96:99],a[24:27]\nM4 a[28:31],v[104:107],v[100:103],a[28:31]\nDR v[12:15],v144 offset:2048\nM4 a[32:35],v[108:111],v[72:75],a[32:35]\n"
"BL v148,s[36:39],s64 offen offset:0 lds\nM4 a[36:39],v[108:111],v[76:79],a[36:39]\nDR v[16:19],v144 offset:4096\nM4 a[40:43],v[108:111],v[80:83],a[40:43]\nM4 a[44:47],v[108:111],v[84:87],a[44:47]\nDR v[20:23],v144 offset:6144\nM4 a[48:51],v[108:111],v[88:91],a[48:51]\nM4 a[52:55],v[108:111],v[92:95],a[52:55]\n"
"DR v[24:27],v144 offset:8192\nBL v149,s[36:39],s64 offen offset:1024 lds\nM4 a[56:59],v[108:111],v[96:99],a[56:59]\nDR v[28:31],v144 offset:10240\nM4 a[60:63],v[108:111],v[100:103],a[60:63]\nM4 a[64:67],v[112:115],v[72:75],a[64:67]\nDR v[32:35],v144 offset:12288\nbuffer_load_dword v245,v251,s[52:55],s77 offen\n"
"M4 a[68:71],v[112:115],v[76:79],a[68:71]\nM4 a[72:75],v[112:115],v[80:83],a[72:75]\nDR v[36:39],v144 offset:14336\nBL v150,s[36:39],s64 offen offset:2048 lds\nM4 a[76:79],v[112:115],v[84:87],a[76:79]\nM4 a[80:83],v[112:115],v[88:91],a[80:83]\nDT v[44:45],v137 offset:32768\nM4 a[84:87],v[112:115],v[92:95],a[84:87]\n"
"M4 a[88:91],v[112:115],v[96:99],a[88:91]\nDT v[46:47],v137 offset:33792\nM4 a[92:95],v[112:115],v[100:103],a[92:95]\nM4 a[96:99],v[116:119],v[72:75],a[96:99]\nDT v[48:49],v138 offset:32768\nBL v151,s[36:39],s64 offen offset:3072 lds\nM4 a[100:103],v[116:119],v[76:79],a[100:103]\nDT v[50:51],v138 offset:33792\n"
"M4 a[104:107],v[116:119],v[80:83],a[104:107]\nM4 a[108:111],v[116:119],v[84:87],a[108:111]\nDT v[52:53],v139 offset:32768\nM4 a[112:115],v[116:119],v[88:91],a[112:115]\nM4 a[116:119],v[116:119],v[92:95],a[116:119]\nDT v[54:55],v139 offset:33792\nSA s64,s64,262144\nM4 a[120:123],v[116:119],v[96:99],a[120:123]\n"
"M4 a[124:127],v[116:119],v[100:103],a[124:127]\nDT v[56:57],v140 offset:32768\nM4 a[128:131],v[120:123],v[72:75],a[128:131]\nSA s76,s76,524288\nM4 a[132:135],v[120:123],v[76:79],a[132:135]\nDT v[58:59],v140 offset:33792\nM4 a[136:139],v[120:123],v[80:83],a[136:139]\nSA m0,s70,32768\n"
"M4 a[140:143],v[120:123],v[84:87],a[140:143]\nDT v[60:61],v141 offset:32768\nM4 a[144:147],v[120:123],v[88:91],a[144:147]\nDT v[62:63],v141 offset:33792\nM4 a[148:151],v[120:123],v[92:95],a[148:151]\nM4 a[152:155],v[120:123],v[96:99],a[152:155]\nDT v[64:65],v142 offset:32768\nM4 a[156:159],v[120:123],v[100:103],a[156:159]\n"
"BL v152,s[40:43],s65 offen offset:0 sc1 lds\nM4 a[160:163],v[124:127],v[72:75],a[160:163]\nDT v[66:67],v142 offset:33792\nM4 a[164:167],v[124:127],v[76:79],a[164:167]\nM4 a[168:171],v[124:127],v[80:83],a[168:171]\nDT v[68:69],v143 offset:32768\nM4 a[172:175],v[124:127],v[84:87],a[172:175]\nM4 a[176:179],v[124:127],v[88:91],a[176:179]\n"
"DT v[70:71],v143 offset:33792\nM4 a[180:183],v[124:127],v[92:95],a[180:183]\nBL v153,s[40:43],s65 offen offset:1024 sc1 lds\nM4 a[184:187],v[124:127],v[96:99],a[184:187]\nM4 a[188:191],v[124:127],v[100:103],a[188:191]\nSA s77,s77,221184\nM4 a[192:195],v[128:131],v[72:75],a[192:195]\nM4 a[196:199],v[128:131],v[76:79],a[196:199]\n"
"M4 a[200:203],v[128:131],v[80:83],a[200:203]\nBL v154,s[40:43],s65 offen offset:2048 sc1 lds\nM4 a[204:207],v[128:131],v[84:87],a[204:207]\nM4 a[208:211],v[128:131],v[88:91],a[208:211]\nM4 a[212:215],v[128:131],v[92:95],a[212:215]\nM4 a[216:219],v[128:131],v[96:99],a[216:219]\nM4 a[220:223],v[128:131],v[100:103],a[220:223]\nBL v155,s[40:43],s65 offen offset:3072 sc1 lds\n"
"M4 a[224:227],v[132:135],v[72:75],a[224:227]\nM4 a[228:231],v[132:135],v[76:79],a[228:231]\nM4 a[232:235],v[132:135],v[80:83],a[232:235]\nM4 a[236:239],v[132:135],v[84:87],a[236:239]\nM4 a[240:243],v[132:135],v[88:91],a[240:243]\nM4 a[244:247],v[132:135],v[92:95],a[244:247]\nM4 a[248:251],v[132:135],v[96:99],a[248:251]\nM4 a[252:255],v[132:135],v[100:103],a[252:255]\n"
"SW vmcnt(24) lgkmcnt(0)\ns_barrier\ns_sub_u32 s71,15,s63\ns_sub_u32 s71,s71,s63\ns_lshl_b32 s71,s71,7\nv_bfe_u32 v170,v200,4,1\nv_mul_lo_u32 v170,v170,s71\nVN v171,15,v200\n"
"v_lshl_add_u32 v170,v171,3,v170\ns_lshl_b32 s71,s63,7\nSA s71,s71,63967232\nVU v170,s71,v170\nv_mov_b32 v171,s93\nv_add_co_u32 v170,vcc,s92,v170\ns_nop 1\nv_addc_co_u32 v171,vcc,0,v171,vcc\n"
"global_load_dwordx2 v[172:173],v[170:171],off sc1\nM4 a[0:3],v[40:43],v[8:11],a[0:3]\nDT v[104:105],v136 offset:49152\nM4 a[4:7],v[40:43],v[12:15],a[4:7]\nDT v[106:107],v136 offset:50176\nSA m0,s69,32768\nbuffer_load_dword v242,v250,s[48:51],s76 offen\nM4 a[8:11],v[40:43],v[16:19],a[8:11]\n"
"M4 a[12:15],v[40:43],v[20:23],a[12:15]\nDR v[72:75],v145 offset:0\nM4 a[16:19],v[40:43],v[24:27],a[16:19]\nM4 a[20:23],v[40:43],v[28:31],a[20:23]\nDR v[76:79],v145 offset:2048\nBL v148,s[36:39],s64 offen offset:0 lds\nM4 a[24:27],v[40:43],v[32:35],a[24:27]\nM4 a[28:31],v[40:43],v[36:39],a[28:31]\n"
"DR v[80:83],v145 offset:4096\nM4 a[32:35],v[44:47],v[8:11],a[32:35]\nM4 a[36:39],v[44:47],v[12:15],a[36:39]\nDR v[84:87],v145 offset:6144\nM4 a[40:43],v[44:47],v[16:19],a[40:43]\nBL v149,s[36:39],s64 offen offset:1024 lds\nM4 a[44:47],v[44:47],v[20:23],a[44:47]\nDR v[88:91],v145 offset:8192\n"
"M4 a[48:51],v[44:47],v[24:27],a[48:51]\nM4 a[52:55],v[44:47],v[28:31],a[52:55]\nDR v[92:95],v145 offset:10240\nM4 a[56:59],v[44:47],v[32:35],a[56:59]\nBL v150,s[36:39],s64 offen offset:2048 lds\nM4 a[60:63],v[44:47],v[36:39],a[60:63]\nDR v[96:99],v145 offset:12288\nM4 a[64:67],v[48:51],v[8:11],a[64:67]\n"
"buffer_load_dword v243,v251,s[52:55],s77 offen\nM4 a[68:71],v[48:51],v[12:15],a[68:71]\nDR v[100:103],v145 offset:14336\nM4 a[72:75],v[48:51],v[16:19],a[72:75]\nM4 a[76:79],v[48:51],v[20:23],a[76:79]\nDT v[108:109],v137 offset:49152\nBL v151,s[36:39],s64 offen offset:3072 lds\nM4 a[80:83],v[48:51],v[24:27],a[80:83]\n"
"M4 a[84:87],v[48:51],v[28:31],a[84:87]\nDT v[110:111],v137 offset:50176\nM4 a[88:91],v[48:51],v[32:35],a[88:91]\nM4 a[92:95],v[48:51],v[36:39],a[92:95]\nDT v[112:113],v138 offset:49152\nM4 a[96:99],v[52:55],v[8:11],a[96:99]\nDT v[114:115],v138 offset:50176\nSA s64,s64,262144\n"
"M4 a[100:103],v[52:55],v[12:15],a[100:103]\nM4 a[104:107],v[52:55],v[16:19],a[104:107]\nDT v[116:117],v139 offset:49152\nM4 a[108:111],v[52:55],v[20:23],a[108:111]\nM4 a[112:115],v[52:55],v[24:27],a[112:115]\nDT v[118:119],v139 offset:50176\nSA m0,s70,36864\nM4 a[116:119],v[52:55],v[28:31],a[116:119]\n"
"M4 a[120:123],v[52:55],v[32:35],a[120:123]\nDT v[120:121],v140 offset:49152\nM4 a[124:127],v[52:55],v[36:39],a[124:127]\nM4 a[128:131],v[56:59],v[8:11],a[128:131]\nDT v[122:123],v140 offset:50176\nSA s76,s76,524288\nM4 a[132:135],v[56:59],v[12:15],a[132:135]\nSA s72,s65,32768\n"
"M4 a[136:139],v[56:59],v[16:19],a[136:139]\nDT v[124:125],v141 offset:49152\nM4 a[140:143],v[56:59],v[20:23],a[140:143]\nM4 a[144:147],v[56:59],v[24:27],a[144:147]\nDT v[126:127],v141 offset:50176\nM4 a[148:151],v[56:59],v[28:31],a[148:151]\nBL v152,s[40:43],s72 offen offset:0 sc1 lds\nM4 a[152:155],v[56:59],v[32:35],a[152:155]\n"
"DT v[128:129],v142 offset:49152\nM4 a[156:159],v[56:59],v[36:39],a[156:159]\nM4 a[160:163],v[60:63],v[8:11],a[160:163]\nDT v[130:131],v142 offset:50176\nM4 a[164:167],v[60:63],v[12:15],a[164:167]\nM4 a[168:171],v[60:63],v[16:19],a[168:171]\nDT v[132:133],v143 offset:49152\nBL v153,s[40:43],s72 offen offset:1024 sc1 lds\n"
"M4 a[172:175],v[60:63],v[20:23],a[172:175]\nM4 a[176:179],v[60:63],v[24:27],a[176:179]\nDT v[134:135],v143 offset:50176\nM4 a[180:183],v[60:63],v[28:31],a[180:183]\nM4 a[184:187],v[60:63],v[32:35],a[184:187]\nM4 a[188:191],v[60:63],v[36:39],a[188:191]\nBL v154,s[40:43],s72 offen offset:2048 sc1 lds\nSA s77,s77,221184\n"
"M4 a[192:195],v[64:67],v[8:11],a[192:195]\nM4 a[196:199],v[64:67],v[12:15],a[196:199]\nM4 a[200:203],v[64:67],v[16:19],a[200:203]\nM4 a[204:207],v[64:67],v[20:23],a[204:207]\nBL v155,s[40:43],s72 offen offset:3072 sc1 lds\nM4 a[208:211],v[64:67],v[24:27],a[208:211]\nM4 a[212:215],v[64:67],v[28:31],a[212:215]\nM4 a[216:219],v[64:67],v[32:35],a[216:219]\n"
"M4 a[220:223],v[64:67],v[36:39],a[220:223]\nM4 a[224:227],v[68:71],v[8:11],a[224:227]\nSA s65,s65,128\nM4 a[228:231],v[68:71],v[12:15],a[228:231]\nM4 a[232:235],v[68:71],v[16:19],a[232:235]\nM4 a[236:239],v[68:71],v[20:23],a[236:239]\nM4 a[240:243],v[68:71],v[24:27],a[240:243]\nM4 a[244:247],v[68:71],v[28:31],a[244:247]\n"
"M4 a[248:251],v[68:71],v[32:35],a[248:251]\nM4 a[252:255],v[68:71],v[36:39],a[252:255]\nSW vmcnt(21) lgkmcnt(0)\ns_barrier\nSW vmcnt(10)\nSM s71,0\nP1_lwK:\nv_xor_b32 v172,s90,v172\n"
"v_xor_b32 v173,s91,v173\nv_or_b32 v172,v172,v173\nv_cmp_ne_u32 vcc,0,v172\ns_nop 4\ns_cbranch_vccz P1_lwK_d\nSA s71,s71,1\ns_cmp_ge_u32 s71,200000\ns_cbranch_scc1 P1_lwK_d\n"
"s_sleep 2\nglobal_load_dwordx2 v[172:173],v[170:171],off sc1\nSW vmcnt(0)\ns_branch P1_lwK\nP1_lwK_d:\nM4 a[0:3],v[104:107],v[72:75],a[0:3]\nVU v144,0,v146\nM4 a[4:7],v[104:107],v[76:79],a[4:7]\n"
"VU v145,0,v147\nM4 a[8:11],v[104:107],v[80:83],a[8:11]\nDT v[40:41],v136 offset:0\nM4 a[12:15],v[104:107],v[84:87],a[12:15]\nDT v[42:43],v136 offset:1024\nSA m0,s69,49152\nM4 a[16:19],v[104:107],v[88:91],a[16:19]\nM4 a[20:23],v[104:107],v[92:95],a[20:23]\n"
"DR v[8:11],v144 offset:0\nM4 a[24:27],v[104:107],v[96:99],a[24:27]\nM4 a[28:31],v[104:107],v[100:103],a[28:31]\nDR v[12:15],v144 offset:2048\nM4 a[32:35],v[108:111],v[72:75],a[32:35]\nBL v148,s[36:39],s64 offen offset:0 lds\nM4 a[36:39],v[108:111],v[76:79],a[36:39]\nDR v[16:19],v144 offset:4096\n"
"M4 a[40:43],v[108:111],v[80:83],a[40:43]\nM4 a[44:47],v[108:111],v[84:87],a[44:47]\nDR v[20:23],v144 offset:6144\nM4 a[48:51],v[108:111],v[88:91],a[48:51]\nM4 a[52:55],v[108:111],v[92:95],a[52:55]\nDR v[24:27],v144 offset:8192\nBL v149,s[36:39],s64 offen offset:1024 lds\nM4 a[56:59],v[108:111],v[96:99],a[56:59]\n"
"DR v[28:31],v144 offset:10240\nM4 a[60:63],v[108:111],v[100:103],a[60:63]\nM4 a[64:67],v[112:115],v[72:75],a[64:67]\nDR v[32:35],v144 offset:12288\nM4 a[68:71],v[112:115],v[76:79],a[68:71]\nM4 a[72:75],v[112:115],v[80:83],a[72:75]\nDR v[36:39],v144 offset:14336\nBL v150,s[36:39],s64 offen offset:2048 lds\n"
"M4 a[76:79],v[112:115],v[84:87],a[76:79]\nM4 a[80:83],v[112:115],v[88:91],a[80:83]\nDT v[44:45],v137 offset:0\nM4 a[84:87],v[112:115],v[92:95],a[84:87]\nM4 a[88:91],v[112:115],v[96:99],a[88:91]\nDT v[46:47],v137 offset:1024\nM4 a[92:95],v[112:115],v[100:103],a[92:95]\nM4 a[96:99],v[116:119],v[72:75],a[96:99]\n"
"DT v[48:49],v138 offset:0\nBL v151,s[36:39],s64 offen offset:3072 lds\nM4 a[100:103],v[116:119],v[76:79],a[100:103]\nDT v[50:51],v138 offset:1024\nM4 a[104:107],v[116:119],v[80:83],a[104:107]\nM4 a[108:111],v[116:119],v[84:87],a[108:111]\nDT v[52:53],v139 offset:0\nM4 a[112:115],v[116:119],v[88:91],a[112:115]\n"
"M4 a[116:119],v[116:119],v[92:95],a[116:119]\nDT v[54:55],v139 offset:1024\nSA s64,s64,262144\nM4 a[120:123],v[116:119],v[96:99],a[120:123]\nM4 a[124:127],v[116:119],v[100:103],a[124:127]\nDT v[56:57],v140 offset:0\nM4 a[128:131],v[120:123],v[72:75],a[128:131]\nM4 a[132:135],v[120:123],v[76:79],a[132:135]\n"
"DT v[58:59],v140 offset:1024\nM4 a[136:139],v[120:123],v[80:83],a[136:139]\nSA m0,s70,65536\nM4 a[140:143],v[120:123],v[84:87],a[140:143]\nDT v[60:61],v141 offset:0\nM4 a[144:147],v[120:123],v[88:91],a[144:147]\nDT v[62:63],v141 offset:1024\nM4 a[148:151],v[120:123],v[92:95],a[148:151]\n"
"M4 a[152:155],v[120:123],v[96:99],a[152:155]\nDT v[64:65],v142 offset:0\nM4 a[156:159],v[120:123],v[100:103],a[156:159]\nBL v160,s[56:59],s68 offen offset:0 sc1 lds\nM4 a[160:163],v[124:127],v[72:75],a[160:163]\nDT v[66:67],v142 offset:1024\nM4 a[164:167],v[124:127],v[76:79],a[164:167]\nM4 a[168:171],v[124:127],v[80:83],a[168:171]\n"
"DT v[68:69],v143 offset:0\nM4 a[172:175],v[124:127],v[84:87],a[172:175]\nM4 a[176:179],v[124:127],v[88:91],a[176:179]\nDT v[70:71],v143 offset:1024\nM4 a[180:183],v[124:127],v[92:95],a[180:183]\nBL v161,s[56:59],s68 offen offset:1024 sc1 lds\nM4 a[184:187],v[124:127],v[96:99],a[184:187]\nM4 a[188:191],v[124:127],v[100:103],a[188:191]\n"
"M4 a[192:195],v[128:131],v[72:75],a[192:195]\nM4 a[196:199],v[128:131],v[76:79],a[196:199]\nM4 a[200:203],v[128:131],v[80:83],a[200:203]\nBL v162,s[56:59],s68 offen offset:2048 sc1 lds\nM4 a[204:207],v[128:131],v[84:87],a[204:207]\nM4 a[208:211],v[128:131],v[88:91],a[208:211]\nM4 a[212:215],v[128:131],v[92:95],a[212:215]\nM4 a[216:219],v[128:131],v[96:99],a[216:219]\n"
"M4 a[220:223],v[128:131],v[100:103],a[220:223]\nBL v163,s[56:59],s68 offen offset:3072 sc1 lds\nM4 a[224:227],v[132:135],v[72:75],a[224:227]\nM4 a[228:231],v[132:135],v[76:79],a[228:231]\nM4 a[232:235],v[132:135],v[80:83],a[232:235]\nM4 a[236:239],v[132:135],v[84:87],a[236:239]\nM4 a[240:243],v[132:135],v[88:91],a[240:243]\nM4 a[244:247],v[132:135],v[92:95],a[244:247]\n"
"M4 a[248:251],v[132:135],v[96:99],a[248:251]\nM4 a[252:255],v[132:135],v[100:103],a[252:255]\nSW vmcnt(23) lgkmcnt(0)\ns_barrier\nM4 a[0:3],v[40:43],v[8:11],a[0:3]\nDT v[104:105],v136 offset:16384\nM4 a[4:7],v[40:43],v[12:15],a[4:7]\nDT v[106:107],v136 offset:17408\n"
"SA m0,s81,0\nM4 a[8:11],v[40:43],v[16:19],a[8:11]\nM4 a[12:15],v[40:43],v[20:23],a[12:15]\nDR v[72:75],v145 offset:0\nM4 a[16:19],v[40:43],v[24:27],a[16:19]\nM4 a[20:23],v[40:43],v[28:31],a[20:23]\nDR v[76:79],v145 offset:2048\nBL v156,s[48:51],s66 offen offset:0 lds\n"
"M4 a[24:27],v[40:43],v[32:35],a[24:27]\nM4 a[28:31],v[40:43],v[36:39],a[28:31]\nDR v[80:83],v145 offset:4096\nM4 a[32:35],v[44:47],v[8:11],a[32:35]\nM4 a[36:39],v[44:47],v[12:15],a[36:39]\nDR v[84:87],v145 offset:6144\nBL v157,s[48:51],s66 offen offset:1024 lds\nM4 a[40:43],v[44:47],v[16:19],a[40:43]\n"
"M4 a[44:47],v[44:47],v[20:23],a[44:47]\nDR v[88:91],v145 offset:8192\nM4 a[48:51],v[44:47],v[24:27],a[48:51]\nM4 a[52:55],v[44:47],v[28:31],a[52:55]\nDR v[92:95],v145 offset:10240\nSA m0,s82,0\nM4 a[56:59],v[44:47],v[32:35],a[56:59]\nM4 a[60:63],v[44:47],v[36:39],a[60:63]\n"
"DR v[96:99],v145 offset:12288\nM4 a[64:67],v[48:51],v[8:11],a[64:67]\nM4 a[68:71],v[48:51],v[12:15],a[68:71]\nDR v[100:103],v145 offset:14336\nM4 a[72:75],v[48:51],v[16:19],a[72:75]\nBL v158,s[52:55],s67 offen lds\nM4 a[76:79],v[48:51],v[20:23],a[76:79]\nDT v[108:109],v137 offset:16384\n"
"M4 a[80:83],v[48:51],v[24:27],a[80:83]\nM4 a[84:87],v[48:51],v[28:31],a[84:87]\nDT v[110:111],v137 offset:17408\nM4 a[88:91],v[48:51],v[32:35],a[88:91]\nSA s66,s66,262144\nM4 a[92:95],v[48:51],v[36:39],a[92:95]\nDT v[112:113],v138 offset:16384\nM4 a[96:99],v[52:55],v[8:11],a[96:99]\n"
"DT v[114:115],v138 offset:17408\nM4 a[100:103],v[52:55],v[12:15],a[100:103]\nM4 a[104:107],v[52:55],v[16:19],a[104:107]\nDT v[116:117],v139 offset:16384\nSA s67,s67,110592\nM4 a[108:111],v[52:55],v[20:23],a[108:111]\nM4 a[112:115],v[52:55],v[24:27],a[112:115]\nDT v[118:119],v139 offset:17408\n"
"M4 a[116:119],v[52:55],v[28:31],a[116:119]\nM4 a[120:123],v[52:55],v[32:35],a[120:123]\nDT v[120:121],v140 offset:16384\nM4 a[124:127],v[52:55],v[36:39],a[124:127]\nSA m0,s70,69632\nM4 a[128:131],v[56:59],v[8:11],a[128:131]\nDT v[122:123],v140 offset:17408\nM4 a[132:135],v[56:59],v[12:15],a[132:135]\n"
"M4 a[136:139],v[56:59],v[16:19],a[136:139]\nDT v[124:125],v141 offset:16384\nM4 a[140:143],v[56:59],v[20:23],a[140:143]\nSA s72,s68,40960\nM4 a[144:147],v[56:59],v[24:27],a[144:147]\nDT v[126:127],v141 offset:17408\nM4 a[148:151],v[56:59],v[28:31],a[148:151]\nM4 a[152:155],v[56:59],v[32:35],a[152:155]\n"
"DT v[128:129],v142 offset:16384\nM4 a[156:159],v[56:59],v[36:39],a[156:159]\nBL v160,s[56:59],s72 offen offset:0 sc1 lds\nM4 a[160:163],v[60:63],v[8:11],a[160:163]\nDT v[130:131],v142 offset:17408\nM4 a[164:167],v[60:63],v[12:15],a[164:167]\nM4 a[168:171],v[60:63],v[16:19],a[168:171]\nDT v[132:133],v143 offset:16384\n"
"M4 a[172:175],v[60:63],v[20:23],a[172:175]\nBL v161,s[56:59],s72 offen offset:1024 sc1 lds\nM4 a[176:179],v[60:63],v[24:27],a[176:179]\nDT v[134:135],v143 offset:17408\nM4 a[180:183],v[60:63],v[28:31],a[180:183]\nM4 a[184:187],v[60:63],v[32:35],a[184:187]\nM4 a[188:191],v[60:63],v[36:39],a[188:191]\nM4 a[192:195],v[64:67],v[8:11],a[192:195]\n"
"BL v162,s[56:59],s72 offen offset:2048 sc1 lds\nM4 a[196:199],v[64:67],v[12:15],a[196:199]\nM4 a[200:203],v[64:67],v[16:19],a[200:203]\nM4 a[204:207],v[64:67],v[20:23],a[204:207]\nM4 a[208:211],v[64:67],v[24:27],a[208:211]\nBL v163,s[56:59],s72 offen offset:3072 sc1 lds\nM4 a[212:215],v[64:67],v[28:31],a[212:215]\nM4 a[216:219],v[64:67],v[32:35],a[216:219]\n"
"M4 a[220:223],v[64:67],v[36:39],a[220:223]\nM4 a[224:227],v[68:71],v[8:11],a[224:227]\nSA s68,s68,128\nM4 a[228:231],v[68:71],v[12:15],a[228:231]\nM4 a[232:235],v[68:71],v[16:19],a[232:235]\nM4 a[236:239],v[68:71],v[20:23],a[236:239]\nM4 a[240:243],v[68:71],v[24:27],a[240:243]\nM4 a[244:247],v[68:71],v[28:31],a[244:247]\n"
"M4 a[248:251],v[68:71],v[32:35],a[248:251]\nM4 a[252:255],v[68:71],v[36:39],a[252:255]\nSW vmcnt(15) lgkmcnt(0)\ns_barrier\nM4 a[0:3],v[104:107],v[72:75],a[0:3]\nVU v144,32768,v146\nM4 a[4:7],v[104:107],v[76:79],a[4:7]\nVU v145,32768,v147\n"
"M4 a[8:11],v[104:107],v[80:83],a[8:11]\nDT v[40:41],v136 offset:32768\nM4 a[12:15],v[104:107],v[84:87],a[12:15]\nDT v[42:43],v136 offset:33792\nSA m0,s81,16384\nM4 a[16:19],v[104:107],v[88:91],a[16:19]\nM4 a[20:23],v[104:107],v[92:95],a[20:23]\nDR v[8:11],v144 offset:0\n"
"M4 a[24:27],v[104:107],v[96:99],a[24:27]\nM4 a[28:31],v[104:107],v[100:103],a[28:31]\nDR v[12:15],v144 offset:2048\nBL v156,s[48:51],s66 offen offset:0 lds\nM4 a[32:35],v[108:111],v[72:75],a[32:35]\nM4 a[36:39],v[108:111],v[76:79],a[36:39]\nDR v[16:19],v144 offset:4096\nM4 a[40:43],v[108:111],v[80:83],a[40:43]\n"
"M4 a[44:47],v[108:111],v[84:87],a[44:47]\nDR v[20:23],v144 offset:6144\nM4 a[48:51],v[108:111],v[88:91],a[48:51]\nBL v157,s[48:51],s66 offen offset:1024 lds\nM4 a[52:55],v[108:111],v[92:95],a[52:55]\nDR v[24:27],v144 offset:8192\nM4 a[56:59],v[108:111],v[96:99],a[56:59]\nDR v[28:31],v144 offset:10240\n"
"M4 a[60:63],v[108:111],v[100:103],a[60:63]\nM4 a[64:67],v[112:115],v[72:75],a[64:67]\nDR v[32:35],v144 offset:12288\nM4 a[68:71],v[112:115],v[76:79],a[68:71]\nSA m0,s82,16384\nM4 a[72:75],v[112:115],v[80:83],a[72:75]\nDR v[36:39],v144 offset:14336\nM4 a[76:79],v[112:115],v[84:87],a[76:79]\n"
"M4 a[80:83],v[112:115],v[88:91],a[80:83]\nDT v[44:45],v137 offset:32768\nM4 a[84:87],v[112:115],v[92:95],a[84:87]\nM4 a[88:91],v[112:115],v[96:99],a[88:91]\nDT v[46:47],v137 offset:33792\nBL v158,s[52:55],s67 offen lds\nM4 a[92:95],v[112:115],v[100:103],a[92:95]\nM4 a[96:99],v[116:119],v[72:75],a[96:99]\n"
"DT v[48:49],v138 offset:32768\nM4 a[100:103],v[116:119],v[76:79],a[100:103]\nDT v[50:51],v138 offset:33792\nM4 a[104:107],v[116:119],v[80:83],a[104:107]\nM4 a[108:111],v[116:119],v[84:87],a[108:111]\nDT v[52:53],v139 offset:32768\nSA s66,s66,262144\nM4 a[112:115],v[116:119],v[88:91],a[112:115]\n"
"M4 a[116:119],v[116:119],v[92:95],a[116:119]\nDT v[54:55],v139 offset:33792\nM4 a[120:123],v[116:119],v[96:99],a[120:123]\nM4 a[124:127],v[116:119],v[100:103],a[124:127]\nDT v[56:57],v140 offset:32768\nM4 a[128:131],v[120:123],v[72:75],a[128:131]\nSA s67,s67,110592\nM4 a[132:135],v[120:123],v[76:79],a[132:135]\n"
"DT v[58:59],v140 offset:33792\nM4 a[136:139],v[120:123],v[80:83],a[136:139]\nM4 a[140:143],v[120:123],v[84:87],a[140:143]\nDT v[60:61],v141 offset:32768\nM4 a[144:147],v[120:123],v[88:91],a[144:147]\nDT v[62:63],v141 offset:33792\nSA m0,s70,0\nM4 a[148:151],v[120:123],v[92:95],a[148:151]\n"
"M4 a[152:155],v[120:123],v[96:99],a[152:155]\nDT v[64:65],v142 offset:32768\nM4 a[156:159],v[120:123],v[100:103],a[156:159]\nM4 a[160:163],v[124:127],v[72:75],a[160:163]\nDT v[66:67],v142 offset:33792\nM4 a[164:167],v[124:127],v[76:79],a[164:167]\nBL v160,s[56:59],s68 offen offset:0 sc1 lds\nM4 a[168:171],v[124:127],v[80:83],a[168:171]\n"
"DT v[68:69],v143 offset:32768\nM4 a[172:175],v[124:127],v[84:87],a[172:175]\nM4 a[176:179],v[124:127],v[88:91],a[176:179]\nDT v[70:71],v143 offset:33792\nM4 a[180:183],v[124:127],v[92:95],a[180:183]\nM4 a[184:187],v[124:127],v[96:99],a[184:187]\nBL v161,s[56:59],s68 offen offset:1024 sc1 lds\nM4 a[188:191],v[124:127],v[100:103],a[188:191]\n"
"M4 a[192:195],v[128:131],v[72:75],a[192:195]\nM4 a[196:199],v[128:131],v[76:79],a[196:199]\nM4 a[200:203],v[128:131],v[80:83],a[200:203]\nM4 a[204:207],v[128:131],v[84:87],a[204:207]\nBL v162,s[56:59],s68 offen offset:2048 sc1 lds\nM4 a[208:211],v[128:131],v[88:91],a[208:211]\nM4 a[212:215],v[128:131],v[92:95],a[212:215]\nM4 a[216:219],v[128:131],v[96:99],a[216:219]\n"
"M4 a[220:223],v[128:131],v[100:103],a[220:223]\nM4 a[224:227],v[132:135],v[72:75],a[224:227]\nBL v163,s[56:59],s68 offen offset:3072 sc1 lds\nM4 a[228:231],v[132:135],v[76:79],a[228:231]\nM4 a[232:235],v[132:135],v[80:83],a[232:235]\nM4 a[236:239],v[132:135],v[84:87],a[236:239]\nM4 a[240:243],v[132:135],v[88:91],a[240:243]\nM4 a[244:247],v[132:135],v[92:95],a[244:247]\n"
"M4 a[248:251],v[132:135],v[96:99],a[248:251]\nM4 a[252:255],v[132:135],v[100:103],a[252:255]\nSW vmcnt(18) lgkmcnt(0)\ns_barrier\nM4 a[0:3],v[40:43],v[8:11],a[0:3]\nDT v[104:105],v136 offset:49152\nM4 a[4:7],v[40:43],v[12:15],a[4:7]\nDT v[106:107],v136 offset:50176\n"
"SA m0,s81,32768\nM4 a[8:11],v[40:43],v[16:19],a[8:11]\nM4 a[12:15],v[40:43],v[20:23],a[12:15]\nDR v[72:75],v145 offset:0\nM4 a[16:19],v[40:43],v[24:27],a[16:19]\nM4 a[20:23],v[40:43],v[28:31],a[20:23]\nDR v[76:79],v145 offset:2048\nBL v156,s[48:51],s66 offen offset:0 lds\n"
"M4 a[24:27],v[40:43],v[32:35],a[24:27]\nM4 a[28:31],v[40:43],v[36:39],a[28:31]\nDR v[80:83],v145 offset:4096\nM4 a[32:35],v[44:47],v[8:11],a[32:35]\nM4 a[36:39],v[44:47],v[12:15],a[36:39]\nDR v[84:87],v145 offset:6144\nBL v157,s[48:51],s66 offen offset:1024 lds\nM4 a[40:43],v[44:47],v[16:19],a[40:43]\n"
"M4 a[44:47],v[44:47],v[20:23],a[44:47]\nDR v[88:91],v145 offset:8192\nM4 a[48:51],v[44:47],v[24:27],a[48:51]\nM4 a[52:55],v[44:47],v[28:31],a[52:55]\nDR v[92:95],v145 offset:10240\nSA m0,s82,32768\nM4 a[56:59],v[44:47],v[32:35],a[56:59]\nM4 a[60:63],v[44:47],v[36:39],a[60:63]\n"
"DR v[96:99],v145 offset:12288\nM4 a[64:67],v[48:51],v[8:11],a[64:67]\nM4 a[68:71],v[48:51],v[12:15],a[68:71]\nDR v[100:103],v145 offset:14336\nM4 a[72:75],v[48:51],v[16:19],a[72:75]\nBL v158,s[52:55],s67 offen lds\nM4 a[76:79],v[48:51],v[20:23],a[76:79]\nDT v[108:109],v137 offset:49152\n"
"M4 a[80:83],v[48:51],v[24:27],a[80:83]\nM4 a[84:87],v[48:51],v[28:31],a[84:87]\nDT v[110:111],v137 offset:50176\nM4 a[88:91],v[48:51],v[32:35],a[88:91]\nSA s66,s66,262144\nM4 a[92:95],v[48:51],v[36:39],a[92:95]\nDT v[112:113],v138 offset:49152\nM4 a[96:99],v[52:55],v[8:11],a[96:99]\n"
"DT v[114:115],v138 offset:50176\nM4 a[100:103],v[52:55],v[12:15],a[100:103]\nM4 a[104:107],v[52:55],v[16:19],a[104:107]\nDT v[116:117],v139 offset:49152\nSA s67,s67,110592\nM4 a[108:111],v[52:55],v[20:23],a[108:111]\nM4 a[112:115],v[52:55],v[24:27],a[112:115]\nDT v[118:119],v139 offset:50176\n"
"M4 a[116:119],v[52:55],v[28:31],a[116:119]\nM4 a[120:123],v[52:55],v[32:35],a[120:123]\nDT v[120:121],v140 offset:49152\nM4 a[124:127],v[52:55],v[36:39],a[124:127]\nSA m0,s70,4096\nM4 a[128:131],v[56:59],v[8:11],a[128:131]\nDT v[122:123],v140 offset:50176\nM4 a[132:135],v[56:59],v[12:15],a[132:135]\n"
"M4 a[136:139],v[56:59],v[16:19],a[136:139]\nDT v[124:125],v141 offset:49152\nM4 a[140:143],v[56:59],v[20:23],a[140:143]\nSA s72,s68,40960\nM4 a[144:147],v[56:59],v[24:27],a[144:147]\nDT v[126:127],v141 offset:50176\nM4 a[148:151],v[56:59],v[28:31],a[148:151]\nM4 a[152:155],v[56:59],v[32:35],a[152:155]\n"
"DT v[128:129],v142 offset:49152\nM4 a[156:159],v[56:59],v[36:39],a[156:159]\nBL v160,s[56:59],s72 offen offset:0 sc1 lds\nM4 a[160:163],v[60:63],v[8:11],a[160:163]\nDT v[130:131],v142 offset:50176\nM4 a[164:167],v[60:63],v[12:15],a[164:167]\nM4 a[168:171],v[60:63],v[16:19],a[168:171]\nDT v[132:133],v143 offset:49152\n"
"M4 a[172:175],v[60:63],v[20:23],a[172:175]\nBL v161,s[56:59],s72 offen offset:1024 sc1 lds\nM4 a[176:179],v[60:63],v[24:27],a[176:179]\nDT v[134:135],v143 offset:50176\nM4 a[180:183],v[60:63],v[28:31],a[180:183]\nM4 a[184:187],v[60:63],v[32:35],a[184:187]\nM4 a[188:191],v[60:63],v[36:39],a[188:191]\nM4 a[192:195],v[64:67],v[8:11],a[192:195]\n"
"BL v162,s[56:59],s72 offen offset:2048 sc1 lds\nM4 a[196:199],v[64:67],v[12:15],a[196:199]\nM4 a[200:203],v[64:67],v[16:19],a[200:203]\nM4 a[204:207],v[64:67],v[20:23],a[204:207]\nM4 a[208:211],v[64:67],v[24:27],a[208:211]\nBL v163,s[56:59],s72 offen offset:3072 sc1 lds\nM4 a[212:215],v[64:67],v[28:31],a[212:215]\nM4 a[216:219],v[64:67],v[32:35],a[216:219]\n"
"M4 a[220:223],v[64:67],v[36:39],a[220:223]\nM4 a[224:227],v[68:71],v[8:11],a[224:227]\nSA s68,s68,128\nM4 a[228:231],v[68:71],v[12:15],a[228:231]\nM4 a[232:235],v[68:71],v[16:19],a[232:235]\nM4 a[236:239],v[68:71],v[20:23],a[236:239]\nM4 a[240:243],v[68:71],v[24:27],a[240:243]\nM4 a[244:247],v[68:71],v[28:31],a[244:247]\n"
"M4 a[248:251],v[68:71],v[32:35],a[248:251]\nM4 a[252:255],v[68:71],v[36:39],a[252:255]\nSW vmcnt(14) lgkmcnt(0)\ns_barrier\nM4 a[0:3],v[104:107],v[72:75],a[0:3]\nVU v144,65536,v146\nM4 a[4:7],v[104:107],v[76:79],a[4:7]\nVU v145,65536,v147\n"
"M4 a[8:11],v[104:107],v[80:83],a[8:11]\nDT v[40:41],v164 offset:0\nM4 a[12:15],v[104:107],v[84:87],a[12:15]\nSA m0,s81,49152\nM4 a[16:19],v[104:107],v[88:91],a[16:19]\nDT v[42:43],v164 offset:1024\nM4 a[20:23],v[104:107],v[92:95],a[20:23]\nM4 a[24:27],v[104:107],v[96:99],a[24:27]\n"
"DR v[8:11],v144 offset:0\nM4 a[28:31],v[104:107],v[100:103],a[28:31]\nBL v156,s[48:51],s66 offen offset:0 lds\nM4 a[32:35],v[108:111],v[72:75],a[32:35]\nDR v[12:15],v144 offset:2048\nM4 a[36:39],v[108:111],v[76:79],a[36:39]\nM4 a[40:43],v[108:111],v[80:83],a[40:43]\nDR v[16:19],v144 offset:4096\n"
"M4 a[44:47],v[108:111],v[84:87],a[44:47]\nM4 a[48:51],v[108:111],v[88:91],a[48:51]\nBL v157,s[48:51],s66 offen offset:1024 lds\nM4 a[52:55],v[108:111],v[92:95],a[52:55]\nDR v[20:23],v144 offset:6144\nM4 a[56:59],v[108:111],v[96:99],a[56:59]\nM4 a[60:63],v[108:111],v[100:103],a[60:63]\nDR v[24:27],v144 offset:8192\n"
"M4 a[64:67],v[112:115],v[72:75],a[64:67]\nM4 a[68:71],v[112:115],v[76:79],a[68:71]\nDR v[28:31],v144 offset:10240\nSA m0,s82,49152\nM4 a[72:75],v[112:115],v[80:83],a[72:75]\nM4 a[76:79],v[112:115],v[84:87],a[76:79]\nDR v[32:35],v144 offset:12288\nM4 a[80:83],v[112:115],v[88:91],a[80:83]\n"
"M4 a[84:87],v[112:115],v[92:95],a[84:87]\nDR v[36:39],v144 offset:14336\nM4 a[88:91],v[112:115],v[96:99],a[88:91]\nBL v158,s[52:55],s67 offen lds\nM4 a[92:95],v[112:115],v[100:103],a[92:95]\nM4 a[96:99],v[116:119],v[72:75],a[96:99]\nDT v[44:45],v165 offset:0\nM4 a[100:103],v[116:119],v[76:79],a[100:103]\n"
"M4 a[104:107],v[116:119],v[80:83],a[104:107]\nDT v[46:47],v165 offset:1024\nM4 a[108:111],v[116:119],v[84:87],a[108:111]\nSA s66,s66,262144\nM4 a[112:115],v[116:119],v[88:91],a[112:115]\nDT v[48:49],v166 offset:0\nM4 a[116:119],v[116:119],v[92:95],a[116:119]\nM4 a[120:123],v[116:119],v[96:99],a[120:123]\n"
"DT v[50:51],v166 offset:1024\nM4 a[124:127],v[116:119],v[100:103],a[124:127]\nM4 a[128:131],v[120:123],v[72:75],a[128:131]\nDT v[52:53],v167 offset:0\nSA s67,s67,110592\nM4 a[132:135],v[120:123],v[76:79],a[132:135]\nM4 a[136:139],v[120:123],v[80:83],a[136:139]\nM4 a[140:143],v[120:123],v[84:87],a[140:143]\n"
"DT v[54:55],v167 offset:1024\nM4 a[144:147],v[120:123],v[88:91],a[144:147]\nSA m0,s70,32768\nM4 a[148:151],v[120:123],v[92:95],a[148:151]\nDT v[56:57],v168 offset:0\nM4 a[152:155],v[120:123],v[96:99],a[152:155]\nM4 a[156:159],v[120:123],v[100:103],a[156:159]\nDT v[58:59],v168 offset:512\n"
"M4 a[160:163],v[124:127],v[72:75],a[160:163]\nM4 a[164:167],v[124:127],v[76:79],a[164:167]\nDT v[60:61],v169 offset:0\nBL v160,s[56:59],s68 offen offset:0 sc1 lds\nM4 a[168:171],v[124:127],v[80:83],a[168:171]\nM4 a[172:175],v[124:127],v[84:87],a[172:175]\nDT v[62:63],v169 offset:512\nM4 a[176:179],v[124:127],v[88:91],a[176:179]\n"
"M4 a[180:183],v[124:127],v[92:95],a[180:183]\nM4 a[184:187],v[124:127],v[96:99],a[184:187]\nBL v161,s[56:59],s68 offen offset:1024 sc1 lds\nM4 a[188:191],v[124:127],v[100:103],a[188:191]\nM4 a[192:195],v[128:131],v[72:75],a[192:195]\nM4 a[196:199],v[128:131],v[76:79],a[196:199]\nM4 a[200:203],v[128:131],v[80:83],a[200:203]\nM4 a[204:207],v[128:131],v[84:87],a[204:207]\n"
"BL v162,s[56:59],s68 offen offset:2048 sc1 lds\nM4 a[208:211],v[128:131],v[88:91],a[208:211]\nM4 a[212:215],v[128:131],v[92:95],a[212:215]\nM4 a[216:219],v[128:131],v[96:99],a[216:219]\nM4 a[220:223],v[128:131],v[100:103],a[220:223]\nM4 a[224:227],v[132:135],v[72:75],a[224:227]\nBL v163,s[56:59],s68 offen offset:3072 sc1 lds\nM4 a[228:231],v[132:135],v[76:79],a[228:231]\n"
"M4 a[232:235],v[132:135],v[80:83],a[232:235]\nM4 a[236:239],v[132:135],v[84:87],a[236:239]\nM4 a[240:243],v[132:135],v[88:91],a[240:243]\nM4 a[244:247],v[132:135],v[92:95],a[244:247]\nM4 a[248:251],v[132:135],v[96:99],a[248:251]\nM4 a[252:255],v[132:135],v[100:103],a[252:255]\nSW vmcnt(18) lgkmcnt(0)\ns_barrier\n"
"s_nop 3\nv_lshrrev_b32 v170,4,v200\nVN v171,15,v200\nv_lshrrev_b32 v172,1,v170\nVL v201,10,v172\nVN v172,1,v170\nv_lshl_add_u32 v201,v172,9,v201\nVN v172,7,v171\n"
"v_lshl_add_u32 v201,v172,4,v201\nv_lshrrev_b32 v172,3,v171\nv_lshl_add_u32 v201,v172,8,v201\nSM s71,6291456\nSA s44,s92,s71\ns_addc_u32 s45,s93,0\ns_and_b32 s45,s45,0xffff\nSM s46,16777216\n"
"SM s47,0x20000\nAR v88,a192\nAR v89,a193\nAR v90,a194\nAR v91,a195\nAR v92,a224\nAR v93,a225\nAR v94,a226\n"
"AR v95,a227\nCV v148,v88,v89\nCV v149,v90,v91\nCV v150,v92,v93\nCV v151,v94,v95\nAR v96,a196\nAR v97,a197\nAR v98,a198\n"
"AR v99,a199\nAR v100,a228\nAR v101,a229\nAR v102,a230\nAR v103,a231\nCV v152,v96,v97\nCV v153,v98,v99\nCV v154,v100,v101\n"
"CV v155,v102,v103\nAR v88,a200\nAR v89,a201\nAR v90,a202\nAR v91,a203\nAR v92,a232\nAR v93,a233\nAR v94,a234\n"
"AR v95,a235\nCV v196,v88,v89\nCV v197,v90,v91\nCV v198,v92,v93\nCV v199,v94,v95\nSA m0,s81,0\nAR v96,a204\nAR v97,a205\n"
"AR v98,a206\nAR v99,a207\nAR v100,a236\nAR v101,a237\nAR v102,a238\nAR v103,a239\nCV v204,v96,v97\nCV v205,v98,v99\n"
"CV v206,v100,v101\nCV v207,v102,v103\nv_mov_b32 v108,0x3f803f80\nAW a200,v108\nAW a201,v108\nAW a202,v108\nAW a203,v108\nBL v156,s[48:51],s66 offen offset:0 lds\n"
"AR v72,a0\nAR v73,a1\nAR v74,a2\nAR v75,a3\nAR v76,a32\nAR v77,a33\nAR v78,a34\nAR v79,a35\n"
"CV v64,v72,v73\nCV v65,v74,v75\nCV v66,v76,v77\nCV v67,v78,v79\nM4 a[0:3],v[40:43],v[8:11],0\nM4 a[32:35],v[44:47],v[8:11],0\nBL v157,s[48:51],s66 offen offset:1024 lds\nAR v80,a4\n"
"AR v81,a5\nAR v82,a6\nAR v83,a7\nAR v84,a36\nAR v85,a37\nAR v86,a38\nAR v87,a39\nCV v68,v80,v81\n"
"CV v69,v82,v83\nCV v70,v84,v85\nCV v71,v86,v87\nM4 a[4:7],v[40:43],v[12:15],0\nM4 a[36:39],v[44:47],v[12:15],0\nSA m0,s82,0\nAR v88,a64\nAR v89,a65\n"
"AR v90,a66\nAR v91,a67\nAR v92,a96\nAR v93,a97\nAR v94,a98\nAR v95,a99\nCV v128,v88,v89\nCV v129,v90,v91\n"
"CV v130,v92,v93\nCV v131,v94,v95\nM4 a[64:67],v[48:51],v[8:11],0\nM4 a[96:99],v[52:55],v[8:11],0\nBL v158,s[52:55],s67 offen lds\nAR v96,a68\nAR v97,a69\nAR v98,a70\n"
"AR v99,a71\nAR v100,a100\nAR v101,a101\nAR v102,a102\nAR v103,a103\nCV v132,v96,v97\nCV v133,v98,v99\nCV v134,v100,v101\n"
"CV v135,v102,v103\nM4 a[68:71],v[48:51],v[12:15],0\nM4 a[100:103],v[52:55],v[12:15],0\nSA s66,s66,262144\nAR v72,a128\nAR v73,a129\nAR v74,a130\nAR v75,a131\n"
"AR v76,a160\nAR v77,a161\nAR v78,a162\nAR v79,a163\nCV v136,v72,v73\nCV v137,v74,v75\nCV v138,v76,v77\nCV v139,v78,v79\n"
"M4 a[128:131],v[56:59],v[8:11],0\nM4 a[160:163],v[60:63],v[8:11],0\nSA s67,s67,110592\nAR v80,a132\nAR v81,a133\nAR v82,a134\nAR v83,a135\nAR v84,a164\n"
"AR v85,a165\nAR v86,a166\nAR v87,a167\nCV v140,v80,v81\nCV v141,v82,v83\nCV v142,v84,v85\nCV v143,v86,v87\nM4 a[132:135],v[56:59],v[12:15],0\n"
"M4 a[164:167],v[60:63],v[12:15],0\nSA m0,s70,36864\nAR v72,a8\nAR v73,a9\nAR v74,a10\nAR v75,a11\nAR v76,a40\nAR v77,a41\n"
"AR v78,a42\nAR v79,a43\nCV v172,v72,v73\nCV v173,v74,v75\nCV v174,v76,v77\nCV v175,v78,v79\nM4 a[8:11],v[40:43],v[16:19],0\nM4 a[40:43],v[44:47],v[16:19],0\n"
"SA s72,s68,40960\nAR v80,a12\nAR v81,a13\nAR v82,a14\nAR v83,a15\nAR v84,a44\nAR v85,a45\nAR v86,a46\n"
"AR v87,a47\nCV v176,v80,v81\nCV v177,v82,v83\nCV v178,v84,v85\nCV v179,v86,v87\nM4 a[12:15],v[40:43],v[20:23],0\nM4 a[44:47],v[44:47],v[20:23],0\nBL v160,s[56:59],s72 offen offset:0 sc1 lds\n"
"AR v88,a72\nAR v89,a73\nAR v90,a74\nAR v91,a75\nAR v92,a104\nAR v93,a105\nAR v94,a106\nAR v95,a107\n"
"CV v180,v88,v89\nCV v181,v90,v91\nCV v182,v92,v93\nCV v183,v94,v95\nM4 a[72:75],v[48:51],v[16:19],0\nM4 a[104:107],v[52:55],v[16:19],0\nBL v161,s[56:59],s72 offen offset:1024 sc1 lds\nAR v96,a76\n"
"AR v97,a77\nAR v98,a78\nAR v99,a79\nAR v100,a108\nAR v101,a109\nAR v102,a110\nAR v103,a111\nCV v184,v96,v97\n"
"CV v185,v98,v99\nCV v186,v100,v101\nCV v187,v102,v103\nM4 a[76:79],v[48:51],v[20:23],0\nM4 a[108:111],v[52:55],v[20:23],0\nBL v162,s[56:59],s72 offen offset:2048 sc1 lds\nAR v72,a136\nAR v73,a137\n"
"AR v74,a138\nAR v75,a139\nAR v76,a168\nAR v77,a169\nAR v78,a170\nAR v79,a171\nCV v188,v72,v73\nCV v189,v74,v75\n"
"CV v190,v76,v77\nCV v191,v78,v79\nM4 a[136:139],v[56:59],v[16:19],0\nM4 a[168:171],v[60:63],v[16:19],0\nBL v163,s[56:59],s72 offen offset:3072 sc1 lds\nAR v80,a140\nAR v81,a141\nAR v82,a142\n"
"AR v83,a143\nAR v84,a172\nAR v85,a173\nAR v86,a174\nAR v87,a175\nCV v192,v80,v81\nCV v193,v82,v83\nCV v194,v84,v85\n"
"CV v195,v86,v87\nM4 a[140:143],v[56:59],v[20:23],0\nM4 a[172:175],v[60:63],v[20:23],0\nSA s68,s68,128\nAR v72,a16\nAR v73,a17\nAR v74,a18\nAR v75,a19\n"
"AR v76,a48\nAR v77,a49\nAR v78,a50\nAR v79,a51\nCV v208,v72,v73\nCV v209,v74,v75\nCV v210,v76,v77\nCV v211,v78,v79\n"
"M4 a[16:19],v[40:43],v[24:27],0\nM4 a[48:51],v[44:47],v[24:27],0\nAR v80,a20\nAR v81,a21\nAR v82,a22\nAR v83,a23\nAR v84,a52\nAR v85,a53\n"
"AR v86,a54\nAR v87,a55\nCV v212,v80,v81\nCV v213,v82,v83\nCV v214,v84,v85\nCV v215,v86,v87\nM4 a[20:23],v[40:43],v[28:31],0\nM4 a[52:55],v[44:47],v[28:31],0\n"
"AR v88,a80\nAR v89,a81\nAR v90,a82\nAR v91,a83\nAR v92,a112\nAR v93,a113\nAR v94,a114\nAR v95,a115\n"
"CV v216,v88,v89\nCV v217,v90,v91\nCV v218,v92,v93\nCV v219,v94,v95\nM4 a[80:83],v[48:51],v[24:27],0\nM4 a[112:115],v[52:55],v[24:27],0\nAR v96,a84\nAR v97,a85\n"
"AR v98,a86\nAR v99,a87\nAR v100,a116\nAR v101,a117\nAR v102,a118\nAR v103,a119\nCV v220,v96,v97\nCV v221,v98,v99\n"
"CV v222,v100,v101\nCV v223,v102,v103\nM4 a[84:87],v[48:51],v[28:31],0\nM4 a[116:119],v[52:55],v[28:31],0\nAR v72,a144\nAR v73,a145\nAR v74,a146\nAR v75,a147\n"
"AR v76,a176\nAR v77,a177\nAR v78,a178\nAR v79,a179\nCV v224,v72,v73\nCV v225,v74,v75\nCV v226,v76,v77\nCV v227,v78,v79\n"
"M4 a[144:147],v[56:59],v[24:27],0\nM4 a[176:179],v[60:63],v[24:27],0\nAR v80,a148\nAR v81,a149\nAR v82,a150\nAR v83,a151\nAR v84,a180\nAR v85,a181\n"
"AR v86,a182\nAR v87,a183\nCV v228,v80,v81\nCV v229,v82,v83\nCV v230,v84,v85\nCV v231,v86,v87\nM4 a[148:151],v[56:59],v[28:31],0\nM4 a[180:183],v[60:63],v[28:31],0\n"
"AR v72,a24\nAR v73,a25\nAR v74,a26\nAR v75,a27\nAR v76,a56\nAR v77,a57\nAR v78,a58\nAR v79,a59\n"
"CV v240,v72,v73\nCV v241,v74,v75\nCV v242,v76,v77\nCV v243,v78,v79\nM4 a[24:27],v[40:43],v[32:35],0\nM4 a[56:59],v[44:47],v[32:35],0\nAR v80,a28\nAR v81,a29\n"
"AR v82,a30\nAR v83,a31\nAR v84,a60\nAR v85,a61\nAR v86,a62\nAR v87,a63\nCV v244,v80,v81\nCV v245,v82,v83\n"
"CV v246,v84,v85\nCV v247,v86,v87\nM4 a[28:31],v[40:43],v[36:39],0\nM4 a[60:63],v[44:47],v[36:39],0\nAR v88,a88\nAR v89,a89\nAR v90,a90\nAR v91,a91\n"
"AR v92,a120\nAR v93,a121\nAR v94,a122\nAR v95,a123\nCV v248,v88,v89\nCV v249,v90,v91\nCV v250,v92,v93\nCV v251,v94,v95\n"
"M4 a[88:91],v[48:51],v[32:35],0\nM4 a[120:123],v[52:55],v[32:35],0\nAR v96,a92\nAR v97,a93\nAR v98,a94\nAR v99,a95\nAR v100,a124\nAR v101,a125\n"
"AR v102,a126\nAR v103,a127\nCV v252,v96,v97\nCV v253,v98,v99\nCV v254,v100,v101\nCV v255,v102,v103\nM4 a[92:95],v[48:51],v[36:39],0\nM4 a[124:127],v[52:55],v[36:39],0\n"
"AR v72,a152\nAR v73,a153\nAR v74,a154\nAR v75,a155\nAR v76,a184\nAR v77,a185\nAR v78,a186\nAR v79,a187\n"
"CV v104,v72,v73\nCV v105,v74,v75\nCV v106,v76,v77\nCV v107,v78,v79\nAW a224,v104\nAW a225,v105\nAW a226,v106\nAW a227,v107\n"
"M4 a[152:155],v[56:59],v[32:35],0\nM4 a[184:187],v[60:63],v[32:35],0\nAR v80,a156\nAR v81,a157\nAR v82,a158\nAR v83,a159\nAR v84,a188\nAR v85,a189\n"
"AR v86,a190\nAR v87,a191\nCV v108,v80,v81\nCV v109,v82,v83\nCV v110,v84,v85\nCV v111,v86,v87\nAW a228,v108\nAW a229,v109\n"
"AW a230,v110\nAW a231,v111\nM4 a[156:159],v[56:59],v[36:39],0\nM4 a[188:191],v[60:63],v[36:39],0\nAR v88,a208\nAR v89,a209\nAR v90,a210\nAR v91,a211\n"
"AR v92,a240\nAR v93,a241\nAR v94,a242\nAR v95,a243\nCV v232,v88,v89\nCV v233,v90,v91\nCV v234,v92,v93\nCV v235,v94,v95\n"
"AR v96,a212\nAR v97,a213\nAR v98,a214\nAR v99,a215\nAR v100,a244\nAR v101,a245\nAR v102,a246\nAR v103,a247\n"
"CV v236,v96,v97\nCV v237,v98,v99\nCV v238,v100,v101\nCV v239,v102,v103\nAR v88,a216\nAR v89,a217\nAR v90,a218\nAR v91,a219\n"
"AR v92,a248\nAR v93,a249\nAR v94,a250\nAR v95,a251\nCV v104,v88,v89\nCV v105,v90,v91\nCV v106,v92,v93\nCV v107,v94,v95\n"
"AW a232,v104\nAW a233,v105\nAW a234,v106\nAW a235,v107\nAR v96,a220\nAR v97,a221\nAR v98,a222\nAR v99,a223\n"
"AR v100,a252\nAR v101,a253\nAR v102,a254\nAR v103,a255\nCV v108,v96,v97\nCV v109,v98,v99\nCV v110,v100,v101\nCV v111,v102,v103\n"
"AW a236,v108\nAW a237,v109\nAW a238,v110\nAW a239,v111\nM4 a[192:195],v[56:59],a[200:203],0\nM4 a[196:199],v[60:63],a[200:203],0\nDT v[104:105],v164 offset:16384\nDT v[106:107],v164 offset:17408\n"
"DR v[72:75],v145 offset:0\nDR v[76:79],v145 offset:2048\nDR v[80:83],v145 offset:4096\nDR v[84:87],v145 offset:6144\nDR v[88:91],v145 offset:8192\nDR v[92:95],v145 offset:10240\nDR v[96:99],v145 offset:12288\nDR v[100:103],v145 offset:14336\n"
"DT v[108:109],v165 offset:16384\nDT v[110:111],v165 offset:17408\nDT v[112:113],v166 offset:16384\nDT v[114:115],v166 offset:17408\nDT v[116:117],v167 offset:16384\nDT v[118:119],v167 offset:17408\nDT v[120:121],v168 offset:16384\nDT v[122:123],v168 offset:16896\n"
"DT v[124:125],v169 offset:16384\nDT v[126:127],v169 offset:16896\nSW vmcnt(14) lgkmcnt(0)\ns_barrier\nM4 a[0:3],v[104:107],v[72:75],a[0:3]\nVU v144,0,v146\nM4 a[4:7],v[104:107],v[76:79],a[4:7]\nVU v145,0,v147\n"
"SA s75,s80,0\nM4 a[8:11],v[104:107],v[80:83],a[8:11]\nDT v[40:41],v164 offset:32768\nM4 a[12:15],v[104:107],v[84:87],a[12:15]\nDT v[42:43],v164 offset:33792\nSA m0,s81,16384\nM4 a[16:19],v[104:107],v[88:91],a[16:19]\nM4 a[20:23],v[104:107],v[92:95],a[20:23]\n"
"DR v[8:11],v144 offset:0\nM4 a[24:27],v[104:107],v[96:99],a[24:27]\nBL v156,s[48:51],s66 offen offset:0 lds\nM4 a[28:31],v[104:107],v[100:103],a[28:31]\nDR v[12:15],v144 offset:2048\nM4 a[32:35],v[108:111],v[72:75],a[32:35]\nDR v[16:19],v144 offset:4096\nM4 a[36:39],v[108:111],v[76:79],a[36:39]\n"
"M4 a[40:43],v[108:111],v[80:83],a[40:43]\nDR v[20:23],v144 offset:6144\nBL v157,s[48:51],s66 offen offset:1024 lds\nM4 a[44:47],v[108:111],v[84:87],a[44:47]\nM4 a[48:51],v[108:111],v[88:91],a[48:51]\nDR v[24:27],v144 offset:8192\nM4 a[52:55],v[108:111],v[92:95],a[52:55]\nDR v[28:31],v144 offset:10240\n"
"BS v[64:67],v201,s[44:47],s75 offen offset:0 sc1\nM4 a[56:59],v[108:111],v[96:99],a[56:59]\nSA m0,s82,16384\nM4 a[60:63],v[108:111],v[100:103],a[60:63]\nDR v[32:35],v144 offset:12288\nM4 a[64:67],v[112:115],v[72:75],a[64:67]\nM4 a[68:71],v[112:115],v[76:79],a[68:71]\nDR v[36:39],v144 offset:14336\n"
"BL v158,s[52:55],s67 offen lds\nM4 a[72:75],v[112:115],v[80:83],a[72:75]\nM4 a[76:79],v[112:115],v[84:87],a[76:79]\nDT v[44:45],v165 offset:32768\nM4 a[80:83],v[112:115],v[88:91],a[80:83]\nDT v[46:47],v165 offset:33792\nM4 a[84:87],v[112:115],v[92:95],a[84:87]\nSA s66,s66,262144\n"
"M4 a[88:91],v[112:115],v[96:99],a[88:91]\nDT v[48:49],v166 offset:32768\nM4 a[92:95],v[112:115],v[100:103],a[92:95]\nM4 a[96:99],v[116:119],v[72:75],a[96:99]\nDT v[50:51],v166 offset:33792\nM4 a[100:103],v[116:119],v[76:79],a[100:103]\nDT v[52:53],v167 offset:32768\nSA s67,s67,110592\n"
"SA s75,s80,0\nM4 a[104:107],v[116:119],v[80:83],a[104:107]\nM4 a[108:111],v[116:119],v[84:87],a[108:111]\nDT v[54:55],v167 offset:33792\nM4 a[112:115],v[116:119],v[88:91],a[112:115]\nSA m0,s70,65536\nM4 a[116:119],v[116:119],v[92:95],a[116:119]\nDT v[56:57],v168 offset:32768\n"
"M4 a[120:123],v[116:119],v[96:99],a[120:123]\nDT v[58:59],v168 offset:33280\nM4 a[124:127],v[116:119],v[100:103],a[124:127]\nM4 a[128:131],v[120:123],v[72:75],a[128:131]\nDT v[60:61],v169 offset:32768\nBL v160,s[56:59],s68 offen offset:0 sc1 lds\nM4 a[132:135],v[120:123],v[76:79],a[132:135]\nM4 a[136:139],v[120:123],v[80:83],a[136:139]\n"
"DT v[62:63],v169 offset:33280\nM4 a[140:143],v[120:123],v[84:87],a[140:143]\nM4 a[144:147],v[120:123],v[88:91],a[144:147]\nBL v161,s[56:59],s68 offen offset:1024 sc1 lds\nM4 a[148:151],v[120:123],v[92:95],a[148:151]\nBS v[68:71],v201,s[44:47],s75 offen offset:128 sc1\nM4 a[152:155],v[120:123],v[96:99],a[152:155]\nM4 a[156:159],v[120:123],v[100:103],a[156:159]\n"
"BL v162,s[56:59],s68 offen offset:2048 sc1 lds\nM4 a[192:195],v[120:123],a[200:203],a[192:195]\nM4 a[160:163],v[124:127],v[72:75],a[160:163]\nM4 a[164:167],v[124:127],v[76:79],a[164:167]\nM4 a[168:171],v[124:127],v[80:83],a[168:171]\nBL v163,s[56:59],s68 offen offset:3072 sc1 lds\nM4 a[172:175],v[124:127],v[84:87],a[172:175]\nM4 a[176:179],v[124:127],v[88:91],a[176:179]\n"
"M4 a[180:183],v[124:127],v[92:95],a[180:183]\nM4 a[184:187],v[124:127],v[96:99],a[184:187]\nM4 a[188:191],v[124:127],v[100:103],a[188:191]\nM4 a[196:199],v[124:127],a[200:203],a[196:199]\nSW vmcnt(20) lgkmcnt(0)\ns_barrier\nM4 a[0:3],v[40:43],v[8:11],a[0:3]\nDT v[104:105],v164 offset:49152\n"
"M4 a[4:7],v[40:43],v[12:15],a[4:7]\nDT v[106:107],v164 offset:50176\nSA m0,s81,32768\nSA s75,s80,0\nM4 a[8:11],v[40:43],v[16:19],a[8:11]\nM4 a[12:15],v[40:43],v[20:23],a[12:15]\nDR v[72:75],v145 offset:0\nM4 a[16:19],v[40:43],v[24:27],a[16:19]\n"
"BL v156,s[48:51],s66 offen offset:0 lds\nM4 a[20:23],v[40:43],v[28:31],a[20:23]\nDR v[76:79],v145 offset:2048\nM4 a[24:27],v[40:43],v[32:35],a[24:27]\nM4 a[28:31],v[40:43],v[36:39],a[28:31]\nDR v[80:83],v145 offset:4096\nBL v157,s[48:51],s66 offen offset:1024 lds\nM4 a[32:35],v[44:47],v[8:11],a[32:35]\n"
"M4 a[36:39],v[44:47],v[12:15],a[36:39]\nDR v[84:87],v145 offset:6144\nM4 a[40:43],v[44:47],v[16:19],a[40:43]\nDR v[88:91],v145 offset:8192\nSA m0,s82,32768\nM4 a[44:47],v[44:47],v[20:23],a[44:47]\nM4 a[48:51],v[44:47],v[24:27],a[48:51]\nDR v[92:95],v145 offset:10240\n"
"M4 a[52:55],v[44:47],v[28:31],a[52:55]\nBS v[128:131],v201,s[44:47],s75 offen offset:2048 sc1\nM4 a[56:59],v[44:47],v[32:35],a[56:59]\nDR v[96:99],v145 offset:12288\nBL v158,s[52:55],s67 offen lds\nM4 a[60:63],v[44:47],v[36:39],a[60:63]\nM4 a[64:67],v[48:51],v[8:11],a[64:67]\nDR v[100:103],v145 offset:14336\n"
"M4 a[68:71],v[48:51],v[12:15],a[68:71]\nSA s66,s66,262144\nM4 a[72:75],v[48:51],v[16:19],a[72:75]\nDT v[108:109],v165 offset:49152\nM4 a[76:79],v[48:51],v[20:23],a[76:79]\nDT v[110:111],v165 offset:50176\nM4 a[80:83],v[48:51],v[24:27],a[80:83]\nSA s67,s67,110592\n"
"M4 a[84:87],v[48:51],v[28:31],a[84:87]\nDT v[112:113],v166 offset:49152\nM4 a[88:91],v[48:51],v[32:35],a[88:91]\nM4 a[92:95],v[48:51],v[36:39],a[92:95]\nDT v[114:115],v166 offset:50176\nM4 a[96:99],v[52:55],v[8:11],a[96:99]\nSA m0,s70,69632\nM4 a[100:103],v[52:55],v[12:15],a[100:103]\n"
"DT v[116:117],v167 offset:49152\nSA s75,s80,0\nM4 a[104:107],v[52:55],v[16:19],a[104:107]\nM4 a[108:111],v[52:55],v[20:23],a[108:111]\nDT v[118:119],v167 offset:50176\nSA s72,s68,40960\nM4 a[112:115],v[52:55],v[24:27],a[112:115]\nDT v[120:121],v168 offset:49152\n"
"M4 a[116:119],v[52:55],v[28:31],a[116:119]\nM4 a[120:123],v[52:55],v[32:35],a[120:123]\nDT v[122:123],v168 offset:49664\nBL v160,s[56:59],s72 offen offset:0 sc1 lds\nM4 a[124:127],v[52:55],v[36:39],a[124:127]\nM4 a[128:131],v[56:59],v[8:11],a[128:131]\nDT v[124:125],v169 offset:49152\nM4 a[132:135],v[56:59],v[12:15],a[132:135]\n"
"BL v161,s[56:59],s72 offen offset:1024 sc1 lds\nM4 a[136:139],v[56:59],v[16:19],a[136:139]\nDT v[126:127],v169 offset:49664\nM4 a[140:143],v[56:59],v[20:23],a[140:143]\nM4 a[144:147],v[56:59],v[24:27],a[144:147]\nM4 a[148:151],v[56:59],v[28:31],a[148:151]\nBL v162,s[56:59],s72 offen offset:2048 sc1 lds\nBS v[132:135],v201,s[44:47],s75 offen offset:2176 sc1\n"
"M4 a[152:155],v[56:59],v[32:35],a[152:155]\nM4 a[156:159],v[56:59],v[36:39],a[156:159]\nM4 a[192:195],v[56:59],a[200:203],a[192:195]\nBL v163,s[56:59],s72 offen offset:3072 sc1 lds\nM4 a[160:163],v[60:63],v[8:11],a[160:163]\nM4 a[164:167],v[60:63],v[12:15],a[164:167]\nM4 a[168:171],v[60:63],v[16:19],a[168:171]\nSA s68,s68,128\n"
"M4 a[172:175],v[60:63],v[20:23],a[172:175]\nM4 a[176:179],v[60:63],v[24:27],a[176:179]\nM4 a[180:183],v[60:63],v[28:31],a[180:183]\nM4 a[184:187],v[60:63],v[32:35],a[184:187]\nM4 a[188:191],v[60:63],v[36:39],a[188:191]\nM4 a[196:199],v[60:63],a[200:203],a[196:199]\nSW vmcnt(18) lgkmcnt(0)\ns_barrier\n"
"M4 a[0:3],v[104:107],v[72:75],a[0:3]\nVU v144,32768,v146\nM4 a[4:7],v[104:107],v[76:79],a[4:7]\nVU v145,32768,v147\nSA s75,s80,4096\nM4 a[8:11],v[104:107],v[80:83],a[8:11]\nDT v[40:41],v164 offset:0\nM4 a[12:15],v[104:107],v[84:87],a[12:15]\n"
"DT v[42:43],v164 offset:1024\nSA m0,s81,49152\nM4 a[16:19],v[104:107],v[88:91],a[16:19]\nM4 a[20:23],v[104:107],v[92:95],a[20:23]\nDR v[8:11],v144 offset:0\nM4 a[24:27],v[104:107],v[96:99],a[24:27]\nBL v156,s[48:51],s66 offen offset:0 lds\nM4 a[28:31],v[104:107],v[100:103],a[28:31]\n"
"DR v[12:15],v144 offset:2048\nM4 a[32:35],v[108:111],v[72:75],a[32:35]\nDR v[16:19],v144 offset:4096\nM4 a[36:39],v[108:111],v[76:79],a[36:39]\nM4 a[40:43],v[108:111],v[80:83],a[40:43]\nDR v[20:23],v144 offset:6144\nBL v157,s[48:51],s66 offen offset:1024 lds\nM4 a[44:47],v[108:111],v[84:87],a[44:47]\n"
"M4 a[48:51],v[108:111],v[88:91],a[48:51]\nDR v[24:27],v144 offset:8192\nM4 a[52:55],v[108:111],v[92:95],a[52:55]\nDR v[28:31],v144 offset:10240\nBS v[136:139],v201,s[44:47],s75 offen offset:0 sc1\nM4 a[56:59],v[108:111],v[96:99],a[56:59]\nSA m0,s82,49152\nM4 a[60:63],v[108:111],v[100:103],a[60:63]\n"
"DR v[32:35],v144 offset:12288\nM4 a[64:67],v[112:115],v[72:75],a[64:67]\nM4 a[68:71],v[112:115],v[76:79],a[68:71]\nDR v[36:39],v144 offset:14336\nBL v158,s[52:55],s67 offen lds\nM4 a[72:75],v[112:115],v[80:83],a[72:75]\nM4 a[76:79],v[112:115],v[84:87],a[76:79]\nDT v[44:45],v165 offset:0\n"
"M4 a[80:83],v[112:115],v[88:91],a[80:83]\nDT v[46:47],v165 offset:1024\nM4 a[84:87],v[112:115],v[92:95],a[84:87]\nSA s66,s66,262144\nM4 a[88:91],v[112:115],v[96:99],a[88:91]\nDT v[48:49],v166 offset:0\nM4 a[92:95],v[112:115],v[100:103],a[92:95]\nM4 a[96:99],v[116:119],v[72:75],a[96:99]\n"
"DT v[50:51],v166 offset:1024\nM4 a[100:103],v[116:119],v[76:79],a[100:103]\nDT v[52:53],v167 offset:0\nSA s67,s67,110592\nSA s75,s80,4096\nM4 a[104:107],v[116:119],v[80:83],a[104:107]\nM4 a[108:111],v[116:119],v[84:87],a[108:111]\nDT v[54:55],v167 offset:1024\n"
"M4 a[112:115],v[116:119],v[88:91],a[112:115]\nSA m0,s70,0\nM4 a[116:119],v[116:119],v[92:95],a[116:119]\nDT v[56:57],v168 offset:0\nM4 a[120:123],v[116:119],v[96:99],a[120:123]\nDT v[58:59],v168 offset:512\nM4 a[124:127],v[116:119],v[100:103],a[124:127]\nM4 a[128:131],v[120:123],v[72:75],a[128:131]\n"
"DT v[60:61],v169 offset:0\nBL v160,s[56:59],s68 offen offset:0 sc1 lds\nM4 a[132:135],v[120:123],v[76:79],a[132:135]\nM4 a[136:139],v[120:123],v[80:83],a[136:139]\nDT v[62:63],v169 offset:512\nM4 a[140:143],v[120:123],v[84:87],a[140:143]\nM4 a[144:147],v[120:123],v[88:91],a[144:147]\nBL v161,s[56:59],s68 offen offset:1024 sc1 lds\n"
"M4 a[148:151],v[120:123],v[92:95],a[148:151]\nBS v[140:143],v201,s[44:47],s75 offen offset:128 sc1\nM4 a[152:155],v[120:123],v[96:99],a[152:155]\nM4 a[156:159],v[120:123],v[100:103],a[156:159]\nBL v162,s[56:59],s68 offen offset:2048 sc1 lds\nM4 a[192:195],v[120:123],a[200:203],a[192:195]\nM4 a[160:163],v[124:127],v[72:75],a[160:163]\nM4 a[164:167],v[124:127],v[76:79],a[164:167]\n"
"M4 a[168:171],v[124:127],v[80:83],a[168:171]\nBL v163,s[56:59],s68 offen offset:3072 sc1 lds\nM4 a[172:175],v[124:127],v[84:87],a[172:175]\nM4 a[176:179],v[124:127],v[88:91],a[176:179]\nM4 a[180:183],v[124:127],v[92:95],a[180:183]\nM4 a[184:187],v[124:127],v[96:99],a[184:187]\nM4 a[188:191],v[124:127],v[100:103],a[188:191]\nM4 a[196:199],v[124:127],a[200:203],a[196:199]\n"
"SW vmcnt(23) lgkmcnt(0)\ns_barrier\nM4 a[0:3],v[40:43],v[8:11],a[0:3]\nDT v[104:105],v164 offset:16384\nM4 a[4:7],v[40:43],v[12:15],a[4:7]\nDT v[106:107],v164 offset:17408\nSA m0,s81,0\nSA s75,s80,4096\n"
"M4 a[8:11],v[40:43],v[16:19],a[8:11]\nM4 a[12:15],v[40:43],v[20:23],a[12:15]\nDR v[72:75],v145 offset:0\nM4 a[16:19],v[40:43],v[24:27],a[16:19]\nBL v156,s[48:51],s66 offen offset:0 lds\nM4 a[20:23],v[40:43],v[28:31],a[20:23]\nDR v[76:79],v145 offset:2048\nM4 a[24:27],v[40:43],v[32:35],a[24:27]\n"
"M4 a[28:31],v[40:43],v[36:39],a[28:31]\nDR v[80:83],v145 offset:4096\nBL v157,s[48:51],s66 offen offset:1024 lds\nM4 a[32:35],v[44:47],v[8:11],a[32:35]\nM4 a[36:39],v[44:47],v[12:15],a[36:39]\nDR v[84:87],v145 offset:6144\nM4 a[40:43],v[44:47],v[16:19],a[40:43]\nDR v[88:91],v145 offset:8192\n"
"SA m0,s82,0\nM4 a[44:47],v[44:47],v[20:23],a[44:47]\nM4 a[48:51],v[44:47],v[24:27],a[48:51]\nDR v[92:95],v145 offset:10240\nM4 a[52:55],v[44:47],v[28:31],a[52:55]\nBS v[148:151],v201,s[44:47],s75 offen offset:2048 sc1\nM4 a[56:59],v[44:47],v[32:35],a[56:59]\nDR v[96:99],v145 offset:12288\n"
"BL v158,s[52:55],s67 offen lds\nM4 a[60:63],v[44:47],v[36:39],a[60:63]\nM4 a[64:67],v[48:51],v[8:11],a[64:67]\nDR v[100:103],v145 offset:14336\nM4 a[68:71],v[48:51],v[12:15],a[68:71]\nSA s66,s66,262144\nM4 a[72:75],v[48:51],v[16:19],a[72:75]\nDT v[108:109],v165 offset:16384\n"
"M4 a[76:79],v[48:51],v[20:23],a[76:79]\nDT v[110:111],v165 offset:17408\nM4 a[80:83],v[48:51],v[24:27],a[80:83]\nSA s67,s67,110592\nM4 a[84:87],v[48:51],v[28:31],a[84:87]\nDT v[112:113],v166 offset:16384\nM4 a[88:91],v[48:51],v[32:35],a[88:91]\nM4 a[92:95],v[48:51],v[36:39],a[92:95]\n"
"DT v[114:115],v166 offset:17408\nM4 a[96:99],v[52:55],v[8:11],a[96:99]\nSA m0,s70,4096\nM4 a[100:103],v[52:55],v[12:15],a[100:103]\nDT v[116:117],v167 offset:16384\nSA s75,s80,4096\nM4 a[104:107],v[52:55],v[16:19],a[104:107]\nM4 a[108:111],v[52:55],v[20:23],a[108:111]\n"
"DT v[118:119],v167 offset:17408\nSA s72,s68,40960\nM4 a[112:115],v[52:55],v[24:27],a[112:115]\nDT v[120:121],v168 offset:16384\nM4 a[116:119],v[52:55],v[28:31],a[116:119]\nM4 a[120:123],v[52:55],v[32:35],a[120:123]\nDT v[122:123],v168 offset:16896\nBL v160,s[56:59],s72 offen offset:0 sc1 lds\n"
"M4 a[124:127],v[52:55],v[36:39],a[124:127]\nM4 a[128:131],v[56:59],v[8:11],a[128:131]\nDT v[124:125],v169 offset:16384\nM4 a[132:135],v[56:59],v[12:15],a[132:135]\nBL v161,s[56:59],s72 offen offset:1024 sc1 lds\nM4 a[136:139],v[56:59],v[16:19],a[136:139]\nDT v[126:127],v169 offset:16896\nM4 a[140:143],v[56:59],v[20:23],a[140:143]\n"
"M4 a[144:147],v[56:59],v[24:27],a[144:147]\nM4 a[148:151],v[56:59],v[28:31],a[148:151]\nBL v162,s[56:59],s72 offen offset:2048 sc1 lds\nBS v[152:155],v201,s[44:47],s75 offen offset:2176 sc1\nM4 a[152:155],v[56:59],v[32:35],a[152:155]\nM4 a[156:159],v[56:59],v[36:39],a[156:159]\nM4 a[192:195],v[56:59],a[200:203],a[192:195]\nBL v163,s[56:59],s72 offen offset:3072 sc1 lds\n"
"M4 a[160:163],v[60:63],v[8:11],a[160:163]\nM4 a[164:167],v[60:63],v[12:15],a[164:167]\nM4 a[168:171],v[60:63],v[16:19],a[168:171]\nSA s68,s68,128\nM4 a[172:175],v[60:63],v[20:23],a[172:175]\nM4 a[176:179],v[60:63],v[24:27],a[176:179]\nM4 a[180:183],v[60:63],v[28:31],a[180:183]\nM4 a[184:187],v[60:63],v[32:35],a[184:187]\n"
"M4 a[188:191],v[60:63],v[36:39],a[188:191]\nM4 a[196:199],v[60:63],a[200:203],a[196:199]\nSW vmcnt(18) lgkmcnt(0)\ns_barrier\nM4 a[0:3],v[104:107],v[72:75],a[0:3]\nVU v144,65536,v146\nM4 a[4:7],v[104:107],v[76:79],a[4:7]\nVU v145,65536,v147\n"
"SA s75,s80,8192\nM4 a[8:11],v[104:107],v[80:83],a[8:11]\nDT v[40:41],v164 offset:32768\nM4 a[12:15],v[104:107],v[84:87],a[12:15]\nDT v[42:43],v164 offset:33792\nSA m0,s81,16384\nM4 a[16:19],v[104:107],v[88:91],a[16:19]\nM4 a[20:23],v[104:107],v[92:95],a[20:23]\n"
"DR v[8:11],v144 offset:0\nM4 a[24:27],v[104:107],v[96:99],a[24:27]\nBL v156,s[48:51],s66 offen offset:0 lds\nM4 a[28:31],v[104:107],v[100:103],a[28:31]\nDR v[12:15],v144 offset:2048\nM4 a[32:35],v[108:111],v[72:75],a[32:35]\nDR v[16:19],v144 offset:4096\nM4 a[36:39],v[108:111],v[76:79],a[36:39]\n"
"M4 a[40:43],v[108:111],v[80:83],a[40:43]\nDR v[20:23],v144 offset:6144\nBL v157,s[48:51],s66 offen offset:1024 lds\nM4 a[44:47],v[108:111],v[84:87],a[44:47]\nM4 a[48:51],v[108:111],v[88:91],a[48:51]\nDR v[24:27],v144 offset:8192\nM4 a[52:55],v[108:111],v[92:95],a[52:55]\nDR v[28:31],v144 offset:10240\n"
"BS v[172:175],v201,s[44:47],s75 offen offset:0 sc1\nM4 a[56:59],v[108:111],v[96:99],a[56:59]\nSA m0,s82,16384\nM4 a[60:63],v[108:111],v[100:103],a[60:63]\nDR v[32:35],v144 offset:12288\nM4 a[64:67],v[112:115],v[72:75],a[64:67]\nM4 a[68:71],v[112:115],v[76:79],a[68:71]\nDR v[36:39],v144 offset:14336\n"
"BL v158,s[52:55],s67 offen lds\nM4 a[72:75],v[112:115],v[80:83],a[72:75]\nM4 a[76:79],v[112:115],v[84:87],a[76:79]\nDT v[44:45],v165 offset:32768\nM4 a[80:83],v[112:115],v[88:91],a[80:83]\nDT v[46:47],v165 offset:33792\nM4 a[84:87],v[112:115],v[92:95],a[84:87]\nSA s66,s66,262144\n"
"M4 a[88:91],v[112:115],v[96:99],a[88:91]\nDT v[48:49],v166 offset:32768\nM4 a[92:95],v[112:115],v[100:103],a[92:95]\nM4 a[96:99],v[116:119],v[72:75],a[96:99]\nDT v[50:51],v166 offset:33792\nM4 a[100:103],v[116:119],v[76:79],a[100:103]\nDT v[52:53],v167 offset:32768\nSA s67,s67,110592\n"
"SA s75,s80,8192\nM4 a[104:107],v[116:119],v[80:83],a[104:107]\nM4 a[108:111],v[116:119],v[84:87],a[108:111]\nDT v[54:55],v167 offset:33792\nM4 a[112:115],v[116:119],v[88:91],a[112:115]\nSA m0,s70,32768\nM4 a[116:119],v[116:119],v[92:95],a[116:119]\nDT v[56:57],v168 offset:32768\n"
"M4 a[120:123],v[116:119],v[96:99],a[120:123]\nDT v[58:59],v168 offset:33280\nM4 a[124:127],v[116:119],v[100:103],a[124:127]\nM4 a[128:131],v[120:123],v[72:75],a[128:131]\nDT v[60:61],v169 offset:32768\nBL v160,s[56:59],s68 offen offset:0 sc1 lds\nM4 a[132:135],v[120:123],v[76:79],a[132:135]\nM4 a[136:139],v[120:123],v[80:83],a[136:139]\n"
"DT v[62:63],v169 offset:33280\nM4 a[140:143],v[120:123],v[84:87],a[140:143]\nM4 a[144:147],v[120:123],v[88:91],a[144:147]\nBL v161,s[56:59],s68 offen offset:1024 sc1 lds\nM4 a[148:151],v[120:123],v[92:95],a[148:151]\nBS v[176:179],v201,s[44:47],s75 offen offset:128 sc1\nM4 a[152:155],v[120:123],v[96:99],a[152:155]\nM4 a[156:159],v[120:123],v[100:103],a[156:159]\n"
"BL v162,s[56:59],s68 offen offset:2048 sc1 lds\nM4 a[192:195],v[120:123],a[200:203],a[192:195]\nM4 a[160:163],v[124:127],v[72:75],a[160:163]\nM4 a[164:167],v[124:127],v[76:79],a[164:167]\nM4 a[168:171],v[124:127],v[80:83],a[168:171]\nBL v163,s[56:59],s68 offen offset:3072 sc1 lds\nM4 a[172:175],v[124:127],v[84:87],a[172:175]\nM4 a[176:179],v[124:127],v[88:91],a[176:179]\n"
"M4 a[180:183],v[124:127],v[92:95],a[180:183]\nM4 a[184:187],v[124:127],v[96:99],a[184:187]\nM4 a[188:191],v[124:127],v[100:103],a[188:191]\nM4 a[196:199],v[124:127],a[200:203],a[196:199]\nSW vmcnt(23) lgkmcnt(0)\ns_barrier\nM4 a[0:3],v[40:43],v[8:11],a[0:3]\nDT v[104:105],v164 offset:49152\n"
"M4 a[4:7],v[40:43],v[12:15],a[4:7]\nDT v[106:107],v164 offset:50176\nSA m0,s81,32768\nSA s75,s80,8192\nM4 a[8:11],v[40:43],v[16:19],a[8:11]\nM4 a[12:15],v[40:43],v[20:23],a[12:15]\nDR v[72:75],v145 offset:0\nM4 a[16:19],v[40:43],v[24:27],a[16:19]\n"
"BL v156,s[48:51],s66 offen offset:0 lds\nM4 a[20:23],v[40:43],v[28:31],a[20:23]\nDR v[76:79],v145 offset:2048\nM4 a[24:27],v[40:43],v[32:35],a[24:27]\nM4 a[28:31],v[40:43],v[36:39],a[28:31]\nDR v[80:83],v145 offset:4096\nBL v157,s[48:51],s66 offen offset:1024 lds\nM4 a[32:35],v[44:47],v[8:11],a[32:35]\n"
"M4 a[36:39],v[44:47],v[12:15],a[36:39]\nDR v[84:87],v145 offset:6144\nM4 a[40:43],v[44:47],v[16:19],a[40:43]\nDR v[88:91],v145 offset:8192\nSA m0,s82,32768\nM4 a[44:47],v[44:47],v[20:23],a[44:47]\nM4 a[48:51],v[44:47],v[24:27],a[48:51]\nDR v[92:95],v145 offset:10240\n"
"M4 a[52:55],v[44:47],v[28:31],a[52:55]\nBS v[180:183],v201,s[44:47],s75 offen offset:2048 sc1\nM4 a[56:59],v[44:47],v[32:35],a[56:59]\nDR v[96:99],v145 offset:12288\nBL v158,s[52:55],s67 offen lds\nM4 a[60:63],v[44:47],v[36:39],a[60:63]\nM4 a[64:67],v[48:51],v[8:11],a[64:67]\nDR v[100:103],v145 offset:14336\n"
"M4 a[68:71],v[48:51],v[12:15],a[68:71]\nSA s66,s66,262144\nM4 a[72:75],v[48:51],v[16:19],a[72:75]\nDT v[108:109],v165 offset:49152\nM4 a[76:79],v[48:51],v[20:23],a[76:79]\nDT v[110:111],v165 offset:50176\nM4 a[80:83],v[48:51],v[24:27],a[80:83]\nSA s67,s67,110592\n"
"M4 a[84:87],v[48:51],v[28:31],a[84:87]\nDT v[112:113],v166 offset:49152\nM4 a[88:91],v[48:51],v[32:35],a[88:91]\nM4 a[92:95],v[48:51],v[36:39],a[92:95]\nDT v[114:115],v166 offset:50176\nM4 a[96:99],v[52:55],v[8:11],a[96:99]\nSA m0,s70,36864\nM4 a[100:103],v[52:55],v[12:15],a[100:103]\n"
"DT v[116:117],v167 offset:49152\nSA s75,s80,8192\nM4 a[104:107],v[52:55],v[16:19],a[104:107]\nM4 a[108:111],v[52:55],v[20:23],a[108:111]\nDT v[118:119],v167 offset:50176\nSA s72,s68,40960\nM4 a[112:115],v[52:55],v[24:27],a[112:115]\nDT v[120:121],v168 offset:49152\n"
"M4 a[116:119],v[52:55],v[28:31],a[116:119]\nM4 a[120:123],v[52:55],v[32:35],a[120:123]\nDT v[122:123],v168 offset:49664\nBL v160,s[56:59],s72 offen offset:0 sc1 lds\nM4 a[124:127],v[52:55],v[36:39],a[124:127]\nM4 a[128:131],v[56:59],v[8:11],a[128:131]\nDT v[124:125],v169 offset:49152\nM4 a[132:135],v[56:59],v[12:15],a[132:135]\n"
"BL v161,s[56:59],s72 offen offset:1024 sc1 lds\nM4 a[136:139],v[56:59],v[16:19],a[136:139]\nDT v[126:127],v169 offset:49664\nM4 a[140:143],v[56:59],v[20:23],a[140:143]\nM4 a[144:147],v[56:59],v[24:27],a[144:147]\nM4 a[148:151],v[56:59],v[28:31],a[148:151]\nBL v162,s[56:59],s72 offen offset:2048 sc1 lds\nBS v[184:187],v201,s[44:47],s75 offen offset:2176 sc1\n"
"M4 a[152:155],v[56:59],v[32:35],a[152:155]\nM4 a[156:159],v[56:59],v[36:39],a[156:159]\nM4 a[192:195],v[56:59],a[200:203],a[192:195]\nBL v163,s[56:59],s72 offen offset:3072 sc1 lds\nM4 a[160:163],v[60:63],v[8:11],a[160:163]\nM4 a[164:167],v[60:63],v[12:15],a[164:167]\nM4 a[168:171],v[60:63],v[16:19],a[168:171]\nSA s68,s68,128\n"
"M4 a[172:175],v[60:63],v[20:23],a[172:175]\nM4 a[176:179],v[60:63],v[24:27],a[176:179]\nM4 a[180:183],v[60:63],v[28:31],a[180:183]\nM4 a[184:187],v[60:63],v[32:35],a[184:187]\nM4 a[188:191],v[60:63],v[36:39],a[188:191]\nM4 a[196:199],v[60:63],a[200:203],a[196:199]\nSW vmcnt(18) lgkmcnt(0)\ns_barrier\n"
"M4 a[0:3],v[104:107],v[72:75],a[0:3]\nVU v144,0,v146\nM4 a[4:7],v[104:107],v[76:79],a[4:7]\nVU v145,0,v147\nSA s75,s80,12288\nM4 a[8:11],v[104:107],v[80:83],a[8:11]\nDT v[40:41],v164 offset:0\nM4 a[12:15],v[104:107],v[84:87],a[12:15]\n"
"DT v[42:43],v164 offset:1024\nSA m0,s81,49152\nM4 a[16:19],v[104:107],v[88:91],a[16:19]\nM4 a[20:23],v[104:107],v[92:95],a[20:23]\nDR v[8:11],v144 offset:0\nM4 a[24:27],v[104:107],v[96:99],a[24:27]\nBL v156,s[48:51],s66 offen offset:0 lds\nM4 a[28:31],v[104:107],v[100:103],a[28:31]\n"
"DR v[12:15],v144 offset:2048\nM4 a[32:35],v[108:111],v[72:75],a[32:35]\nDR v[16:19],v144 offset:4096\nM4 a[36:39],v[108:111],v[76:79],a[36:39]\nM4 a[40:43],v[108:111],v[80:83],a[40:43]\nDR v[20:23],v144 offset:6144\nBL v157,s[48:51],s66 offen offset:1024 lds\nM4 a[44:47],v[108:111],v[84:87],a[44:47]\n"
"M4 a[48:51],v[108:111],v[88:91],a[48:51]\nDR v[24:27],v144 offset:8192\nM4 a[52:55],v[108:111],v[92:95],a[52:55]\nDR v[28:31],v144 offset:10240\nBS v[188:191],v201,s[44:47],s75 offen offset:0 sc1\nM4 a[56:59],v[108:111],v[96:99],a[56:59]\nSA m0,s82,49152\nM4 a[60:63],v[108:111],v[100:103],a[60:63]\n"
"DR v[32:35],v144 offset:12288\nM4 a[64:67],v[112:115],v[72:75],a[64:67]\nM4 a[68:71],v[112:115],v[76:79],a[68:71]\nDR v[36:39],v144 offset:14336\nBL v158,s[52:55],s67 offen lds\nM4 a[72:75],v[112:115],v[80:83],a[72:75]\nM4 a[76:79],v[112:115],v[84:87],a[76:79]\nDT v[44:45],v165 offset:0\n"
"M4 a[80:83],v[112:115],v[88:91],a[80:83]\nDT v[46:47],v165 offset:1024\nM4 a[84:87],v[112:115],v[92:95],a[84:87]\nSA s66,s66,262144\nM4 a[88:91],v[112:115],v[96:99],a[88:91]\nDT v[48:49],v166 offset:0\nM4 a[92:95],v[112:115],v[100:103],a[92:95]\nM4 a[96:99],v[116:119],v[72:75],a[96:99]\n"
"DT v[50:51],v166 offset:1024\nM4 a[100:103],v[116:119],v[76:79],a[100:103]\nDT v[52:53],v167 offset:0\nSA s67,s67,110592\nSA s75,s80,12288\nM4 a[104:107],v[116:119],v[80:83],a[104:107]\nM4 a[108:111],v[116:119],v[84:87],a[108:111]\nDT v[54:55],v167 offset:1024\n"
"M4 a[112:115],v[116:119],v[88:91],a[112:115]\nSA m0,s70,65536\nM4 a[116:119],v[116:119],v[92:95],a[116:119]\nDT v[56:57],v168 offset:0\nM4 a[120:123],v[116:119],v[96:99],a[120:123]\nDT v[58:59],v168 offset:512\nM4 a[124:127],v[116:119],v[100:103],a[124:127]\nM4 a[128:131],v[120:123],v[72:75],a[128:131]\n"
"DT v[60:61],v169 offset:0\nBL v160,s[56:59],s68 offen offset:0 sc1 lds\nM4 a[132:135],v[120:123],v[76:79],a[132:135]\nM4 a[136:139],v[120:123],v[80:83],a[136:139]\nDT v[62:63],v169 offset:512\nM4 a[140:143],v[120:123],v[84:87],a[140:143]\nM4 a[144:147],v[120:123],v[88:91],a[144:147]\nBL v161,s[56:59],s68 offen offset:1024 sc1 lds\n"
"M4 a[148:151],v[120:123],v[92:95],a[148:151]\nBS v[192:195],v201,s[44:47],s75 offen offset:128 sc1\nM4 a[152:155],v[120:123],v[96:99],a[152:155]\nM4 a[156:159],v[120:123],v[100:103],a[156:159]\nBL v162,s[56:59],s68 offen offset:2048 sc1 lds\nM4 a[192:195],v[120:123],a[200:203],a[192:195]\nM4 a[160:163],v[124:127],v[72:75],a[160:163]\nM4 a[164:167],v[124:127],v[76:79],a[164:167]\n"
"M4 a[168:171],v[124:127],v[80:83],a[168:171]\nBL v163,s[56:59],s68 offen offset:3072 sc1 lds\nM4 a[172:175],v[124:127],v[84:87],a[172:175]\nM4 a[176:179],v[124:127],v[88:91],a[176:179]\nM4 a[180:183],v[124:127],v[92:95],a[180:183]\nM4 a[184:187],v[124:127],v[96:99],a[184:187]\nM4 a[188:191],v[124:127],v[100:103],a[188:191]\nM4 a[196:199],v[124:127],a[200:203],a[196:199]\n"
"SW vmcnt(23) lgkmcnt(0)\ns_barrier\nM4 a[0:3],v[40:43],v[8:11],a[0:3]\nDT v[104:105],v164 offset:16384\nM4 a[4:7],v[40:43],v[12:15],a[4:7]\nDT v[106:107],v164 offset:17408\nSA m0,s81,0\nSA s75,s80,12288\n"
"M4 a[8:11],v[40:43],v[16:19],a[8:11]\nM4 a[12:15],v[40:43],v[20:23],a[12:15]\nDR v[72:75],v145 offset:0\nM4 a[16:19],v[40:43],v[24:27],a[16:19]\nBL v156,s[48:51],s66 offen offset:0 lds\nM4 a[20:23],v[40:43],v[28:31],a[20:23]\nDR v[76:79],v145 offset:2048\nM4 a[24:27],v[40:43],v[32:35],a[24:27]\n"
"M4 a[28:31],v[40:43],v[36:39],a[28:31]\nDR v[80:83],v145 offset:4096\nBL v157,s[48:51],s66 offen offset:1024 lds\nM4 a[32:35],v[44:47],v[8:11],a[32:35]\nM4 a[36:39],v[44:47],v[12:15],a[36:39]\nDR v[84:87],v145 offset:6144\nM4 a[40:43],v[44:47],v[16:19],a[40:43]\nDR v[88:91],v145 offset:8192\n"
"SA m0,s82,0\nM4 a[44:47],v[44:47],v[20:23],a[44:47]\nM4 a[48:51],v[44:47],v[24:27],a[48:51]\nDR v[92:95],v145 offset:10240\nM4 a[52:55],v[44:47],v[28:31],a[52:55]\nBS v[196:199],v201,s[44:47],s75 offen offset:2048 sc1\nM4 a[56:59],v[44:47],v[32:35],a[56:59]\nDR v[96:99],v145 offset:12288\n"
"BL v158,s[52:55],s67 offen lds\nM4 a[60:63],v[44:47],v[36:39],a[60:63]\nM4 a[64:67],v[48:51],v[8:11],a[64:67]\nDR v[100:103],v145 offset:14336\nM4 a[68:71],v[48:51],v[12:15],a[68:71]\nSA s66,s66,262144\nM4 a[72:75],v[48:51],v[16:19],a[72:75]\nDT v[108:109],v165 offset:16384\n"
"M4 a[76:79],v[48:51],v[20:23],a[76:79]\nDT v[110:111],v165 offset:17408\nM4 a[80:83],v[48:51],v[24:27],a[80:83]\nSA s67,s67,110592\nM4 a[84:87],v[48:51],v[28:31],a[84:87]\nDT v[112:113],v166 offset:16384\nM4 a[88:91],v[48:51],v[32:35],a[88:91]\nM4 a[92:95],v[48:51],v[36:39],a[92:95]\n"
"DT v[114:115],v166 offset:17408\nM4 a[96:99],v[52:55],v[8:11],a[96:99]\nSA m0,s70,69632\nM4 a[100:103],v[52:55],v[12:15],a[100:103]\nDT v[116:117],v167 offset:16384\nSA s75,s80,12288\nM4 a[104:107],v[52:55],v[16:19],a[104:107]\nM4 a[108:111],v[52:55],v[20:23],a[108:111]\n"
"DT v[118:119],v167 offset:17408\nSA s72,s68,40960\nM4 a[112:115],v[52:55],v[24:27],a[112:115]\nDT v[120:121],v168 offset:16384\nM4 a[116:119],v[52:55],v[28:31],a[116:119]\nM4 a[120:123],v[52:55],v[32:35],a[120:123]\nDT v[122:123],v168 offset:16896\nBL v160,s[56:59],s72 offen offset:0 sc1 lds\n"
"M4 a[124:127],v[52:55],v[36:39],a[124:127]\nM4 a[128:131],v[56:59],v[8:11],a[128:131]\nDT v[124:125],v169 offset:16384\nM4 a[132:135],v[56:59],v[12:15],a[132:135]\nBL v161,s[56:59],s72 offen offset:1024 sc1 lds\nM4 a[136:139],v[56:59],v[16:19],a[136:139]\nDT v[126:127],v169 offset:16896\nM4 a[140:143],v[56:59],v[20:23],a[140:143]\n"
"M4 a[144:147],v[56:59],v[24:27],a[144:147]\nM4 a[148:151],v[56:59],v[28:31],a[148:151]\nBL v162,s[56:59],s72 offen offset:2048 sc1 lds\nBS v[204:207],v201,s[44:47],s75 offen offset:2176 sc1\nM4 a[152:155],v[56:59],v[32:35],a[152:155]\nM4 a[156:159],v[56:59],v[36:39],a[156:159]\nM4 a[192:195],v[56:59],a[200:203],a[192:195]\nBL v163,s[56:59],s72 offen offset:3072 sc1 lds\n"
"M4 a[160:163],v[60:63],v[8:11],a[160:163]\nM4 a[164:167],v[60:63],v[12:15],a[164:167]\nM4 a[168:171],v[60:63],v[16:19],a[168:171]\nSA s68,s68,128\nM4 a[172:175],v[60:63],v[20:23],a[172:175]\nM4 a[176:179],v[60:63],v[24:27],a[176:179]\nM4 a[180:183],v[60:63],v[28:31],a[180:183]\nM4 a[184:187],v[60:63],v[32:35],a[184:187]\n"
"M4 a[188:191],v[60:63],v[36:39],a[188:191]\nM4 a[196:199],v[60:63],a[200:203],a[196:199]\nSW vmcnt(18) lgkmcnt(0)\ns_barrier\nM4 a[0:3],v[104:107],v[72:75],a[0:3]\nVU v144,32768,v146\nM4 a[4:7],v[104:107],v[76:79],a[4:7]\nVU v145,32768,v147\n"
"SA s75,s80,16384\nM4 a[8:11],v[104:107],v[80:83],a[8:11]\nDT v[40:41],v164 offset:32768\nM4 a[12:15],v[104:107],v[84:87],a[12:15]\nDT v[42:43],v164 offset:33792\nSA m0,s81,16384\nM4 a[16:19],v[104:107],v[88:91],a[16:19]\nM4 a[20:23],v[104:107],v[92:95],a[20:23]\n"
"DR v[8:11],v144 offset:0\nM4 a[24:27],v[104:107],v[96:99],a[24:27]\nBL v156,s[48:51],s66 offen offset:0 lds\nM4 a[28:31],v[104:107],v[100:103],a[28:31]\nDR v[12:15],v144 offset:2048\nM4 a[32:35],v[108:111],v[72:75],a[32:35]\nDR v[16:19],v144 offset:4096\nM4 a[36:39],v[108:111],v[76:79],a[36:39]\n"
"M4 a[40:43],v[108:111],v[80:83],a[40:43]\nDR v[20:23],v144 offset:6144\nBL v157,s[48:51],s66 offen offset:1024 lds\nM4 a[44:47],v[108:111],v[84:87],a[44:47]\nM4 a[48:51],v[108:111],v[88:91],a[48:51]\nDR v[24:27],v144 offset:8192\nM4 a[52:55],v[108:111],v[92:95],a[52:55]\nDR v[28:31],v144 offset:10240\n"
"BS v[208:211],v201,s[44:47],s75 offen offset:0 sc1\nM4 a[56:59],v[108:111],v[96:99],a[56:59]\nSA m0,s82,16384\nM4 a[60:63],v[108:111],v[100:103],a[60:63]\nDR v[32:35],v144 offset:12288\nM4 a[64:67],v[112:115],v[72:75],a[64:67]\nM4 a[68:71],v[112:115],v[76:79],a[68:71]\nDR v[36:39],v144 offset:14336\n"
"BL v158,s[52:55],s67 offen lds\nM4 a[72:75],v[112:115],v[80:83],a[72:75]\nM4 a[76:79],v[112:115],v[84:87],a[76:79]\nDT v[44:45],v165 offset:32768\nM4 a[80:83],v[112:115],v[88:91],a[80:83]\nDT v[46:47],v165 offset:33792\nM4 a[84:87],v[112:115],v[92:95],a[84:87]\nSA s66,s66,262144\n"
"M4 a[88:91],v[112:115],v[96:99],a[88:91]\nDT v[48:49],v166 offset:32768\nM4 a[92:95],v[112:115],v[100:103],a[92:95]\nM4 a[96:99],v[116:119],v[72:75],a[96:99]\nDT v[50:51],v166 offset:33792\nM4 a[100:103],v[116:119],v[76:79],a[100:103]\nDT v[52:53],v167 offset:32768\nSA s67,s67,110592\n"
"SA s75,s80,16384\nM4 a[104:107],v[116:119],v[80:83],a[104:107]\nM4 a[108:111],v[116:119],v[84:87],a[108:111]\nDT v[54:55],v167 offset:33792\nM4 a[112:115],v[116:119],v[88:91],a[112:115]\nSA m0,s70,0\nM4 a[116:119],v[116:119],v[92:95],a[116:119]\nDT v[56:57],v168 offset:32768\n"
"M4 a[120:123],v[116:119],v[96:99],a[120:123]\nDT v[58:59],v168 offset:33280\nM4 a[124:127],v[116:119],v[100:103],a[124:127]\nM4 a[128:131],v[120:123],v[72:75],a[128:131]\nDT v[60:61],v169 offset:32768\nBL v160,s[56:59],s68 offen offset:0 sc1 lds\nM4 a[132:135],v[120:123],v[76:79],a[132:135]\nM4 a[136:139],v[120:123],v[80:83],a[136:139]\n"
"DT v[62:63],v169 offset:33280\nM4 a[140:143],v[120:123],v[84:87],a[140:143]\nM4 a[144:147],v[120:123],v[88:91],a[144:147]\nBL v161,s[56:59],s68 offen offset:1024 sc1 lds\nM4 a[148:151],v[120:123],v[92:95],a[148:151]\nBS v[212:215],v201,s[44:47],s75 offen offset:128 sc1\nM4 a[152:155],v[120:123],v[96:99],a[152:155]\nM4 a[156:159],v[120:123],v[100:103],a[156:159]\n"
"BL v162,s[56:59],s68 offen offset:2048 sc1 lds\nM4 a[192:195],v[120:123],a[200:203],a[192:195]\nM4 a[160:163],v[124:127],v[72:75],a[160:163]\nM4 a[164:167],v[124:127],v[76:79],a[164:167]\nM4 a[168:171],v[124:127],v[80:83],a[168:171]\nBL v163,s[56:59],s68 offen offset:3072 sc1 lds\nM4 a[172:175],v[124:127],v[84:87],a[172:175]\nM4 a[176:179],v[124:127],v[88:91],a[176:179]\n"
"M4 a[180:183],v[124:127],v[92:95],a[180:183]\nM4 a[184:187],v[124:127],v[96:99],a[184:187]\nM4 a[188:191],v[124:127],v[100:103],a[188:191]\nM4 a[196:199],v[124:127],a[200:203],a[196:199]\nSW vmcnt(23) lgkmcnt(0)\ns_barrier\nM4 a[0:3],v[40:43],v[8:11],a[0:3]\nDT v[104:105],v164 offset:49152\n"
"M4 a[4:7],v[40:43],v[12:15],a[4:7]\nDT v[106:107],v164 offset:50176\nSA m0,s81,32768\nSA s75,s80,16384\nM4 a[8:11],v[40:43],v[16:19],a[8:11]\nM4 a[12:15],v[40:43],v[20:23],a[12:15]\nDR v[72:75],v145 offset:0\nM4 a[16:19],v[40:43],v[24:27],a[16:19]\n"
"BL v156,s[48:51],s66 offen offset:0 lds\nM4 a[20:23],v[40:43],v[28:31],a[20:23]\nDR v[76:79],v145 offset:2048\nM4 a[24:27],v[40:43],v[32:35],a[24:27]\nM4 a[28:31],v[40:43],v[36:39],a[28:31]\nDR v[80:83],v145 offset:4096\nBL v157,s[48:51],s66 offen offset:1024 lds\nM4 a[32:35],v[44:47],v[8:11],a[32:35]\n"
"M4 a[36:39],v[44:47],v[12:15],a[36:39]\nDR v[84:87],v145 offset:6144\nM4 a[40:43],v[44:47],v[16:19],a[40:43]\nDR v[88:91],v145 offset:8192\nSA m0,s82,32768\nM4 a[44:47],v[44:47],v[20:23],a[44:47]\nM4 a[48:51],v[44:47],v[24:27],a[48:51]\nDR v[92:95],v145 offset:10240\n"
"M4 a[52:55],v[44:47],v[28:31],a[52:55]\nBS v[216:219],v201,s[44:47],s75 offen offset:2048 sc1\nM4 a[56:59],v[44:47],v[32:35],a[56:59]\nDR v[96:99],v145 offset:12288\nBL v158,s[52:55],s67 offen lds\nM4 a[60:63],v[44:47],v[36:39],a[60:63]\nM4 a[64:67],v[48:51],v[8:11],a[64:67]\nDR v[100:103],v145 offset:14336\n"
"M4 a[68:71],v[48:51],v[12:15],a[68:71]\nSA s66,s66,262144\nM4 a[72:75],v[48:51],v[16:19],a[72:75]\nDT v[108:109],v165 offset:49152\nM4 a[76:79],v[48:51],v[20:23],a[76:79]\nDT v[110:111],v165 offset:50176\nM4 a[80:83],v[48:51],v[24:27],a[80:83]\nSA s67,s67,110592\n"
"M4 a[84:87],v[48:51],v[28:31],a[84:87]\nDT v[112:113],v166 offset:49152\nM4 a[88:91],v[48:51],v[32:35],a[88:91]\nM4 a[92:95],v[48:51],v[36:39],a[92:95]\nDT v[114:115],v166 offset:50176\nM4 a[96:99],v[52:55],v[8:11],a[96:99]\nSA m0,s70,4096\nM4 a[100:103],v[52:55],v[12:15],a[100:103]\n"
"DT v[116:117],v167 offset:49152\nSA s75,s80,16384\nM4 a[104:107],v[52:55],v[16:19],a[104:107]\nM4 a[108:111],v[52:55],v[20:23],a[108:111]\nDT v[118:119],v167 offset:50176\nSA s72,s68,40960\nM4 a[112:115],v[52:55],v[24:27],a[112:115]\nDT v[120:121],v168 offset:49152\n"
"M4 a[116:119],v[52:55],v[28:31],a[116:119]\nM4 a[120:123],v[52:55],v[32:35],a[120:123]\nDT v[122:123],v168 offset:49664\nBL v160,s[56:59],s72 offen offset:0 sc1 lds\nM4 a[124:127],v[52:55],v[36:39],a[124:127]\nM4 a[128:131],v[56:59],v[8:11],a[128:131]\nDT v[124:125],v169 offset:49152\nM4 a[132:135],v[56:59],v[12:15],a[132:135]\n"
"BL v161,s[56:59],s72 offen offset:1024 sc1 lds\nM4 a[136:139],v[56:59],v[16:19],a[136:139]\nDT v[126:127],v169 offset:49664\nM4 a[140:143],v[56:59],v[20:23],a[140:143]\nM4 a[144:147],v[56:59],v[24:27],a[144:147]\nM4 a[148:151],v[56:59],v[28:31],a[148:151]\nBL v162,s[56:59],s72 offen offset:2048 sc1 lds\nBS v[220:223],v201,s[44:47],s75 offen offset:2176 sc1\n"
"M4 a[152:155],v[56:59],v[32:35],a[152:155]\nM4 a[156:159],v[56:59],v[36:39],a[156:159]\nM4 a[192:195],v[56:59],a[200:203],a[192:195]\nBL v163,s[56:59],s72 offen offset:3072 sc1 lds\nM4 a[160:163],v[60:63],v[8:11],a[160:163]\nM4 a[164:167],v[60:63],v[12:15],a[164:167]\nM4 a[168:171],v[60:63],v[16:19],a[168:171]\nSA s68,s68,128\n"
"M4 a[172:175],v[60:63],v[20:23],a[172:175]\nM4 a[176:179],v[60:63],v[24:27],a[176:179]\nM4 a[180:183],v[60:63],v[28:31],a[180:183]\nM4 a[184:187],v[60:63],v[32:35],a[184:187]\nM4 a[188:191],v[60:63],v[36:39],a[188:191]\nM4 a[196:199],v[60:63],a[200:203],a[196:199]\nSW vmcnt(18) lgkmcnt(0)\ns_barrier\n"
"M4 a[0:3],v[104:107],v[72:75],a[0:3]\nVU v144,65536,v146\nM4 a[4:7],v[104:107],v[76:79],a[4:7]\nVU v145,65536,v147\nSA s75,s80,20480\nM4 a[8:11],v[104:107],v[80:83],a[8:11]\nDT v[40:41],v164 offset:0\nM4 a[12:15],v[104:107],v[84:87],a[12:15]\n"
"DT v[42:43],v164 offset:1024\nSA m0,s81,49152\nM4 a[16:19],v[104:107],v[88:91],a[16:19]\nM4 a[20:23],v[104:107],v[92:95],a[20:23]\nDR v[8:11],v144 offset:0\nM4 a[24:27],v[104:107],v[96:99],a[24:27]\nBL v156,s[48:51],s66 offen offset:0 lds\nM4 a[28:31],v[104:107],v[100:103],a[28:31]\n"
"DR v[12:15],v144 offset:2048\nM4 a[32:35],v[108:111],v[72:75],a[32:35]\nDR v[16:19],v144 offset:4096\nM4 a[36:39],v[108:111],v[76:79],a[36:39]\nM4 a[40:43],v[108:111],v[80:83],a[40:43]\nDR v[20:23],v144 offset:6144\nBL v157,s[48:51],s66 offen offset:1024 lds\nM4 a[44:47],v[108:111],v[84:87],a[44:47]\n"
"M4 a[48:51],v[108:111],v[88:91],a[48:51]\nDR v[24:27],v144 offset:8192\nM4 a[52:55],v[108:111],v[92:95],a[52:55]\nDR v[28:31],v144 offset:10240\nBS v[224:227],v201,s[44:47],s75 offen offset:0 sc1\nM4 a[56:59],v[108:111],v[96:99],a[56:59]\nSA m0,s82,49152\nM4 a[60:63],v[108:111],v[100:103],a[60:63]\n"
"DR v[32:35],v144 offset:12288\nM4 a[64:67],v[112:115],v[72:75],a[64:67]\nM4 a[68:71],v[112:115],v[76:79],a[68:71]\nDR v[36:39],v144 offset:14336\nBL v158,s[52:55],s67 offen lds\nM4 a[72:75],v[112:115],v[80:83],a[72:75]\nM4 a[76:79],v[112:115],v[84:87],a[76:79]\nDT v[44:45],v165 offset:0\n"
"M4 a[80:83],v[112:115],v[88:91],a[80:83]\nDT v[46:47],v165 offset:1024\nM4 a[84:87],v[112:115],v[92:95],a[84:87]\nSA s66,s66,262144\nM4 a[88:91],v[112:115],v[96:99],a[88:91]\nDT v[48:49],v166 offset:0\nM4 a[92:95],v[112:115],v[100:103],a[92:95]\nM4 a[96:99],v[116:119],v[72:75],a[96:99]\n"
"DT v[50:51],v166 offset:1024\nM4 a[100:103],v[116:119],v[76:79],a[100:103]\nDT v[52:53],v167 offset:0\nSA s67,s67,110592\nSA s75,s80,20480\nM4 a[104:107],v[116:119],v[80:83],a[104:107]\nM4 a[108:111],v[116:119],v[84:87],a[108:111]\nDT v[54:55],v167 offset:1024\n"
"M4 a[112:115],v[116:119],v[88:91],a[112:115]\nSA m0,s70,32768\nM4 a[116:119],v[116:119],v[92:95],a[116:119]\nDT v[56:57],v168 offset:0\nM4 a[120:123],v[116:119],v[96:99],a[120:123]\nDT v[58:59],v168 offset:512\nM4 a[124:127],v[116:119],v[100:103],a[124:127]\nM4 a[128:131],v[120:123],v[72:75],a[128:131]\n"
"DT v[60:61],v169 offset:0\nBL v160,s[56:59],s68 offen offset:0 sc1 lds\nM4 a[132:135],v[120:123],v[76:79],a[132:135]\nM4 a[136:139],v[120:123],v[80:83],a[136:139]\nDT v[62:63],v169 offset:512\nM4 a[140:143],v[120:123],v[84:87],a[140:143]\nM4 a[144:147],v[120:123],v[88:91],a[144:147]\nBL v161,s[56:59],s68 offen offset:1024 sc1 lds\n"
"M4 a[148:151],v[120:123],v[92:95],a[148:151]\nBS v[228:231],v201,s[44:47],s75 offen offset:128 sc1\nM4 a[152:155],v[120:123],v[96:99],a[152:155]\nM4 a[156:159],v[120:123],v[100:103],a[156:159]\nBL v162,s[56:59],s68 offen offset:2048 sc1 lds\nM4 a[192:195],v[120:123],a[200:203],a[192:195]\nM4 a[160:163],v[124:127],v[72:75],a[160:163]\nM4 a[164:167],v[124:127],v[76:79],a[164:167]\n"
"M4 a[168:171],v[124:127],v[80:83],a[168:171]\nBL v163,s[56:59],s68 offen offset:3072 sc1 lds\nM4 a[172:175],v[124:127],v[84:87],a[172:175]\nM4 a[176:179],v[124:127],v[88:91],a[176:179]\nM4 a[180:183],v[124:127],v[92:95],a[180:183]\nM4 a[184:187],v[124:127],v[96:99],a[184:187]\nM4 a[188:191],v[124:127],v[100:103],a[188:191]\nM4 a[196:199],v[124:127],a[200:203],a[196:199]\n"
"SW vmcnt(23) lgkmcnt(0)\ns_barrier\nM4 a[0:3],v[40:43],v[8:11],a[0:3]\nDT v[104:105],v164 offset:16384\nM4 a[4:7],v[40:43],v[12:15],a[4:7]\nDT v[106:107],v164 offset:17408\nSA m0,s81,0\nSA s75,s80,20480\n"
"M4 a[8:11],v[40:43],v[16:19],a[8:11]\nM4 a[12:15],v[40:43],v[20:23],a[12:15]\nDR v[72:75],v145 offset:0\nM4 a[16:19],v[40:43],v[24:27],a[16:19]\nBL v156,s[48:51],s66 offen offset:0 lds\nM4 a[20:23],v[40:43],v[28:31],a[20:23]\nDR v[76:79],v145 offset:2048\nM4 a[24:27],v[40:43],v[32:35],a[24:27]\n"
"M4 a[28:31],v[40:43],v[36:39],a[28:31]\nDR v[80:83],v145 offset:4096\nBL v157,s[48:51],s66 offen offset:1024 lds\nM4 a[32:35],v[44:47],v[8:11],a[32:35]\nM4 a[36:39],v[44:47],v[12:15],a[36:39]\nDR v[84:87],v145 offset:6144\nM4 a[40:43],v[44:47],v[16:19],a[40:43]\nDR v[88:91],v145 offset:8192\n"
"SA m0,s82,0\nM4 a[44:47],v[44:47],v[20:23],a[44:47]\nM4 a[48:51],v[44:47],v[24:27],a[48:51]\nDR v[92:95],v145 offset:10240\nM4 a[52:55],v[44:47],v[28:31],a[52:55]\nBS v[232:235],v201,s[44:47],s75 offen offset:2048 sc1\nM4 a[56:59],v[44:47],v[32:35],a[56:59]\nDR v[96:99],v145 offset:12288\n"
"BL v158,s[52:55],s67 offen lds\nM4 a[60:63],v[44:47],v[36:39],a[60:63]\nM4 a[64:67],v[48:51],v[8:11],a[64:67]\nDR v[100:103],v145 offset:14336\nM4 a[68:71],v[48:51],v[12:15],a[68:71]\nSA s66,s66,262144\nM4 a[72:75],v[48:51],v[16:19],a[72:75]\nDT v[108:109],v165 offset:16384\n"
"M4 a[76:79],v[48:51],v[20:23],a[76:79]\nDT v[110:111],v165 offset:17408\nM4 a[80:83],v[48:51],v[24:27],a[80:83]\nSA s67,s67,110592\nM4 a[84:87],v[48:51],v[28:31],a[84:87]\nDT v[112:113],v166 offset:16384\nM4 a[88:91],v[48:51],v[32:35],a[88:91]\nM4 a[92:95],v[48:51],v[36:39],a[92:95]\n"
"DT v[114:115],v166 offset:17408\nM4 a[96:99],v[52:55],v[8:11],a[96:99]\nSA m0,s70,36864\nM4 a[100:103],v[52:55],v[12:15],a[100:103]\nDT v[116:117],v167 offset:16384\nSA s75,s80,20480\nM4 a[104:107],v[52:55],v[16:19],a[104:107]\nM4 a[108:111],v[52:55],v[20:23],a[108:111]\n"
"DT v[118:119],v167 offset:17408\nSA s72,s68,40960\nM4 a[112:115],v[52:55],v[24:27],a[112:115]\nDT v[120:121],v168 offset:16384\nM4 a[116:119],v[52:55],v[28:31],a[116:119]\nM4 a[120:123],v[52:55],v[32:35],a[120:123]\nDT v[122:123],v168 offset:16896\nBL v160,s[56:59],s72 offen offset:0 sc1 lds\n"
"M4 a[124:127],v[52:55],v[36:39],a[124:127]\nM4 a[128:131],v[56:59],v[8:11],a[128:131]\nDT v[124:125],v169 offset:16384\nM4 a[132:135],v[56:59],v[12:15],a[132:135]\nBL v161,s[56:59],s72 offen offset:1024 sc1 lds\nM4 a[136:139],v[56:59],v[16:19],a[136:139]\nDT v[126:127],v169 offset:16896\nM4 a[140:143],v[56:59],v[20:23],a[140:143]\n"
"M4 a[144:147],v[56:59],v[24:27],a[144:147]\nM4 a[148:151],v[56:59],v[28:31],a[148:151]\nBL v162,s[56:59],s72 offen offset:2048 sc1 lds\nBS v[236:239],v201,s[44:47],s75 offen offset:2176 sc1\nM4 a[152:155],v[56:59],v[32:35],a[152:155]\nM4 a[156:159],v[56:59],v[36:39],a[156:159]\nM4 a[192:195],v[56:59],a[200:203],a[192:195]\nBL v163,s[56:59],s72 offen offset:3072 sc1 lds\n"
"M4 a[160:163],v[60:63],v[8:11],a[160:163]\nM4 a[164:167],v[60:63],v[12:15],a[164:167]\nM4 a[168:171],v[60:63],v[16:19],a[168:171]\nSA s68,s68,128\nM4 a[172:175],v[60:63],v[20:23],a[172:175]\nM4 a[176:179],v[60:63],v[24:27],a[176:179]\nM4 a[180:183],v[60:63],v[28:31],a[180:183]\nM4 a[184:187],v[60:63],v[32:35],a[184:187]\n"
"M4 a[188:191],v[60:63],v[36:39],a[188:191]\nM4 a[196:199],v[60:63],a[200:203],a[196:199]\nSW vmcnt(18) lgkmcnt(0)\ns_barrier\nM4 a[0:3],v[104:107],v[72:75],a[0:3]\nVU v144,0,v146\nM4 a[4:7],v[104:107],v[76:79],a[4:7]\nVU v145,0,v147\n"
"SA s75,s80,24576\nM4 a[8:11],v[104:107],v[80:83],a[8:11]\nDT v[40:41],v164 offset:32768\nM4 a[12:15],v[104:107],v[84:87],a[12:15]\nDT v[42:43],v164 offset:33792\nSA m0,s81,16384\nM4 a[16:19],v[104:107],v[88:91],a[16:19]\nM4 a[20:23],v[104:107],v[92:95],a[20:23]\n"
"DR v[8:11],v144 offset:0\nM4 a[24:27],v[104:107],v[96:99],a[24:27]\nBL v156,s[48:51],s66 offen offset:0 lds\nM4 a[28:31],v[104:107],v[100:103],a[28:31]\nDR v[12:15],v144 offset:2048\nM4 a[32:35],v[108:111],v[72:75],a[32:35]\nDR v[16:19],v144 offset:4096\nM4 a[36:39],v[108:111],v[76:79],a[36:39]\n"
"M4 a[40:43],v[108:111],v[80:83],a[40:43]\nDR v[20:23],v144 offset:6144\nBL v157,s[48:51],s66 offen offset:1024 lds\nM4 a[44:47],v[108:111],v[84:87],a[44:47]\nM4 a[48:51],v[108:111],v[88:91],a[48:51]\nDR v[24:27],v144 offset:8192\nM4 a[52:55],v[108:111],v[92:95],a[52:55]\nDR v[28:31],v144 offset:10240\n"
"BS v[240:243],v201,s[44:47],s75 offen offset:0 sc1\nM4 a[56:59],v[108:111],v[96:99],a[56:59]\nSA m0,s82,16384\nM4 a[60:63],v[108:111],v[100:103],a[60:63]\nDR v[32:35],v144 offset:12288\nM4 a[64:67],v[112:115],v[72:75],a[64:67]\nM4 a[68:71],v[112:115],v[76:79],a[68:71]\nDR v[36:39],v144 offset:14336\n"
"BL v158,s[52:55],s67 offen lds\nM4 a[72:75],v[112:115],v[80:83],a[72:75]\nM4 a[76:79],v[112:115],v[84:87],a[76:79]\nDT v[44:45],v165 offset:32768\nM4 a[80:83],v[112:115],v[88:91],a[80:83]\nDT v[46:47],v165 offset:33792\nM4 a[84:87],v[112:115],v[92:95],a[84:87]\nSA s66,s66,262144\n"
"M4 a[88:91],v[112:115],v[96:99],a[88:91]\nDT v[48:49],v166 offset:32768\nM4 a[92:95],v[112:115],v[100:103],a[92:95]\nM4 a[96:99],v[116:119],v[72:75],a[96:99]\nDT v[50:51],v166 offset:33792\nM4 a[100:103],v[116:119],v[76:79],a[100:103]\nDT v[52:53],v167 offset:32768\nSA s67,s67,110592\n"
"SA s75,s80,24576\nM4 a[104:107],v[116:119],v[80:83],a[104:107]\nM4 a[108:111],v[116:119],v[84:87],a[108:111]\nDT v[54:55],v167 offset:33792\nM4 a[112:115],v[116:119],v[88:91],a[112:115]\nSA m0,s70,65536\nM4 a[116:119],v[116:119],v[92:95],a[116:119]\nDT v[56:57],v168 offset:32768\n"
"M4 a[120:123],v[116:119],v[96:99],a[120:123]\nDT v[58:59],v168 offset:33280\nM4 a[124:127],v[116:119],v[100:103],a[124:127]\nM4 a[128:131],v[120:123],v[72:75],a[128:131]\nDT v[60:61],v169 offset:32768\nBL v160,s[56:59],s68 offen offset:0 sc1 lds\nM4 a[132:135],v[120:123],v[76:79],a[132:135]\nM4 a[136:139],v[120:123],v[80:83],a[136:139]\n"
"DT v[62:63],v169 offset:33280\nM4 a[140:143],v[120:123],v[84:87],a[140:143]\nM4 a[144:147],v[120:123],v[88:91],a[144:147]\nBL v161,s[56:59],s68 offen offset:1024 sc1 lds\nM4 a[148:151],v[120:123],v[92:95],a[148:151]\nBS v[244:247],v201,s[44:47],s75 offen offset:128 sc1\nM4 a[152:155],v[120:123],v[96:99],a[152:155]\nM4 a[156:159],v[120:123],v[100:103],a[156:159]\n"
"BL v162,s[56:59],s68 offen offset:2048 sc1 lds\nM4 a[192:195],v[120:123],a[200:203],a[192:195]\nM4 a[160:163],v[124:127],v[72:75],a[160:163]\nM4 a[164:167],v[124:127],v[76:79],a[164:167]\nM4 a[168:171],v[124:127],v[80:83],a[168:171]\nBL v163,s[56:59],s68 offen offset:3072 sc1 lds\nM4 a[172:175],v[124:127],v[84:87],a[172:175]\nM4 a[176:179],v[124:127],v[88:91],a[176:179]\n"
"M4 a[180:183],v[124:127],v[92:95],a[180:183]\nM4 a[184:187],v[124:127],v[96:99],a[184:187]\nM4 a[188:191],v[124:127],v[100:103],a[188:191]\nM4 a[196:199],v[124:127],a[200:203],a[196:199]\nSW vmcnt(23) lgkmcnt(0)\ns_barrier\nM4 a[0:3],v[40:43],v[8:11],a[0:3]\nDT v[104:105],v164 offset:49152\n"
"M4 a[4:7],v[40:43],v[12:15],a[4:7]\nDT v[106:107],v164 offset:50176\nSA m0,s81,32768\nSA s75,s80,24576\nM4 a[8:11],v[40:43],v[16:19],a[8:11]\nM4 a[12:15],v[40:43],v[20:23],a[12:15]\nDR v[72:75],v145 offset:0\nM4 a[16:19],v[40:43],v[24:27],a[16:19]\n"
"BL v156,s[48:51],s66 offen offset:0 lds\nM4 a[20:23],v[40:43],v[28:31],a[20:23]\nDR v[76:79],v145 offset:2048\nM4 a[24:27],v[40:43],v[32:35],a[24:27]\nM4 a[28:31],v[40:43],v[36:39],a[28:31]\nDR v[80:83],v145 offset:4096\nBL v157,s[48:51],s66 offen offset:1024 lds\nM4 a[32:35],v[44:47],v[8:11],a[32:35]\n"
"M4 a[36:39],v[44:47],v[12:15],a[36:39]\nDR v[84:87],v145 offset:6144\nM4 a[40:43],v[44:47],v[16:19],a[40:43]\nDR v[88:91],v145 offset:8192\nSA m0,s82,32768\nM4 a[44:47],v[44:47],v[20:23],a[44:47]\nM4 a[48:51],v[44:47],v[24:27],a[48:51]\nDR v[92:95],v145 offset:10240\n"
"M4 a[52:55],v[44:47],v[28:31],a[52:55]\nBS v[248:251],v201,s[44:47],s75 offen offset:2048 sc1\nM4 a[56:59],v[44:47],v[32:35],a[56:59]\nDR v[96:99],v145 offset:12288\nBL v158,s[52:55],s67 offen lds\nM4 a[60:63],v[44:47],v[36:39],a[60:63]\nM4 a[64:67],v[48:51],v[8:11],a[64:67]\nDR v[100:103],v145 offset:14336\n"
"M4 a[68:71],v[48:51],v[12:15],a[68:71]\nSA s66,s66,262144\nM4 a[72:75],v[48:51],v[16:19],a[72:75]\nDT v[108:109],v165 offset:49152\nM4 a[76:79],v[48:51],v[20:23],a[76:79]\nDT v[110:111],v165 offset:50176\nM4 a[80:83],v[48:51],v[24:27],a[80:83]\nSA s67,s67,110592\n"
"M4 a[84:87],v[48:51],v[28:31],a[84:87]\nDT v[112:113],v166 offset:49152\nM4 a[88:91],v[48:51],v[32:35],a[88:91]\nM4 a[92:95],v[48:51],v[36:39],a[92:95]\nDT v[114:115],v166 offset:50176\nM4 a[96:99],v[52:55],v[8:11],a[96:99]\nSA m0,s70,69632\nM4 a[100:103],v[52:55],v[12:15],a[100:103]\n"
"DT v[116:117],v167 offset:49152\nSA s75,s80,24576\nM4 a[104:107],v[52:55],v[16:19],a[104:107]\nM4 a[108:111],v[52:55],v[20:23],a[108:111]\nDT v[118:119],v167 offset:50176\nSA s72,s68,40960\nM4 a[112:115],v[52:55],v[24:27],a[112:115]\nDT v[120:121],v168 offset:49152\n"
"M4 a[116:119],v[52:55],v[28:31],a[116:119]\nM4 a[120:123],v[52:55],v[32:35],a[120:123]\nDT v[122:123],v168 offset:49664\nBL v160,s[56:59],s72 offen offset:0 sc1 lds\nM4 a[124:127],v[52:55],v[36:39],a[124:127]\nM4 a[128:131],v[56:59],v[8:11],a[128:131]\nDT v[124:125],v169 offset:49152\nM4 a[132:135],v[56:59],v[12:15],a[132:135]\n"
"BL v161,s[56:59],s72 offen offset:1024 sc1 lds\nM4 a[136:139],v[56:59],v[16:19],a[136:139]\nDT v[126:127],v169 offset:49664\nM4 a[140:143],v[56:59],v[20:23],a[140:143]\nM4 a[144:147],v[56:59],v[24:27],a[144:147]\nM4 a[148:151],v[56:59],v[28:31],a[148:151]\nBL v162,s[56:59],s72 offen offset:2048 sc1 lds\nBS v[252:255],v201,s[44:47],s75 offen offset:2176 sc1\n"
"M4 a[152:155],v[56:59],v[32:35],a[152:155]\nM4 a[156:159],v[56:59],v[36:39],a[156:159]\nM4 a[192:195],v[56:59],a[200:203],a[192:195]\nBL v163,s[56:59],s72 offen offset:3072 sc1 lds\nM4 a[160:163],v[60:63],v[8:11],a[160:163]\nM4 a[164:167],v[60:63],v[12:15],a[164:167]\nM4 a[168:171],v[60:63],v[16:19],a[168:171]\nSA s68,s68,128\n"
"M4 a[172:175],v[60:63],v[20:23],a[172:175]\nM4 a[176:179],v[60:63],v[24:27],a[176:179]\nM4 a[180:183],v[60:63],v[28:31],a[180:183]\nM4 a[184:187],v[60:63],v[32:35],a[184:187]\nM4 a[188:191],v[60:63],v[36:39],a[188:191]\nM4 a[196:199],v[60:63],a[200:203],a[196:199]\nSW vmcnt(18) lgkmcnt(0)\ns_barrier\n"
"M4 a[0:3],v[104:107],v[72:75],a[0:3]\nVU v144,32768,v146\nM4 a[4:7],v[104:107],v[76:79],a[4:7]\nVU v145,32768,v147\nSA s75,s80,28672\nM4 a[8:11],v[104:107],v[80:83],a[8:11]\nDT v[40:41],v164 offset:0\nM4 a[12:15],v[104:107],v[84:87],a[12:15]\n"
"DT v[42:43],v164 offset:1024\nSA m0,s81,49152\nM4 a[16:19],v[104:107],v[88:91],a[16:19]\nM4 a[20:23],v[104:107],v[92:95],a[20:23]\nDR v[8:11],v144 offset:0\nM4 a[24:27],v[104:107],v[96:99],a[24:27]\nM4 a[28:31],v[104:107],v[100:103],a[28:31]\nDR v[12:15],v144 offset:2048\n"
"M4 a[32:35],v[108:111],v[72:75],a[32:35]\nDR v[16:19],v144 offset:4096\nM4 a[36:39],v[108:111],v[76:79],a[36:39]\nBL v156,s[48:51],s66 offen offset:0 lds\nM4 a[40:43],v[108:111],v[80:83],a[40:43]\nDR v[20:23],v144 offset:6144\nM4 a[44:47],v[108:111],v[84:87],a[44:47]\nM4 a[48:51],v[108:111],v[88:91],a[48:51]\n"
"DR v[24:27],v144 offset:8192\nM4 a[52:55],v[108:111],v[92:95],a[52:55]\nDR v[28:31],v144 offset:10240\nBS a[224:227],v201,s[44:47],s75 offen offset:0 sc1\nM4 a[56:59],v[108:111],v[96:99],a[56:59]\nM4 a[60:63],v[108:111],v[100:103],a[60:63]\nDR v[32:35],v144 offset:12288\nBL v157,s[48:51],s66 offen offset:1024 lds\n"
"M4 a[64:67],v[112:115],v[72:75],a[64:67]\nM4 a[68:71],v[112:115],v[76:79],a[68:71]\nDR v[36:39],v144 offset:14336\nM4 a[72:75],v[112:115],v[80:83],a[72:75]\nM4 a[76:79],v[112:115],v[84:87],a[76:79]\nDT v[44:45],v165 offset:0\nM4 a[80:83],v[112:115],v[88:91],a[80:83]\nDT v[46:47],v165 offset:1024\n"
"M4 a[84:87],v[112:115],v[92:95],a[84:87]\nSA m0,s82,49152\nM4 a[88:91],v[112:115],v[96:99],a[88:91]\nDT v[48:49],v166 offset:0\nM4 a[92:95],v[112:115],v[100:103],a[92:95]\nM4 a[96:99],v[116:119],v[72:75],a[96:99]\nDT v[50:51],v166 offset:1024\nM4 a[100:103],v[116:119],v[76:79],a[100:103]\n"
"DT v[52:53],v167 offset:0\nSA s75,s80,28672\nM4 a[104:107],v[116:119],v[80:83],a[104:107]\nM4 a[108:111],v[116:119],v[84:87],a[108:111]\nDT v[54:55],v167 offset:1024\nM4 a[112:115],v[116:119],v[88:91],a[112:115]\nBL v158,s[52:55],s67 offen lds\nM4 a[116:119],v[116:119],v[92:95],a[116:119]\n"
"DT v[56:57],v168 offset:0\nM4 a[120:123],v[116:119],v[96:99],a[120:123]\nDT v[58:59],v168 offset:512\nM4 a[124:127],v[116:119],v[100:103],a[124:127]\nM4 a[128:131],v[120:123],v[72:75],a[128:131]\nDT v[60:61],v169 offset:0\nM4 a[132:135],v[120:123],v[76:79],a[132:135]\nM4 a[136:139],v[120:123],v[80:83],a[136:139]\n"
"DT v[62:63],v169 offset:512\nSA s66,s66,262144\nM4 a[140:143],v[120:123],v[84:87],a[140:143]\nM4 a[144:147],v[120:123],v[88:91],a[144:147]\nM4 a[148:151],v[120:123],v[92:95],a[148:151]\nBS a[228:231],v201,s[44:47],s75 offen offset:128 sc1\nM4 a[152:155],v[120:123],v[96:99],a[152:155]\nM4 a[156:159],v[120:123],v[100:103],a[156:159]\n"
"M4 a[192:195],v[120:123],a[200:203],a[192:195]\nSA s67,s67,110592\nM4 a[160:163],v[124:127],v[72:75],a[160:163]\nM4 a[164:167],v[124:127],v[76:79],a[164:167]\nM4 a[168:171],v[124:127],v[80:83],a[168:171]\nM4 a[172:175],v[124:127],v[84:87],a[172:175]\nM4 a[176:179],v[124:127],v[88:91],a[176:179]\nM4 a[180:183],v[124:127],v[92:95],a[180:183]\n"
"M4 a[184:187],v[124:127],v[96:99],a[184:187]\nM4 a[188:191],v[124:127],v[100:103],a[188:191]\nM4 a[196:199],v[124:127],a[200:203],a[196:199]\nSW vmcnt(19) lgkmcnt(0)\ns_barrier\nM4 a[0:3],v[40:43],v[8:11],a[0:3]\nDT v[104:105],v164 offset:16384\nM4 a[4:7],v[40:43],v[12:15],a[4:7]\n"
"DT v[106:107],v164 offset:17408\nSA s75,s80,28672\nM4 a[8:11],v[40:43],v[16:19],a[8:11]\nM4 a[12:15],v[40:43],v[20:23],a[12:15]\nDR v[72:75],v145 offset:0\nM4 a[16:19],v[40:43],v[24:27],a[16:19]\nM4 a[20:23],v[40:43],v[28:31],a[20:23]\nDR v[76:79],v145 offset:2048\n"
"M4 a[24:27],v[40:43],v[32:35],a[24:27]\nM4 a[28:31],v[40:43],v[36:39],a[28:31]\nDR v[80:83],v145 offset:4096\nM4 a[32:35],v[44:47],v[8:11],a[32:35]\nM4 a[36:39],v[44:47],v[12:15],a[36:39]\nDR v[84:87],v145 offset:6144\nM4 a[40:43],v[44:47],v[16:19],a[40:43]\nDR v[88:91],v145 offset:8192\n"
"M4 a[44:47],v[44:47],v[20:23],a[44:47]\nM4 a[48:51],v[44:47],v[24:27],a[48:51]\nDR v[92:95],v145 offset:10240\nM4 a[52:55],v[44:47],v[28:31],a[52:55]\nBS a[232:235],v201,s[44:47],s75 offen offset:2048 sc1\nM4 a[56:59],v[44:47],v[32:35],a[56:59]\nDR v[96:99],v145 offset:12288\nM4 a[60:63],v[44:47],v[36:39],a[60:63]\n"
"M4 a[64:67],v[48:51],v[8:11],a[64:67]\nDR v[100:103],v145 offset:14336\nM4 a[68:71],v[48:51],v[12:15],a[68:71]\nM4 a[72:75],v[48:51],v[16:19],a[72:75]\nDT v[108:109],v165 offset:16384\nM4 a[76:79],v[48:51],v[20:23],a[76:79]\nDT v[110:111],v165 offset:17408\nM4 a[80:83],v[48:51],v[24:27],a[80:83]\n"
"M4 a[84:87],v[48:51],v[28:31],a[84:87]\nDT v[112:113],v166 offset:16384\nM4 a[88:91],v[48:51],v[32:35],a[88:91]\nM4 a[92:95],v[48:51],v[36:39],a[92:95]\nDT v[114:115],v166 offset:17408\nM4 a[96:99],v[52:55],v[8:11],a[96:99]\nM4 a[100:103],v[52:55],v[12:15],a[100:103]\nDT v[116:117],v167 offset:16384\n"
"SA s75,s80,28672\nM4 a[104:107],v[52:55],v[16:19],a[104:107]\nM4 a[108:111],v[52:55],v[20:23],a[108:111]\nDT v[118:119],v167 offset:17408\nM4 a[112:115],v[52:55],v[24:27],a[112:115]\nDT v[120:121],v168 offset:16384\nM4 a[116:119],v[52:55],v[28:31],a[116:119]\nM4 a[120:123],v[52:55],v[32:35],a[120:123]\n"
"DT v[122:123],v168 offset:16896\nM4 a[124:127],v[52:55],v[36:39],a[124:127]\nM4 a[128:131],v[56:59],v[8:11],a[128:131]\nDT v[124:125],v169 offset:16384\nM4 a[132:135],v[56:59],v[12:15],a[132:135]\nM4 a[136:139],v[56:59],v[16:19],a[136:139]\nDT v[126:127],v169 offset:16896\nM4 a[140:143],v[56:59],v[20:23],a[140:143]\n"
"M4 a[144:147],v[56:59],v[24:27],a[144:147]\nM4 a[148:151],v[56:59],v[28:31],a[148:151]\nBS a[236:239],v201,s[44:47],s75 offen offset:2176 sc1\nM4 a[152:155],v[56:59],v[32:35],a[152:155]\nM4 a[156:159],v[56:59],v[36:39],a[156:159]\nM4 a[192:195],v[56:59],a[200:203],a[192:195]\nM4 a[160:163],v[60:63],v[8:11],a[160:163]\nM4 a[164:167],v[60:63],v[12:15],a[164:167]\n"
"M4 a[168:171],v[60:63],v[16:19],a[168:171]\nM4 a[172:175],v[60:63],v[20:23],a[172:175]\nM4 a[176:179],v[60:63],v[24:27],a[176:179]\nM4 a[180:183],v[60:63],v[28:31],a[180:183]\nM4 a[184:187],v[60:63],v[32:35],a[184:187]\nM4 a[188:191],v[60:63],v[36:39],a[188:191]\nM4 a[196:199],v[60:63],a[200:203],a[196:199]\nSW vmcnt(7) lgkmcnt(0)\n"
"s_barrier\nM4 a[0:3],v[104:107],v[72:75],a[0:3]\nVU v144,65536,v146\nM4 a[4:7],v[104:107],v[76:79],a[4:7]\nVU v145,65536,v147\nSM s80,s60\nM4 a[8:11],v[104:107],v[80:83],a[8:11]\nDT v[40:41],v164 offset:32768\n"
"M4 a[12:15],v[104:107],v[84:87],a[12:15]\nDT v[42:43],v164 offset:33792\nM4 a[16:19],v[104:107],v[88:91],a[16:19]\nSM s81,s61\nM4 a[20:23],v[104:107],v[92:95],a[20:23]\nDR v[8:11],v144 offset:0\nM4 a[24:27],v[104:107],v[96:99],a[24:27]\nM4 a[28:31],v[104:107],v[100:103],a[28:31]\n"
"DR v[12:15],v144 offset:2048\nSM s82,s62\nM4 a[32:35],v[108:111],v[72:75],a[32:35]\nDR v[16:19],v144 offset:4096\nM4 a[36:39],v[108:111],v[76:79],a[36:39]\nM4 a[40:43],v[108:111],v[80:83],a[40:43]\nDR v[20:23],v144 offset:6144\nSM s83,s63\n"
"M4 a[44:47],v[108:111],v[84:87],a[44:47]\nM4 a[48:51],v[108:111],v[88:91],a[48:51]\nDR v[24:27],v144 offset:8192\nM4 a[52:55],v[108:111],v[92:95],a[52:55]\nDR v[28:31],v144 offset:10240\ns_load_dwordx2 s[84:85],s[94:95],0x38\nM4 a[56:59],v[108:111],v[96:99],a[56:59]\nM4 a[60:63],v[108:111],v[100:103],a[60:63]\n"
"DR v[32:35],v144 offset:12288\nM4 a[64:67],v[112:115],v[72:75],a[64:67]\ns_sub_u32 s34,15,s83\ns_cmp_eq_u32 s80,0\ns_cselect_b32 s34,s34,s83\ns_lshl_b32 s34,s34,10\nSA s34,s34,2621440\nSA s96,s92,s34\n"
"s_addc_u32 s97,s93,0\ns_and_b32 s97,s97,0xffff\nSM s98,1024\nSM s99,0x20000\nM4 a[68:71],v[112:115],v[76:79],a[68:71]\nDR v[36:39],v144 offset:14336\nM4 a[72:75],v[112:115],v[80:83],a[72:75]\nM4 a[76:79],v[112:115],v[84:87],a[76:79]\n"
"DT v[44:45],v165 offset:32768\nVN v228,15,v200\nM4 a[80:83],v[112:115],v[88:91],a[80:83]\nDT v[46:47],v165 offset:33792\nM4 a[84:87],v[112:115],v[92:95],a[84:87]\nM4 a[88:91],v[112:115],v[96:99],a[88:91]\nDT v[48:49],v166 offset:32768\nVL v228,3,v228\n"
"M4 a[92:95],v[112:115],v[100:103],a[92:95]\nM4 a[96:99],v[116:119],v[72:75],a[96:99]\nDT v[50:51],v166 offset:33792\nM4 a[100:103],v[116:119],v[76:79],a[100:103]\nDT v[52:53],v167 offset:32768\nbuffer_load_dwordx2 v[204:205],v228,s[96:99],0 offen offset:0 sc1\nM4 a[104:107],v[116:119],v[80:83],a[104:107]\nM4 a[108:111],v[116:119],v[84:87],a[108:111]\n"
"DT v[54:55],v167 offset:33792\nM4 a[112:115],v[116:119],v[88:91],a[112:115]\nbuffer_load_dwordx2 v[206:207],v228,s[96:99],0 offen offset:128 sc1\nM4 a[116:119],v[116:119],v[92:95],a[116:119]\nDT v[56:57],v168 offset:32768\nM4 a[120:123],v[116:119],v[96:99],a[120:123]\nDT v[58:59],v168 offset:33280\nM4 a[124:127],v[116:119],v[100:103],a[124:127]\n"
"buffer_load_dwordx2 v[208:209],v228,s[96:99],0 offen offset:256 sc1\nM4 a[128:131],v[120:123],v[72:75],a[128:131]\nDT v[60:61],v169 offset:32768\nM4 a[132:135],v[120:123],v[76:79],a[132:135]\nM4 a[136:139],v[120:123],v[80:83],a[136:139]\nDT v[62:63],v169 offset:33280\nbuffer_load_dwordx2 v[210:211],v228,s[96:99],0 offen offset:384 sc1\nM4 a[140:143],v[120:123],v[84:87],a[140:143]\n"
"M4 a[144:147],v[120:123],v[88:91],a[144:147]\nM4 a[148:151],v[120:123],v[92:95],a[148:151]\nbuffer_load_dwordx2 v[212:213],v228,s[96:99],0 offen offset:512 sc1\nM4 a[152:155],v[120:123],v[96:99],a[152:155]\nM4 a[156:159],v[120:123],v[100:103],a[156:159]\nM4 a[192:195],v[120:123],a[200:203],a[192:195]\nbuffer_load_dwordx2 v[214:215],v228,s[96:99],0 offen offset:640 sc1\nM4 a[160:163],v[124:127],v[72:75],a[160:163]\n"
"M4 a[164:167],v[124:127],v[76:79],a[164:167]\nM4 a[168:171],v[124:127],v[80:83],a[168:171]\nbuffer_load_dwordx2 v[216:217],v228,s[96:99],0 offen offset:768 sc1\nM4 a[172:175],v[124:127],v[84:87],a[172:175]\nM4 a[176:179],v[124:127],v[88:91],a[176:179]\nM4 a[180:183],v[124:127],v[92:95],a[180:183]\nbuffer_load_dwordx2 v[218:219],v228,s[96:99],0 offen offset:896 sc1\nM4 a[184:187],v[124:127],v[96:99],a[184:187]\n"
"M4 a[188:191],v[124:127],v[100:103],a[188:191]\nM4 a[196:199],v[124:127],a[200:203],a[196:199]\nSW vmcnt(11) lgkmcnt(0)\ns_barrier\nSW vmcnt(8)\nM4 a[0:3],v[40:43],v[8:11],a[0:3]\nDT v[104:105],v164 offset:49152\nM4 a[4:7],v[40:43],v[12:15],a[4:7]\n"
"DT v[106:107],v164 offset:50176\nSM s84,s84\ns_and_b32 s85,s85,0xffff\nSM s86,3456\nSM s87,0x20000\ns_lshl_b32 s34,s82,7\ns_lshl_b32 s35,s81,6\nSA s34,s34,s35\n"
"M4 a[8:11],v[40:43],v[16:19],a[8:11]\nM4 a[12:15],v[40:43],v[20:23],a[12:15]\nDR v[72:75],v145 offset:0\nM4 a[16:19],v[40:43],v[24:27],a[16:19]\nM4 a[20:23],v[40:43],v[28:31],a[20:23]\nDR v[76:79],v145 offset:2048\nM4 a[24:27],v[40:43],v[32:35],a[24:27]\nM4 a[28:31],v[40:43],v[36:39],a[28:31]\n"
"DR v[80:83],v145 offset:4096\nM4 a[32:35],v[44:47],v[8:11],a[32:35]\nM4 a[36:39],v[44:47],v[12:15],a[36:39]\nDR v[84:87],v145 offset:6144\nM4 a[40:43],v[44:47],v[16:19],a[40:43]\nDR v[88:91],v145 offset:8192\nv_lshrrev_b32 v228,4,v200\nM4 a[44:47],v[44:47],v[20:23],a[44:47]\n"
"M4 a[48:51],v[44:47],v[24:27],a[48:51]\nDR v[92:95],v145 offset:10240\nM4 a[52:55],v[44:47],v[28:31],a[52:55]\nM4 a[56:59],v[44:47],v[32:35],a[56:59]\nDR v[96:99],v145 offset:12288\nM4 a[60:63],v[44:47],v[36:39],a[60:63]\nM4 a[64:67],v[48:51],v[8:11],a[64:67]\nDR v[100:103],v145 offset:14336\n"
"M4 a[68:71],v[48:51],v[12:15],a[68:71]\nM4 a[72:75],v[48:51],v[16:19],a[72:75]\nDT v[108:109],v165 offset:49152\nM4 a[76:79],v[48:51],v[20:23],a[76:79]\nDT v[110:111],v165 offset:50176\nM4 a[80:83],v[48:51],v[24:27],a[80:83]\nVL v228,3,v228\nM4 a[84:87],v[48:51],v[28:31],a[84:87]\n"
"DT v[112:113],v166 offset:49152\nM4 a[88:91],v[48:51],v[32:35],a[88:91]\nM4 a[92:95],v[48:51],v[36:39],a[92:95]\nDT v[114:115],v166 offset:50176\nM4 a[96:99],v[52:55],v[8:11],a[96:99]\nM4 a[100:103],v[52:55],v[12:15],a[100:103]\nDT v[116:117],v167 offset:49152\nM4 a[104:107],v[52:55],v[16:19],a[104:107]\n"
"M4 a[108:111],v[52:55],v[20:23],a[108:111]\nDT v[118:119],v167 offset:50176\nM4 a[112:115],v[52:55],v[24:27],a[112:115]\nDT v[120:121],v168 offset:49152\nM4 a[116:119],v[52:55],v[28:31],a[116:119]\nbuffer_load_dwordx2 v[220:221],v228,s[84:87],s34 offen offset:0\nM4 a[120:123],v[52:55],v[32:35],a[120:123]\nDT v[122:123],v168 offset:49664\n"
"M4 a[124:127],v[52:55],v[36:39],a[124:127]\nM4 a[128:131],v[56:59],v[8:11],a[128:131]\nDT v[124:125],v169 offset:49152\nM4 a[132:135],v[56:59],v[12:15],a[132:135]\nM4 a[136:139],v[56:59],v[16:19],a[136:139]\nDT v[126:127],v169 offset:49664\nM4 a[140:143],v[56:59],v[20:23],a[140:143]\nM4 a[144:147],v[56:59],v[24:27],a[144:147]\n"
"M4 a[148:151],v[56:59],v[28:31],a[148:151]\nM4 a[152:155],v[56:59],v[32:35],a[152:155]\nM4 a[156:159],v[56:59],v[36:39],a[156:159]\nbuffer_load_dwordx2 v[222:223],v228,s[84:87],s34 offen offset:32\nM4 a[192:195],v[56:59],a[200:203],a[192:195]\nM4 a[160:163],v[60:63],v[8:11],a[160:163]\nM4 a[164:167],v[60:63],v[12:15],a[164:167]\nM4 a[168:171],v[60:63],v[16:19],a[168:171]\n"
"M4 a[172:175],v[60:63],v[20:23],a[172:175]\nM4 a[176:179],v[60:63],v[24:27],a[176:179]\nM4 a[180:183],v[60:63],v[28:31],a[180:183]\nM4 a[184:187],v[60:63],v[32:35],a[184:187]\nM4 a[188:191],v[60:63],v[36:39],a[188:191]\nM4 a[196:199],v[60:63],a[200:203],a[196:199]\nSW lgkmcnt(0)\ns_barrier\n"
"s_lshl_b32 s71,s88,3\nSA s71,s71,63971328\ns_cmp_eq_u32 s89,0\ns_cbranch_scc0 P1_kvsK\nv_mov_b32 v170,s90\nv_mov_b32 v171,s91\nv_mov_b32 v172,0\nVU v172,s71,v172\n"
"v_mov_b32 v173,s93\nv_add_co_u32 v172,vcc,s92,v172\ns_nop 1\nv_addc_co_u32 v173,vcc,0,v173,vcc\ns_mov_b64 exec,1\nglobal_store_dwordx2 v[172:173],v[170:171],off sc1\ns_mov_b64 exec,-1\nP1_kvsK:\n"
"M4 a[0:3],v[104:107],v[72:75],a[0:3]\nVU v144,0,v146\nM4 a[4:7],v[104:107],v[76:79],a[4:7]\nVU v145,0,v147\ns_and_b32 s34,s88,7\ns_lshl_b32 s34,s34,3\ns_lshr_b32 s35,s88,6\ns_lshl_b32 s35,s35,9\n"
"SA s34,s34,s35\nSA s34,s34,63971328\nVN v224,7,v200\nVL v224,6,v224\nVU v224,s34,v224\nv_mov_b32 v225,s93\nv_add_co_u32 v224,vcc,s92,v224\ns_nop 1\n"
"v_addc_co_u32 v225,vcc,0,v225,vcc\nM4 a[8:11],v[104:107],v[80:83],a[8:11]\nM4 a[12:15],v[104:107],v[84:87],a[12:15]\nM4 a[16:19],v[104:107],v[88:91],a[16:19]\nM4 a[20:23],v[104:107],v[92:95],a[20:23]\nM4 a[24:27],v[104:107],v[96:99],a[24:27]\nM4 a[28:31],v[104:107],v[100:103],a[28:31]\nM4 a[32:35],v[108:111],v[72:75],a[32:35]\n"
"M4 a[36:39],v[108:111],v[76:79],a[36:39]\nM4 a[40:43],v[108:111],v[80:83],a[40:43]\nM4 a[44:47],v[108:111],v[84:87],a[44:47]\nM4 a[48:51],v[108:111],v[88:91],a[48:51]\nM4 a[52:55],v[108:111],v[92:95],a[52:55]\nM4 a[56:59],v[108:111],v[96:99],a[56:59]\nM4 a[60:63],v[108:111],v[100:103],a[60:63]\nM4 a[64:67],v[112:115],v[72:75],a[64:67]\n"
"M4 a[68:71],v[112:115],v[76:79],a[68:71]\nM4 a[72:75],v[112:115],v[80:83],a[72:75]\nM4 a[76:79],v[112:115],v[84:87],a[76:79]\nM4 a[80:83],v[112:115],v[88:91],a[80:83]\nM4 a[84:87],v[112:115],v[92:95],a[84:87]\nM4 a[88:91],v[112:115],v[96:99],a[88:91]\nM4 a[92:95],v[112:115],v[100:103],a[92:95]\nM4 a[96:99],v[116:119],v[72:75],a[96:99]\n"
"M4 a[100:103],v[116:119],v[76:79],a[100:103]\nglobal_load_dwordx2 v[226:227],v[224:225],off sc1\nM4 a[104:107],v[116:119],v[80:83],a[104:107]\nM4 a[108:111],v[116:119],v[84:87],a[108:111]\nM4 a[112:115],v[116:119],v[88:91],a[112:115]\nM4 a[116:119],v[116:119],v[92:95],a[116:119]\nM4 a[120:123],v[116:119],v[96:99],a[120:123]\nM4 a[124:127],v[116:119],v[100:103],a[124:127]\n"
"M4 a[128:131],v[120:123],v[72:75],a[128:131]\nM4 a[132:135],v[120:123],v[76:79],a[132:135]\nM4 a[136:139],v[120:123],v[80:83],a[136:139]\nM4 a[140:143],v[120:123],v[84:87],a[140:143]\nM4 a[144:147],v[120:123],v[88:91],a[144:147]\nM4 a[148:151],v[120:123],v[92:95],a[148:151]\nM4 a[152:155],v[120:123],v[96:99],a[152:155]\nM4 a[156:159],v[120:123],v[100:103],a[156:159]\n"
"M4 a[192:195],v[120:123],a[200:203],a[192:195]\nM4 a[160:163],v[124:127],v[72:75],a[160:163]\nM4 a[164:167],v[124:127],v[76:79],a[164:167]\nM4 a[168:171],v[124:127],v[80:83],a[168:171]\nM4 a[172:175],v[124:127],v[84:87],a[172:175]\nM4 a[176:179],v[124:127],v[88:91],a[176:179]\nM4 a[180:183],v[124:127],v[92:95],a[180:183]\nM4 a[184:187],v[124:127],v[96:99],a[184:187]\n"
"M4 a[188:191],v[124:127],v[100:103],a[188:191]\nM4 a[196:199],v[124:127],a[200:203],a[196:199]\nSW lgkmcnt(0)\ns_branch P1_t2d\nP1_V:\nM4 a[0:3],v[8:11],v[40:43],0\nDT v[104:105],v136 offset:16384\nM4 a[4:7],v[12:15],v[40:43],0\n"
"DT v[106:107],v136 offset:17408\nSA m0,s70,32768\nVL v214,16,v204\nVN v215,0xffff0000,v204\nM4 a[8:11],v[16:19],v[40:43],0\nVL v216,16,v205\nVN v217,0xffff0000,v205\nM4 a[12:15],v[20:23],v[40:43],0\n"
"DR v[72:75],v145 offset:0\nBL v152,s[40:43],s65 offen offset:0 sc1 lds\nVL v218,16,v206\nM4 a[16:19],v[24:27],v[40:43],0\nVN v219,0xffff0000,v206\nVL v220,16,v207\nM4 a[20:23],v[28:31],v[40:43],0\nDR v[76:79],v145 offset:2048\n"
"VN v221,0xffff0000,v207\nVL v222,16,v208\nM4 a[24:27],v[32:35],v[40:43],0\nBL v153,s[40:43],s65 offen offset:1024 sc1 lds\nVN v223,0xffff0000,v208\nM4 a[28:31],v[36:39],v[40:43],0\nDR v[80:83],v145 offset:4096\nVL v224,16,v209\n"
"VN v225,0xffff0000,v209\nM4 a[32:35],v[8:11],v[44:47],0\nBL v154,s[40:43],s65 offen offset:2048 sc1 lds\nVL v226,16,v210\nM4 a[36:39],v[12:15],v[44:47],0\nDR v[84:87],v145 offset:6144\nVN v227,0xffff0000,v210\nVL v228,16,v211\n"
"M4 a[40:43],v[16:19],v[44:47],0\nVN v229,0xffff0000,v211\nVL v230,16,v212\nM4 a[44:47],v[20:23],v[44:47],0\nDR v[88:91],v145 offset:8192\nBL v155,s[40:43],s65 offen offset:3072 sc1 lds\nVN v231,0xffff0000,v212\nM4 a[48:51],v[24:27],v[44:47],0\n"
"VL v232,16,v213\nVN v233,0xffff0000,v213\nM4 a[52:55],v[28:31],v[44:47],0\nDR v[92:95],v145 offset:10240\nSA m0,s69,32768\nVA v236,v214,v215\nM4 a[56:59],v[32:35],v[44:47],0\nVA v236,v236,v216\n"
"VA v236,v236,v217\nM4 a[60:63],v[36:39],v[44:47],0\nDR v[96:99],v145 offset:12288\nVA v236,v236,v218\nVA v236,v236,v219\nM4 a[64:67],v[8:11],v[48:51],0\nBL v148,s[36:39],s64 offen offset:0 lds\nVA v236,v236,v220\n"
"M4 a[68:71],v[12:15],v[48:51],0\nDR v[100:103],v145 offset:14336\nVA v236,v236,v221\nVA v236,v236,v222\nM4 a[72:75],v[16:19],v[48:51],0\nBL v149,s[36:39],s64 offen offset:1024 lds\nVA v236,v236,v223\nVA v236,v236,v224\n"
"M4 a[76:79],v[20:23],v[48:51],0\nDT v[108:109],v137 offset:16384\nVA v236,v236,v225\nM4 a[80:83],v[24:27],v[48:51],0\nVA v236,v236,v226\nVA v236,v236,v227\nM4 a[84:87],v[28:31],v[48:51],0\nDT v[110:111],v137 offset:17408\n"
"BL v150,s[36:39],s64 offen offset:2048 lds\nVA v236,v236,v228\nM4 a[88:91],v[32:35],v[48:51],0\nVA v236,v236,v229\nVA v236,v236,v230\nM4 a[92:95],v[36:39],v[48:51],0\nDT v[112:113],v138 offset:16384\nBL v151,s[36:39],s64 offen offset:3072 lds\n"
"VA v236,v236,v231\nVA v236,v236,v232\nM4 a[96:99],v[8:11],v[52:55],0\nDT v[114:115],v138 offset:17408\nVA v236,v236,v233\nM4 a[100:103],v[12:15],v[52:55],0\ns_nop 0\nv_add_f32_dpp v236,v236,v236 row_ror:8 row_mask:0xf bank_mask:0xf\n"
"s_nop 1\nv_add_f32_dpp v236,v236,v236 row_ror:4 row_mask:0xf bank_mask:0xf\nM4 a[104:107],v[16:19],v[52:55],0\nDT v[116:117],v139 offset:16384\nSA s64,s64,262144\nv_add_f32_dpp v236,v236,v236 row_ror:2 row_mask:0xf bank_mask:0xf\nM4 a[108:111],v[20:23],v[52:55],0\ns_nop 0\n"
"v_add_f32_dpp v236,v236,v236 row_ror:1 row_mask:0xf bank_mask:0xf\nv_mov_b32 v237,v236\nM4 a[112:115],v[24:27],v[52:55],0\nDT v[118:119],v139 offset:17408\nSA m0,s70,36864\nv_permlane16_swap_b32 v236,v237\nVA v236,v236,v237\nM4 a[116:119],v[28:31],v[52:55],0\n"
"VM v236,0x3acccccd,v236\nM4 a[120:123],v[32:35],v[52:55],0\nDT v[120:121],v140 offset:16384\nVS v238,v214,v236\nVM v239,v238,v238\nM4 a[124:127],v[36:39],v[52:55],0\nSA s72,s65,32768\nVS v238,v215,v236\n"
"M4 a[128:131],v[8:11],v[56:59],0\nDT v[122:123],v140 offset:17408\nv_fmac_f32 v239,v238,v238\nVS v238,v216,v236\nM4 a[132:135],v[12:15],v[56:59],0\nBL v152,s[40:43],s72 offen offset:0 sc1 lds\nv_fmac_f32 v239,v238,v238\nVS v238,v217,v236\n"
"M4 a[136:139],v[16:19],v[56:59],0\nDT v[124:125],v141 offset:16384\nv_fmac_f32 v239,v238,v238\nM4 a[140:143],v[20:23],v[56:59],0\nVS v238,v218,v236\nv_fmac_f32 v239,v238,v238\nM4 a[144:147],v[24:27],v[56:59],0\nDT v[126:127],v141 offset:17408\n"
"BL v153,s[40:43],s72 offen offset:1024 sc1 lds\nVS v238,v219,v236\nv_fmac_f32 v239,v238,v238\nM4 a[148:151],v[28:31],v[56:59],0\nVS v238,v220,v236\nM4 a[152:155],v[32:35],v[56:59],0\nDT v[128:129],v142 offset:16384\nBL v154,s[40:43],s72 offen offset:2048 sc1 lds\n"
"v_fmac_f32 v239,v238,v238\nVS v238,v221,v236\nM4 a[156:159],v[36:39],v[56:59],0\nv_fmac_f32 v239,v238,v238\nM4 a[160:163],v[8:11],v[60:63],0\nDT v[130:131],v142 offset:17408\nVS v238,v222,v236\nv_fmac_f32 v239,v238,v238\n"
"M4 a[164:167],v[12:15],v[60:63],0\nBL v155,s[40:43],s72 offen offset:3072 sc1 lds\nVS v238,v223,v236\nv_fmac_f32 v239,v238,v238\nM4 a[168:171],v[16:19],v[60:63],0\nDT v[132:133],v143 offset:16384\nVS v238,v224,v236\nM4 a[172:175],v[20:23],v[60:63],0\n"
"SA s65,s65,128\nv_fmac_f32 v239,v238,v238\nVS v238,v225,v236\nM4 a[176:179],v[24:27],v[60:63],0\nDT v[134:135],v143 offset:17408\nv_fmac_f32 v239,v238,v238\nM4 a[180:183],v[28:31],v[60:63],0\nVS v238,v226,v236\n"
"v_fmac_f32 v239,v238,v238\nM4 a[184:187],v[32:35],v[60:63],0\nSA m0,s69,49152\nVS v238,v227,v236\nv_fmac_f32 v239,v238,v238\nM4 a[188:191],v[36:39],v[60:63],0\nVS v238,v228,v236\nM4 a[192:195],v[8:11],v[64:67],0\n"
"BL v148,s[36:39],s64 offen offset:0 lds\nv_fmac_f32 v239,v238,v238\nVS v238,v229,v236\nM4 a[196:199],v[12:15],v[64:67],0\nv_fmac_f32 v239,v238,v238\nVS v238,v230,v236\nM4 a[200:203],v[16:19],v[64:67],0\nv_fmac_f32 v239,v238,v238\n"
"M4 a[204:207],v[20:23],v[64:67],0\nBL v149,s[36:39],s64 offen offset:1024 lds\nVS v238,v231,v236\nv_fmac_f32 v239,v238,v238\nM4 a[208:211],v[24:27],v[64:67],0\nVS v238,v232,v236\nM4 a[212:215],v[28:31],v[64:67],0\nBL v150,s[36:39],s64 offen offset:2048 lds\n"
"v_fmac_f32 v239,v238,v238\nVS v238,v233,v236\nM4 a[216:219],v[32:35],v[64:67],0\nv_fmac_f32 v239,v238,v238\ns_nop 1\nv_add_f32_dpp v239,v239,v239 row_ror:8 row_mask:0xf bank_mask:0xf\nM4 a[220:223],v[36:39],v[64:67],0\ns_nop 0\n"
"v_add_f32_dpp v239,v239,v239 row_ror:4 row_mask:0xf bank_mask:0xf\nM4 a[224:227],v[8:11],v[68:71],0\nBL v151,s[36:39],s64 offen offset:3072 lds\nv_add_f32_dpp v239,v239,v239 row_ror:2 row_mask:0xf bank_mask:0xf\ns_nop 1\nv_add_f32_dpp v239,v239,v239 row_ror:1 row_mask:0xf bank_mask:0xf\nM4 a[228:231],v[12:15],v[68:71],0\nv_mov_b32 v237,v239\n"
"M4 a[232:235],v[16:19],v[68:71],0\nSA s64,s64,262144\nv_permlane16_swap_b32 v239,v237\nVA v239,v239,v237\nM4 a[236:239],v[20:23],v[68:71],0\nVM v239,0x3acccccd,v239\nCV v237,v236,v236\nM4 a[240:243],v[24:27],v[68:71],0\n"
"VL v236,16,v237\nM4 a[244:247],v[28:31],v[68:71],0\nCV v237,v239,v239\nVL v239,16,v237\nM4 a[248:251],v[32:35],v[68:71],0\nVA v239,0x3727c5ac,v239\nM4 a[252:255],v[36:39],v[68:71],0\nSW vmcnt(4) lgkmcnt(0)\n"
"s_barrier\nM4 a[0:3],v[72:75],v[104:107],a[0:3]\nVU v144,32768,v146\nM4 a[4:7],v[76:79],v[104:107],a[4:7]\nVU v145,32768,v147\nCV v237,v239,v239\nVL v239,16,v237\nM4 a[8:11],v[80:83],v[104:107],a[8:11]\n"
"DT v[40:41],v136 offset:32768\nv_sqrt_f32 v239,v239\ns_nop 0\nCV v237,v239,v239\nM4 a[12:15],v[84:87],v[104:107],a[12:15]\nDT v[42:43],v136 offset:33792\nSA m0,s70,65536\nVL v239,16,v237\n"
"RC v239,v239\nM4 a[16:19],v[88:91],v[104:107],a[16:19]\nVS v214,v214,v236\nM4 a[20:23],v[92:95],v[104:107],a[20:23]\nDR v[8:11],v144 offset:0\nBL v152,s[40:43],s65 offen offset:0 sc1 lds\nVS v215,v215,v236\nVS v216,v216,v236\n"
"M4 a[24:27],v[96:99],v[104:107],a[24:27]\nVS v217,v217,v236\nVS v218,v218,v236\nM4 a[28:31],v[100:103],v[104:107],a[28:31]\nDR v[12:15],v144 offset:2048\nBL v153,s[40:43],s65 offen offset:1024 sc1 lds\nVS v219,v219,v236\nM4 a[32:35],v[72:75],v[108:111],a[32:35]\n"
"VS v220,v220,v236\nVS v221,v221,v236\nM4 a[36:39],v[76:79],v[108:111],a[36:39]\nDR v[16:19],v144 offset:4096\nBL v154,s[40:43],s65 offen offset:2048 sc1 lds\nVS v222,v222,v236\nVS v223,v223,v236\nM4 a[40:43],v[80:83],v[108:111],a[40:43]\n"
"VS v224,v224,v236\nM4 a[44:47],v[84:87],v[108:111],a[44:47]\nDR v[20:23],v144 offset:6144\nBL v155,s[40:43],s65 offen offset:3072 sc1 lds\nVS v225,v225,v236\nVS v226,v226,v236\nM4 a[48:51],v[88:91],v[108:111],a[48:51]\nVS v227,v227,v236\n"
"VS v228,v228,v236\nM4 a[52:55],v[92:95],v[108:111],a[52:55]\nDR v[24:27],v144 offset:8192\nSA m0,s69,0\nVS v229,v229,v236\nM4 a[56:59],v[96:99],v[108:111],a[56:59]\nDR v[28:31],v144 offset:10240\nVS v230,v230,v236\n"
"VS v231,v231,v236\nM4 a[60:63],v[100:103],v[108:111],a[60:63]\nBL v148,s[36:39],s64 offen offset:0 lds\nVS v232,v232,v236\nVS v233,v233,v236\nM4 a[64:67],v[72:75],v[112:115],a[64:67]\nDR v[32:35],v144 offset:12288\nCV v204,v214,v215\n"
"M4 a[68:71],v[76:79],v[112:115],a[68:71]\nBL v149,s[36:39],s64 offen offset:1024 lds\nCV v205,v216,v217\nCV v206,v218,v219\nM4 a[72:75],v[80:83],v[112:115],a[72:75]\nDR v[36:39],v144 offset:14336\nCV v207,v220,v221\nCV v208,v222,v223\n"
"M4 a[76:79],v[84:87],v[112:115],a[76:79]\nBL v150,s[36:39],s64 offen offset:2048 lds\nCV v209,v224,v225\nM4 a[80:83],v[88:91],v[112:115],a[80:83]\nDT v[44:45],v137 offset:32768\nCV v210,v226,v227\nCV v211,v228,v229\nM4 a[84:87],v[92:95],v[112:115],a[84:87]\n"
"BL v151,s[36:39],s64 offen offset:3072 lds\nCV v212,v230,v231\nCV v213,v232,v233\nM4 a[88:91],v[96:99],v[112:115],a[88:91]\nDT v[46:47],v137 offset:33792\nVL v214,16,v204\nM4 a[92:95],v[100:103],v[112:115],a[92:95]\nSA s64,s64,262144\n"
"VN v215,0xffff0000,v204\nVL v216,16,v205\nM4 a[96:99],v[72:75],v[116:119],a[96:99]\nDT v[48:49],v138 offset:32768\nVN v217,0xffff0000,v205\nVL v218,16,v206\nM4 a[100:103],v[76:79],v[116:119],a[100:103]\nDT v[50:51],v138 offset:33792\n"
"SA m0,s70,69632\nVN v219,0xffff0000,v206\nM4 a[104:107],v[80:83],v[116:119],a[104:107]\nVL v220,16,v207\nVN v221,0xffff0000,v207\nM4 a[108:111],v[84:87],v[116:119],a[108:111]\nDT v[52:53],v139 offset:32768\nSA s72,s65,32768\n"
"VL v222,16,v208\nVN v223,0xffff0000,v208\nM4 a[112:115],v[88:91],v[116:119],a[112:115]\nVL v224,16,v209\nM4 a[116:119],v[92:95],v[116:119],a[116:119]\nDT v[54:55],v139 offset:33792\nBL v152,s[40:43],s72 offen offset:0 sc1 lds\nVN v225,0xffff0000,v209\n"
"VL v226,16,v210\nM4 a[120:123],v[96:99],v[116:119],a[120:123]\nVN v227,0xffff0000,v210\nVL v228,16,v211\nM4 a[124:127],v[100:103],v[116:119],a[124:127]\nDT v[56:57],v140 offset:32768\nBL v153,s[40:43],s72 offen offset:1024 sc1 lds\nVN v229,0xffff0000,v211\n"
"M4 a[128:131],v[72:75],v[120:123],a[128:131]\nVL v230,16,v212\nVN v231,0xffff0000,v212\nM4 a[132:135],v[76:79],v[120:123],a[132:135]\nDT v[58:59],v140 offset:33792\nBL v154,s[40:43],s72 offen offset:2048 sc1 lds\nVL v232,16,v213\nVN v233,0xffff0000,v213\n"
"M4 a[136:139],v[80:83],v[120:123],a[136:139]\nVM v214,v214,v239\nVM v215,v215,v239\nM4 a[140:143],v[84:87],v[120:123],a[140:143]\nDT v[60:61],v141 offset:32768\nBL v155,s[40:43],s72 offen offset:3072 sc1 lds\nVM v216,v216,v239\nM4 a[144:147],v[88:91],v[120:123],a[144:147]\n"
"DT v[62:63],v141 offset:33792\nVM v217,v217,v239\nVM v218,v218,v239\nM4 a[148:151],v[92:95],v[120:123],a[148:151]\nSA s65,s65,128\nVM v219,v219,v239\nVM v220,v220,v239\nM4 a[152:155],v[96:99],v[120:123],a[152:155]\n"
"DT v[64:65],v142 offset:32768\nVM v221,v221,v239\nM4 a[156:159],v[100:103],v[120:123],a[156:159]\nSA m0,s69,16384\nVM v222,v222,v239\nVM v223,v223,v239\nM4 a[160:163],v[72:75],v[124:127],a[160:163]\nDT v[66:67],v142 offset:33792\n"
"VM v224,v224,v239\nVM v225,v225,v239\nM4 a[164:167],v[76:79],v[124:127],a[164:167]\nBL v148,s[36:39],s64 offen offset:0 lds\nVM v226,v226,v239\nM4 a[168:171],v[80:83],v[124:127],a[168:171]\nDT v[68:69],v143 offset:32768\nVM v227,v227,v239\n"
"VM v228,v228,v239\nM4 a[172:175],v[84:87],v[124:127],a[172:175]\nBL v149,s[36:39],s64 offen offset:1024 lds\nVM v229,v229,v239\nVM v230,v230,v239\nM4 a[176:179],v[88:91],v[124:127],a[176:179]\nDT v[70:71],v143 offset:33792\nVM v231,v231,v239\n"
"M4 a[180:183],v[92:95],v[124:127],a[180:183]\nBL v150,s[36:39],s64 offen offset:2048 lds\nVM v232,v232,v239\nVM v233,v233,v239\nM4 a[184:187],v[96:99],v[124:127],a[184:187]\nCV v204,v214,v215\nCV v205,v216,v217\nM4 a[188:191],v[100:103],v[124:127],a[188:191]\n"
"BL v151,s[36:39],s64 offen offset:3072 lds\nCV v206,v218,v219\nM4 a[192:195],v[72:75],v[128:131],a[192:195]\nCV v207,v220,v221\nCV v208,v222,v223\nM4 a[196:199],v[76:79],v[128:131],a[196:199]\nSA s64,s64,262144\nCV v209,v224,v225\n"
"CV v210,v226,v227\nM4 a[200:203],v[80:83],v[128:131],a[200:203]\nCV v211,v228,v229\nM4 a[204:207],v[84:87],v[128:131],a[204:207]\nSA m0,s70,0\nCV v212,v230,v231\nCV v213,v232,v233\nM4 a[208:211],v[88:91],v[128:131],a[208:211]\n"
"buffer_store_dwordx2 v[204:205],v234,s[96:99],s78 offen offset:0 sc1\nbuffer_store_dwordx2 v[206:207],v234,s[96:99],s78 offen offset:256 sc1\nM4 a[212:215],v[92:95],v[128:131],a[212:215]\nBL v152,s[40:43],s65 offen offset:0 sc1 lds\nbuffer_store_dwordx2 v[208:209],v234,s[96:99],s78 offen offset:512 sc1\nM4 a[216:219],v[96:99],v[128:131],a[216:219]\nbuffer_store_dwordx2 v[210:211],v234,s[96:99],s78 offen offset:768 sc1\nbuffer_store_dwordx2 v[212:213],v234,s[96:99],s78 offen offset:1024 sc1\n"
"M4 a[220:223],v[100:103],v[128:131],a[220:223]\nBL v153,s[40:43],s65 offen offset:1024 sc1 lds\nVL v214,16,v204\nVN v215,0xffff0000,v204\nM4 a[224:227],v[72:75],v[132:135],a[224:227]\nVL v216,16,v205\nM4 a[228:231],v[76:79],v[132:135],a[228:231]\nBL v154,s[40:43],s65 offen offset:2048 sc1 lds\n"
"VN v217,0xffff0000,v205\nVL v218,16,v206\nM4 a[232:235],v[80:83],v[132:135],a[232:235]\nVN v219,0xffff0000,v206\nVL v220,16,v207\nM4 a[236:239],v[84:87],v[132:135],a[236:239]\nBL v155,s[40:43],s65 offen offset:3072 sc1 lds\nVN v221,0xffff0000,v207\n"
"M4 a[240:243],v[88:91],v[132:135],a[240:243]\nVL v222,16,v208\nbuffer_load_dword v244,v250,s[48:51],s76 offen\nM4 a[244:247],v[92:95],v[132:135],a[244:247]\nbuffer_load_dword v245,v251,s[52:55],s77 offen\nSA s76,s76,524288\nM4 a[248:251],v[96:99],v[132:135],a[248:251]\nSA s77,s77,221184\n"
"M4 a[252:255],v[100:103],v[132:135],a[252:255]\nSW vmcnt(27) lgkmcnt(0)\ns_barrier\nM4 a[0:3],v[8:11],v[40:43],a[0:3]\nDT v[104:105],v136 offset:49152\nM4 a[4:7],v[12:15],v[40:43],a[4:7]\nDT v[106:107],v136 offset:50176\nSA m0,s69,32768\n"
"VN v223,0xffff0000,v208\nVL v224,16,v209\nM4 a[8:11],v[16:19],v[40:43],a[8:11]\nVN v225,0xffff0000,v209\nVL v226,16,v210\nM4 a[12:15],v[20:23],v[40:43],a[12:15]\nDR v[72:75],v145 offset:0\nVN v227,0xffff0000,v210\n"
"VL v228,16,v211\nM4 a[16:19],v[24:27],v[40:43],a[16:19]\nVN v229,0xffff0000,v211\nM4 a[20:23],v[28:31],v[40:43],a[20:23]\nDR v[76:79],v145 offset:2048\nBL v148,s[36:39],s64 offen offset:0 lds\nVL v230,16,v212\nVN v231,0xffff0000,v212\n"
"M4 a[24:27],v[32:35],v[40:43],a[24:27]\nVL v232,16,v213\nVN v233,0xffff0000,v213\nM4 a[28:31],v[36:39],v[40:43],a[28:31]\nDR v[80:83],v145 offset:4096\nVA v236,v214,v215\nM4 a[32:35],v[8:11],v[44:47],a[32:35]\nVA v236,v236,v216\n"
"VA v236,v236,v217\nM4 a[36:39],v[12:15],v[44:47],a[36:39]\nDR v[84:87],v145 offset:6144\nVA v236,v236,v218\nVA v236,v236,v219\nM4 a[40:43],v[16:19],v[44:47],a[40:43]\nBL v149,s[36:39],s64 offen offset:1024 lds\nVA v236,v236,v220\n"
"M4 a[44:47],v[20:23],v[44:47],a[44:47]\nDR v[88:91],v145 offset:8192\nVA v236,v236,v221\nVA v236,v236,v222\nM4 a[48:51],v[24:27],v[44:47],a[48:51]\nVA v236,v236,v223\nVA v236,v236,v224\nM4 a[52:55],v[28:31],v[44:47],a[52:55]\n"
"DR v[92:95],v145 offset:10240\nVA v236,v236,v225\nM4 a[56:59],v[32:35],v[44:47],a[56:59]\nBL v150,s[36:39],s64 offen offset:2048 lds\nVA v236,v236,v226\nVA v236,v236,v227\nM4 a[60:63],v[36:39],v[44:47],a[60:63]\nDR v[96:99],v145 offset:12288\n"
"VA v236,v236,v228\nVA v236,v236,v229\nM4 a[64:67],v[8:11],v[48:51],a[64:67]\nVA v236,v236,v230\nM4 a[68:71],v[12:15],v[48:51],a[68:71]\nDR v[100:103],v145 offset:14336\nVA v236,v236,v231\nVA v236,v236,v232\n"
"M4 a[72:75],v[16:19],v[48:51],a[72:75]\nVA v236,v236,v233\ns_nop 1\nv_add_f32_dpp v236,v236,v236 row_ror:8 row_mask:0xf bank_mask:0xf\nM4 a[76:79],v[20:23],v[48:51],a[76:79]\nDT v[108:109],v137 offset:49152\nBL v151,s[36:39],s64 offen offset:3072 lds\nv_add_f32_dpp v236,v236,v236 row_ror:4 row_mask:0xf bank_mask:0xf\n"
"M4 a[80:83],v[24:27],v[48:51],a[80:83]\ns_nop 0\nv_add_f32_dpp v236,v236,v236 row_ror:2 row_mask:0xf bank_mask:0xf\ns_nop 1\nv_add_f32_dpp v236,v236,v236 row_ror:1 row_mask:0xf bank_mask:0xf\nM4 a[84:87],v[28:31],v[48:51],a[84:87]\nDT v[110:111],v137 offset:50176\nv_mov_b32 v237,v236\n"
"s_nop 1\nv_permlane16_swap_b32 v236,v237\nM4 a[88:91],v[32:35],v[48:51],a[88:91]\nVA v236,v236,v237\nM4 a[92:95],v[36:39],v[48:51],a[92:95]\nDT v[112:113],v138 offset:49152\nVM v236,0x3acccccd,v236\nVS v238,v214,v236\n"
"M4 a[96:99],v[8:11],v[52:55],a[96:99]\nDT v[114:115],v138 offset:50176\nSA s64,s64,262144\nVM v239,v238,v238\nVS v238,v215,v236\nM4 a[100:103],v[12:15],v[52:55],a[100:103]\nv_fmac_f32 v239,v238,v238\nM4 a[104:107],v[16:19],v[52:55],a[104:107]\n"
"DT v[116:117],v139 offset:49152\nVS v238,v216,v236\nv_fmac_f32 v239,v238,v238\nM4 a[108:111],v[20:23],v[52:55],a[108:111]\nVS v238,v217,v236\nv_fmac_f32 v239,v238,v238\nM4 a[112:115],v[24:27],v[52:55],a[112:115]\nDT v[118:119],v139 offset:50176\n"
"SA m0,s70,4096\nVS v238,v218,v236\nM4 a[116:119],v[28:31],v[52:55],a[116:119]\nv_fmac_f32 v239,v238,v238\nVS v238,v219,v236\nM4 a[120:123],v[32:35],v[52:55],a[120:123]\nDT v[120:121],v140 offset:49152\nv_fmac_f32 v239,v238,v238\n"
"VS v238,v220,v236\nM4 a[124:127],v[36:39],v[52:55],a[124:127]\nv_fmac_f32 v239,v238,v238\nM4 a[128:131],v[8:11],v[56:59],a[128:131]\nDT v[122:123],v140 offset:50176\nVS v238,v221,v236\nv_fmac_f32 v239,v238,v238\nM4 a[132:135],v[12:15],v[56:59],a[132:135]\n"
"SA s72,s65,32768\nVS v238,v222,v236\nv_fmac_f32 v239,v238,v238\nM4 a[136:139],v[16:19],v[56:59],a[136:139]\nDT v[124:125],v141 offset:49152\nVS v238,v223,v236\nv_fmac_f32 v239,v238,v238\nM4 a[140:143],v[20:23],v[56:59],a[140:143]\n"
"VS v238,v224,v236\nM4 a[144:147],v[24:27],v[56:59],a[144:147]\nDT v[126:127],v141 offset:50176\nv_fmac_f32 v239,v238,v238\nVS v238,v225,v236\nM4 a[148:151],v[28:31],v[56:59],a[148:151]\nBL v152,s[40:43],s72 offen offset:0 sc1 lds\nv_fmac_f32 v239,v238,v238\n"
"VS v238,v226,v236\nM4 a[152:155],v[32:35],v[56:59],a[152:155]\nDT v[128:129],v142 offset:49152\nv_fmac_f32 v239,v238,v238\nM4 a[156:159],v[36:39],v[56:59],a[156:159]\nVS v238,v227,v236\nv_fmac_f32 v239,v238,v238\nM4 a[160:163],v[8:11],v[60:63],a[160:163]\n"
"DT v[130:131],v142 offset:50176\nVS v238,v228,v236\nv_fmac_f32 v239,v238,v238\nM4 a[164:167],v[12:15],v[60:63],a[164:167]\nVS v238,v229,v236\nM4 a[168:171],v[16:19],v[60:63],a[168:171]\nDT v[132:133],v143 offset:49152\nBL v153,s[40:43],s72 offen offset:1024 sc1 lds\n"
"v_fmac_f32 v239,v238,v238\nVS v238,v230,v236\nM4 a[172:175],v[20:23],v[60:63],a[172:175]\nv_fmac_f32 v239,v238,v238\nVS v238,v231,v236\nM4 a[176:179],v[24:27],v[60:63],a[176:179]\nDT v[134:135],v143 offset:50176\nv_fmac_f32 v239,v238,v238\n"
"M4 a[180:183],v[28:31],v[60:63],a[180:183]\nVS v238,v232,v236\nv_fmac_f32 v239,v238,v238\nM4 a[184:187],v[32:35],v[60:63],a[184:187]\nVS v238,v233,v236\nv_fmac_f32 v239,v238,v238\nM4 a[188:191],v[36:39],v[60:63],a[188:191]\nBL v154,s[40:43],s72 offen offset:2048 sc1 lds\n"
"v_add_f32_dpp v239,v239,v239 row_ror:8 row_mask:0xf bank_mask:0xf\nM4 a[192:195],v[8:11],v[64:67],a[192:195]\ns_nop 0\nv_add_f32_dpp v239,v239,v239 row_ror:4 row_mask:0xf bank_mask:0xf\ns_nop 1\nv_add_f32_dpp v239,v239,v239 row_ror:2 row_mask:0xf bank_mask:0xf\nM4 a[196:199],v[12:15],v[64:67],a[196:199]\ns_nop 0\n"
"v_add_f32_dpp v239,v239,v239 row_ror:1 row_mask:0xf bank_mask:0xf\nv_mov_b32 v237,v239\nM4 a[200:203],v[16:19],v[64:67],a[200:203]\ns_nop 0\nv_permlane16_swap_b32 v239,v237\nM4 a[204:207],v[20:23],v[64:67],a[204:207]\nBL v155,s[40:43],s72 offen offset:3072 sc1 lds\nVA v239,v239,v237\n"
"VM v239,0x3acccccd,v239\nM4 a[208:211],v[24:27],v[64:67],a[208:211]\nCV v237,v236,v236\nVL v236,16,v237\nM4 a[212:215],v[28:31],v[64:67],a[212:215]\nCV v237,v239,v239\nM4 a[216:219],v[32:35],v[64:67],a[216:219]\nVL v239,16,v237\n"
"VA v239,0x3727c5ac,v239\nM4 a[220:223],v[36:39],v[64:67],a[220:223]\nCV v237,v239,v239\nVL v239,16,v237\nM4 a[224:227],v[8:11],v[68:71],a[224:227]\nSA s65,s65,128\nv_sqrt_f32 v239,v239\nM4 a[228:231],v[12:15],v[68:71],a[228:231]\n"
"CV v237,v239,v239\nVL v239,16,v237\nM4 a[232:235],v[16:19],v[68:71],a[232:235]\nRC v239,v239\ns_nop 0\nv_mov_b32 v240,v239\nM4 a[236:239],v[20:23],v[68:71],a[236:239]\nVM v241,v236,v239\n"
"M4 a[240:243],v[24:27],v[68:71],a[240:243]\nbuffer_store_dwordx2 v[240:241],v235,s[96:99],s79 offen sc1\nbuffer_load_dword v242,v250,s[48:51],s76 offen\nM4 a[244:247],v[28:31],v[68:71],a[244:247]\nbuffer_load_dword v243,v251,s[52:55],s77 offen\nSA s76,s76,524288\nM4 a[248:251],v[32:35],v[68:71],a[248:251]\nSA s77,s77,221184\n"
"M4 a[252:255],v[36:39],v[68:71],a[252:255]\nSW vmcnt(26) lgkmcnt(0)\ns_barrier\nM4 a[0:3],v[72:75],v[104:107],a[0:3]\nVU v144,65536,v146\nM4 a[4:7],v[76:79],v[104:107],a[4:7]\nVU v145,65536,v147\nbuffer_load_dword v244,v250,s[48:51],s76 offen\n"
"M4 a[8:11],v[80:83],v[104:107],a[8:11]\nDT v[40:41],v136 offset:0\nM4 a[12:15],v[84:87],v[104:107],a[12:15]\nDT v[42:43],v136 offset:1024\nSA m0,s69,49152\nM4 a[16:19],v[88:91],v[104:107],a[16:19]\nM4 a[20:23],v[92:95],v[104:107],a[20:23]\nDR v[8:11],v144 offset:0\n"
"M4 a[24:27],v[96:99],v[104:107],a[24:27]\nM4 a[28:31],v[100:103],v[104:107],a[28:31]\nDR v[12:15],v144 offset:2048\nM4 a[32:35],v[72:75],v[108:111],a[32:35]\nBL v148,s[36:39],s64 offen offset:0 lds\nM4 a[36:39],v[76:79],v[108:111],a[36:39]\nDR v[16:19],v144 offset:4096\nM4 a[40:43],v[80:83],v[108:111],a[40:43]\n"
"M4 a[44:47],v[84:87],v[108:111],a[44:47]\nDR v[20:23],v144 offset:6144\nM4 a[48:51],v[88:91],v[108:111],a[48:51]\nM4 a[52:55],v[92:95],v[108:111],a[52:55]\nDR v[24:27],v144 offset:8192\nBL v149,s[36:39],s64 offen offset:1024 lds\nM4 a[56:59],v[96:99],v[108:111],a[56:59]\nDR v[28:31],v144 offset:10240\n"
"M4 a[60:63],v[100:103],v[108:111],a[60:63]\nM4 a[64:67],v[72:75],v[112:115],a[64:67]\nDR v[32:35],v144 offset:12288\nbuffer_load_dword v245,v251,s[52:55],s77 offen\nM4 a[68:71],v[76:79],v[112:115],a[68:71]\nM4 a[72:75],v[80:83],v[112:115],a[72:75]\nDR v[36:39],v144 offset:14336\nBL v150,s[36:39],s64 offen offset:2048 lds\n"
"M4 a[76:79],v[84:87],v[112:115],a[76:79]\nM4 a[80:83],v[88:91],v[112:115],a[80:83]\nDT v[44:45],v137 offset:0\nM4 a[84:87],v[92:95],v[112:115],a[84:87]\nM4 a[88:91],v[96:99],v[112:115],a[88:91]\nDT v[46:47],v137 offset:1024\nM4 a[92:95],v[100:103],v[112:115],a[92:95]\nM4 a[96:99],v[72:75],v[116:119],a[96:99]\n"
"DT v[48:49],v138 offset:0\nBL v151,s[36:39],s64 offen offset:3072 lds\nM4 a[100:103],v[76:79],v[116:119],a[100:103]\nDT v[50:51],v138 offset:1024\nM4 a[104:107],v[80:83],v[116:119],a[104:107]\nM4 a[108:111],v[84:87],v[116:119],a[108:111]\nDT v[52:53],v139 offset:0\nM4 a[112:115],v[88:91],v[116:119],a[112:115]\n"
"M4 a[116:119],v[92:95],v[116:119],a[116:119]\nDT v[54:55],v139 offset:1024\nSA s64,s64,262144\nM4 a[120:123],v[96:99],v[116:119],a[120:123]\nM4 a[124:127],v[100:103],v[116:119],a[124:127]\nDT v[56:57],v140 offset:0\nM4 a[128:131],v[72:75],v[120:123],a[128:131]\nSA s76,s76,524288\n"
"M4 a[132:135],v[76:79],v[120:123],a[132:135]\nDT v[58:59],v140 offset:1024\nM4 a[136:139],v[80:83],v[120:123],a[136:139]\nSA m0,s70,32768\nM4 a[140:143],v[84:87],v[120:123],a[140:143]\nDT v[60:61],v141 offset:0\nM4 a[144:147],v[88:91],v[120:123],a[144:147]\nDT v[62:63],v141 offset:1024\n"
"M4 a[148:151],v[92:95],v[120:123],a[148:151]\nM4 a[152:155],v[96:99],v[120:123],a[152:155]\nDT v[64:65],v142 offset:0\nM4 a[156:159],v[100:103],v[120:123],a[156:159]\nBL v152,s[40:43],s65 offen offset:0 sc1 lds\nM4 a[160:163],v[72:75],v[124:127],a[160:163]\nDT v[66:67],v142 offset:1024\nM4 a[164:167],v[76:79],v[124:127],a[164:167]\n"
"M4 a[168:171],v[80:83],v[124:127],a[168:171]\nDT v[68:69],v143 offset:0\nM4 a[172:175],v[84:87],v[124:127],a[172:175]\nM4 a[176:179],v[88:91],v[124:127],a[176:179]\nDT v[70:71],v143 offset:1024\nM4 a[180:183],v[92:95],v[124:127],a[180:183]\nBL v153,s[40:43],s65 offen offset:1024 sc1 lds\nM4 a[184:187],v[96:99],v[124:127],a[184:187]\n"
"M4 a[188:191],v[100:103],v[124:127],a[188:191]\nSA s77,s77,221184\nM4 a[192:195],v[72:75],v[128:131],a[192:195]\nM4 a[196:199],v[76:79],v[128:131],a[196:199]\nM4 a[200:203],v[80:83],v[128:131],a[200:203]\nBL v154,s[40:43],s65 offen offset:2048 sc1 lds\nM4 a[204:207],v[84:87],v[128:131],a[204:207]\nM4 a[208:211],v[88:91],v[128:131],a[208:211]\n"
"M4 a[212:215],v[92:95],v[128:131],a[212:215]\nM4 a[216:219],v[96:99],v[128:131],a[216:219]\nM4 a[220:223],v[100:103],v[128:131],a[220:223]\nBL v155,s[40:43],s65 offen offset:3072 sc1 lds\nM4 a[224:227],v[72:75],v[132:135],a[224:227]\nM4 a[228:231],v[76:79],v[132:135],a[228:231]\nM4 a[232:235],v[80:83],v[132:135],a[232:235]\nM4 a[236:239],v[84:87],v[132:135],a[236:239]\n"
"M4 a[240:243],v[88:91],v[132:135],a[240:243]\nM4 a[244:247],v[92:95],v[132:135],a[244:247]\nM4 a[248:251],v[96:99],v[132:135],a[248:251]\nM4 a[252:255],v[100:103],v[132:135],a[252:255]\nSW vmcnt(32) lgkmcnt(0)\ns_barrier\nM4 a[0:3],v[8:11],v[40:43],a[0:3]\nDT v[104:105],v136 offset:16384\n"
"M4 a[4:7],v[12:15],v[40:43],a[4:7]\nDT v[106:107],v136 offset:17408\nSA m0,s69,0\nbuffer_load_dword v242,v250,s[48:51],s76 offen\nM4 a[8:11],v[16:19],v[40:43],a[8:11]\nM4 a[12:15],v[20:23],v[40:43],a[12:15]\nDR v[72:75],v145 offset:0\nM4 a[16:19],v[24:27],v[40:43],a[16:19]\n"
"M4 a[20:23],v[28:31],v[40:43],a[20:23]\nDR v[76:79],v145 offset:2048\nBL v148,s[36:39],s64 offen offset:0 lds\nM4 a[24:27],v[32:35],v[40:43],a[24:27]\nM4 a[28:31],v[36:39],v[40:43],a[28:31]\nDR v[80:83],v145 offset:4096\nM4 a[32:35],v[8:11],v[44:47],a[32:35]\nM4 a[36:39],v[12:15],v[44:47],a[36:39]\n"
"DR v[84:87],v145 offset:6144\nM4 a[40:43],v[16:19],v[44:47],a[40:43]\nBL v149,s[36:39],s64 offen offset:1024 lds\nM4 a[44:47],v[20:23],v[44:47],a[44:47]\nDR v[88:91],v145 offset:8192\nM4 a[48:51],v[24:27],v[44:47],a[48:51]\nM4 a[52:55],v[28:31],v[44:47],a[52:55]\nDR v[92:95],v145 offset:10240\n"
"M4 a[56:59],v[32:35],v[44:47],a[56:59]\nBL v150,s[36:39],s64 offen offset:2048 lds\nM4 a[60:63],v[36:39],v[44:47],a[60:63]\nDR v[96:99],v145 offset:12288\nM4 a[64:67],v[8:11],v[48:51],a[64:67]\nbuffer_load_dword v243,v251,s[52:55],s77 offen\nM4 a[68:71],v[12:15],v[48:51],a[68:71]\nDR v[100:103],v145 offset:14336\n"
"M4 a[72:75],v[16:19],v[48:51],a[72:75]\nM4 a[76:79],v[20:23],v[48:51],a[76:79]\nDT v[108:109],v137 offset:16384\nBL v151,s[36:39],s64 offen offset:3072 lds\nM4 a[80:83],v[24:27],v[48:51],a[80:83]\nM4 a[84:87],v[28:31],v[48:51],a[84:87]\nDT v[110:111],v137 offset:17408\nM4 a[88:91],v[32:35],v[48:51],a[88:91]\n"
"M4 a[92:95],v[36:39],v[48:51],a[92:95]\nDT v[112:113],v138 offset:16384\nM4 a[96:99],v[8:11],v[52:55],a[96:99]\nDT v[114:115],v138 offset:17408\nSA s64,s64,262144\nM4 a[100:103],v[12:15],v[52:55],a[100:103]\nM4 a[104:107],v[16:19],v[52:55],a[104:107]\nDT v[116:117],v139 offset:16384\n"
"M4 a[108:111],v[20:23],v[52:55],a[108:111]\nM4 a[112:115],v[24:27],v[52:55],a[112:115]\nDT v[118:119],v139 offset:17408\nSA m0,s70,36864\nM4 a[116:119],v[28:31],v[52:55],a[116:119]\nM4 a[120:123],v[32:35],v[52:55],a[120:123]\nDT v[120:121],v140 offset:16384\nM4 a[124:127],v[36:39],v[52:55],a[124:127]\n"
"M4 a[128:131],v[8:11],v[56:59],a[128:131]\nDT v[122:123],v140 offset:17408\nSA s76,s76,524288\nM4 a[132:135],v[12:15],v[56:59],a[132:135]\nSA s72,s65,32768\nM4 a[136:139],v[16:19],v[56:59],a[136:139]\nDT v[124:125],v141 offset:16384\nM4 a[140:143],v[20:23],v[56:59],a[140:143]\n"
"M4 a[144:147],v[24:27],v[56:59],a[144:147]\nDT v[126:127],v141 offset:17408\nM4 a[148:151],v[28:31],v[56:59],a[148:151]\nBL v152,s[40:43],s72 offen offset:0 sc1 lds\nM4 a[152:155],v[32:35],v[56:59],a[152:155]\nDT v[128:129],v142 offset:16384\nM4 a[156:159],v[36:39],v[56:59],a[156:159]\nM4 a[160:163],v[8:11],v[60:63],a[160:163]\n"
"DT v[130:131],v142 offset:17408\nM4 a[164:167],v[12:15],v[60:63],a[164:167]\nM4 a[168:171],v[16:19],v[60:63],a[168:171]\nDT v[132:133],v143 offset:16384\nBL v153,s[40:43],s72 offen offset:1024 sc1 lds\nM4 a[172:175],v[20:23],v[60:63],a[172:175]\nM4 a[176:179],v[24:27],v[60:63],a[176:179]\nDT v[134:135],v143 offset:17408\n"
"M4 a[180:183],v[28:31],v[60:63],a[180:183]\nM4 a[184:187],v[32:35],v[60:63],a[184:187]\nM4 a[188:191],v[36:39],v[60:63],a[188:191]\nBL v154,s[40:43],s72 offen offset:2048 sc1 lds\nSA s77,s77,221184\nM4 a[192:195],v[8:11],v[64:67],a[192:195]\nM4 a[196:199],v[12:15],v[64:67],a[196:199]\nM4 a[200:203],v[16:19],v[64:67],a[200:203]\n"
"M4 a[204:207],v[20:23],v[64:67],a[204:207]\nBL v155,s[40:43],s72 offen offset:3072 sc1 lds\nM4 a[208:211],v[24:27],v[64:67],a[208:211]\nM4 a[212:215],v[28:31],v[64:67],a[212:215]\nM4 a[216:219],v[32:35],v[64:67],a[216:219]\nM4 a[220:223],v[36:39],v[64:67],a[220:223]\nM4 a[224:227],v[8:11],v[68:71],a[224:227]\nSA s65,s65,128\n"
"M4 a[228:231],v[12:15],v[68:71],a[228:231]\nM4 a[232:235],v[16:19],v[68:71],a[232:235]\nM4 a[236:239],v[20:23],v[68:71],a[236:239]\nM4 a[240:243],v[24:27],v[68:71],a[240:243]\nM4 a[244:247],v[28:31],v[68:71],a[244:247]\nM4 a[248:251],v[32:35],v[68:71],a[248:251]\nM4 a[252:255],v[36:39],v[68:71],a[252:255]\nSW vmcnt(23) lgkmcnt(0)\n"
"s_barrier\nSW vmcnt(22)\nM4 a[0:3],v[72:75],v[104:107],a[0:3]\nVU v144,0,v146\nM4 a[4:7],v[76:79],v[104:107],a[4:7]\nVU v145,0,v147\nbuffer_load_dword v244,v250,s[48:51],s76 offen\nM4 a[8:11],v[80:83],v[104:107],a[8:11]\n"
"DT v[40:41],v136 offset:32768\nM4 a[12:15],v[84:87],v[104:107],a[12:15]\nDT v[42:43],v136 offset:33792\nSA m0,s69,16384\nM4 a[16:19],v[88:91],v[104:107],a[16:19]\nM4 a[20:23],v[92:95],v[104:107],a[20:23]\nDR v[8:11],v144 offset:0\nM4 a[24:27],v[96:99],v[104:107],a[24:27]\n"
"M4 a[28:31],v[100:103],v[104:107],a[28:31]\nDR v[12:15],v144 offset:2048\nM4 a[32:35],v[72:75],v[108:111],a[32:35]\nBL v148,s[36:39],s64 offen offset:0 lds\nM4 a[36:39],v[76:79],v[108:111],a[36:39]\nDR v[16:19],v144 offset:4096\nM4 a[40:43],v[80:83],v[108:111],a[40:43]\nM4 a[44:47],v[84:87],v[108:111],a[44:47]\n"
"DR v[20:23],v144 offset:6144\nM4 a[48:51],v[88:91],v[108:111],a[48:51]\nM4 a[52:55],v[92:95],v[108:111],a[52:55]\nDR v[24:27],v144 offset:8192\nBL v149,s[36:39],s64 offen offset:1024 lds\nM4 a[56:59],v[96:99],v[108:111],a[56:59]\nDR v[28:31],v144 offset:10240\nM4 a[60:63],v[100:103],v[108:111],a[60:63]\n"
"M4 a[64:67],v[72:75],v[112:115],a[64:67]\nDR v[32:35],v144 offset:12288\nbuffer_load_dword v245,v251,s[52:55],s77 offen\nM4 a[68:71],v[76:79],v[112:115],a[68:71]\nM4 a[72:75],v[80:83],v[112:115],a[72:75]\nDR v[36:39],v144 offset:14336\nBL v150,s[36:39],s64 offen offset:2048 lds\nM4 a[76:79],v[84:87],v[112:115],a[76:79]\n"
"M4 a[80:83],v[88:91],v[112:115],a[80:83]\nDT v[44:45],v137 offset:32768\nM4 a[84:87],v[92:95],v[112:115],a[84:87]\nM4 a[88:91],v[96:99],v[112:115],a[88:91]\nDT v[46:47],v137 offset:33792\nM4 a[92:95],v[100:103],v[112:115],a[92:95]\nM4 a[96:99],v[72:75],v[116:119],a[96:99]\nDT v[48:49],v138 offset:32768\n"
"BL v151,s[36:39],s64 offen offset:3072 lds\nM4 a[100:103],v[76:79],v[116:119],a[100:103]\nDT v[50:51],v138 offset:33792\nM4 a[104:107],v[80:83],v[116:119],a[104:107]\nM4 a[108:111],v[84:87],v[116:119],a[108:111]\nDT v[52:53],v139 offset:32768\nM4 a[112:115],v[88:91],v[116:119],a[112:115]\nM4 a[116:119],v[92:95],v[116:119],a[116:119]\n"
"DT v[54:55],v139 offset:33792\nSA s64,s64,262144\nM4 a[120:123],v[96:99],v[116:119],a[120:123]\nM4 a[124:127],v[100:103],v[116:119],a[124:127]\nDT v[56:57],v140 offset:32768\nM4 a[128:131],v[72:75],v[120:123],a[128:131]\nSA s76,s76,524288\nM4 a[132:135],v[76:79],v[120:123],a[132:135]\n"
"DT v[58:59],v140 offset:33792\nM4 a[136:139],v[80:83],v[120:123],a[136:139]\nSA m0,s70,65536\nM4 a[140:143],v[84:87],v[120:123],a[140:143]\nDT v[60:61],v141 offset:32768\nM4 a[144:147],v[88:91],v[120:123],a[144:147]\nDT v[62:63],v141 offset:33792\nM4 a[148:151],v[92:95],v[120:123],a[148:151]\n"
"M4 a[152:155],v[96:99],v[120:123],a[152:155]\nDT v[64:65],v142 offset:32768\nM4 a[156:159],v[100:103],v[120:123],a[156:159]\nBL v152,s[40:43],s65 offen offset:0 sc1 lds\nM4 a[160:163],v[72:75],v[124:127],a[160:163]\nDT v[66:67],v142 offset:33792\nM4 a[164:167],v[76:79],v[124:127],a[164:167]\nM4 a[168:171],v[80:83],v[124:127],a[168:171]\n"
"DT v[68:69],v143 offset:32768\nM4 a[172:175],v[84:87],v[124:127],a[172:175]\nM4 a[176:179],v[88:91],v[124:127],a[176:179]\nDT v[70:71],v143 offset:33792\nM4 a[180:183],v[92:95],v[124:127],a[180:183]\nBL v153,s[40:43],s65 offen offset:1024 sc1 lds\nM4 a[184:187],v[96:99],v[124:127],a[184:187]\nM4 a[188:191],v[100:103],v[124:127],a[188:191]\n"
"SA s77,s77,221184\nM4 a[192:195],v[72:75],v[128:131],a[192:195]\nM4 a[196:199],v[76:79],v[128:131],a[196:199]\nM4 a[200:203],v[80:83],v[128:131],a[200:203]\nBL v154,s[40:43],s65 offen offset:2048 sc1 lds\nM4 a[204:207],v[84:87],v[128:131],a[204:207]\nM4 a[208:211],v[88:91],v[128:131],a[208:211]\nM4 a[212:215],v[92:95],v[128:131],a[212:215]\n"
"M4 a[216:219],v[96:99],v[128:131],a[216:219]\nM4 a[220:223],v[100:103],v[128:131],a[220:223]\nBL v155,s[40:43],s65 offen offset:3072 sc1 lds\nM4 a[224:227],v[72:75],v[132:135],a[224:227]\nM4 a[228:231],v[76:79],v[132:135],a[228:231]\nM4 a[232:235],v[80:83],v[132:135],a[232:235]\nM4 a[236:239],v[84:87],v[132:135],a[236:239]\nM4 a[240:243],v[88:91],v[132:135],a[240:243]\n"
"M4 a[244:247],v[92:95],v[132:135],a[244:247]\nM4 a[248:251],v[96:99],v[132:135],a[248:251]\nM4 a[252:255],v[100:103],v[132:135],a[252:255]\nSW vmcnt(24) lgkmcnt(0)\ns_barrier\ns_lshl_b32 s71,s88,3\nSA s71,s71,63967232\ns_cmp_eq_u32 s89,0\n"
"s_cbranch_scc0 P1_lnsV\nv_mov_b32 v170,s90\nv_mov_b32 v171,s91\nv_mov_b32 v172,0\nVU v172,s71,v172\nv_mov_b32 v173,s93\nv_add_co_u32 v172,vcc,s92,v172\ns_nop 1\n"
"v_addc_co_u32 v173,vcc,0,v173,vcc\ns_mov_b64 exec,1\nglobal_store_dwordx2 v[172:173],v[170:171],off sc1\ns_mov_b64 exec,-1\nP1_lnsV:\nM4 a[0:3],v[8:11],v[40:43],a[0:3]\nDT v[104:105],v136 offset:49152\nM4 a[4:7],v[12:15],v[40:43],a[4:7]\n"
"DT v[106:107],v136 offset:50176\nSA m0,s69,32768\nbuffer_load_dword v242,v250,s[48:51],s76 offen\nM4 a[8:11],v[16:19],v[40:43],a[8:11]\nM4 a[12:15],v[20:23],v[40:43],a[12:15]\nDR v[72:75],v145 offset:0\nM4 a[16:19],v[24:27],v[40:43],a[16:19]\nM4 a[20:23],v[28:31],v[40:43],a[20:23]\n"
"DR v[76:79],v145 offset:2048\nBL v148,s[36:39],s64 offen offset:0 lds\nM4 a[24:27],v[32:35],v[40:43],a[24:27]\nM4 a[28:31],v[36:39],v[40:43],a[28:31]\nDR v[80:83],v145 offset:4096\nM4 a[32:35],v[8:11],v[44:47],a[32:35]\nM4 a[36:39],v[12:15],v[44:47],a[36:39]\nDR v[84:87],v145 offset:6144\n"
"M4 a[40:43],v[16:19],v[44:47],a[40:43]\nBL v149,s[36:39],s64 offen offset:1024 lds\nM4 a[44:47],v[20:23],v[44:47],a[44:47]\nDR v[88:91],v145 offset:8192\nM4 a[48:51],v[24:27],v[44:47],a[48:51]\nM4 a[52:55],v[28:31],v[44:47],a[52:55]\nDR v[92:95],v145 offset:10240\nM4 a[56:59],v[32:35],v[44:47],a[56:59]\n"
"BL v150,s[36:39],s64 offen offset:2048 lds\nM4 a[60:63],v[36:39],v[44:47],a[60:63]\nDR v[96:99],v145 offset:12288\nM4 a[64:67],v[8:11],v[48:51],a[64:67]\nbuffer_load_dword v243,v251,s[52:55],s77 offen\nM4 a[68:71],v[12:15],v[48:51],a[68:71]\nDR v[100:103],v145 offset:14336\nM4 a[72:75],v[16:19],v[48:51],a[72:75]\n"
"M4 a[76:79],v[20:23],v[48:51],a[76:79]\nDT v[108:109],v137 offset:49152\nBL v151,s[36:39],s64 offen offset:3072 lds\nM4 a[80:83],v[24:27],v[48:51],a[80:83]\nM4 a[84:87],v[28:31],v[48:51],a[84:87]\nDT v[110:111],v137 offset:50176\nM4 a[88:91],v[32:35],v[48:51],a[88:91]\nM4 a[92:95],v[36:39],v[48:51],a[92:95]\n"
"DT v[112:113],v138 offset:49152\nM4 a[96:99],v[8:11],v[52:55],a[96:99]\nDT v[114:115],v138 offset:50176\nSA s64,s64,262144\nM4 a[100:103],v[12:15],v[52:55],a[100:103]\nM4 a[104:107],v[16:19],v[52:55],a[104:107]\nDT v[116:117],v139 offset:49152\nM4 a[108:111],v[20:23],v[52:55],a[108:111]\n"
"M4 a[112:115],v[24:27],v[52:55],a[112:115]\nDT v[118:119],v139 offset:50176\nSA m0,s70,69632\nM4 a[116:119],v[28:31],v[52:55],a[116:119]\nM4 a[120:123],v[32:35],v[52:55],a[120:123]\nDT v[120:121],v140 offset:49152\nM4 a[124:127],v[36:39],v[52:55],a[124:127]\nM4 a[128:131],v[8:11],v[56:59],a[128:131]\n"
"DT v[122:123],v140 offset:50176\nSA s76,s76,524288\nM4 a[132:135],v[12:15],v[56:59],a[132:135]\nSA s72,s65,32768\nM4 a[136:139],v[16:19],v[56:59],a[136:139]\nDT v[124:125],v141 offset:49152\nM4 a[140:143],v[20:23],v[56:59],a[140:143]\nM4 a[144:147],v[24:27],v[56:59],a[144:147]\n"
"DT v[126:127],v141 offset:50176\nM4 a[148:151],v[28:31],v[56:59],a[148:151]\nBL v152,s[40:43],s72 offen offset:0 sc1 lds\nM4 a[152:155],v[32:35],v[56:59],a[152:155]\nDT v[128:129],v142 offset:49152\nM4 a[156:159],v[36:39],v[56:59],a[156:159]\nM4 a[160:163],v[8:11],v[60:63],a[160:163]\nDT v[130:131],v142 offset:50176\n"
"M4 a[164:167],v[12:15],v[60:63],a[164:167]\nM4 a[168:171],v[16:19],v[60:63],a[168:171]\nDT v[132:133],v143 offset:49152\nBL v153,s[40:43],s72 offen offset:1024 sc1 lds\nM4 a[172:175],v[20:23],v[60:63],a[172:175]\nM4 a[176:179],v[24:27],v[60:63],a[176:179]\nDT v[134:135],v143 offset:50176\nM4 a[180:183],v[28:31],v[60:63],a[180:183]\n"
"M4 a[184:187],v[32:35],v[60:63],a[184:187]\nM4 a[188:191],v[36:39],v[60:63],a[188:191]\nBL v154,s[40:43],s72 offen offset:2048 sc1 lds\nSA s77,s77,221184\nM4 a[192:195],v[8:11],v[64:67],a[192:195]\nM4 a[196:199],v[12:15],v[64:67],a[196:199]\nM4 a[200:203],v[16:19],v[64:67],a[200:203]\nM4 a[204:207],v[20:23],v[64:67],a[204:207]\n"
"BL v155,s[40:43],s72 offen offset:3072 sc1 lds\nM4 a[208:211],v[24:27],v[64:67],a[208:211]\nM4 a[212:215],v[28:31],v[64:67],a[212:215]\nM4 a[216:219],v[32:35],v[64:67],a[216:219]\nM4 a[220:223],v[36:39],v[64:67],a[220:223]\nM4 a[224:227],v[8:11],v[68:71],a[224:227]\nSA s65,s65,128\nM4 a[228:231],v[12:15],v[68:71],a[228:231]\n"
"M4 a[232:235],v[16:19],v[68:71],a[232:235]\nM4 a[236:239],v[20:23],v[68:71],a[236:239]\nM4 a[240:243],v[24:27],v[68:71],a[240:243]\nM4 a[244:247],v[28:31],v[68:71],a[244:247]\nM4 a[248:251],v[32:35],v[68:71],a[248:251]\nM4 a[252:255],v[36:39],v[68:71],a[252:255]\nSW vmcnt(20) lgkmcnt(0)\ns_barrier\n"
"M4 a[0:3],v[72:75],v[104:107],a[0:3]\nVU v144,32768,v146\nM4 a[4:7],v[76:79],v[104:107],a[4:7]\nVU v145,32768,v147\nbuffer_load_dword v244,v250,s[48:51],s76 offen\nM4 a[8:11],v[80:83],v[104:107],a[8:11]\nDT v[40:41],v136 offset:0\nM4 a[12:15],v[84:87],v[104:107],a[12:15]\n"
"DT v[42:43],v136 offset:1024\nSA m0,s69,49152\nM4 a[16:19],v[88:91],v[104:107],a[16:19]\nM4 a[20:23],v[92:95],v[104:107],a[20:23]\nDR v[8:11],v144 offset:0\nM4 a[24:27],v[96:99],v[104:107],a[24:27]\nM4 a[28:31],v[100:103],v[104:107],a[28:31]\nDR v[12:15],v144 offset:2048\n"
"M4 a[32:35],v[72:75],v[108:111],a[32:35]\nBL v148,s[36:39],s64 offen offset:0 lds\nM4 a[36:39],v[76:79],v[108:111],a[36:39]\nDR v[16:19],v144 offset:4096\nM4 a[40:43],v[80:83],v[108:111],a[40:43]\nM4 a[44:47],v[84:87],v[108:111],a[44:47]\nDR v[20:23],v144 offset:6144\nM4 a[48:51],v[88:91],v[108:111],a[48:51]\n"
"M4 a[52:55],v[92:95],v[108:111],a[52:55]\nDR v[24:27],v144 offset:8192\nBL v149,s[36:39],s64 offen offset:1024 lds\nM4 a[56:59],v[96:99],v[108:111],a[56:59]\nDR v[28:31],v144 offset:10240\nM4 a[60:63],v[100:103],v[108:111],a[60:63]\nM4 a[64:67],v[72:75],v[112:115],a[64:67]\nDR v[32:35],v144 offset:12288\n"
"buffer_load_dword v245,v251,s[52:55],s77 offen\nM4 a[68:71],v[76:79],v[112:115],a[68:71]\nM4 a[72:75],v[80:83],v[112:115],a[72:75]\nDR v[36:39],v144 offset:14336\nBL v150,s[36:39],s64 offen offset:2048 lds\nM4 a[76:79],v[84:87],v[112:115],a[76:79]\nM4 a[80:83],v[88:91],v[112:115],a[80:83]\nDT v[44:45],v137 offset:0\n"
"M4 a[84:87],v[92:95],v[112:115],a[84:87]\nM4 a[88:91],v[96:99],v[112:115],a[88:91]\nDT v[46:47],v137 offset:1024\nM4 a[92:95],v[100:103],v[112:115],a[92:95]\nM4 a[96:99],v[72:75],v[116:119],a[96:99]\nDT v[48:49],v138 offset:0\nBL v151,s[36:39],s64 offen offset:3072 lds\nM4 a[100:103],v[76:79],v[116:119],a[100:103]\n"
"DT v[50:51],v138 offset:1024\nM4 a[104:107],v[80:83],v[116:119],a[104:107]\nM4 a[108:111],v[84:87],v[116:119],a[108:111]\nDT v[52:53],v139 offset:0\nM4 a[112:115],v[88:91],v[116:119],a[112:115]\nM4 a[116:119],v[92:95],v[116:119],a[116:119]\nDT v[54:55],v139 offset:1024\nSA s64,s64,262144\n"
"M4 a[120:123],v[96:99],v[116:119],a[120:123]\nM4 a[124:127],v[100:103],v[116:119],a[124:127]\nDT v[56:57],v140 offset:0\nM4 a[128:131],v[72:75],v[120:123],a[128:131]\nSA s76,s76,524288\nM4 a[132:135],v[76:79],v[120:123],a[132:135]\nDT v[58:59],v140 offset:1024\nM4 a[136:139],v[80:83],v[120:123],a[136:139]\n"
"SA m0,s70,0\nM4 a[140:143],v[84:87],v[120:123],a[140:143]\nDT v[60:61],v141 offset:0\nM4 a[144:147],v[88:91],v[120:123],a[144:147]\nDT v[62:63],v141 offset:1024\nM4 a[148:151],v[92:95],v[120:123],a[148:151]\nM4 a[152:155],v[96:99],v[120:123],a[152:155]\nDT v[64:65],v142 offset:0\n"
"M4 a[156:159],v[100:103],v[120:123],a[156:159]\nBL v152,s[40:43],s65 offen offset:0 sc1 lds\nM4 a[160:163],v[72:75],v[124:127],a[160:163]\nDT v[66:67],v142 offset:1024\nM4 a[164:167],v[76:79],v[124:127],a[164:167]\nM4 a[168:171],v[80:83],v[124:127],a[168:171]\nDT v[68:69],v143 offset:0\nM4 a[172:175],v[84:87],v[124:127],a[172:175]\n"
"M4 a[176:179],v[88:91],v[124:127],a[176:179]\nDT v[70:71],v143 offset:1024\nM4 a[180:183],v[92:95],v[124:127],a[180:183]\nBL v153,s[40:43],s65 offen offset:1024 sc1 lds\nM4 a[184:187],v[96:99],v[124:127],a[184:187]\nM4 a[188:191],v[100:103],v[124:127],a[188:191]\nSA s77,s77,221184\nM4 a[192:195],v[72:75],v[128:131],a[192:195]\n"
"M4 a[196:199],v[76:79],v[128:131],a[196:199]\nM4 a[200:203],v[80:83],v[128:131],a[200:203]\nBL v154,s[40:43],s65 offen offset:2048 sc1 lds\nM4 a[204:207],v[84:87],v[128:131],a[204:207]\nM4 a[208:211],v[88:91],v[128:131],a[208:211]\nM4 a[212:215],v[92:95],v[128:131],a[212:215]\nM4 a[216:219],v[96:99],v[128:131],a[216:219]\nM4 a[220:223],v[100:103],v[128:131],a[220:223]\n"
"BL v155,s[40:43],s65 offen offset:3072 sc1 lds\nM4 a[224:227],v[72:75],v[132:135],a[224:227]\nM4 a[228:231],v[76:79],v[132:135],a[228:231]\nM4 a[232:235],v[80:83],v[132:135],a[232:235]\nM4 a[236:239],v[84:87],v[132:135],a[236:239]\nM4 a[240:243],v[88:91],v[132:135],a[240:243]\nM4 a[244:247],v[92:95],v[132:135],a[244:247]\nM4 a[248:251],v[96:99],v[132:135],a[248:251]\n"
"M4 a[252:255],v[100:103],v[132:135],a[252:255]\nSW vmcnt(24) lgkmcnt(0)\ns_barrier\nM4 a[0:3],v[8:11],v[40:43],a[0:3]\nDT v[104:105],v136 offset:16384\nM4 a[4:7],v[12:15],v[40:43],a[4:7]\nDT v[106:107],v136 offset:17408\nSA m0,s69,0\n"
"buffer_load_dword v242,v250,s[48:51],s76 offen\nM4 a[8:11],v[16:19],v[40:43],a[8:11]\nM4 a[12:15],v[20:23],v[40:43],a[12:15]\nDR v[72:75],v145 offset:0\nM4 a[16:19],v[24:27],v[40:43],a[16:19]\nM4 a[20:23],v[28:31],v[40:43],a[20:23]\nDR v[76:79],v145 offset:2048\nBL v148,s[36:39],s64 offen offset:0 lds\n"
"M4 a[24:27],v[32:35],v[40:43],a[24:27]\nM4 a[28:31],v[36:39],v[40:43],a[28:31]\nDR v[80:83],v145 offset:4096\nM4 a[32:35],v[8:11],v[44:47],a[32:35]\nM4 a[36:39],v[12:15],v[44:47],a[36:39]\nDR v[84:87],v145 offset:6144\nM4 a[40:43],v[16:19],v[44:47],a[40:43]\nBL v149,s[36:39],s64 offen offset:1024 lds\n"
"M4 a[44:47],v[20:23],v[44:47],a[44:47]\nDR v[88:91],v145 offset:8192\nM4 a[48:51],v[24:27],v[44:47],a[48:51]\nM4 a[52:55],v[28:31],v[44:47],a[52:55]\nDR v[92:95],v145 offset:10240\nM4 a[56:59],v[32:35],v[44:47],a[56:59]\nBL v150,s[36:39],s64 offen offset:2048 lds\nM4 a[60:63],v[36:39],v[44:47],a[60:63]\n"
"DR v[96:99],v145 offset:12288\nM4 a[64:67],v[8:11],v[48:51],a[64:67]\nbuffer_load_dword v243,v251,s[52:55],s77 offen\nM4 a[68:71],v[12:15],v[48:51],a[68:71]\nDR v[100:103],v145 offset:14336\nM4 a[72:75],v[16:19],v[48:51],a[72:75]\nM4 a[76:79],v[20:23],v[48:51],a[76:79]\nDT v[108:109],v137 offset:16384\n"
"BL v151,s[36:39],s64 offen offset:3072 lds\nM4 a[80:83],v[24:27],v[48:51],a[80:83]\nM4 a[84:87],v[28:31],v[48:51],a[84:87]\nDT v[110:111],v137 offset:17408\nM4 a[88:91],v[32:35],v[48:51],a[88:91]\nM4 a[92:95],v[36:39],v[48:51],a[92:95]\nDT v[112:113],v138 offset:16384\nM4 a[96:99],v[8:11],v[52:55],a[96:99]\n"
"DT v[114:115],v138 offset:17408\nSA s64,s64,262144\nM4 a[100:103],v[12:15],v[52:55],a[100:103]\nM4 a[104:107],v[16:19],v[52:55],a[104:107]\nDT v[116:117],v139 offset:16384\nM4 a[108:111],v[20:23],v[52:55],a[108:111]\nM4 a[112:115],v[24:27],v[52:55],a[112:115]\nDT v[118:119],v139 offset:17408\n"
"SA m0,s70,4096\nM4 a[116:119],v[28:31],v[52:55],a[116:119]\nM4 a[120:123],v[32:35],v[52:55],a[120:123]\nDT v[120:121],v140 offset:16384\nM4 a[124:127],v[36:39],v[52:55],a[124:127]\nM4 a[128:131],v[8:11],v[56:59],a[128:131]\nDT v[122:123],v140 offset:17408\nSA s76,s76,524288\n"
"M4 a[132:135],v[12:15],v[56:59],a[132:135]\nSA s72,s65,32768\nM4 a[136:139],v[16:19],v[56:59],a[136:139]\nDT v[124:125],v141 offset:16384\nM4 a[140:143],v[20:23],v[56:59],a[140:143]\nM4 a[144:147],v[24:27],v[56:59],a[144:147]\nDT v[126:127],v141 offset:17408\nM4 a[148:151],v[28:31],v[56:59],a[148:151]\n"
"BL v152,s[40:43],s72 offen offset:0 sc1 lds\nM4 a[152:155],v[32:35],v[56:59],a[152:155]\nDT v[128:129],v142 offset:16384\nM4 a[156:159],v[36:39],v[56:59],a[156:159]\nM4 a[160:163],v[8:11],v[60:63],a[160:163]\nDT v[130:131],v142 offset:17408\nM4 a[164:167],v[12:15],v[60:63],a[164:167]\nM4 a[168:171],v[16:19],v[60:63],a[168:171]\n"
"DT v[132:133],v143 offset:16384\nBL v153,s[40:43],s72 offen offset:1024 sc1 lds\nM4 a[172:175],v[20:23],v[60:63],a[172:175]\nM4 a[176:179],v[24:27],v[60:63],a[176:179]\nDT v[134:135],v143 offset:17408\nM4 a[180:183],v[28:31],v[60:63],a[180:183]\nM4 a[184:187],v[32:35],v[60:63],a[184:187]\nM4 a[188:191],v[36:39],v[60:63],a[188:191]\n"
"BL v154,s[40:43],s72 offen offset:2048 sc1 lds\nSA s77,s77,221184\nM4 a[192:195],v[8:11],v[64:67],a[192:195]\nM4 a[196:199],v[12:15],v[64:67],a[196:199]\nM4 a[200:203],v[16:19],v[64:67],a[200:203]\nM4 a[204:207],v[20:23],v[64:67],a[204:207]\nBL v155,s[40:43],s72 offen offset:3072 sc1 lds\nM4 a[208:211],v[24:27],v[64:67],a[208:211]\n"
"M4 a[212:215],v[28:31],v[64:67],a[212:215]\nM4 a[216:219],v[32:35],v[64:67],a[216:219]\nM4 a[220:223],v[36:39],v[64:67],a[220:223]\nM4 a[224:227],v[8:11],v[68:71],a[224:227]\nSA s65,s65,128\nM4 a[228:231],v[12:15],v[68:71],a[228:231]\nM4 a[232:235],v[16:19],v[68:71],a[232:235]\nM4 a[236:239],v[20:23],v[68:71],a[236:239]\n"
"M4 a[240:243],v[24:27],v[68:71],a[240:243]\nM4 a[244:247],v[28:31],v[68:71],a[244:247]\nM4 a[248:251],v[32:35],v[68:71],a[248:251]\nM4 a[252:255],v[36:39],v[68:71],a[252:255]\nSW vmcnt(20) lgkmcnt(0)\ns_barrier\nM4 a[0:3],v[72:75],v[104:107],a[0:3]\nVU v144,65536,v146\n"
"M4 a[4:7],v[76:79],v[104:107],a[4:7]\nVU v145,65536,v147\nbuffer_load_dword v244,v250,s[48:51],s76 offen\nM4 a[8:11],v[80:83],v[104:107],a[8:11]\nDT v[40:41],v136 offset:32768\nM4 a[12:15],v[84:87],v[104:107],a[12:15]\nDT v[42:43],v136 offset:33792\nSA m0,s69,16384\n"
"M4 a[16:19],v[88:91],v[104:107],a[16:19]\nM4 a[20:23],v[92:95],v[104:107],a[20:23]\nDR v[8:11],v144 offset:0\nM4 a[24:27],v[96:99],v[104:107],a[24:27]\nM4 a[28:31],v[100:103],v[104:107],a[28:31]\nDR v[12:15],v144 offset:2048\nM4 a[32:35],v[72:75],v[108:111],a[32:35]\nBL v148,s[36:39],s64 offen offset:0 lds\n"
"M4 a[36:39],v[76:79],v[108:111],a[36:39]\nDR v[16:19],v144 offset:4096\nM4 a[40:43],v[80:83],v[108:111],a[40:43]\nM4 a[44:47],v[84:87],v[108:111],a[44:47]\nDR v[20:23],v144 offset:6144\nM4 a[48:51],v[88:91],v[108:111],a[48:51]\nM4 a[52:55],v[92:95],v[108:111],a[52:55]\nDR v[24:27],v144 offset:8192\n"
"BL v149,s[36:39],s64 offen offset:1024 lds\nM4 a[56:59],v[96:99],v[108:111],a[56:59]\nDR v[28:31],v144 offset:10240\nM4 a[60:63],v[100:103],v[108:111],a[60:63]\nM4 a[64:67],v[72:75],v[112:115],a[64:67]\nDR v[32:35],v144 offset:12288\nbuffer_load_dword v245,v251,s[52:55],s77 offen\nM4 a[68:71],v[76:79],v[112:115],a[68:71]\n"
"M4 a[72:75],v[80:83],v[112:115],a[72:75]\nDR v[36:39],v144 offset:14336\nBL v150,s[36:39],s64 offen offset:2048 lds\nM4 a[76:79],v[84:87],v[112:115],a[76:79]\nM4 a[80:83],v[88:91],v[112:115],a[80:83]\nDT v[44:45],v137 offset:32768\nM4 a[84:87],v[92:95],v[112:115],a[84:87]\nM4 a[88:91],v[96:99],v[112:115],a[88:91]\n"
"DT v[46:47],v137 offset:33792\nM4 a[92:95],v[100:103],v[112:115],a[92:95]\nM4 a[96:99],v[72:75],v[116:119],a[96:99]\nDT v[48:49],v138 offset:32768\nBL v151,s[36:39],s64 offen offset:3072 lds\nM4 a[100:103],v[76:79],v[116:119],a[100:103]\nDT v[50:51],v138 offset:33792\nM4 a[104:107],v[80:83],v[116:119],a[104:107]\n"
"M4 a[108:111],v[84:87],v[116:119],a[108:111]\nDT v[52:53],v139 offset:32768\nM4 a[112:115],v[88:91],v[116:119],a[112:115]\nM4 a[116:119],v[92:95],v[116:119],a[116:119]\nDT v[54:55],v139 offset:33792\nSA s64,s64,262144\nM4 a[120:123],v[96:99],v[116:119],a[120:123]\nM4 a[124:127],v[100:103],v[116:119],a[124:127]\n"
"DT v[56:57],v140 offset:32768\nM4 a[128:131],v[72:75],v[120:123],a[128:131]\nSA s76,s76,524288\nM4 a[132:135],v[76:79],v[120:123],a[132:135]\nDT v[58:59],v140 offset:33792\nM4 a[136:139],v[80:83],v[120:123],a[136:139]\nSA m0,s70,32768\nM4 a[140:143],v[84:87],v[120:123],a[140:143]\n"
"DT v[60:61],v141 offset:32768\nM4 a[144:147],v[88:91],v[120:123],a[144:147]\nDT v[62:63],v141 offset:33792\nM4 a[148:151],v[92:95],v[120:123],a[148:151]\nM4 a[152:155],v[96:99],v[120:123],a[152:155]\nDT v[64:65],v142 offset:32768\nM4 a[156:159],v[100:103],v[120:123],a[156:159]\nBL v152,s[40:43],s65 offen offset:0 sc1 lds\n"
"M4 a[160:163],v[72:75],v[124:127],a[160:163]\nDT v[66:67],v142 offset:33792\nM4 a[164:167],v[76:79],v[124:127],a[164:167]\nM4 a[168:171],v[80:83],v[124:127],a[168:171]\nDT v[68:69],v143 offset:32768\nM4 a[172:175],v[84:87],v[124:127],a[172:175]\nM4 a[176:179],v[88:91],v[124:127],a[176:179]\nDT v[70:71],v143 offset:33792\n"
"M4 a[180:183],v[92:95],v[124:127],a[180:183]\nBL v153,s[40:43],s65 offen offset:1024 sc1 lds\nM4 a[184:187],v[96:99],v[124:127],a[184:187]\nM4 a[188:191],v[100:103],v[124:127],a[188:191]\nSA s77,s77,221184\nM4 a[192:195],v[72:75],v[128:131],a[192:195]\nM4 a[196:199],v[76:79],v[128:131],a[196:199]\nM4 a[200:203],v[80:83],v[128:131],a[200:203]\n"
"BL v154,s[40:43],s65 offen offset:2048 sc1 lds\nM4 a[204:207],v[84:87],v[128:131],a[204:207]\nM4 a[208:211],v[88:91],v[128:131],a[208:211]\nM4 a[212:215],v[92:95],v[128:131],a[212:215]\nM4 a[216:219],v[96:99],v[128:131],a[216:219]\nM4 a[220:223],v[100:103],v[128:131],a[220:223]\nBL v155,s[40:43],s65 offen offset:3072 sc1 lds\nM4 a[224:227],v[72:75],v[132:135],a[224:227]\n"
"M4 a[228:231],v[76:79],v[132:135],a[228:231]\nM4 a[232:235],v[80:83],v[132:135],a[232:235]\nM4 a[236:239],v[84:87],v[132:135],a[236:239]\nM4 a[240:243],v[88:91],v[132:135],a[240:243]\nM4 a[244:247],v[92:95],v[132:135],a[244:247]\nM4 a[248:251],v[96:99],v[132:135],a[248:251]\nM4 a[252:255],v[100:103],v[132:135],a[252:255]\nSW vmcnt(24) lgkmcnt(0)\n"
"s_barrier\ns_sub_u32 s71,15,s63\ns_sub_u32 s71,s71,s63\ns_lshl_b32 s71,s71,7\nv_bfe_u32 v170,v200,4,1\nv_mul_lo_u32 v170,v170,s71\nVN v171,15,v200\nv_lshl_add_u32 v170,v171,3,v170\n"
"s_lshl_b32 s71,s63,7\nSA s71,s71,63967232\nVU v170,s71,v170\nv_mov_b32 v171,s93\nv_add_co_u32 v170,vcc,s92,v170\ns_nop 1\nv_addc_co_u32 v171,vcc,0,v171,vcc\nglobal_load_dwordx2 v[172:173],v[170:171],off sc1\n"
"M4 a[0:3],v[8:11],v[40:43],a[0:3]\nDT v[104:105],v136 offset:49152\nM4 a[4:7],v[12:15],v[40:43],a[4:7]\nDT v[106:107],v136 offset:50176\nSA m0,s69,32768\nbuffer_load_dword v242,v250,s[48:51],s76 offen\nM4 a[8:11],v[16:19],v[40:43],a[8:11]\nM4 a[12:15],v[20:23],v[40:43],a[12:15]\n"
"DR v[72:75],v145 offset:0\nM4 a[16:19],v[24:27],v[40:43],a[16:19]\nM4 a[20:23],v[28:31],v[40:43],a[20:23]\nDR v[76:79],v145 offset:2048\nBL v148,s[36:39],s64 offen offset:0 lds\nM4 a[24:27],v[32:35],v[40:43],a[24:27]\nM4 a[28:31],v[36:39],v[40:43],a[28:31]\nDR v[80:83],v145 offset:4096\n"
"M4 a[32:35],v[8:11],v[44:47],a[32:35]\nM4 a[36:39],v[12:15],v[44:47],a[36:39]\nDR v[84:87],v145 offset:6144\nM4 a[40:43],v[16:19],v[44:47],a[40:43]\nBL v149,s[36:39],s64 offen offset:1024 lds\nM4 a[44:47],v[20:23],v[44:47],a[44:47]\nDR v[88:91],v145 offset:8192\nM4 a[48:51],v[24:27],v[44:47],a[48:51]\n"
"M4 a[52:55],v[28:31],v[44:47],a[52:55]\nDR v[92:95],v145 offset:10240\nM4 a[56:59],v[32:35],v[44:47],a[56:59]\nBL v150,s[36:39],s64 offen offset:2048 lds\nM4 a[60:63],v[36:39],v[44:47],a[60:63]\nDR v[96:99],v145 offset:12288\nM4 a[64:67],v[8:11],v[48:51],a[64:67]\nbuffer_load_dword v243,v251,s[52:55],s77 offen\n"
"M4 a[68:71],v[12:15],v[48:51],a[68:71]\nDR v[100:103],v145 offset:14336\nM4 a[72:75],v[16:19],v[48:51],a[72:75]\nM4 a[76:79],v[20:23],v[48:51],a[76:79]\nDT v[108:109],v137 offset:49152\nBL v151,s[36:39],s64 offen offset:3072 lds\nM4 a[80:83],v[24:27],v[48:51],a[80:83]\nM4 a[84:87],v[28:31],v[48:51],a[84:87]\n"
"DT v[110:111],v137 offset:50176\nM4 a[88:91],v[32:35],v[48:51],a[88:91]\nM4 a[92:95],v[36:39],v[48:51],a[92:95]\nDT v[112:113],v138 offset:49152\nM4 a[96:99],v[8:11],v[52:55],a[96:99]\nDT v[114:115],v138 offset:50176\nSA s64,s64,262144\nM4 a[100:103],v[12:15],v[52:55],a[100:103]\n"
"M4 a[104:107],v[16:19],v[52:55],a[104:107]\nDT v[116:117],v139 offset:49152\nM4 a[108:111],v[20:23],v[52:55],a[108:111]\nM4 a[112:115],v[24:27],v[52:55],a[112:115]\nDT v[118:119],v139 offset:50176\nSA m0,s70,36864\nM4 a[116:119],v[28:31],v[52:55],a[116:119]\nM4 a[120:123],v[32:35],v[52:55],a[120:123]\n"
"DT v[120:121],v140 offset:49152\nM4 a[124:127],v[36:39],v[52:55],a[124:127]\nM4 a[128:131],v[8:11],v[56:59],a[128:131]\nDT v[122:123],v140 offset:50176\nSA s76,s76,524288\nM4 a[132:135],v[12:15],v[56:59],a[132:135]\nSA s72,s65,32768\nM4 a[136:139],v[16:19],v[56:59],a[136:139]\n"
"DT v[124:125],v141 offset:49152\nM4 a[140:143],v[20:23],v[56:59],a[140:143]\nM4 a[144:147],v[24:27],v[56:59],a[144:147]\nDT v[126:127],v141 offset:50176\nM4 a[148:151],v[28:31],v[56:59],a[148:151]\nBL v152,s[40:43],s72 offen offset:0 sc1 lds\nM4 a[152:155],v[32:35],v[56:59],a[152:155]\nDT v[128:129],v142 offset:49152\n"
"M4 a[156:159],v[36:39],v[56:59],a[156:159]\nM4 a[160:163],v[8:11],v[60:63],a[160:163]\nDT v[130:131],v142 offset:50176\nM4 a[164:167],v[12:15],v[60:63],a[164:167]\nM4 a[168:171],v[16:19],v[60:63],a[168:171]\nDT v[132:133],v143 offset:49152\nBL v153,s[40:43],s72 offen offset:1024 sc1 lds\nM4 a[172:175],v[20:23],v[60:63],a[172:175]\n"
"M4 a[176:179],v[24:27],v[60:63],a[176:179]\nDT v[134:135],v143 offset:50176\nM4 a[180:183],v[28:31],v[60:63],a[180:183]\nM4 a[184:187],v[32:35],v[60:63],a[184:187]\nM4 a[188:191],v[36:39],v[60:63],a[188:191]\nBL v154,s[40:43],s72 offen offset:2048 sc1 lds\nSA s77,s77,221184\nM4 a[192:195],v[8:11],v[64:67],a[192:195]\n"
"M4 a[196:199],v[12:15],v[64:67],a[196:199]\nM4 a[200:203],v[16:19],v[64:67],a[200:203]\nM4 a[204:207],v[20:23],v[64:67],a[204:207]\nBL v155,s[40:43],s72 offen offset:3072 sc1 lds\nM4 a[208:211],v[24:27],v[64:67],a[208:211]\nM4 a[212:215],v[28:31],v[64:67],a[212:215]\nM4 a[216:219],v[32:35],v[64:67],a[216:219]\nM4 a[220:223],v[36:39],v[64:67],a[220:223]\n"
"M4 a[224:227],v[8:11],v[68:71],a[224:227]\nSA s65,s65,128\nM4 a[228:231],v[12:15],v[68:71],a[228:231]\nM4 a[232:235],v[16:19],v[68:71],a[232:235]\nM4 a[236:239],v[20:23],v[68:71],a[236:239]\nM4 a[240:243],v[24:27],v[68:71],a[240:243]\nM4 a[244:247],v[28:31],v[68:71],a[244:247]\nM4 a[248:251],v[32:35],v[68:71],a[248:251]\n"
"M4 a[252:255],v[36:39],v[68:71],a[252:255]\nSW vmcnt(21) lgkmcnt(0)\ns_barrier\nSW vmcnt(10)\nSM s71,0\nP1_lwV:\nv_xor_b32 v172,s90,v172\nv_xor_b32 v173,s91,v173\n"
"v_or_b32 v172,v172,v173\nv_cmp_ne_u32 vcc,0,v172\ns_nop 4\ns_cbranch_vccz P1_lwV_d\nSA s71,s71,1\ns_cmp_ge_u32 s71,200000\ns_cbranch_scc1 P1_lwV_d\ns_sleep 2\n"
"global_load_dwordx2 v[172:173],v[170:171],off sc1\nSW vmcnt(0)\ns_branch P1_lwV\nP1_lwV_d:\nM4 a[0:3],v[72:75],v[104:107],a[0:3]\nVU v144,0,v146\nM4 a[4:7],v[76:79],v[104:107],a[4:7]\nVU v145,0,v147\n"
"M4 a[8:11],v[80:83],v[104:107],a[8:11]\nDT v[40:41],v136 offset:0\nM4 a[12:15],v[84:87],v[104:107],a[12:15]\nDT v[42:43],v136 offset:1024\nSA m0,s69,49152\nM4 a[16:19],v[88:91],v[104:107],a[16:19]\nM4 a[20:23],v[92:95],v[104:107],a[20:23]\nDR v[8:11],v144 offset:0\n"
"M4 a[24:27],v[96:99],v[104:107],a[24:27]\nM4 a[28:31],v[100:103],v[104:107],a[28:31]\nDR v[12:15],v144 offset:2048\nM4 a[32:35],v[72:75],v[108:111],a[32:35]\nBL v148,s[36:39],s64 offen offset:0 lds\nM4 a[36:39],v[76:79],v[108:111],a[36:39]\nDR v[16:19],v144 offset:4096\nM4 a[40:43],v[80:83],v[108:111],a[40:43]\n"
"M4 a[44:47],v[84:87],v[108:111],a[44:47]\nDR v[20:23],v144 offset:6144\nM4 a[48:51],v[88:91],v[108:111],a[48:51]\nM4 a[52:55],v[92:95],v[108:111],a[52:55]\nDR v[24:27],v144 offset:8192\nBL v149,s[36:39],s64 offen offset:1024 lds\nM4 a[56:59],v[96:99],v[108:111],a[56:59]\nDR v[28:31],v144 offset:10240\n"
"M4 a[60:63],v[100:103],v[108:111],a[60:63]\nM4 a[64:67],v[72:75],v[112:115],a[64:67]\nDR v[32:35],v144 offset:12288\nM4 a[68:71],v[76:79],v[112:115],a[68:71]\nM4 a[72:75],v[80:83],v[112:115],a[72:75]\nDR v[36:39],v144 offset:14336\nBL v150,s[36:39],s64 offen offset:2048 lds\nM4 a[76:79],v[84:87],v[112:115],a[76:79]\n"
"M4 a[80:83],v[88:91],v[112:115],a[80:83]\nDT v[44:45],v137 offset:0\nM4 a[84:87],v[92:95],v[112:115],a[84:87]\nM4 a[88:91],v[96:99],v[112:115],a[88:91]\nDT v[46:47],v137 offset:1024\nM4 a[92:95],v[100:103],v[112:115],a[92:95]\nM4 a[96:99],v[72:75],v[116:119],a[96:99]\nDT v[48:49],v138 offset:0\n"
"BL v151,s[36:39],s64 offen offset:3072 lds\nM4 a[100:103],v[76:79],v[116:119],a[100:103]\nDT v[50:51],v138 offset:1024\nM4 a[104:107],v[80:83],v[116:119],a[104:107]\nM4 a[108:111],v[84:87],v[116:119],a[108:111]\nDT v[52:53],v139 offset:0\nM4 a[112:115],v[88:91],v[116:119],a[112:115]\nM4 a[116:119],v[92:95],v[116:119],a[116:119]\n"
"DT v[54:55],v139 offset:1024\nSA s64,s64,262144\nM4 a[120:123],v[96:99],v[116:119],a[120:123]\nM4 a[124:127],v[100:103],v[116:119],a[124:127]\nDT v[56:57],v140 offset:0\nM4 a[128:131],v[72:75],v[120:123],a[128:131]\nM4 a[132:135],v[76:79],v[120:123],a[132:135]\nDT v[58:59],v140 offset:1024\n"
"M4 a[136:139],v[80:83],v[120:123],a[136:139]\nSA m0,s70,65536\nM4 a[140:143],v[84:87],v[120:123],a[140:143]\nDT v[60:61],v141 offset:0\nM4 a[144:147],v[88:91],v[120:123],a[144:147]\nDT v[62:63],v141 offset:1024\nM4 a[148:151],v[92:95],v[120:123],a[148:151]\nM4 a[152:155],v[96:99],v[120:123],a[152:155]\n"
"DT v[64:65],v142 offset:0\nM4 a[156:159],v[100:103],v[120:123],a[156:159]\nBL v160,s[56:59],s68 offen offset:0 sc1 lds\nM4 a[160:163],v[72:75],v[124:127],a[160:163]\nDT v[66:67],v142 offset:1024\nM4 a[164:167],v[76:79],v[124:127],a[164:167]\nM4 a[168:171],v[80:83],v[124:127],a[168:171]\nDT v[68:69],v143 offset:0\n"
"M4 a[172:175],v[84:87],v[124:127],a[172:175]\nM4 a[176:179],v[88:91],v[124:127],a[176:179]\nDT v[70:71],v143 offset:1024\nM4 a[180:183],v[92:95],v[124:127],a[180:183]\nBL v161,s[56:59],s68 offen offset:1024 sc1 lds\nM4 a[184:187],v[96:99],v[124:127],a[184:187]\nM4 a[188:191],v[100:103],v[124:127],a[188:191]\nM4 a[192:195],v[72:75],v[128:131],a[192:195]\n"
"M4 a[196:199],v[76:79],v[128:131],a[196:199]\nM4 a[200:203],v[80:83],v[128:131],a[200:203]\nBL v162,s[56:59],s68 offen offset:2048 sc1 lds\nM4 a[204:207],v[84:87],v[128:131],a[204:207]\nM4 a[208:211],v[88:91],v[128:131],a[208:211]\nM4 a[212:215],v[92:95],v[128:131],a[212:215]\nM4 a[216:219],v[96:99],v[128:131],a[216:219]\nM4 a[220:223],v[100:103],v[128:131],a[220:223]\n"
"BL v163,s[56:59],s68 offen offset:3072 sc1 lds\nM4 a[224:227],v[72:75],v[132:135],a[224:227]\nM4 a[228:231],v[76:79],v[132:135],a[228:231]\nM4 a[232:235],v[80:83],v[132:135],a[232:235]\nM4 a[236:239],v[84:87],v[132:135],a[236:239]\nM4 a[240:243],v[88:91],v[132:135],a[240:243]\nM4 a[244:247],v[92:95],v[132:135],a[244:247]\nM4 a[248:251],v[96:99],v[132:135],a[248:251]\n"
"M4 a[252:255],v[100:103],v[132:135],a[252:255]\nSW vmcnt(23) lgkmcnt(0)\ns_barrier\nM4 a[0:3],v[8:11],v[40:43],a[0:3]\nDT v[104:105],v136 offset:16384\nM4 a[4:7],v[12:15],v[40:43],a[4:7]\nDT v[106:107],v136 offset:17408\nSA m0,s81,0\n"
"M4 a[8:11],v[16:19],v[40:43],a[8:11]\nM4 a[12:15],v[20:23],v[40:43],a[12:15]\nDR v[72:75],v145 offset:0\nM4 a[16:19],v[24:27],v[40:43],a[16:19]\nM4 a[20:23],v[28:31],v[40:43],a[20:23]\nDR v[76:79],v145 offset:2048\nBL v156,s[48:51],s66 offen offset:0 lds\nM4 a[24:27],v[32:35],v[40:43],a[24:27]\n"
"M4 a[28:31],v[36:39],v[40:43],a[28:31]\nDR v[80:83],v145 offset:4096\nM4 a[32:35],v[8:11],v[44:47],a[32:35]\nM4 a[36:39],v[12:15],v[44:47],a[36:39]\nDR v[84:87],v145 offset:6144\nBL v157,s[48:51],s66 offen offset:1024 lds\nM4 a[40:43],v[16:19],v[44:47],a[40:43]\nM4 a[44:47],v[20:23],v[44:47],a[44:47]\n"
"DR v[88:91],v145 offset:8192\nM4 a[48:51],v[24:27],v[44:47],a[48:51]\nM4 a[52:55],v[28:31],v[44:47],a[52:55]\nDR v[92:95],v145 offset:10240\nSA m0,s82,0\nM4 a[56:59],v[32:35],v[44:47],a[56:59]\nM4 a[60:63],v[36:39],v[44:47],a[60:63]\nDR v[96:99],v145 offset:12288\n"
"M4 a[64:67],v[8:11],v[48:51],a[64:67]\nM4 a[68:71],v[12:15],v[48:51],a[68:71]\nDR v[100:103],v145 offset:14336\nM4 a[72:75],v[16:19],v[48:51],a[72:75]\nBL v158,s[52:55],s67 offen lds\nM4 a[76:79],v[20:23],v[48:51],a[76:79]\nDT v[108:109],v137 offset:16384\nM4 a[80:83],v[24:27],v[48:51],a[80:83]\n"
"M4 a[84:87],v[28:31],v[48:51],a[84:87]\nDT v[110:111],v137 offset:17408\nM4 a[88:91],v[32:35],v[48:51],a[88:91]\nSA s66,s66,262144\nM4 a[92:95],v[36:39],v[48:51],a[92:95]\nDT v[112:113],v138 offset:16384\nM4 a[96:99],v[8:11],v[52:55],a[96:99]\nDT v[114:115],v138 offset:17408\n"
"M4 a[100:103],v[12:15],v[52:55],a[100:103]\nM4 a[104:107],v[16:19],v[52:55],a[104:107]\nDT v[116:117],v139 offset:16384\nSA s67,s67,110592\nM4 a[108:111],v[20:23],v[52:55],a[108:111]\nM4 a[112:115],v[24:27],v[52:55],a[112:115]\nDT v[118:119],v139 offset:17408\nM4 a[116:119],v[28:31],v[52:55],a[116:119]\n"
"M4 a[120:123],v[32:35],v[52:55],a[120:123]\nDT v[120:121],v140 offset:16384\nM4 a[124:127],v[36:39],v[52:55],a[124:127]\nSA m0,s70,69632\nM4 a[128:131],v[8:11],v[56:59],a[128:131]\nDT v[122:123],v140 offset:17408\nM4 a[132:135],v[12:15],v[56:59],a[132:135]\nM4 a[136:139],v[16:19],v[56:59],a[136:139]\n"
"DT v[124:125],v141 offset:16384\nM4 a[140:143],v[20:23],v[56:59],a[140:143]\nSA s72,s68,40960\nM4 a[144:147],v[24:27],v[56:59],a[144:147]\nDT v[126:127],v141 offset:17408\nM4 a[148:151],v[28:31],v[56:59],a[148:151]\nM4 a[152:155],v[32:35],v[56:59],a[152:155]\nDT v[128:129],v142 offset:16384\n"
"M4 a[156:159],v[36:39],v[56:59],a[156:159]\nBL v160,s[56:59],s72 offen offset:0 sc1 lds\nM4 a[160:163],v[8:11],v[60:63],a[160:163]\nDT v[130:131],v142 offset:17408\nM4 a[164:167],v[12:15],v[60:63],a[164:167]\nM4 a[168:171],v[16:19],v[60:63],a[168:171]\nDT v[132:133],v143 offset:16384\nM4 a[172:175],v[20:23],v[60:63],a[172:175]\n"
"BL v161,s[56:59],s72 offen offset:1024 sc1 lds\nM4 a[176:179],v[24:27],v[60:63],a[176:179]\nDT v[134:135],v143 offset:17408\nM4 a[180:183],v[28:31],v[60:63],a[180:183]\nM4 a[184:187],v[32:35],v[60:63],a[184:187]\nM4 a[188:191],v[36:39],v[60:63],a[188:191]\nM4 a[192:195],v[8:11],v[64:67],a[192:195]\nBL v162,s[56:59],s72 offen offset:2048 sc1 lds\n"
"M4 a[196:199],v[12:15],v[64:67],a[196:199]\nM4 a[200:203],v[16:19],v[64:67],a[200:203]\nM4 a[204:207],v[20:23],v[64:67],a[204:207]\nM4 a[208:211],v[24:27],v[64:67],a[208:211]\nBL v163,s[56:59],s72 offen offset:3072 sc1 lds\nM4 a[212:215],v[28:31],v[64:67],a[212:215]\nM4 a[216:219],v[32:35],v[64:67],a[216:219]\nM4 a[220:223],v[36:39],v[64:67],a[220:223]\n"
"M4 a[224:227],v[8:11],v[68:71],a[224:227]\nSA s68,s68,128\nM4 a[228:231],v[12:15],v[68:71],a[228:231]\nM4 a[232:235],v[16:19],v[68:71],a[232:235]\nM4 a[236:239],v[20:23],v[68:71],a[236:239]\nM4 a[240:243],v[24:27],v[68:71],a[240:243]\nM4 a[244:247],v[28:31],v[68:71],a[244:247]\nM4 a[248:251],v[32:35],v[68:71],a[248:251]\n"
"M4 a[252:255],v[36:39],v[68:71],a[252:255]\nSW vmcnt(15) lgkmcnt(0)\ns_barrier\nM4 a[0:3],v[72:75],v[104:107],a[0:3]\nVU v144,32768,v146\nM4 a[4:7],v[76:79],v[104:107],a[4:7]\nVU v145,32768,v147\nM4 a[8:11],v[80:83],v[104:107],a[8:11]\n"
"DT v[40:41],v136 offset:32768\nM4 a[12:15],v[84:87],v[104:107],a[12:15]\nDT v[42:43],v136 offset:33792\nSA m0,s81,16384\nM4 a[16:19],v[88:91],v[104:107],a[16:19]\nM4 a[20:23],v[92:95],v[104:107],a[20:23]\nDR v[8:11],v144 offset:0\nM4 a[24:27],v[96:99],v[104:107],a[24:27]\n"
"M4 a[28:31],v[100:103],v[104:107],a[28:31]\nDR v[12:15],v144 offset:2048\nBL v156,s[48:51],s66 offen offset:0 lds\nM4 a[32:35],v[72:75],v[108:111],a[32:35]\nM4 a[36:39],v[76:79],v[108:111],a[36:39]\nDR v[16:19],v144 offset:4096\nM4 a[40:43],v[80:83],v[108:111],a[40:43]\nM4 a[44:47],v[84:87],v[108:111],a[44:47]\n"
"DR v[20:23],v144 offset:6144\nM4 a[48:51],v[88:91],v[108:111],a[48:51]\nBL v157,s[48:51],s66 offen offset:1024 lds\nM4 a[52:55],v[92:95],v[108:111],a[52:55]\nDR v[24:27],v144 offset:8192\nM4 a[56:59],v[96:99],v[108:111],a[56:59]\nDR v[28:31],v144 offset:10240\nM4 a[60:63],v[100:103],v[108:111],a[60:63]\n"
"M4 a[64:67],v[72:75],v[112:115],a[64:67]\nDR v[32:35],v144 offset:12288\nM4 a[68:71],v[76:79],v[112:115],a[68:71]\nSA m0,s82,16384\nM4 a[72:75],v[80:83],v[112:115],a[72:75]\nDR v[36:39],v144 offset:14336\nM4 a[76:79],v[84:87],v[112:115],a[76:79]\nM4 a[80:83],v[88:91],v[112:115],a[80:83]\n"
"DT v[44:45],v137 offset:32768\nM4 a[84:87],v[92:95],v[112:115],a[84:87]\nM4 a[88:91],v[96:99],v[112:115],a[88:91]\nDT v[46:47],v137 offset:33792\nBL v158,s[52:55],s67 offen lds\nM4 a[92:95],v[100:103],v[112:115],a[92:95]\nM4 a[96:99],v[72:75],v[116:119],a[96:99]\nDT v[48:49],v138 offset:32768\n"
"M4 a[100:103],v[76:79],v[116:119],a[100:103]\nDT v[50:51],v138 offset:33792\nM4 a[104:107],v[80:83],v[116:119],a[104:107]\nM4 a[108:111],v[84:87],v[116:119],a[108:111]\nDT v[52:53],v139 offset:32768\nSA s66,s66,262144\nM4 a[112:115],v[88:91],v[116:119],a[112:115]\nM4 a[116:119],v[92:95],v[116:119],a[116:119]\n"
"DT v[54:55],v139 offset:33792\nM4 a[120:123],v[96:99],v[116:119],a[120:123]\nM4 a[124:127],v[100:103],v[116:119],a[124:127]\nDT v[56:57],v140 offset:32768\nM4 a[128:131],v[72:75],v[120:123],a[128:131]\nSA s67,s67,110592\nM4 a[132:135],v[76:79],v[120:123],a[132:135]\nDT v[58:59],v140 offset:33792\n"
"M4 a[136:139],v[80:83],v[120:123],a[136:139]\nM4 a[140:143],v[84:87],v[120:123],a[140:143]\nDT v[60:61],v141 offset:32768\nM4 a[144:147],v[88:91],v[120:123],a[144:147]\nDT v[62:63],v141 offset:33792\nSA m0,s70,0\nM4 a[148:151],v[92:95],v[120:123],a[148:151]\nM4 a[152:155],v[96:99],v[120:123],a[152:155]\n"
"DT v[64:65],v142 offset:32768\nM4 a[156:159],v[100:103],v[120:123],a[156:159]\nM4 a[160:163],v[72:75],v[124:127],a[160:163]\nDT v[66:67],v142 offset:33792\nM4 a[164:167],v[76:79],v[124:127],a[164:167]\nBL v160,s[56:59],s68 offen offset:0 sc1 lds\nM4 a[168:171],v[80:83],v[124:127],a[168:171]\nDT v[68:69],v143 offset:32768\n"
"M4 a[172:175],v[84:87],v[124:127],a[172:175]\nM4 a[176:179],v[88:91],v[124:127],a[176:179]\nDT v[70:71],v143 offset:33792\nM4 a[180:183],v[92:95],v[124:127],a[180:183]\nM4 a[184:187],v[96:99],v[124:127],a[184:187]\nBL v161,s[56:59],s68 offen offset:1024 sc1 lds\nM4 a[188:191],v[100:103],v[124:127],a[188:191]\nM4 a[192:195],v[72:75],v[128:131],a[192:195]\n"
"M4 a[196:199],v[76:79],v[128:131],a[196:199]\nM4 a[200:203],v[80:83],v[128:131],a[200:203]\nM4 a[204:207],v[84:87],v[128:131],a[204:207]\nBL v162,s[56:59],s68 offen offset:2048 sc1 lds\nM4 a[208:211],v[88:91],v[128:131],a[208:211]\nM4 a[212:215],v[92:95],v[128:131],a[212:215]\nM4 a[216:219],v[96:99],v[128:131],a[216:219]\nM4 a[220:223],v[100:103],v[128:131],a[220:223]\n"
"M4 a[224:227],v[72:75],v[132:135],a[224:227]\nBL v163,s[56:59],s68 offen offset:3072 sc1 lds\nM4 a[228:231],v[76:79],v[132:135],a[228:231]\nM4 a[232:235],v[80:83],v[132:135],a[232:235]\nM4 a[236:239],v[84:87],v[132:135],a[236:239]\nM4 a[240:243],v[88:91],v[132:135],a[240:243]\nM4 a[244:247],v[92:95],v[132:135],a[244:247]\nM4 a[248:251],v[96:99],v[132:135],a[248:251]\n"
"M4 a[252:255],v[100:103],v[132:135],a[252:255]\nSW vmcnt(18) lgkmcnt(0)\ns_barrier\nM4 a[0:3],v[8:11],v[40:43],a[0:3]\nDT v[104:105],v136 offset:49152\nM4 a[4:7],v[12:15],v[40:43],a[4:7]\nDT v[106:107],v136 offset:50176\nSA m0,s81,32768\n"
"M4 a[8:11],v[16:19],v[40:43],a[8:11]\nM4 a[12:15],v[20:23],v[40:43],a[12:15]\nDR v[72:75],v145 offset:0\nM4 a[16:19],v[24:27],v[40:43],a[16:19]\nM4 a[20:23],v[28:31],v[40:43],a[20:23]\nDR v[76:79],v145 offset:2048\nBL v156,s[48:51],s66 offen offset:0 lds\nM4 a[24:27],v[32:35],v[40:43],a[24:27]\n"
"M4 a[28:31],v[36:39],v[40:43],a[28:31]\nDR v[80:83],v145 offset:4096\nM4 a[32:35],v[8:11],v[44:47],a[32:35]\nM4 a[36:39],v[12:15],v[44:47],a[36:39]\nDR v[84:87],v145 offset:6144\nBL v157,s[48:51],s66 offen offset:1024 lds\nM4 a[40:43],v[16:19],v[44:47],a[40:43]\nM4 a[44:47],v[20:23],v[44:47],a[44:47]\n"
"DR v[88:91],v145 offset:8192\nM4 a[48:51],v[24:27],v[44:47],a[48:51]\nM4 a[52:55],v[28:31],v[44:47],a[52:55]\nDR v[92:95],v145 offset:10240\nSA m0,s82,32768\nM4 a[56:59],v[32:35],v[44:47],a[56:59]\nM4 a[60:63],v[36:39],v[44:47],a[60:63]\nDR v[96:99],v145 offset:12288\n"
"M4 a[64:67],v[8:11],v[48:51],a[64:67]\nM4 a[68:71],v[12:15],v[48:51],a[68:71]\nDR v[100:103],v145 offset:14336\nM4 a[72:75],v[16:19],v[48:51],a[72:75]\nBL v158,s[52:55],s67 offen lds\nM4 a[76:79],v[20:23],v[48:51],a[76:79]\nDT v[108:109],v137 offset:49152\nM4 a[80:83],v[24:27],v[48:51],a[80:83]\n"
"M4 a[84:87],v[28:31],v[48:51],a[84:87]\nDT v[110:111],v137 offset:50176\nM4 a[88:91],v[32:35],v[48:51],a[88:91]\nSA s66,s66,262144\nM4 a[92:95],v[36:39],v[48:51],a[92:95]\nDT v[112:113],v138 offset:49152\nM4 a[96:99],v[8:11],v[52:55],a[96:99]\nDT v[114:115],v138 offset:50176\n"
"M4 a[100:103],v[12:15],v[52:55],a[100:103]\nM4 a[104:107],v[16:19],v[52:55],a[104:107]\nDT v[116:117],v139 offset:49152\nSA s67,s67,110592\nM4 a[108:111],v[20:23],v[52:55],a[108:111]\nM4 a[112:115],v[24:27],v[52:55],a[112:115]\nDT v[118:119],v139 offset:50176\nM4 a[116:119],v[28:31],v[52:55],a[116:119]\n"
"M4 a[120:123],v[32:35],v[52:55],a[120:123]\nDT v[120:121],v140 offset:49152\nM4 a[124:127],v[36:39],v[52:55],a[124:127]\nSA m0,s70,4096\nM4 a[128:131],v[8:11],v[56:59],a[128:131]\nDT v[122:123],v140 offset:50176\nM4 a[132:135],v[12:15],v[56:59],a[132:135]\nM4 a[136:139],v[16:19],v[56:59],a[136:139]\n"
"DT v[124:125],v141 offset:49152\nM4 a[140:143],v[20:23],v[56:59],a[140:143]\nSA s72,s68,40960\nM4 a[144:147],v[24:27],v[56:59],a[144:147]\nDT v[126:127],v141 offset:50176\nM4 a[148:151],v[28:31],v[56:59],a[148:151]\nM4 a[152:155],v[32:35],v[56:59],a[152:155]\nDT v[128:129],v142 offset:49152\n"
"M4 a[156:159],v[36:39],v[56:59],a[156:159]\nBL v160,s[56:59],s72 offen offset:0 sc1 lds\nM4 a[160:163],v[8:11],v[60:63],a[160:163]\nDT v[130:131],v142 offset:50176\nM4 a[164:167],v[12:15],v[60:63],a[164:167]\nM4 a[168:171],v[16:19],v[60:63],a[168:171]\nDT v[132:133],v143 offset:49152\nM4 a[172:175],v[20:23],v[60:63],a[172:175]\n"
"BL v161,s[56:59],s72 offen offset:1024 sc1 lds\nM4 a[176:179],v[24:27],v[60:63],a[176:179]\nDT v[134:135],v143 offset:50176\nM4 a[180:183],v[28:31],v[60:63],a[180:183]\nM4 a[184:187],v[32:35],v[60:63],a[184:187]\nM4 a[188:191],v[36:39],v[60:63],a[188:191]\nM4 a[192:195],v[8:11],v[64:67],a[192:195]\nBL v162,s[56:59],s72 offen offset:2048 sc1 lds\n"
"M4 a[196:199],v[12:15],v[64:67],a[196:199]\nM4 a[200:203],v[16:19],v[64:67],a[200:203]\nM4 a[204:207],v[20:23],v[64:67],a[204:207]\nM4 a[208:211],v[24:27],v[64:67],a[208:211]\nBL v163,s[56:59],s72 offen offset:3072 sc1 lds\nM4 a[212:215],v[28:31],v[64:67],a[212:215]\nM4 a[216:219],v[32:35],v[64:67],a[216:219]\nM4 a[220:223],v[36:39],v[64:67],a[220:223]\n"
"M4 a[224:227],v[8:11],v[68:71],a[224:227]\nSA s68,s68,128\nM4 a[228:231],v[12:15],v[68:71],a[228:231]\nM4 a[232:235],v[16:19],v[68:71],a[232:235]\nM4 a[236:239],v[20:23],v[68:71],a[236:239]\nM4 a[240:243],v[24:27],v[68:71],a[240:243]\nM4 a[244:247],v[28:31],v[68:71],a[244:247]\nM4 a[248:251],v[32:35],v[68:71],a[248:251]\n"
"M4 a[252:255],v[36:39],v[68:71],a[252:255]\nSW vmcnt(14) lgkmcnt(0)\ns_barrier\nM4 a[0:3],v[72:75],v[104:107],a[0:3]\nVU v144,65536,v146\nM4 a[4:7],v[76:79],v[104:107],a[4:7]\nVU v145,65536,v147\nM4 a[8:11],v[80:83],v[104:107],a[8:11]\n"
"DT v[40:41],v164 offset:0\nM4 a[12:15],v[84:87],v[104:107],a[12:15]\nSA m0,s81,49152\nM4 a[16:19],v[88:91],v[104:107],a[16:19]\nDT v[42:43],v164 offset:1024\nM4 a[20:23],v[92:95],v[104:107],a[20:23]\nM4 a[24:27],v[96:99],v[104:107],a[24:27]\nDR v[8:11],v144 offset:0\n"
"M4 a[28:31],v[100:103],v[104:107],a[28:31]\nBL v156,s[48:51],s66 offen offset:0 lds\nM4 a[32:35],v[72:75],v[108:111],a[32:35]\nDR v[12:15],v144 offset:2048\nM4 a[36:39],v[76:79],v[108:111],a[36:39]\nM4 a[40:43],v[80:83],v[108:111],a[40:43]\nDR v[16:19],v144 offset:4096\nM4 a[44:47],v[84:87],v[108:111],a[44:47]\n"
"M4 a[48:51],v[88:91],v[108:111],a[48:51]\nBL v157,s[48:51],s66 offen offset:1024 lds\nM4 a[52:55],v[92:95],v[108:111],a[52:55]\nDR v[20:23],v144 offset:6144\nM4 a[56:59],v[96:99],v[108:111],a[56:59]\nM4 a[60:63],v[100:103],v[108:111],a[60:63]\nDR v[24:27],v144 offset:8192\nM4 a[64:67],v[72:75],v[112:115],a[64:67]\n"
"M4 a[68:71],v[76:79],v[112:115],a[68:71]\nDR v[28:31],v144 offset:10240\nSA m0,s82,49152\nM4 a[72:75],v[80:83],v[112:115],a[72:75]\nM4 a[76:79],v[84:87],v[112:115],a[76:79]\nDR v[32:35],v144 offset:12288\nM4 a[80:83],v[88:91],v[112:115],a[80:83]\nM4 a[84:87],v[92:95],v[112:115],a[84:87]\n"
"DR v[36:39],v144 offset:14336\nM4 a[88:91],v[96:99],v[112:115],a[88:91]\nBL v158,s[52:55],s67 offen lds\nM4 a[92:95],v[100:103],v[112:115],a[92:95]\nM4 a[96:99],v[72:75],v[116:119],a[96:99]\nDT v[44:45],v165 offset:0\nM4 a[100:103],v[76:79],v[116:119],a[100:103]\nM4 a[104:107],v[80:83],v[116:119],a[104:107]\n"
"DT v[46:47],v165 offset:1024\nM4 a[108:111],v[84:87],v[116:119],a[108:111]\nSA s66,s66,262144\nM4 a[112:115],v[88:91],v[116:119],a[112:115]\nDT v[48:49],v166 offset:0\nM4 a[116:119],v[92:95],v[116:119],a[116:119]\nM4 a[120:123],v[96:99],v[116:119],a[120:123]\nDT v[50:51],v166 offset:1024\n"
"M4 a[124:127],v[100:103],v[116:119],a[124:127]\nM4 a[128:131],v[72:75],v[120:123],a[128:131]\nDT v[52:53],v167 offset:0\nSA s67,s67,110592\nM4 a[132:135],v[76:79],v[120:123],a[132:135]\nM4 a[136:139],v[80:83],v[120:123],a[136:139]\nM4 a[140:143],v[84:87],v[120:123],a[140:143]\nDT v[54:55],v167 offset:1024\n"
"M4 a[144:147],v[88:91],v[120:123],a[144:147]\nSA m0,s70,32768\nM4 a[148:151],v[92:95],v[120:123],a[148:151]\nDT v[56:57],v168 offset:0\nM4 a[152:155],v[96:99],v[120:123],a[152:155]\nM4 a[156:159],v[100:103],v[120:123],a[156:159]\nDT v[58:59],v168 offset:512\nM4 a[160:163],v[72:75],v[124:127],a[160:163]\n"
"M4 a[164:167],v[76:79],v[124:127],a[164:167]\nDT v[60:61],v169 offset:0\nBL v160,s[56:59],s68 offen offset:0 sc1 lds\nM4 a[168:171],v[80:83],v[124:127],a[168:171]\nM4 a[172:175],v[84:87],v[124:127],a[172:175]\nDT v[62:63],v169 offset:512\nM4 a[176:179],v[88:91],v[124:127],a[176:179]\nM4 a[180:183],v[92:95],v[124:127],a[180:183]\n"
"M4 a[184:187],v[96:99],v[124:127],a[184:187]\nBL v161,s[56:59],s68 offen offset:1024 sc1 lds\nM4 a[188:191],v[100:103],v[124:127],a[188:191]\nM4 a[192:195],v[72:75],v[128:131],a[192:195]\nM4 a[196:199],v[76:79],v[128:131],a[196:199]\nM4 a[200:203],v[80:83],v[128:131],a[200:203]\nM4 a[204:207],v[84:87],v[128:131],a[204:207]\nBL v162,s[56:59],s68 offen offset:2048 sc1 lds\n"
"M4 a[208:211],v[88:91],v[128:131],a[208:211]\nM4 a[212:215],v[92:95],v[128:131],a[212:215]\nM4 a[216:219],v[96:99],v[128:131],a[216:219]\nM4 a[220:223],v[100:103],v[128:131],a[220:223]\nM4 a[224:227],v[72:75],v[132:135],a[224:227]\nBL v163,s[56:59],s68 offen offset:3072 sc1 lds\nM4 a[228:231],v[76:79],v[132:135],a[228:231]\nM4 a[232:235],v[80:83],v[132:135],a[232:235]\n"
"M4 a[236:239],v[84:87],v[132:135],a[236:239]\nM4 a[240:243],v[88:91],v[132:135],a[240:243]\nM4 a[244:247],v[92:95],v[132:135],a[244:247]\nM4 a[248:251],v[96:99],v[132:135],a[248:251]\nM4 a[252:255],v[100:103],v[132:135],a[252:255]\nSW vmcnt(18) lgkmcnt(0)\ns_barrier\ns_nop 3\n"
"v_lshrrev_b32 v170,4,v200\nVN v171,15,v200\nv_lshrrev_b32 v172,1,v170\nVL v201,10,v172\nVN v172,1,v170\nv_lshl_add_u32 v201,v172,9,v201\nv_lshl_add_u32 v201,v171,4,v201\nSM s71,23068672\n"
"SA s44,s92,s71\ns_addc_u32 s45,s93,0\ns_and_b32 s45,s45,0xffff\nSM s46,16777216\nSM s47,0x20000\nAR v88,a192\nAR v89,a193\nAR v90,a194\n"
"AR v91,a195\nAR v92,a196\nAR v93,a197\nAR v94,a198\nAR v95,a199\nCV v148,v88,v89\nCV v149,v90,v91\nCV v150,v92,v93\n"
"CV v151,v94,v95\nAR v96,a224\nAR v97,a225\nAR v98,a226\nAR v99,a227\nAR v100,a228\nAR v101,a229\nAR v102,a230\n"
"AR v103,a231\nCV v152,v96,v97\nCV v153,v98,v99\nCV v154,v100,v101\nCV v155,v102,v103\nAR v88,a200\nAR v89,a201\nAR v90,a202\n"
"AR v91,a203\nAR v92,a204\nAR v93,a205\nAR v94,a206\nAR v95,a207\nCV v196,v88,v89\nCV v197,v90,v91\nCV v198,v92,v93\n"
"CV v199,v94,v95\nSA m0,s81,0\nAR v96,a232\nAR v97,a233\nAR v98,a234\nAR v99,a235\nAR v100,a236\nAR v101,a237\n"
"AR v102,a238\nAR v103,a239\nCV v204,v96,v97\nCV v205,v98,v99\nCV v206,v100,v101\nCV v207,v102,v103\nv_mov_b32 v108,0x3f803f80\nAW a200,v108\n"
"AW a201,v108\nAW a202,v108\nAW a203,v108\nBL v156,s[48:51],s66 offen offset:0 lds\nAR v72,a0\nAR v73,a1\nAR v74,a2\nAR v75,a3\n"
"AR v76,a4\nAR v77,a5\nAR v78,a6\nAR v79,a7\nCV v64,v72,v73\nCV v65,v74,v75\nCV v66,v76,v77\nCV v67,v78,v79\n"
"M4 a[0:3],v[40:43],v[8:11],0\nM4 a[4:7],v[40:43],v[12:15],0\nBL v157,s[48:51],s66 offen offset:1024 lds\nAR v80,a32\nAR v81,a33\nAR v82,a34\nAR v83,a35\nAR v84,a36\n"
"AR v85,a37\nAR v86,a38\nAR v87,a39\nCV v68,v80,v81\nCV v69,v82,v83\nCV v70,v84,v85\nCV v71,v86,v87\nM4 a[32:35],v[44:47],v[8:11],0\n"
"M4 a[36:39],v[44:47],v[12:15],0\nSA m0,s82,0\nAR v88,a64\nAR v89,a65\nAR v90,a66\nAR v91,a67\nAR v92,a68\nAR v93,a69\n"
"AR v94,a70\nAR v95,a71\nCV v128,v88,v89\nCV v129,v90,v91\nCV v130,v92,v93\nCV v131,v94,v95\nM4 a[64:67],v[48:51],v[8:11],0\nM4 a[68:71],v[48:51],v[12:15],0\n"
"BL v158,s[52:55],s67 offen lds\nAR v96,a96\nAR v97,a97\nAR v98,a98\nAR v99,a99\nAR v100,a100\nAR v101,a101\nAR v102,a102\n"
"AR v103,a103\nCV v132,v96,v97\nCV v133,v98,v99\nCV v134,v100,v101\nCV v135,v102,v103\nM4 a[96:99],v[52:55],v[8:11],0\nM4 a[100:103],v[52:55],v[12:15],0\nSA s66,s66,262144\n"
"AR v72,a128\nAR v73,a129\nAR v74,a130\nAR v75,a131\nAR v76,a132\nAR v77,a133\nAR v78,a134\nAR v79,a135\n"
"CV v136,v72,v73\nCV v137,v74,v75\nCV v138,v76,v77\nCV v139,v78,v79\nM4 a[128:131],v[56:59],v[8:11],0\nM4 a[132:135],v[56:59],v[12:15],0\nSA s67,s67,110592\nAR v80,a160\n"
"AR v81,a161\nAR v82,a162\nAR v83,a163\nAR v84,a164\nAR v85,a165\nAR v86,a166\nAR v87,a167\nCV v140,v80,v81\n"
"CV v141,v82,v83\nCV v142,v84,v85\nCV v143,v86,v87\nM4 a[160:163],v[60:63],v[8:11],0\nM4 a[164:167],v[60:63],v[12:15],0\nSA m0,s70,36864\nAR v72,a8\nAR v73,a9\n"
"AR v74,a10\nAR v75,a11\nAR v76,a12\nAR v77,a13\nAR v78,a14\nAR v79,a15\nCV v172,v72,v73\nCV v173,v74,v75\n"
"CV v174,v76,v77\nCV v175,v78,v79\nM4 a[8:11],v[40:43],v[16:19],0\nM4 a[12:15],v[40:43],v[20:23],0\nSA s72,s68,40960\nAR v80,a40\nAR v81,a41\nAR v82,a42\n"
"AR v83,a43\nAR v84,a44\nAR v85,a45\nAR v86,a46\nAR v87,a47\nCV v176,v80,v81\nCV v177,v82,v83\nCV v178,v84,v85\n"
"CV v179,v86,v87\nM4 a[40:43],v[44:47],v[16:19],0\nM4 a[44:47],v[44:47],v[20:23],0\nBL v160,s[56:59],s72 offen offset:0 sc1 lds\nAR v88,a72\nAR v89,a73\nAR v90,a74\nAR v91,a75\n"
"AR v92,a76\nAR v93,a77\nAR v94,a78\nAR v95,a79\nCV v180,v88,v89\nCV v181,v90,v91\nCV v182,v92,v93\nCV v183,v94,v95\n"
"M4 a[72:75],v[48:51],v[16:19],0\nM4 a[76:79],v[48:51],v[20:23],0\nBL v161,s[56:59],s72 offen offset:1024 sc1 lds\nAR v96,a104\nAR v97,a105\nAR v98,a106\nAR v99,a107\nAR v100,a108\n"
"AR v101,a109\nAR v102,a110\nAR v103,a111\nCV v184,v96,v97\nCV v185,v98,v99\nCV v186,v100,v101\nCV v187,v102,v103\nM4 a[104:107],v[52:55],v[16:19],0\n"
"M4 a[108:111],v[52:55],v[20:23],0\nBL v162,s[56:59],s72 offen offset:2048 sc1 lds\nAR v72,a136\nAR v73,a137\nAR v74,a138\nAR v75,a139\nAR v76,a140\nAR v77,a141\n"
"AR v78,a142\nAR v79,a143\nCV v188,v72,v73\nCV v189,v74,v75\nCV v190,v76,v77\nCV v191,v78,v79\nM4 a[136:139],v[56:59],v[16:19],0\nM4 a[140:143],v[56:59],v[20:23],0\n"
"BL v163,s[56:59],s72 offen offset:3072 sc1 lds\nAR v80,a168\nAR v81,a169\nAR v82,a170\nAR v83,a171\nAR v84,a172\nAR v85,a173\nAR v86,a174\n"
"AR v87,a175\nCV v192,v80,v81\nCV v193,v82,v83\nCV v194,v84,v85\nCV v195,v86,v87\nM4 a[168:171],v[60:63],v[16:19],0\nM4 a[172:175],v[60:63],v[20:23],0\nSA s68,s68,128\n"
"AR v72,a16\nAR v73,a17\nAR v74,a18\nAR v75,a19\nAR v76,a20\nAR v77,a21\nAR v78,a22\nAR v79,a23\n"
"CV v208,v72,v73\nCV v209,v74,v75\nCV v210,v76,v77\nCV v211,v78,v79\nM4 a[16:19],v[40:43],v[24:27],0\nM4 a[20:23],v[40:43],v[28:31],0\nAR v80,a48\nAR v81,a49\n"
"AR v82,a50\nAR v83,a51\nAR v84,a52\nAR v85,a53\nAR v86,a54\nAR v87,a55\nCV v212,v80,v81\nCV v213,v82,v83\n"
"CV v214,v84,v85\nCV v215,v86,v87\nM4 a[48:51],v[44:47],v[24:27],0\nM4 a[52:55],v[44:47],v[28:31],0\nAR v88,a80\nAR v89,a81\nAR v90,a82\nAR v91,a83\n"
"AR v92,a84\nAR v93,a85\nAR v94,a86\nAR v95,a87\nCV v216,v88,v89\nCV v217,v90,v91\nCV v218,v92,v93\nCV v219,v94,v95\n"
"M4 a[80:83],v[48:51],v[24:27],0\nM4 a[84:87],v[48:51],v[28:31],0\nAR v96,a112\nAR v97,a113\nAR v98,a114\nAR v99,a115\nAR v100,a116\nAR v101,a117\n"
"AR v102,a118\nAR v103,a119\nCV v220,v96,v97\nCV v221,v98,v99\nCV v222,v100,v101\nCV v223,v102,v103\nM4 a[112:115],v[52:55],v[24:27],0\nM4 a[116:119],v[52:55],v[28:31],0\n"
"AR v72,a144\nAR v73,a145\nAR v74,a146\nAR v75,a147\nAR v76,a148\nAR v77,a149\nAR v78,a150\nAR v79,a151\n"
"CV v224,v72,v73\nCV v225,v74,v75\nCV v226,v76,v77\nCV v227,v78,v79\nM4 a[144:147],v[56:59],v[24:27],0\nM4 a[148:151],v[56:59],v[28:31],0\nAR v80,a176\nAR v81,a177\n"
"AR v82,a178\nAR v83,a179\nAR v84,a180\nAR v85,a181\nAR v86,a182\nAR v87,a183\nCV v228,v80,v81\nCV v229,v82,v83\n"
"CV v230,v84,v85\nCV v231,v86,v87\nM4 a[176:179],v[60:63],v[24:27],0\nM4 a[180:183],v[60:63],v[28:31],0\nAR v72,a24\nAR v73,a25\nAR v74,a26\nAR v75,a27\n"
"AR v76,a28\nAR v77,a29\nAR v78,a30\nAR v79,a31\nCV v240,v72,v73\nCV v241,v74,v75\nCV v242,v76,v77\nCV v243,v78,v79\n"
"M4 a[24:27],v[40:43],v[32:35],0\nM4 a[28:31],v[40:43],v[36:39],0\nAR v80,a56\nAR v81,a57\nAR v82,a58\nAR v83,a59\nAR v84,a60\nAR v85,a61\n"
"AR v86,a62\nAR v87,a63\nCV v244,v80,v81\nCV v245,v82,v83\nCV v246,v84,v85\nCV v247,v86,v87\nM4 a[56:59],v[44:47],v[32:35],0\nM4 a[60:63],v[44:47],v[36:39],0\n"
"AR v88,a88\nAR v89,a89\nAR v90,a90\nAR v91,a91\nAR v92,a92\nAR v93,a93\nAR v94,a94\nAR v95,a95\n"
"CV v248,v88,v89\nCV v249,v90,v91\nCV v250,v92,v93\nCV v251,v94,v95\nM4 a[88:91],v[48:51],v[32:35],0\nM4 a[92:95],v[48:51],v[36:39],0\nAR v96,a120\nAR v97,a121\n"
"AR v98,a122\nAR v99,a123\nAR v100,a124\nAR v101,a125\nAR v102,a126\nAR v103,a127\nCV v252,v96,v97\nCV v253,v98,v99\n"
"CV v254,v100,v101\nCV v255,v102,v103\nM4 a[120:123],v[52:55],v[32:35],0\nM4 a[124:127],v[52:55],v[36:39],0\nAR v72,a152\nAR v73,a153\nAR v74,a154\nAR v75,a155\n"
"AR v76,a156\nAR v77,a157\nAR v78,a158\nAR v79,a159\nCV v104,v72,v73\nCV v105,v74,v75\nCV v106,v76,v77\nCV v107,v78,v79\n"
"AW a224,v104\nAW a225,v105\nAW a226,v106\nAW a227,v107\nM4 a[152:155],v[56:59],v[32:35],0\nM4 a[156:159],v[56:59],v[36:39],0\nAR v80,a184\nAR v81,a185\n"
"AR v82,a186\nAR v83,a187\nAR v84,a188\nAR v85,a189\nAR v86,a190\nAR v87,a191\nCV v108,v80,v81\nCV v109,v82,v83\n"
"CV v110,v84,v85\nCV v111,v86,v87\nAW a228,v108\nAW a229,v109\nAW a230,v110\nAW a231,v111\nM4 a[184:187],v[60:63],v[32:35],0\nM4 a[188:191],v[60:63],v[36:39],0\n"
"AR v88,a208\nAR v89,a209\nAR v90,a210\nAR v91,a211\nAR v92,a212\nAR v93,a213\nAR v94,a214\nAR v95,a215\n"
"CV v232,v88,v89\nCV v233,v90,v91\nCV v234,v92,v93\nCV v235,v94,v95\nAR v96,a240\nAR v97,a241\nAR v98,a242\nAR v99,a243\n"
"AR v100,a244\nAR v101,a245\nAR v102,a246\nAR v103,a247\nCV v236,v96,v97\nCV v237,v98,v99\nCV v238,v100,v101\nCV v239,v102,v103\n"
"AR v88,a216\nAR v89,a217\nAR v90,a218\nAR v91,a219\nAR v92,a220\nAR v93,a221\nAR v94,a222\nAR v95,a223\n"
"CV v104,v88,v89\nCV v105,v90,v91\nCV v106,v92,v93\nCV v107,v94,v95\nAW a232,v104\nAW a233,v105\nAW a234,v106\nAW a235,v107\n"
"AR v96,a248\nAR v97,a249\nAR v98,a250\nAR v99,a251\nAR v100,a252\nAR v101,a253\nAR v102,a254\nAR v103,a255\n"
"CV v108,v96,v97\nCV v109,v98,v99\nCV v110,v100,v101\nCV v111,v102,v103\nAW a236,v108\nAW a237,v109\nAW a238,v110\nAW a239,v111\n"
"M4 a[192:195],v[56:59],a[200:203],0\nM4 a[196:199],v[60:63],a[200:203],0\nDT v[104:105],v164 offset:16384\nDT v[106:107],v164 offset:17408\nDR v[72:75],v145 offset:0\nDR v[76:79],v145 offset:2048\nDR v[80:83],v145 offset:4096\nDR v[84:87],v145 offset:6144\n"
"DR v[88:91],v145 offset:8192\nDR v[92:95],v145 offset:10240\nDR v[96:99],v145 offset:12288\nDR v[100:103],v145 offset:14336\nDT v[108:109],v165 offset:16384\nDT v[110:111],v165 offset:17408\nDT v[112:113],v166 offset:16384\nDT v[114:115],v166 offset:17408\n"
"DT v[116:117],v167 offset:16384\nDT v[118:119],v167 offset:17408\nDT v[120:121],v168 offset:16384\nDT v[122:123],v168 offset:16896\nDT v[124:125],v169 offset:16384\nDT v[126:127],v169 offset:16896\nSW vmcnt(14) lgkmcnt(0)\ns_barrier\n"
"M4 a[0:3],v[104:107],v[72:75],a[0:3]\nVU v144,0,v146\nM4 a[4:7],v[104:107],v[76:79],a[4:7]\nVU v145,0,v147\nSA s75,s80,0\nM4 a[8:11],v[104:107],v[80:83],a[8:11]\nDT v[40:41],v164 offset:32768\nM4 a[12:15],v[104:107],v[84:87],a[12:15]\n"
"DT v[42:43],v164 offset:33792\nSA m0,s81,16384\nM4 a[16:19],v[104:107],v[88:91],a[16:19]\nM4 a[20:23],v[104:107],v[92:95],a[20:23]\nDR v[8:11],v144 offset:0\nM4 a[24:27],v[104:107],v[96:99],a[24:27]\nBL v156,s[48:51],s66 offen offset:0 lds\nM4 a[28:31],v[104:107],v[100:103],a[28:31]\n"
"DR v[12:15],v144 offset:2048\nM4 a[32:35],v[108:111],v[72:75],a[32:35]\nDR v[16:19],v144 offset:4096\nM4 a[36:39],v[108:111],v[76:79],a[36:39]\nM4 a[40:43],v[108:111],v[80:83],a[40:43]\nDR v[20:23],v144 offset:6144\nBL v157,s[48:51],s66 offen offset:1024 lds\nM4 a[44:47],v[108:111],v[84:87],a[44:47]\n"
"M4 a[48:51],v[108:111],v[88:91],a[48:51]\nDR v[24:27],v144 offset:8192\nM4 a[52:55],v[108:111],v[92:95],a[52:55]\nDR v[28:31],v144 offset:10240\nBS v[64:67],v201,s[44:47],s75 offen offset:0 sc1\nM4 a[56:59],v[108:111],v[96:99],a[56:59]\nSA m0,s82,16384\nM4 a[60:63],v[108:111],v[100:103],a[60:63]\n"
"DR v[32:35],v144 offset:12288\nM4 a[64:67],v[112:115],v[72:75],a[64:67]\nM4 a[68:71],v[112:115],v[76:79],a[68:71]\nDR v[36:39],v144 offset:14336\nBL v158,s[52:55],s67 offen lds\nM4 a[72:75],v[112:115],v[80:83],a[72:75]\nM4 a[76:79],v[112:115],v[84:87],a[76:79]\nDT v[44:45],v165 offset:32768\n"
"M4 a[80:83],v[112:115],v[88:91],a[80:83]\nDT v[46:47],v165 offset:33792\nM4 a[84:87],v[112:115],v[92:95],a[84:87]\nSA s66,s66,262144\nM4 a[88:91],v[112:115],v[96:99],a[88:91]\nDT v[48:49],v166 offset:32768\nM4 a[92:95],v[112:115],v[100:103],a[92:95]\nM4 a[96:99],v[116:119],v[72:75],a[96:99]\n"
"DT v[50:51],v166 offset:33792\nM4 a[100:103],v[116:119],v[76:79],a[100:103]\nDT v[52:53],v167 offset:32768\nSA s67,s67,110592\nSA s75,s80,0\nM4 a[104:107],v[116:119],v[80:83],a[104:107]\nM4 a[108:111],v[116:119],v[84:87],a[108:111]\nDT v[54:55],v167 offset:33792\n"
"M4 a[112:115],v[116:119],v[88:91],a[112:115]\nSA m0,s70,65536\nM4 a[116:119],v[116:119],v[92:95],a[116:119]\nDT v[56:57],v168 offset:32768\nM4 a[120:123],v[116:119],v[96:99],a[120:123]\nDT v[58:59],v168 offset:33280\nM4 a[124:127],v[116:119],v[100:103],a[124:127]\nM4 a[128:131],v[120:123],v[72:75],a[128:131]\n"
"DT v[60:61],v169 offset:32768\nBL v160,s[56:59],s68 offen offset:0 sc1 lds\nM4 a[132:135],v[120:123],v[76:79],a[132:135]\nM4 a[136:139],v[120:123],v[80:83],a[136:139]\nDT v[62:63],v169 offset:33280\nM4 a[140:143],v[120:123],v[84:87],a[140:143]\nM4 a[144:147],v[120:123],v[88:91],a[144:147]\nBL v161,s[56:59],s68 offen offset:1024 sc1 lds\n"
"M4 a[148:151],v[120:123],v[92:95],a[148:151]\nBS v[68:71],v201,s[44:47],s75 offen offset:256 sc1\nM4 a[152:155],v[120:123],v[96:99],a[152:155]\nM4 a[156:159],v[120:123],v[100:103],a[156:159]\nBL v162,s[56:59],s68 offen offset:2048 sc1 lds\nM4 a[192:195],v[120:123],a[200:203],a[192:195]\nM4 a[160:163],v[124:127],v[72:75],a[160:163]\nM4 a[164:167],v[124:127],v[76:79],a[164:167]\n"
"M4 a[168:171],v[124:127],v[80:83],a[168:171]\nBL v163,s[56:59],s68 offen offset:3072 sc1 lds\nM4 a[172:175],v[124:127],v[84:87],a[172:175]\nM4 a[176:179],v[124:127],v[88:91],a[176:179]\nM4 a[180:183],v[124:127],v[92:95],a[180:183]\nM4 a[184:187],v[124:127],v[96:99],a[184:187]\nM4 a[188:191],v[124:127],v[100:103],a[188:191]\nM4 a[196:199],v[124:127],a[200:203],a[196:199]\n"
"SW vmcnt(20) lgkmcnt(0)\ns_barrier\nM4 a[0:3],v[40:43],v[8:11],a[0:3]\nDT v[104:105],v164 offset:49152\nM4 a[4:7],v[40:43],v[12:15],a[4:7]\nDT v[106:107],v164 offset:50176\nSA m0,s81,32768\nSA s75,s80,0\n"
"M4 a[8:11],v[40:43],v[16:19],a[8:11]\nM4 a[12:15],v[40:43],v[20:23],a[12:15]\nDR v[72:75],v145 offset:0\nM4 a[16:19],v[40:43],v[24:27],a[16:19]\nBL v156,s[48:51],s66 offen offset:0 lds\nM4 a[20:23],v[40:43],v[28:31],a[20:23]\nDR v[76:79],v145 offset:2048\nM4 a[24:27],v[40:43],v[32:35],a[24:27]\n"
"M4 a[28:31],v[40:43],v[36:39],a[28:31]\nDR v[80:83],v145 offset:4096\nBL v157,s[48:51],s66 offen offset:1024 lds\nM4 a[32:35],v[44:47],v[8:11],a[32:35]\nM4 a[36:39],v[44:47],v[12:15],a[36:39]\nDR v[84:87],v145 offset:6144\nM4 a[40:43],v[44:47],v[16:19],a[40:43]\nDR v[88:91],v145 offset:8192\n"
"SA m0,s82,32768\nM4 a[44:47],v[44:47],v[20:23],a[44:47]\nM4 a[48:51],v[44:47],v[24:27],a[48:51]\nDR v[92:95],v145 offset:10240\nM4 a[52:55],v[44:47],v[28:31],a[52:55]\nBS v[128:131],v201,s[44:47],s75 offen offset:2048 sc1\nM4 a[56:59],v[44:47],v[32:35],a[56:59]\nDR v[96:99],v145 offset:12288\n"
"BL v158,s[52:55],s67 offen lds\nM4 a[60:63],v[44:47],v[36:39],a[60:63]\nM4 a[64:67],v[48:51],v[8:11],a[64:67]\nDR v[100:103],v145 offset:14336\nM4 a[68:71],v[48:51],v[12:15],a[68:71]\nSA s66,s66,262144\nM4 a[72:75],v[48:51],v[16:19],a[72:75]\nDT v[108:109],v165 offset:49152\n"
"M4 a[76:79],v[48:51],v[20:23],a[76:79]\nDT v[110:111],v165 offset:50176\nM4 a[80:83],v[48:51],v[24:27],a[80:83]\nSA s67,s67,110592\nM4 a[84:87],v[48:51],v[28:31],a[84:87]\nDT v[112:113],v166 offset:49152\nM4 a[88:91],v[48:51],v[32:35],a[88:91]\nM4 a[92:95],v[48:51],v[36:39],a[92:95]\n"
"DT v[114:115],v166 offset:50176\nM4 a[96:99],v[52:55],v[8:11],a[96:99]\nSA m0,s70,69632\nM4 a[100:103],v[52:55],v[12:15],a[100:103]\nDT v[116:117],v167 offset:49152\nSA s75,s80,0\nM4 a[104:107],v[52:55],v[16:19],a[104:107]\nM4 a[108:111],v[52:55],v[20:23],a[108:111]\n"
"DT v[118:119],v167 offset:50176\nSA s72,s68,40960\nM4 a[112:115],v[52:55],v[24:27],a[112:115]\nDT v[120:121],v168 offset:49152\nM4 a[116:119],v[52:55],v[28:31],a[116:119]\nM4 a[120:123],v[52:55],v[32:35],a[120:123]\nDT v[122:123],v168 offset:49664\nBL v160,s[56:59],s72 offen offset:0 sc1 lds\n"
"M4 a[124:127],v[52:55],v[36:39],a[124:127]\nM4 a[128:131],v[56:59],v[8:11],a[128:131]\nDT v[124:125],v169 offset:49152\nM4 a[132:135],v[56:59],v[12:15],a[132:135]\nBL v161,s[56:59],s72 offen offset:1024 sc1 lds\nM4 a[136:139],v[56:59],v[16:19],a[136:139]\nDT v[126:127],v169 offset:49664\nM4 a[140:143],v[56:59],v[20:23],a[140:143]\n"
"M4 a[144:147],v[56:59],v[24:27],a[144:147]\nM4 a[148:151],v[56:59],v[28:31],a[148:151]\nBL v162,s[56:59],s72 offen offset:2048 sc1 lds\nBS v[132:135],v201,s[44:47],s75 offen offset:2304 sc1\nM4 a[152:155],v[56:59],v[32:35],a[152:155]\nM4 a[156:159],v[56:59],v[36:39],a[156:159]\nM4 a[192:195],v[56:59],a[200:203],a[192:195]\nBL v163,s[56:59],s72 offen offset:3072 sc1 lds\n"
"M4 a[160:163],v[60:63],v[8:11],a[160:163]\nM4 a[164:167],v[60:63],v[12:15],a[164:167]\nM4 a[168:171],v[60:63],v[16:19],a[168:171]\nSA s68,s68,128\nM4 a[172:175],v[60:63],v[20:23],a[172:175]\nM4 a[176:179],v[60:63],v[24:27],a[176:179]\nM4 a[180:183],v[60:63],v[28:31],a[180:183]\nM4 a[184:187],v[60:63],v[32:35],a[184:187]\n"
"M4 a[188:191],v[60:63],v[36:39],a[188:191]\nM4 a[196:199],v[60:63],a[200:203],a[196:199]\nSW vmcnt(18) lgkmcnt(0)\ns_barrier\nM4 a[0:3],v[104:107],v[72:75],a[0:3]\nVU v144,32768,v146\nM4 a[4:7],v[104:107],v[76:79],a[4:7]\nVU v145,32768,v147\n"
"SA s75,s80,4096\nM4 a[8:11],v[104:107],v[80:83],a[8:11]\nDT v[40:41],v164 offset:0\nM4 a[12:15],v[104:107],v[84:87],a[12:15]\nDT v[42:43],v164 offset:1024\nSA m0,s81,49152\nM4 a[16:19],v[104:107],v[88:91],a[16:19]\nM4 a[20:23],v[104:107],v[92:95],a[20:23]\n"
"DR v[8:11],v144 offset:0\nM4 a[24:27],v[104:107],v[96:99],a[24:27]\nBL v156,s[48:51],s66 offen offset:0 lds\nM4 a[28:31],v[104:107],v[100:103],a[28:31]\nDR v[12:15],v144 offset:2048\nM4 a[32:35],v[108:111],v[72:75],a[32:35]\nDR v[16:19],v144 offset:4096\nM4 a[36:39],v[108:111],v[76:79],a[36:39]\n"
"M4 a[40:43],v[108:111],v[80:83],a[40:43]\nDR v[20:23],v144 offset:6144\nBL v157,s[48:51],s66 offen offset:1024 lds\nM4 a[44:47],v[108:111],v[84:87],a[44:47]\nM4 a[48:51],v[108:111],v[88:91],a[48:51]\nDR v[24:27],v144 offset:8192\nM4 a[52:55],v[108:111],v[92:95],a[52:55]\nDR v[28:31],v144 offset:10240\n"
"BS v[136:139],v201,s[44:47],s75 offen offset:0 sc1\nM4 a[56:59],v[108:111],v[96:99],a[56:59]\nSA m0,s82,49152\nM4 a[60:63],v[108:111],v[100:103],a[60:63]\nDR v[32:35],v144 offset:12288\nM4 a[64:67],v[112:115],v[72:75],a[64:67]\nM4 a[68:71],v[112:115],v[76:79],a[68:71]\nDR v[36:39],v144 offset:14336\n"
"BL v158,s[52:55],s67 offen lds\nM4 a[72:75],v[112:115],v[80:83],a[72:75]\nM4 a[76:79],v[112:115],v[84:87],a[76:79]\nDT v[44:45],v165 offset:0\nM4 a[80:83],v[112:115],v[88:91],a[80:83]\nDT v[46:47],v165 offset:1024\nM4 a[84:87],v[112:115],v[92:95],a[84:87]\nSA s66,s66,262144\n"
"M4 a[88:91],v[112:115],v[96:99],a[88:91]\nDT v[48:49],v166 offset:0\nM4 a[92:95],v[112:115],v[100:103],a[92:95]\nM4 a[96:99],v[116:119],v[72:75],a[96:99]\nDT v[50:51],v166 offset:1024\nM4 a[100:103],v[116:119],v[76:79],a[100:103]\nDT v[52:53],v167 offset:0\nSA s67,s67,110592\n"
"SA s75,s80,4096\nM4 a[104:107],v[116:119],v[80:83],a[104:107]\nM4 a[108:111],v[116:119],v[84:87],a[108:111]\nDT v[54:55],v167 offset:1024\nM4 a[112:115],v[116:119],v[88:91],a[112:115]\nSA m0,s70,0\nM4 a[116:119],v[116:119],v[92:95],a[116:119]\nDT v[56:57],v168 offset:0\n"
"M4 a[120:123],v[116:119],v[96:99],a[120:123]\nDT v[58:59],v168 offset:512\nM4 a[124:127],v[116:119],v[100:103],a[124:127]\nM4 a[128:131],v[120:123],v[72:75],a[128:131]\nDT v[60:61],v169 offset:0\nBL v160,s[56:59],s68 offen offset:0 sc1 lds\nM4 a[132:135],v[120:123],v[76:79],a[132:135]\nM4 a[136:139],v[120:123],v[80:83],a[136:139]\n"
"DT v[62:63],v169 offset:512\nM4 a[140:143],v[120:123],v[84:87],a[140:143]\nM4 a[144:147],v[120:123],v[88:91],a[144:147]\nBL v161,s[56:59],s68 offen offset:1024 sc1 lds\nM4 a[148:151],v[120:123],v[92:95],a[148:151]\nBS v[140:143],v201,s[44:47],s75 offen offset:256 sc1\nM4 a[152:155],v[120:123],v[96:99],a[152:155]\nM4 a[156:159],v[120:123],v[100:103],a[156:159]\n"
"BL v162,s[56:59],s68 offen offset:2048 sc1 lds\nM4 a[192:195],v[120:123],a[200:203],a[192:195]\nM4 a[160:163],v[124:127],v[72:75],a[160:163]\nM4 a[164:167],v[124:127],v[76:79],a[164:167]\nM4 a[168:171],v[124:127],v[80:83],a[168:171]\nBL v163,s[56:59],s68 offen offset:3072 sc1 lds\nM4 a[172:175],v[124:127],v[84:87],a[172:175]\nM4 a[176:179],v[124:127],v[88:91],a[176:179]\n"
"M4 a[180:183],v[124:127],v[92:95],a[180:183]\nM4 a[184:187],v[124:127],v[96:99],a[184:187]\nM4 a[188:191],v[124:127],v[100:103],a[188:191]\nM4 a[196:199],v[124:127],a[200:203],a[196:199]\nSW vmcnt(23) lgkmcnt(0)\ns_barrier\nM4 a[0:3],v[40:43],v[8:11],a[0:3]\nDT v[104:105],v164 offset:16384\n"
"M4 a[4:7],v[40:43],v[12:15],a[4:7]\nDT v[106:107],v164 offset:17408\nSA m0,s81,0\nSA s75,s80,4096\nM4 a[8:11],v[40:43],v[16:19],a[8:11]\nM4 a[12:15],v[40:43],v[20:23],a[12:15]\nDR v[72:75],v145 offset:0\nM4 a[16:19],v[40:43],v[24:27],a[16:19]\n"
"BL v156,s[48:51],s66 offen offset:0 lds\nM4 a[20:23],v[40:43],v[28:31],a[20:23]\nDR v[76:79],v145 offset:2048\nM4 a[24:27],v[40:43],v[32:35],a[24:27]\nM4 a[28:31],v[40:43],v[36:39],a[28:31]\nDR v[80:83],v145 offset:4096\nBL v157,s[48:51],s66 offen offset:1024 lds\nM4 a[32:35],v[44:47],v[8:11],a[32:35]\n"
"M4 a[36:39],v[44:47],v[12:15],a[36:39]\nDR v[84:87],v145 offset:6144\nM4 a[40:43],v[44:47],v[16:19],a[40:43]\nDR v[88:91],v145 offset:8192\nSA m0,s82,0\nM4 a[44:47],v[44:47],v[20:23],a[44:47]\nM4 a[48:51],v[44:47],v[24:27],a[48:51]\nDR v[92:95],v145 offset:10240\n"
"M4 a[52:55],v[44:47],v[28:31],a[52:55]\nBS v[148:151],v201,s[44:47],s75 offen offset:2048 sc1\nM4 a[56:59],v[44:47],v[32:35],a[56:59]\nDR v[96:99],v145 offset:12288\nBL v158,s[52:55],s67 offen lds\nM4 a[60:63],v[44:47],v[36:39],a[60:63]\nM4 a[64:67],v[48:51],v[8:11],a[64:67]\nDR v[100:103],v145 offset:14336\n"
"M4 a[68:71],v[48:51],v[12:15],a[68:71]\nSA s66,s66,262144\nM4 a[72:75],v[48:51],v[16:19],a[72:75]\nDT v[108:109],v165 offset:16384\nM4 a[76:79],v[48:51],v[20:23],a[76:79]\nDT v[110:111],v165 offset:17408\nM4 a[80:83],v[48:51],v[24:27],a[80:83]\nSA s67,s67,110592\n"
"M4 a[84:87],v[48:51],v[28:31],a[84:87]\nDT v[112:113],v166 offset:16384\nM4 a[88:91],v[48:51],v[32:35],a[88:91]\nM4 a[92:95],v[48:51],v[36:39],a[92:95]\nDT v[114:115],v166 offset:17408\nM4 a[96:99],v[52:55],v[8:11],a[96:99]\nSA m0,s70,4096\nM4 a[100:103],v[52:55],v[12:15],a[100:103]\n"
"DT v[116:117],v167 offset:16384\nSA s75,s80,4096\nM4 a[104:107],v[52:55],v[16:19],a[104:107]\nM4 a[108:111],v[52:55],v[20:23],a[108:111]\nDT v[118:119],v167 offset:17408\nSA s72,s68,40960\nM4 a[112:115],v[52:55],v[24:27],a[112:115]\nDT v[120:121],v168 offset:16384\n"
"M4 a[116:119],v[52:55],v[28:31],a[116:119]\nM4 a[120:123],v[52:55],v[32:35],a[120:123]\nDT v[122:123],v168 offset:16896\nBL v160,s[56:59],s72 offen offset:0 sc1 lds\nM4 a[124:127],v[52:55],v[36:39],a[124:127]\nM4 a[128:131],v[56:59],v[8:11],a[128:131]\nDT v[124:125],v169 offset:16384\nM4 a[132:135],v[56:59],v[12:15],a[132:135]\n"
"BL v161,s[56:59],s72 offen offset:1024 sc1 lds\nM4 a[136:139],v[56:59],v[16:19],a[136:139]\nDT v[126:127],v169 offset:16896\nM4 a[140:143],v[56:59],v[20:23],a[140:143]\nM4 a[144:147],v[56:59],v[24:27],a[144:147]\nM4 a[148:151],v[56:59],v[28:31],a[148:151]\nBL v162,s[56:59],s72 offen offset:2048 sc1 lds\nBS v[152:155],v201,s[44:47],s75 offen offset:2304 sc1\n"
"M4 a[152:155],v[56:59],v[32:35],a[152:155]\nM4 a[156:159],v[56:59],v[36:39],a[156:159]\nM4 a[192:195],v[56:59],a[200:203],a[192:195]\nBL v163,s[56:59],s72 offen offset:3072 sc1 lds\nM4 a[160:163],v[60:63],v[8:11],a[160:163]\nM4 a[164:167],v[60:63],v[12:15],a[164:167]\nM4 a[168:171],v[60:63],v[16:19],a[168:171]\nSA s68,s68,128\n"
"M4 a[172:175],v[60:63],v[20:23],a[172:175]\nM4 a[176:179],v[60:63],v[24:27],a[176:179]\nM4 a[180:183],v[60:63],v[28:31],a[180:183]\nM4 a[184:187],v[60:63],v[32:35],a[184:187]\nM4 a[188:191],v[60:63],v[36:39],a[188:191]\nM4 a[196:199],v[60:63],a[200:203],a[196:199]\nSW vmcnt(18) lgkmcnt(0)\ns_barrier\n"
"M4 a[0:3],v[104:107],v[72:75],a[0:3]\nVU v144,65536,v146\nM4 a[4:7],v[104:107],v[76:79],a[4:7]\nVU v145,65536,v147\nSA s75,s80,8192\nM4 a[8:11],v[104:107],v[80:83],a[8:11]\nDT v[40:41],v164 offset:32768\nM4 a[12:15],v[104:107],v[84:87],a[12:15]\n"
"DT v[42:43],v164 offset:33792\nSA m0,s81,16384\nM4 a[16:19],v[104:107],v[88:91],a[16:19]\nM4 a[20:23],v[104:107],v[92:95],a[20:23]\nDR v[8:11],v144 offset:0\nM4 a[24:27],v[104:107],v[96:99],a[24:27]\nBL v156,s[48:51],s66 offen offset:0 lds\nM4 a[28:31],v[104:107],v[100:103],a[28:31]\n"
"DR v[12:15],v144 offset:2048\nM4 a[32:35],v[108:111],v[72:75],a[32:35]\nDR v[16:19],v144 offset:4096\nM4 a[36:39],v[108:111],v[76:79],a[36:39]\nM4 a[40:43],v[108:111],v[80:83],a[40:43]\nDR v[20:23],v144 offset:6144\nBL v157,s[48:51],s66 offen offset:1024 lds\nM4 a[44:47],v[108:111],v[84:87],a[44:47]\n"
"M4 a[48:51],v[108:111],v[88:91],a[48:51]\nDR v[24:27],v144 offset:8192\nM4 a[52:55],v[108:111],v[92:95],a[52:55]\nDR v[28:31],v144 offset:10240\nBS v[172:175],v201,s[44:47],s75 offen offset:0 sc1\nM4 a[56:59],v[108:111],v[96:99],a[56:59]\nSA m0,s82,16384\nM4 a[60:63],v[108:111],v[100:103],a[60:63]\n"
"DR v[32:35],v144 offset:12288\nM4 a[64:67],v[112:115],v[72:75],a[64:67]\nM4 a[68:71],v[112:115],v[76:79],a[68:71]\nDR v[36:39],v144 offset:14336\nBL v158,s[52:55],s67 offen lds\nM4 a[72:75],v[112:115],v[80:83],a[72:75]\nM4 a[76:79],v[112:115],v[84:87],a[76:79]\nDT v[44:45],v165 offset:32768\n"
"M4 a[80:83],v[112:115],v[88:91],a[80:83]\nDT v[46:47],v165 offset:33792\nM4 a[84:87],v[112:115],v[92:95],a[84:87]\nSA s66,s66,262144\nM4 a[88:91],v[112:115],v[96:99],a[88:91]\nDT v[48:49],v166 offset:32768\nM4 a[92:95],v[112:115],v[100:103],a[92:95]\nM4 a[96:99],v[116:119],v[72:75],a[96:99]\n"
"DT v[50:51],v166 offset:33792\nM4 a[100:103],v[116:119],v[76:79],a[100:103]\nDT v[52:53],v167 offset:32768\nSA s67,s67,110592\nSA s75,s80,8192\nM4 a[104:107],v[116:119],v[80:83],a[104:107]\nM4 a[108:111],v[116:119],v[84:87],a[108:111]\nDT v[54:55],v167 offset:33792\n"
"M4 a[112:115],v[116:119],v[88:91],a[112:115]\nSA m0,s70,32768\nM4 a[116:119],v[116:119],v[92:95],a[116:119]\nDT v[56:57],v168 offset:32768\nM4 a[120:123],v[116:119],v[96:99],a[120:123]\nDT v[58:59],v168 offset:33280\nM4 a[124:127],v[116:119],v[100:103],a[124:127]\nM4 a[128:131],v[120:123],v[72:75],a[128:131]\n"
"DT v[60:61],v169 offset:32768\nBL v160,s[56:59],s68 offen offset:0 sc1 lds\nM4 a[132:135],v[120:123],v[76:79],a[132:135]\nM4 a[136:139],v[120:123],v[80:83],a[136:139]\nDT v[62:63],v169 offset:33280\nM4 a[140:143],v[120:123],v[84:87],a[140:143]\nM4 a[144:147],v[120:123],v[88:91],a[144:147]\nBL v161,s[56:59],s68 offen offset:1024 sc1 lds\n"
"M4 a[148:151],v[120:123],v[92:95],a[148:151]\nBS v[176:179],v201,s[44:47],s75 offen offset:256 sc1\nM4 a[152:155],v[120:123],v[96:99],a[152:155]\nM4 a[156:159],v[120:123],v[100:103],a[156:159]\nBL v162,s[56:59],s68 offen offset:2048 sc1 lds\nM4 a[192:195],v[120:123],a[200:203],a[192:195]\nM4 a[160:163],v[124:127],v[72:75],a[160:163]\nM4 a[164:167],v[124:127],v[76:79],a[164:167]\n"
"M4 a[168:171],v[124:127],v[80:83],a[168:171]\nBL v163,s[56:59],s68 offen offset:3072 sc1 lds\nM4 a[172:175],v[124:127],v[84:87],a[172:175]\nM4 a[176:179],v[124:127],v[88:91],a[176:179]\nM4 a[180:183],v[124:127],v[92:95],a[180:183]\nM4 a[184:187],v[124:127],v[96:99],a[184:187]\nM4 a[188:191],v[124:127],v[100:103],a[188:191]\nM4 a[196:199],v[124:127],a[200:203],a[196:199]\n"
"SW vmcnt(23) lgkmcnt(0)\ns_barrier\nM4 a[0:3],v[40:43],v[8:11],a[0:3]\nDT v[104:105],v164 offset:49152\nM4 a[4:7],v[40:43],v[12:15],a[4:7]\nDT v[106:107],v164 offset:50176\nSA m0,s81,32768\nSA s75,s80,8192\n"
"M4 a[8:11],v[40:43],v[16:19],a[8:11]\nM4 a[12:15],v[40:43],v[20:23],a[12:15]\nDR v[72:75],v145 offset:0\nM4 a[16:19],v[40:43],v[24:27],a[16:19]\nBL v156,s[48:51],s66 offen offset:0 lds\nM4 a[20:23],v[40:43],v[28:31],a[20:23]\nDR v[76:79],v145 offset:2048\nM4 a[24:27],v[40:43],v[32:35],a[24:27]\n"
"M4 a[28:31],v[40:43],v[36:39],a[28:31]\nDR v[80:83],v145 offset:4096\nBL v157,s[48:51],s66 offen offset:1024 lds\nM4 a[32:35],v[44:47],v[8:11],a[32:35]\nM4 a[36:39],v[44:47],v[12:15],a[36:39]\nDR v[84:87],v145 offset:6144\nM4 a[40:43],v[44:47],v[16:19],a[40:43]\nDR v[88:91],v145 offset:8192\n"
"SA m0,s82,32768\nM4 a[44:47],v[44:47],v[20:23],a[44:47]\nM4 a[48:51],v[44:47],v[24:27],a[48:51]\nDR v[92:95],v145 offset:10240\nM4 a[52:55],v[44:47],v[28:31],a[52:55]\nBS v[180:183],v201,s[44:47],s75 offen offset:2048 sc1\nM4 a[56:59],v[44:47],v[32:35],a[56:59]\nDR v[96:99],v145 offset:12288\n"
"BL v158,s[52:55],s67 offen lds\nM4 a[60:63],v[44:47],v[36:39],a[60:63]\nM4 a[64:67],v[48:51],v[8:11],a[64:67]\nDR v[100:103],v145 offset:14336\nM4 a[68:71],v[48:51],v[12:15],a[68:71]\nSA s66,s66,262144\nM4 a[72:75],v[48:51],v[16:19],a[72:75]\nDT v[108:109],v165 offset:49152\n"
"M4 a[76:79],v[48:51],v[20:23],a[76:79]\nDT v[110:111],v165 offset:50176\nM4 a[80:83],v[48:51],v[24:27],a[80:83]\nSA s67,s67,110592\nM4 a[84:87],v[48:51],v[28:31],a[84:87]\nDT v[112:113],v166 offset:49152\nM4 a[88:91],v[48:51],v[32:35],a[88:91]\nM4 a[92:95],v[48:51],v[36:39],a[92:95]\n"
"DT v[114:115],v166 offset:50176\nM4 a[96:99],v[52:55],v[8:11],a[96:99]\nSA m0,s70,36864\nM4 a[100:103],v[52:55],v[12:15],a[100:103]\nDT v[116:117],v167 offset:49152\nSA s75,s80,8192\nM4 a[104:107],v[52:55],v[16:19],a[104:107]\nM4 a[108:111],v[52:55],v[20:23],a[108:111]\n"
"DT v[118:119],v167 offset:50176\nSA s72,s68,40960\nM4 a[112:115],v[52:55],v[24:27],a[112:115]\nDT v[120:121],v168 offset:49152\nM4 a[116:119],v[52:55],v[28:31],a[116:119]\nM4 a[120:123],v[52:55],v[32:35],a[120:123]\nDT v[122:123],v168 offset:49664\nBL v160,s[56:59],s72 offen offset:0 sc1 lds\n"
"M4 a[124:127],v[52:55],v[36:39],a[124:127]\nM4 a[128:131],v[56:59],v[8:11],a[128:131]\nDT v[124:125],v169 offset:49152\nM4 a[132:135],v[56:59],v[12:15],a[132:135]\nBL v161,s[56:59],s72 offen offset:1024 sc1 lds\nM4 a[136:139],v[56:59],v[16:19],a[136:139]\nDT v[126:127],v169 offset:49664\nM4 a[140:143],v[56:59],v[20:23],a[140:143]\n"
"M4 a[144:147],v[56:59],v[24:27],a[144:147]\nM4 a[148:151],v[56:59],v[28:31],a[148:151]\nBL v162,s[56:59],s72 offen offset:2048 sc1 lds\nBS v[184:187],v201,s[44:47],s75 offen offset:2304 sc1\nM4 a[152:155],v[56:59],v[32:35],a[152:155]\nM4 a[156:159],v[56:59],v[36:39],a[156:159]\nM4 a[192:195],v[56:59],a[200:203],a[192:195]\nBL v163,s[56:59],s72 offen offset:3072 sc1 lds\n"
"M4 a[160:163],v[60:63],v[8:11],a[160:163]\nM4 a[164:167],v[60:63],v[12:15],a[164:167]\nM4 a[168:171],v[60:63],v[16:19],a[168:171]\nSA s68,s68,128\nM4 a[172:175],v[60:63],v[20:23],a[172:175]\nM4 a[176:179],v[60:63],v[24:27],a[176:179]\nM4 a[180:183],v[60:63],v[28:31],a[180:183]\nM4 a[184:187],v[60:63],v[32:35],a[184:187]\n"
"M4 a[188:191],v[60:63],v[36:39],a[188:191]\nM4 a[196:199],v[60:63],a[200:203],a[196:199]\nSW vmcnt(18) lgkmcnt(0)\ns_barrier\nM4 a[0:3],v[104:107],v[72:75],a[0:3]\nVU v144,0,v146\nM4 a[4:7],v[104:107],v[76:79],a[4:7]\nVU v145,0,v147\n"
"SA s75,s80,12288\nM4 a[8:11],v[104:107],v[80:83],a[8:11]\nDT v[40:41],v164 offset:0\nM4 a[12:15],v[104:107],v[84:87],a[12:15]\nDT v[42:43],v164 offset:1024\nSA m0,s81,49152\nM4 a[16:19],v[104:107],v[88:91],a[16:19]\nM4 a[20:23],v[104:107],v[92:95],a[20:23]\n"
"DR v[8:11],v144 offset:0\nM4 a[24:27],v[104:107],v[96:99],a[24:27]\nBL v156,s[48:51],s66 offen offset:0 lds\nM4 a[28:31],v[104:107],v[100:103],a[28:31]\nDR v[12:15],v144 offset:2048\nM4 a[32:35],v[108:111],v[72:75],a[32:35]\nDR v[16:19],v144 offset:4096\nM4 a[36:39],v[108:111],v[76:79],a[36:39]\n"
"M4 a[40:43],v[108:111],v[80:83],a[40:43]\nDR v[20:23],v144 offset:6144\nBL v157,s[48:51],s66 offen offset:1024 lds\nM4 a[44:47],v[108:111],v[84:87],a[44:47]\nM4 a[48:51],v[108:111],v[88:91],a[48:51]\nDR v[24:27],v144 offset:8192\nM4 a[52:55],v[108:111],v[92:95],a[52:55]\nDR v[28:31],v144 offset:10240\n"
"BS v[188:191],v201,s[44:47],s75 offen offset:0 sc1\nM4 a[56:59],v[108:111],v[96:99],a[56:59]\nSA m0,s82,49152\nM4 a[60:63],v[108:111],v[100:103],a[60:63]\nDR v[32:35],v144 offset:12288\nM4 a[64:67],v[112:115],v[72:75],a[64:67]\nM4 a[68:71],v[112:115],v[76:79],a[68:71]\nDR v[36:39],v144 offset:14336\n"
"BL v158,s[52:55],s67 offen lds\nM4 a[72:75],v[112:115],v[80:83],a[72:75]\nM4 a[76:79],v[112:115],v[84:87],a[76:79]\nDT v[44:45],v165 offset:0\nM4 a[80:83],v[112:115],v[88:91],a[80:83]\nDT v[46:47],v165 offset:1024\nM4 a[84:87],v[112:115],v[92:95],a[84:87]\nSA s66,s66,262144\n"
"M4 a[88:91],v[112:115],v[96:99],a[88:91]\nDT v[48:49],v166 offset:0\nM4 a[92:95],v[112:115],v[100:103],a[92:95]\nM4 a[96:99],v[116:119],v[72:75],a[96:99]\nDT v[50:51],v166 offset:1024\nM4 a[100:103],v[116:119],v[76:79],a[100:103]\nDT v[52:53],v167 offset:0\nSA s67,s67,110592\n"
"SA s75,s80,12288\nM4 a[104:107],v[116:119],v[80:83],a[104:107]\nM4 a[108:111],v[116:119],v[84:87],a[108:111]\nDT v[54:55],v167 offset:1024\nM4 a[112:115],v[116:119],v[88:91],a[112:115]\nSA m0,s70,65536\nM4 a[116:119],v[116:119],v[92:95],a[116:119]\nDT v[56:57],v168 offset:0\n"
"M4 a[120:123],v[116:119],v[96:99],a[120:123]\nDT v[58:59],v168 offset:512\nM4 a[124:127],v[116:119],v[100:103],a[124:127]\nM4 a[128:131],v[120:123],v[72:75],a[128:131]\nDT v[60:61],v169 offset:0\nBL v160,s[56:59],s68 offen offset:0 sc1 lds\nM4 a[132:135],v[120:123],v[76:79],a[132:135]\nM4 a[136:139],v[120:123],v[80:83],a[136:139]\n"
"DT v[62:63],v169 offset:512\nM4 a[140:143],v[120:123],v[84:87],a[140:143]\nM4 a[144:147],v[120:123],v[88:91],a[144:147]\nBL v161,s[56:59],s68 offen offset:1024 sc1 lds\nM4 a[148:151],v[120:123],v[92:95],a[148:151]\nBS v[192:195],v201,s[44:47],s75 offen offset:256 sc1\nM4 a[152:155],v[120:123],v[96:99],a[152:155]\nM4 a[156:159],v[120:123],v[100:103],a[156:159]\n"
"BL v162,s[56:59],s68 offen offset:2048 sc1 lds\nM4 a[192:195],v[120:123],a[200:203],a[192:195]\nM4 a[160:163],v[124:127],v[72:75],a[160:163]\nM4 a[164:167],v[124:127],v[76:79],a[164:167]\nM4 a[168:171],v[124:127],v[80:83],a[168:171]\nBL v163,s[56:59],s68 offen offset:3072 sc1 lds\nM4 a[172:175],v[124:127],v[84:87],a[172:175]\nM4 a[176:179],v[124:127],v[88:91],a[176:179]\n"
"M4 a[180:183],v[124:127],v[92:95],a[180:183]\nM4 a[184:187],v[124:127],v[96:99],a[184:187]\nM4 a[188:191],v[124:127],v[100:103],a[188:191]\nM4 a[196:199],v[124:127],a[200:203],a[196:199]\nSW vmcnt(23) lgkmcnt(0)\ns_barrier\nM4 a[0:3],v[40:43],v[8:11],a[0:3]\nDT v[104:105],v164 offset:16384\n"
"M4 a[4:7],v[40:43],v[12:15],a[4:7]\nDT v[106:107],v164 offset:17408\nSA m0,s81,0\nSA s75,s80,12288\nM4 a[8:11],v[40:43],v[16:19],a[8:11]\nM4 a[12:15],v[40:43],v[20:23],a[12:15]\nDR v[72:75],v145 offset:0\nM4 a[16:19],v[40:43],v[24:27],a[16:19]\n"
"BL v156,s[48:51],s66 offen offset:0 lds\nM4 a[20:23],v[40:43],v[28:31],a[20:23]\nDR v[76:79],v145 offset:2048\nM4 a[24:27],v[40:43],v[32:35],a[24:27]\nM4 a[28:31],v[40:43],v[36:39],a[28:31]\nDR v[80:83],v145 offset:4096\nBL v157,s[48:51],s66 offen offset:1024 lds\nM4 a[32:35],v[44:47],v[8:11],a[32:35]\n"
"M4 a[36:39],v[44:47],v[12:15],a[36:39]\nDR v[84:87],v145 offset:6144\nM4 a[40:43],v[44:47],v[16:19],a[40:43]\nDR v[88:91],v145 offset:8192\nSA m0,s82,0\nM4 a[44:47],v[44:47],v[20:23],a[44:47]\nM4 a[48:51],v[44:47],v[24:27],a[48:51]\nDR v[92:95],v145 offset:10240\n"
"M4 a[52:55],v[44:47],v[28:31],a[52:55]\nBS v[196:199],v201,s[44:47],s75 offen offset:2048 sc1\nM4 a[56:59],v[44:47],v[32:35],a[56:59]\nDR v[96:99],v145 offset:12288\nBL v158,s[52:55],s67 offen lds\nM4 a[60:63],v[44:47],v[36:39],a[60:63]\nM4 a[64:67],v[48:51],v[8:11],a[64:67]\nDR v[100:103],v145 offset:14336\n"
"M4 a[68:71],v[48:51],v[12:15],a[68:71]\nSA s66,s66,262144\nM4 a[72:75],v[48:51],v[16:19],a[72:75]\nDT v[108:109],v165 offset:16384\nM4 a[76:79],v[48:51],v[20:23],a[76:79]\nDT v[110:111],v165 offset:17408\nM4 a[80:83],v[48:51],v[24:27],a[80:83]\nSA s67,s67,110592\n"
"M4 a[84:87],v[48:51],v[28:31],a[84:87]\nDT v[112:113],v166 offset:16384\nM4 a[88:91],v[48:51],v[32:35],a[88:91]\nM4 a[92:95],v[48:51],v[36:39],a[92:95]\nDT v[114:115],v166 offset:17408\nM4 a[96:99],v[52:55],v[8:11],a[96:99]\nSA m0,s70,69632\nM4 a[100:103],v[52:55],v[12:15],a[100:103]\n"
"DT v[116:117],v167 offset:16384\nSA s75,s80,12288\nM4 a[104:107],v[52:55],v[16:19],a[104:107]\nM4 a[108:111],v[52:55],v[20:23],a[108:111]\nDT v[118:119],v167 offset:17408\nSA s72,s68,40960\nM4 a[112:115],v[52:55],v[24:27],a[112:115]\nDT v[120:121],v168 offset:16384\n"
"M4 a[116:119],v[52:55],v[28:31],a[116:119]\nM4 a[120:123],v[52:55],v[32:35],a[120:123]\nDT v[122:123],v168 offset:16896\nBL v160,s[56:59],s72 offen offset:0 sc1 lds\nM4 a[124:127],v[52:55],v[36:39],a[124:127]\nM4 a[128:131],v[56:59],v[8:11],a[128:131]\nDT v[124:125],v169 offset:16384\nM4 a[132:135],v[56:59],v[12:15],a[132:135]\n"
"BL v161,s[56:59],s72 offen offset:1024 sc1 lds\nM4 a[136:139],v[56:59],v[16:19],a[136:139]\nDT v[126:127],v169 offset:16896\nM4 a[140:143],v[56:59],v[20:23],a[140:143]\nM4 a[144:147],v[56:59],v[24:27],a[144:147]\nM4 a[148:151],v[56:59],v[28:31],a[148:151]\nBL v162,s[56:59],s72 offen offset:2048 sc1 lds\nBS v[204:207],v201,s[44:47],s75 offen offset:2304 sc1\n"
"M4 a[152:155],v[56:59],v[32:35],a[152:155]\nM4 a[156:159],v[56:59],v[36:39],a[156:159]\nM4 a[192:195],v[56:59],a[200:203],a[192:195]\nBL v163,s[56:59],s72 offen offset:3072 sc1 lds\nM4 a[160:163],v[60:63],v[8:11],a[160:163]\nM4 a[164:167],v[60:63],v[12:15],a[164:167]\nM4 a[168:171],v[60:63],v[16:19],a[168:171]\nSA s68,s68,128\n"
"M4 a[172:175],v[60:63],v[20:23],a[172:175]\nM4 a[176:179],v[60:63],v[24:27],a[176:179]\nM4 a[180:183],v[60:63],v[28:31],a[180:183]\nM4 a[184:187],v[60:63],v[32:35],a[184:187]\nM4 a[188:191],v[60:63],v[36:39],a[188:191]\nM4 a[196:199],v[60:63],a[200:203],a[196:199]\nSW vmcnt(18) lgkmcnt(0)\ns_barrier\n"
"M4 a[0:3],v[104:107],v[72:75],a[0:3]\nVU v144,32768,v146\nM4 a[4:7],v[104:107],v[76:79],a[4:7]\nVU v145,32768,v147\nSA s75,s80,16384\nM4 a[8:11],v[104:107],v[80:83],a[8:11]\nDT v[40:41],v164 offset:32768\nM4 a[12:15],v[104:107],v[84:87],a[12:15]\n"
"DT v[42:43],v164 offset:33792\nSA m0,s81,16384\nM4 a[16:19],v[104:107],v[88:91],a[16:19]\nM4 a[20:23],v[104:107],v[92:95],a[20:23]\nDR v[8:11],v144 offset:0\nM4 a[24:27],v[104:107],v[96:99],a[24:27]\nBL v156,s[48:51],s66 offen offset:0 lds\nM4 a[28:31],v[104:107],v[100:103],a[28:31]\n"
"DR v[12:15],v144 offset:2048\nM4 a[32:35],v[108:111],v[72:75],a[32:35]\nDR v[16:19],v144 offset:4096\nM4 a[36:39],v[108:111],v[76:79],a[36:39]\nM4 a[40:43],v[108:111],v[80:83],a[40:43]\nDR v[20:23],v144 offset:6144\nBL v157,s[48:51],s66 offen offset:1024 lds\nM4 a[44:47],v[108:111],v[84:87],a[44:47]\n"
"M4 a[48:51],v[108:111],v[88:91],a[48:51]\nDR v[24:27],v144 offset:8192\nM4 a[52:55],v[108:111],v[92:95],a[52:55]\nDR v[28:31],v144 offset:10240\nBS v[208:211],v201,s[44:47],s75 offen offset:0 sc1\nM4 a[56:59],v[108:111],v[96:99],a[56:59]\nSA m0,s82,16384\nM4 a[60:63],v[108:111],v[100:103],a[60:63]\n"
"DR v[32:35],v144 offset:12288\nM4 a[64:67],v[112:115],v[72:75],a[64:67]\nM4 a[68:71],v[112:115],v[76:79],a[68:71]\nDR v[36:39],v144 offset:14336\nBL v158,s[52:55],s67 offen lds\nM4 a[72:75],v[112:115],v[80:83],a[72:75]\nM4 a[76:79],v[112:115],v[84:87],a[76:79]\nDT v[44:45],v165 offset:32768\n"
"M4 a[80:83],v[112:115],v[88:91],a[80:83]\nDT v[46:47],v165 offset:33792\nM4 a[84:87],v[112:115],v[92:95],a[84:87]\nSA s66,s66,262144\nM4 a[88:91],v[112:115],v[96:99],a[88:91]\nDT v[48:49],v166 offset:32768\nM4 a[92:95],v[112:115],v[100:103],a[92:95]\nM4 a[96:99],v[116:119],v[72:75],a[96:99]\n"
"DT v[50:51],v166 offset:33792\nM4 a[100:103],v[116:119],v[76:79],a[100:103]\nDT v[52:53],v167 offset:32768\nSA s67,s67,110592\nSA s75,s80,16384\nM4 a[104:107],v[116:119],v[80:83],a[104:107]\nM4 a[108:111],v[116:119],v[84:87],a[108:111]\nDT v[54:55],v167 offset:33792\n"
"M4 a[112:115],v[116:119],v[88:91],a[112:115]\nSA m0,s70,0\nM4 a[116:119],v[116:119],v[92:95],a[116:119]\nDT v[56:57],v168 offset:32768\nM4 a[120:123],v[116:119],v[96:99],a[120:123]\nDT v[58:59],v168 offset:33280\nM4 a[124:127],v[116:119],v[100:103],a[124:127]\nM4 a[128:131],v[120:123],v[72:75],a[128:131]\n"
"DT v[60:61],v169 offset:32768\nBL v160,s[56:59],s68 offen offset:0 sc1 lds\nM4 a[132:135],v[120:123],v[76:79],a[132:135]\nM4 a[136:139],v[120:123],v[80:83],a[136:139]\nDT v[62:63],v169 offset:33280\nM4 a[140:143],v[120:123],v[84:87],a[140:143]\nM4 a[144:147],v[120:123],v[88:91],a[144:147]\nBL v161,s[56:59],s68 offen offset:1024 sc1 lds\n"
"M4 a[148:151],v[120:123],v[92:95],a[148:151]\nBS v[212:215],v201,s[44:47],s75 offen offset:256 sc1\nM4 a[152:155],v[120:123],v[96:99],a[152:155]\nM4 a[156:159],v[120:123],v[100:103],a[156:159]\nBL v162,s[56:59],s68 offen offset:2048 sc1 lds\nM4 a[192:195],v[120:123],a[200:203],a[192:195]\nM4 a[160:163],v[124:127],v[72:75],a[160:163]\nM4 a[164:167],v[124:127],v[76:79],a[164:167]\n"
"M4 a[168:171],v[124:127],v[80:83],a[168:171]\nBL v163,s[56:59],s68 offen offset:3072 sc1 lds\nM4 a[172:175],v[124:127],v[84:87],a[172:175]\nM4 a[176:179],v[124:127],v[88:91],a[176:179]\nM4 a[180:183],v[124:127],v[92:95],a[180:183]\nM4 a[184:187],v[124:127],v[96:99],a[184:187]\nM4 a[188:191],v[124:127],v[100:103],a[188:191]\nM4 a[196:199],v[124:127],a[200:203],a[196:199]\n"
"SW vmcnt(23) lgkmcnt(0)\ns_barrier\nM4 a[0:3],v[40:43],v[8:11],a[0:3]\nDT v[104:105],v164 offset:49152\nM4 a[4:7],v[40:43],v[12:15],a[4:7]\nDT v[106:107],v164 offset:50176\nSA m0,s81,32768\nSA s75,s80,16384\n"
"M4 a[8:11],v[40:43],v[16:19],a[8:11]\nM4 a[12:15],v[40:43],v[20:23],a[12:15]\nDR v[72:75],v145 offset:0\nM4 a[16:19],v[40:43],v[24:27],a[16:19]\nBL v156,s[48:51],s66 offen offset:0 lds\nM4 a[20:23],v[40:43],v[28:31],a[20:23]\nDR v[76:79],v145 offset:2048\nM4 a[24:27],v[40:43],v[32:35],a[24:27]\n"
"M4 a[28:31],v[40:43],v[36:39],a[28:31]\nDR v[80:83],v145 offset:4096\nBL v157,s[48:51],s66 offen offset:1024 lds\nM4 a[32:35],v[44:47],v[8:11],a[32:35]\nM4 a[36:39],v[44:47],v[12:15],a[36:39]\nDR v[84:87],v145 offset:6144\nM4 a[40:43],v[44:47],v[16:19],a[40:43]\nDR v[88:91],v145 offset:8192\n"
"SA m0,s82,32768\nM4 a[44:47],v[44:47],v[20:23],a[44:47]\nM4 a[48:51],v[44:47],v[24:27],a[48:51]\nDR v[92:95],v145 offset:10240\nM4 a[52:55],v[44:47],v[28:31],a[52:55]\nBS v[216:219],v201,s[44:47],s75 offen offset:2048 sc1\nM4 a[56:59],v[44:47],v[32:35],a[56:59]\nDR v[96:99],v145 offset:12288\n"
"BL v158,s[52:55],s67 offen lds\nM4 a[60:63],v[44:47],v[36:39],a[60:63]\nM4 a[64:67],v[48:51],v[8:11],a[64:67]\nDR v[100:103],v145 offset:14336\nM4 a[68:71],v[48:51],v[12:15],a[68:71]\nSA s66,s66,262144\nM4 a[72:75],v[48:51],v[16:19],a[72:75]\nDT v[108:109],v165 offset:49152\n"
"M4 a[76:79],v[48:51],v[20:23],a[76:79]\nDT v[110:111],v165 offset:50176\nM4 a[80:83],v[48:51],v[24:27],a[80:83]\nSA s67,s67,110592\nM4 a[84:87],v[48:51],v[28:31],a[84:87]\nDT v[112:113],v166 offset:49152\nM4 a[88:91],v[48:51],v[32:35],a[88:91]\nM4 a[92:95],v[48:51],v[36:39],a[92:95]\n"
"DT v[114:115],v166 offset:50176\nM4 a[96:99],v[52:55],v[8:11],a[96:99]\nSA m0,s70,4096\nM4 a[100:103],v[52:55],v[12:15],a[100:103]\nDT v[116:117],v167 offset:49152\nSA s75,s80,16384\nM4 a[104:107],v[52:55],v[16:19],a[104:107]\nM4 a[108:111],v[52:55],v[20:23],a[108:111]\n"
"DT v[118:119],v167 offset:50176\nSA s72,s68,40960\nM4 a[112:115],v[52:55],v[24:27],a[112:115]\nDT v[120:121],v168 offset:49152\nM4 a[116:119],v[52:55],v[28:31],a[116:119]\nM4 a[120:123],v[52:55],v[32:35],a[120:123]\nDT v[122:123],v168 offset:49664\nBL v160,s[56:59],s72 offen offset:0 sc1 lds\n"
"M4 a[124:127],v[52:55],v[36:39],a[124:127]\nM4 a[128:131],v[56:59],v[8:11],a[128:131]\nDT v[124:125],v169 offset:49152\nM4 a[132:135],v[56:59],v[12:15],a[132:135]\nBL v161,s[56:59],s72 offen offset:1024 sc1 lds\nM4 a[136:139],v[56:59],v[16:19],a[136:139]\nDT v[126:127],v169 offset:49664\nM4 a[140:143],v[56:59],v[20:23],a[140:143]\n"
"M4 a[144:147],v[56:59],v[24:27],a[144:147]\nM4 a[148:151],v[56:59],v[28:31],a[148:151]\nBL v162,s[56:59],s72 offen offset:2048 sc1 lds\nBS v[220:223],v201,s[44:47],s75 offen offset:2304 sc1\nM4 a[152:155],v[56:59],v[32:35],a[152:155]\nM4 a[156:159],v[56:59],v[36:39],a[156:159]\nM4 a[192:195],v[56:59],a[200:203],a[192:195]\nBL v163,s[56:59],s72 offen offset:3072 sc1 lds\n"
"M4 a[160:163],v[60:63],v[8:11],a[160:163]\nM4 a[164:167],v[60:63],v[12:15],a[164:167]\nM4 a[168:171],v[60:63],v[16:19],a[168:171]\nSA s68,s68,128\nM4 a[172:175],v[60:63],v[20:23],a[172:175]\nM4 a[176:179],v[60:63],v[24:27],a[176:179]\nM4 a[180:183],v[60:63],v[28:31],a[180:183]\nM4 a[184:187],v[60:63],v[32:35],a[184:187]\n"
"M4 a[188:191],v[60:63],v[36:39],a[188:191]\nM4 a[196:199],v[60:63],a[200:203],a[196:199]\nSW vmcnt(18) lgkmcnt(0)\ns_barrier\nM4 a[0:3],v[104:107],v[72:75],a[0:3]\nVU v144,65536,v146\nM4 a[4:7],v[104:107],v[76:79],a[4:7]\nVU v145,65536,v147\n"
"SA s75,s80,20480\nM4 a[8:11],v[104:107],v[80:83],a[8:11]\nDT v[40:41],v164 offset:0\nM4 a[12:15],v[104:107],v[84:87],a[12:15]\nDT v[42:43],v164 offset:1024\nSA m0,s81,49152\nM4 a[16:19],v[104:107],v[88:91],a[16:19]\nM4 a[20:23],v[104:107],v[92:95],a[20:23]\n"
"DR v[8:11],v144 offset:0\nM4 a[24:27],v[104:107],v[96:99],a[24:27]\nBL v156,s[48:51],s66 offen offset:0 lds\nM4 a[28:31],v[104:107],v[100:103],a[28:31]\nDR v[12:15],v144 offset:2048\nM4 a[32:35],v[108:111],v[72:75],a[32:35]\nDR v[16:19],v144 offset:4096\nM4 a[36:39],v[108:111],v[76:79],a[36:39]\n"
"M4 a[40:43],v[108:111],v[80:83],a[40:43]\nDR v[20:23],v144 offset:6144\nBL v157,s[48:51],s66 offen offset:1024 lds\nM4 a[44:47],v[108:111],v[84:87],a[44:47]\nM4 a[48:51],v[108:111],v[88:91],a[48:51]\nDR v[24:27],v144 offset:8192\nM4 a[52:55],v[108:111],v[92:95],a[52:55]\nDR v[28:31],v144 offset:10240\n"
"BS v[224:227],v201,s[44:47],s75 offen offset:0 sc1\nM4 a[56:59],v[108:111],v[96:99],a[56:59]\nSA m0,s82,49152\nM4 a[60:63],v[108:111],v[100:103],a[60:63]\nDR v[32:35],v144 offset:12288\nM4 a[64:67],v[112:115],v[72:75],a[64:67]\nM4 a[68:71],v[112:115],v[76:79],a[68:71]\nDR v[36:39],v144 offset:14336\n"
"BL v158,s[52:55],s67 offen lds\nM4 a[72:75],v[112:115],v[80:83],a[72:75]\nM4 a[76:79],v[112:115],v[84:87],a[76:79]\nDT v[44:45],v165 offset:0\nM4 a[80:83],v[112:115],v[88:91],a[80:83]\nDT v[46:47],v165 offset:1024\nM4 a[84:87],v[112:115],v[92:95],a[84:87]\nSA s66,s66,262144\n"
"M4 a[88:91],v[112:115],v[96:99],a[88:91]\nDT v[48:49],v166 offset:0\nM4 a[92:95],v[112:115],v[100:103],a[92:95]\nM4 a[96:99],v[116:119],v[72:75],a[96:99]\nDT v[50:51],v166 offset:1024\nM4 a[100:103],v[116:119],v[76:79],a[100:103]\nDT v[52:53],v167 offset:0\nSA s67,s67,110592\n"
"SA s75,s80,20480\nM4 a[104:107],v[116:119],v[80:83],a[104:107]\nM4 a[108:111],v[116:119],v[84:87],a[108:111]\nDT v[54:55],v167 offset:1024\nM4 a[112:115],v[116:119],v[88:91],a[112:115]\nSA m0,s70,32768\nM4 a[116:119],v[116:119],v[92:95],a[116:119]\nDT v[56:57],v168 offset:0\n"
"M4 a[120:123],v[116:119],v[96:99],a[120:123]\nDT v[58:59],v168 offset:512\nM4 a[124:127],v[116:119],v[100:103],a[124:127]\nM4 a[128:131],v[120:123],v[72:75],a[128:131]\nDT v[60:61],v169 offset:0\nBL v160,s[56:59],s68 offen offset:0 sc1 lds\nM4 a[132:135],v[120:123],v[76:79],a[132:135]\nM4 a[136:139],v[120:123],v[80:83],a[136:139]\n"
"DT v[62:63],v169 offset:512\nM4 a[140:143],v[120:123],v[84:87],a[140:143]\nM4 a[144:147],v[120:123],v[88:91],a[144:147]\nBL v161,s[56:59],s68 offen offset:1024 sc1 lds\nM4 a[148:151],v[120:123],v[92:95],a[148:151]\nBS v[228:231],v201,s[44:47],s75 offen offset:256 sc1\nM4 a[152:155],v[120:123],v[96:99],a[152:155]\nM4 a[156:159],v[120:123],v[100:103],a[156:159]\n"
"BL v162,s[56:59],s68 offen offset:2048 sc1 lds\nM4 a[192:195],v[120:123],a[200:203],a[192:195]\nM4 a[160:163],v[124:127],v[72:75],a[160:163]\nM4 a[164:167],v[124:127],v[76:79],a[164:167]\nM4 a[168:171],v[124:127],v[80:83],a[168:171]\nBL v163,s[56:59],s68 offen offset:3072 sc1 lds\nM4 a[172:175],v[124:127],v[84:87],a[172:175]\nM4 a[176:179],v[124:127],v[88:91],a[176:179]\n"
"M4 a[180:183],v[124:127],v[92:95],a[180:183]\nM4 a[184:187],v[124:127],v[96:99],a[184:187]\nM4 a[188:191],v[124:127],v[100:103],a[188:191]\nM4 a[196:199],v[124:127],a[200:203],a[196:199]\nSW vmcnt(23) lgkmcnt(0)\ns_barrier\nM4 a[0:3],v[40:43],v[8:11],a[0:3]\nDT v[104:105],v164 offset:16384\n"
"M4 a[4:7],v[40:43],v[12:15],a[4:7]\nDT v[106:107],v164 offset:17408\nSA m0,s81,0\nSA s75,s80,20480\nM4 a[8:11],v[40:43],v[16:19],a[8:11]\nM4 a[12:15],v[40:43],v[20:23],a[12:15]\nDR v[72:75],v145 offset:0\nM4 a[16:19],v[40:43],v[24:27],a[16:19]\n"
"BL v156,s[48:51],s66 offen offset:0 lds\nM4 a[20:23],v[40:43],v[28:31],a[20:23]\nDR v[76:79],v145 offset:2048\nM4 a[24:27],v[40:43],v[32:35],a[24:27]\nM4 a[28:31],v[40:43],v[36:39],a[28:31]\nDR v[80:83],v145 offset:4096\nBL v157,s[48:51],s66 offen offset:1024 lds\nM4 a[32:35],v[44:47],v[8:11],a[32:35]\n"
"M4 a[36:39],v[44:47],v[12:15],a[36:39]\nDR v[84:87],v145 offset:6144\nM4 a[40:43],v[44:47],v[16:19],a[40:43]\nDR v[88:91],v145 offset:8192\nSA m0,s82,0\nM4 a[44:47],v[44:47],v[20:23],a[44:47]\nM4 a[48:51],v[44:47],v[24:27],a[48:51]\nDR v[92:95],v145 offset:10240\n"
"M4 a[52:55],v[44:47],v[28:31],a[52:55]\nBS v[232:235],v201,s[44:47],s75 offen offset:2048 sc1\nM4 a[56:59],v[44:47],v[32:35],a[56:59]\nDR v[96:99],v145 offset:12288\nBL v158,s[52:55],s67 offen lds\nM4 a[60:63],v[44:47],v[36:39],a[60:63]\nM4 a[64:67],v[48:51],v[8:11],a[64:67]\nDR v[100:103],v145 offset:14336\n"
"M4 a[68:71],v[48:51],v[12:15],a[68:71]\nSA s66,s66,262144\nM4 a[72:75],v[48:51],v[16:19],a[72:75]\nDT v[108:109],v165 offset:16384\nM4 a[76:79],v[48:51],v[20:23],a[76:79]\nDT v[110:111],v165 offset:17408\nM4 a[80:83],v[48:51],v[24:27],a[80:83]\nSA s67,s67,110592\n"
"M4 a[84:87],v[48:51],v[28:31],a[84:87]\nDT v[112:113],v166 offset:16384\nM4 a[88:91],v[48:51],v[32:35],a[88:91]\nM4 a[92:95],v[48:51],v[36:39],a[92:95]\nDT v[114:115],v166 offset:17408\nM4 a[96:99],v[52:55],v[8:11],a[96:99]\nSA m0,s70,36864\nM4 a[100:103],v[52:55],v[12:15],a[100:103]\n"
"DT v[116:117],v167 offset:16384\nSA s75,s80,20480\nM4 a[104:107],v[52:55],v[16:19],a[104:107]\nM4 a[108:111],v[52:55],v[20:23],a[108:111]\nDT v[118:119],v167 offset:17408\nSA s72,s68,40960\nM4 a[112:115],v[52:55],v[24:27],a[112:115]\nDT v[120:121],v168 offset:16384\n"
"M4 a[116:119],v[52:55],v[28:31],a[116:119]\nM4 a[120:123],v[52:55],v[32:35],a[120:123]\nDT v[122:123],v168 offset:16896\nBL v160,s[56:59],s72 offen offset:0 sc1 lds\nM4 a[124:127],v[52:55],v[36:39],a[124:127]\nM4 a[128:131],v[56:59],v[8:11],a[128:131]\nDT v[124:125],v169 offset:16384\nM4 a[132:135],v[56:59],v[12:15],a[132:135]\n"
"BL v161,s[56:59],s72 offen offset:1024 sc1 lds\nM4 a[136:139],v[56:59],v[16:19],a[136:139]\nDT v[126:127],v169 offset:16896\nM4 a[140:143],v[56:59],v[20:23],a[140:143]\nM4 a[144:147],v[56:59],v[24:27],a[144:147]\nM4 a[148:151],v[56:59],v[28:31],a[148:151]\nBL v162,s[56:59],s72 offen offset:2048 sc1 lds\nBS v[236:239],v201,s[44:47],s75 offen offset:2304 sc1\n"
"M4 a[152:155],v[56:59],v[32:35],a[152:155]\nM4 a[156:159],v[56:59],v[36:39],a[156:159]\nM4 a[192:195],v[56:59],a[200:203],a[192:195]\nBL v163,s[56:59],s72 offen offset:3072 sc1 lds\nM4 a[160:163],v[60:63],v[8:11],a[160:163]\nM4 a[164:167],v[60:63],v[12:15],a[164:167]\nM4 a[168:171],v[60:63],v[16:19],a[168:171]\nSA s68,s68,128\n"
"M4 a[172:175],v[60:63],v[20:23],a[172:175]\nM4 a[176:179],v[60:63],v[24:27],a[176:179]\nM4 a[180:183],v[60:63],v[28:31],a[180:183]\nM4 a[184:187],v[60:63],v[32:35],a[184:187]\nM4 a[188:191],v[60:63],v[36:39],a[188:191]\nM4 a[196:199],v[60:63],a[200:203],a[196:199]\nSW vmcnt(18) lgkmcnt(0)\ns_barrier\n"
"M4 a[0:3],v[104:107],v[72:75],a[0:3]\nVU v144,0,v146\nM4 a[4:7],v[104:107],v[76:79],a[4:7]\nVU v145,0,v147\nSA s75,s80,24576\nM4 a[8:11],v[104:107],v[80:83],a[8:11]\nDT v[40:41],v164 offset:32768\nM4 a[12:15],v[104:107],v[84:87],a[12:15]\n"
"DT v[42:43],v164 offset:33792\nSA m0,s81,16384\nM4 a[16:19],v[104:107],v[88:91],a[16:19]\nM4 a[20:23],v[104:107],v[92:95],a[20:23]\nDR v[8:11],v144 offset:0\nM4 a[24:27],v[104:107],v[96:99],a[24:27]\nBL v156,s[48:51],s66 offen offset:0 lds\nM4 a[28:31],v[104:107],v[100:103],a[28:31]\n"
"DR v[12:15],v144 offset:2048\nM4 a[32:35],v[108:111],v[72:75],a[32:35]\nDR v[16:19],v144 offset:4096\nM4 a[36:39],v[108:111],v[76:79],a[36:39]\nM4 a[40:43],v[108:111],v[80:83],a[40:43]\nDR v[20:23],v144 offset:6144\nBL v157,s[48:51],s66 offen offset:1024 lds\nM4 a[44:47],v[108:111],v[84:87],a[44:47]\n"
"M4 a[48:51],v[108:111],v[88:91],a[48:51]\nDR v[24:27],v144 offset:8192\nM4 a[52:55],v[108:111],v[92:95],a[52:55]\nDR v[28:31],v144 offset:10240\nBS v[240:243],v201,s[44:47],s75 offen offset:0 sc1\nM4 a[56:59],v[108:111],v[96:99],a[56:59]\nSA m0,s82,16384\nM4 a[60:63],v[108:111],v[100:103],a[60:63]\n"
"DR v[32:35],v144 offset:12288\nM4 a[64:67],v[112:115],v[72:75],a[64:67]\nM4 a[68:71],v[112:115],v[76:79],a[68:71]\nDR v[36:39],v144 offset:14336\nBL v158,s[52:55],s67 offen lds\nM4 a[72:75],v[112:115],v[80:83],a[72:75]\nM4 a[76:79],v[112:115],v[84:87],a[76:79]\nDT v[44:45],v165 offset:32768\n"
"M4 a[80:83],v[112:115],v[88:91],a[80:83]\nDT v[46:47],v165 offset:33792\nM4 a[84:87],v[112:115],v[92:95],a[84:87]\nSA s66,s66,262144\nM4 a[88:91],v[112:115],v[96:99],a[88:91]\nDT v[48:49],v166 offset:32768\nM4 a[92:95],v[112:115],v[100:103],a[92:95]\nM4 a[96:99],v[116:119],v[72:75],a[96:99]\n"
"DT v[50:51],v166 offset:33792\nM4 a[100:103],v[116:119],v[76:79],a[100:103]\nDT v[52:53],v167 offset:32768\nSA s67,s67,110592\nSA s75,s80,24576\nM4 a[104:107],v[116:119],v[80:83],a[104:107]\nM4 a[108:111],v[116:119],v[84:87],a[108:111]\nDT v[54:55],v167 offset:33792\n"
"M4 a[112:115],v[116:119],v[88:91],a[112:115]\nSA m0,s70,65536\nM4 a[116:119],v[116:119],v[92:95],a[116:119]\nDT v[56:57],v168 offset:32768\nM4 a[120:123],v[116:119],v[96:99],a[120:123]\nDT v[58:59],v168 offset:33280\nM4 a[124:127],v[116:119],v[100:103],a[124:127]\nM4 a[128:131],v[120:123],v[72:75],a[128:131]\n"
"DT v[60:61],v169 offset:32768\nBL v160,s[56:59],s68 offen offset:0 sc1 lds\nM4 a[132:135],v[120:123],v[76:79],a[132:135]\nM4 a[136:139],v[120:123],v[80:83],a[136:139]\nDT v[62:63],v169 offset:33280\nM4 a[140:143],v[120:123],v[84:87],a[140:143]\nM4 a[144:147],v[120:123],v[88:91],a[144:147]\nBL v161,s[56:59],s68 offen offset:1024 sc1 lds\n"
"M4 a[148:151],v[120:123],v[92:95],a[148:151]\nBS v[244:247],v201,s[44:47],s75 offen offset:256 sc1\nM4 a[152:155],v[120:123],v[96:99],a[152:155]\nM4 a[156:159],v[120:123],v[100:103],a[156:159]\nBL v162,s[56:59],s68 offen offset:2048 sc1 lds\nM4 a[192:195],v[120:123],a[200:203],a[192:195]\nM4 a[160:163],v[124:127],v[72:75],a[160:163]\nM4 a[164:167],v[124:127],v[76:79],a[164:167]\n"
"M4 a[168:171],v[124:127],v[80:83],a[168:171]\nBL v163,s[56:59],s68 offen offset:3072 sc1 lds\nM4 a[172:175],v[124:127],v[84:87],a[172:175]\nM4 a[176:179],v[124:127],v[88:91],a[176:179]\nM4 a[180:183],v[124:127],v[92:95],a[180:183]\nM4 a[184:187],v[124:127],v[96:99],a[184:187]\nM4 a[188:191],v[124:127],v[100:103],a[188:191]\nM4 a[196:199],v[124:127],a[200:203],a[196:199]\n"
"SW vmcnt(23) lgkmcnt(0)\ns_barrier\nM4 a[0:3],v[40:43],v[8:11],a[0:3]\nDT v[104:105],v164 offset:49152\nM4 a[4:7],v[40:43],v[12:15],a[4:7]\nDT v[106:107],v164 offset:50176\nSA m0,s81,32768\nSA s75,s80,24576\n"
"M4 a[8:11],v[40:43],v[16:19],a[8:11]\nM4 a[12:15],v[40:43],v[20:23],a[12:15]\nDR v[72:75],v145 offset:0\nM4 a[16:19],v[40:43],v[24:27],a[16:19]\nBL v156,s[48:51],s66 offen offset:0 lds\nM4 a[20:23],v[40:43],v[28:31],a[20:23]\nDR v[76:79],v145 offset:2048\nM4 a[24:27],v[40:43],v[32:35],a[24:27]\n"
"M4 a[28:31],v[40:43],v[36:39],a[28:31]\nDR v[80:83],v145 offset:4096\nBL v157,s[48:51],s66 offen offset:1024 lds\nM4 a[32:35],v[44:47],v[8:11],a[32:35]\nM4 a[36:39],v[44:47],v[12:15],a[36:39]\nDR v[84:87],v145 offset:6144\nM4 a[40:43],v[44:47],v[16:19],a[40:43]\nDR v[88:91],v145 offset:8192\n"
"SA m0,s82,32768\nM4 a[44:47],v[44:47],v[20:23],a[44:47]\nM4 a[48:51],v[44:47],v[24:27],a[48:51]\nDR v[92:95],v145 offset:10240\nM4 a[52:55],v[44:47],v[28:31],a[52:55]\nBS v[248:251],v201,s[44:47],s75 offen offset:2048 sc1\nM4 a[56:59],v[44:47],v[32:35],a[56:59]\nDR v[96:99],v145 offset:12288\n"
"BL v158,s[52:55],s67 offen lds\nM4 a[60:63],v[44:47],v[36:39],a[60:63]\nM4 a[64:67],v[48:51],v[8:11],a[64:67]\nDR v[100:103],v145 offset:14336\nM4 a[68:71],v[48:51],v[12:15],a[68:71]\nSA s66,s66,262144\nM4 a[72:75],v[48:51],v[16:19],a[72:75]\nDT v[108:109],v165 offset:49152\n"
"M4 a[76:79],v[48:51],v[20:23],a[76:79]\nDT v[110:111],v165 offset:50176\nM4 a[80:83],v[48:51],v[24:27],a[80:83]\nSA s67,s67,110592\nM4 a[84:87],v[48:51],v[28:31],a[84:87]\nDT v[112:113],v166 offset:49152\nM4 a[88:91],v[48:51],v[32:35],a[88:91]\nM4 a[92:95],v[48:51],v[36:39],a[92:95]\n"
"DT v[114:115],v166 offset:50176\nM4 a[96:99],v[52:55],v[8:11],a[96:99]\nSA m0,s70,69632\nM4 a[100:103],v[52:55],v[12:15],a[100:103]\nDT v[116:117],v167 offset:49152\nSA s75,s80,24576\nM4 a[104:107],v[52:55],v[16:19],a[104:107]\nM4 a[108:111],v[52:55],v[20:23],a[108:111]\n"
"DT v[118:119],v167 offset:50176\nSA s72,s68,40960\nM4 a[112:115],v[52:55],v[24:27],a[112:115]\nDT v[120:121],v168 offset:49152\nM4 a[116:119],v[52:55],v[28:31],a[116:119]\nM4 a[120:123],v[52:55],v[32:35],a[120:123]\nDT v[122:123],v168 offset:49664\nBL v160,s[56:59],s72 offen offset:0 sc1 lds\n"
"M4 a[124:127],v[52:55],v[36:39],a[124:127]\nM4 a[128:131],v[56:59],v[8:11],a[128:131]\nDT v[124:125],v169 offset:49152\nM4 a[132:135],v[56:59],v[12:15],a[132:135]\nBL v161,s[56:59],s72 offen offset:1024 sc1 lds\nM4 a[136:139],v[56:59],v[16:19],a[136:139]\nDT v[126:127],v169 offset:49664\nM4 a[140:143],v[56:59],v[20:23],a[140:143]\n"
"M4 a[144:147],v[56:59],v[24:27],a[144:147]\nM4 a[148:151],v[56:59],v[28:31],a[148:151]\nBL v162,s[56:59],s72 offen offset:2048 sc1 lds\nBS v[252:255],v201,s[44:47],s75 offen offset:2304 sc1\nM4 a[152:155],v[56:59],v[32:35],a[152:155]\nM4 a[156:159],v[56:59],v[36:39],a[156:159]\nM4 a[192:195],v[56:59],a[200:203],a[192:195]\nBL v163,s[56:59],s72 offen offset:3072 sc1 lds\n"
"M4 a[160:163],v[60:63],v[8:11],a[160:163]\nM4 a[164:167],v[60:63],v[12:15],a[164:167]\nM4 a[168:171],v[60:63],v[16:19],a[168:171]\nSA s68,s68,128\nM4 a[172:175],v[60:63],v[20:23],a[172:175]\nM4 a[176:179],v[60:63],v[24:27],a[176:179]\nM4 a[180:183],v[60:63],v[28:31],a[180:183]\nM4 a[184:187],v[60:63],v[32:35],a[184:187]\n"
"M4 a[188:191],v[60:63],v[36:39],a[188:191]\nM4 a[196:199],v[60:63],a[200:203],a[196:199]\nSW vmcnt(18) lgkmcnt(0)\ns_barrier\nM4 a[0:3],v[104:107],v[72:75],a[0:3]\nVU v144,32768,v146\nM4 a[4:7],v[104:107],v[76:79],a[4:7]\nVU v145,32768,v147\n"
"SA s75,s80,28672\nM4 a[8:11],v[104:107],v[80:83],a[8:11]\nDT v[40:41],v164 offset:0\nM4 a[12:15],v[104:107],v[84:87],a[12:15]\nDT v[42:43],v164 offset:1024\nSA m0,s81,49152\nM4 a[16:19],v[104:107],v[88:91],a[16:19]\nM4 a[20:23],v[104:107],v[92:95],a[20:23]\n"
"DR v[8:11],v144 offset:0\nM4 a[24:27],v[104:107],v[96:99],a[24:27]\nM4 a[28:31],v[104:107],v[100:103],a[28:31]\nDR v[12:15],v144 offset:2048\nM4 a[32:35],v[108:111],v[72:75],a[32:35]\nDR v[16:19],v144 offset:4096\nM4 a[36:39],v[108:111],v[76:79],a[36:39]\nBL v156,s[48:51],s66 offen offset:0 lds\n"
"M4 a[40:43],v[108:111],v[80:83],a[40:43]\nDR v[20:23],v144 offset:6144\nM4 a[44:47],v[108:111],v[84:87],a[44:47]\nM4 a[48:51],v[108:111],v[88:91],a[48:51]\nDR v[24:27],v144 offset:8192\nM4 a[52:55],v[108:111],v[92:95],a[52:55]\nDR v[28:31],v144 offset:10240\nBS a[224:227],v201,s[44:47],s75 offen offset:0 sc1\n"
"M4 a[56:59],v[108:111],v[96:99],a[56:59]\nM4 a[60:63],v[108:111],v[100:103],a[60:63]\nDR v[32:35],v144 offset:12288\nBL v157,s[48:51],s66 offen offset:1024 lds\nM4 a[64:67],v[112:115],v[72:75],a[64:67]\nM4 a[68:71],v[112:115],v[76:79],a[68:71]\nDR v[36:39],v144 offset:14336\nM4 a[72:75],v[112:115],v[80:83],a[72:75]\n"
"M4 a[76:79],v[112:115],v[84:87],a[76:79]\nDT v[44:45],v165 offset:0\nM4 a[80:83],v[112:115],v[88:91],a[80:83]\nDT v[46:47],v165 offset:1024\nM4 a[84:87],v[112:115],v[92:95],a[84:87]\nSA m0,s82,49152\nM4 a[88:91],v[112:115],v[96:99],a[88:91]\nDT v[48:49],v166 offset:0\n"
"M4 a[92:95],v[112:115],v[100:103],a[92:95]\nM4 a[96:99],v[116:119],v[72:75],a[96:99]\nDT v[50:51],v166 offset:1024\nM4 a[100:103],v[116:119],v[76:79],a[100:103]\nDT v[52:53],v167 offset:0\nSA s75,s80,28672\nM4 a[104:107],v[116:119],v[80:83],a[104:107]\nM4 a[108:111],v[116:119],v[84:87],a[108:111]\n"
"DT v[54:55],v167 offset:1024\nM4 a[112:115],v[116:119],v[88:91],a[112:115]\nBL v158,s[52:55],s67 offen lds\nM4 a[116:119],v[116:119],v[92:95],a[116:119]\nDT v[56:57],v168 offset:0\nM4 a[120:123],v[116:119],v[96:99],a[120:123]\nDT v[58:59],v168 offset:512\nM4 a[124:127],v[116:119],v[100:103],a[124:127]\n"
"M4 a[128:131],v[120:123],v[72:75],a[128:131]\nDT v[60:61],v169 offset:0\nM4 a[132:135],v[120:123],v[76:79],a[132:135]\nM4 a[136:139],v[120:123],v[80:83],a[136:139]\nDT v[62:63],v169 offset:512\nSA s66,s66,262144\nM4 a[140:143],v[120:123],v[84:87],a[140:143]\nM4 a[144:147],v[120:123],v[88:91],a[144:147]\n"
"M4 a[148:151],v[120:123],v[92:95],a[148:151]\nBS a[228:231],v201,s[44:47],s75 offen offset:256 sc1\nM4 a[152:155],v[120:123],v[96:99],a[152:155]\nM4 a[156:159],v[120:123],v[100:103],a[156:159]\nM4 a[192:195],v[120:123],a[200:203],a[192:195]\nSA s67,s67,110592\nM4 a[160:163],v[124:127],v[72:75],a[160:163]\nM4 a[164:167],v[124:127],v[76:79],a[164:167]\n"
"M4 a[168:171],v[124:127],v[80:83],a[168:171]\nM4 a[172:175],v[124:127],v[84:87],a[172:175]\nM4 a[176:179],v[124:127],v[88:91],a[176:179]\nM4 a[180:183],v[124:127],v[92:95],a[180:183]\nM4 a[184:187],v[124:127],v[96:99],a[184:187]\nM4 a[188:191],v[124:127],v[100:103],a[188:191]\nM4 a[196:199],v[124:127],a[200:203],a[196:199]\nSW vmcnt(19) lgkmcnt(0)\n"
"s_barrier\nM4 a[0:3],v[40:43],v[8:11],a[0:3]\nDT v[104:105],v164 offset:16384\nM4 a[4:7],v[40:43],v[12:15],a[4:7]\nDT v[106:107],v164 offset:17408\nSA s75,s80,28672\nM4 a[8:11],v[40:43],v[16:19],a[8:11]\nM4 a[12:15],v[40:43],v[20:23],a[12:15]\n"
"DR v[72:75],v145 offset:0\nM4 a[16:19],v[40:43],v[24:27],a[16:19]\nM4 a[20:23],v[40:43],v[28:31],a[20:23]\nDR v[76:79],v145 offset:2048\nM4 a[24:27],v[40:43],v[32:35],a[24:27]\nM4 a[28:31],v[40:43],v[36:39],a[28:31]\nDR v[80:83],v145 offset:4096\nM4 a[32:35],v[44:47],v[8:11],a[32:35]\n"
"M4 a[36:39],v[44:47],v[12:15],a[36:39]\nDR v[84:87],v145 offset:6144\nM4 a[40:43],v[44:47],v[16:19],a[40:43]\nDR v[88:91],v145 offset:8192\nM4 a[44:47],v[44:47],v[20:23],a[44:47]\nM4 a[48:51],v[44:47],v[24:27],a[48:51]\nDR v[92:95],v145 offset:10240\nM4 a[52:55],v[44:47],v[28:31],a[52:55]\n"
"BS a[232:235],v201,s[44:47],s75 offen offset:2048 sc1\nM4 a[56:59],v[44:47],v[32:35],a[56:59]\nDR v[96:99],v145 offset:12288\nM4 a[60:63],v[44:47],v[36:39],a[60:63]\nM4 a[64:67],v[48:51],v[8:11],a[64:67]\nDR v[100:103],v145 offset:14336\nM4 a[68:71],v[48:51],v[12:15],a[68:71]\nM4 a[72:75],v[48:51],v[16:19],a[72:75]\n"
"DT v[108:109],v165 offset:16384\nM4 a[76:79],v[48:51],v[20:23],a[76:79]\nDT v[110:111],v165 offset:17408\nM4 a[80:83],v[48:51],v[24:27],a[80:83]\nM4 a[84:87],v[48:51],v[28:31],a[84:87]\nDT v[112:113],v166 offset:16384\nM4 a[88:91],v[48:51],v[32:35],a[88:91]\nM4 a[92:95],v[48:51],v[36:39],a[92:95]\n"
"DT v[114:115],v166 offset:17408\nM4 a[96:99],v[52:55],v[8:11],a[96:99]\nM4 a[100:103],v[52:55],v[12:15],a[100:103]\nDT v[116:117],v167 offset:16384\nSA s75,s80,28672\nM4 a[104:107],v[52:55],v[16:19],a[104:107]\nM4 a[108:111],v[52:55],v[20:23],a[108:111]\nDT v[118:119],v167 offset:17408\n"
"M4 a[112:115],v[52:55],v[24:27],a[112:115]\nDT v[120:121],v168 offset:16384\nM4 a[116:119],v[52:55],v[28:31],a[116:119]\nM4 a[120:123],v[52:55],v[32:35],a[120:123]\nDT v[122:123],v168 offset:16896\nM4 a[124:127],v[52:55],v[36:39],a[124:127]\nM4 a[128:131],v[56:59],v[8:11],a[128:131]\nDT v[124:125],v169 offset:16384\n"
"M4 a[132:135],v[56:59],v[12:15],a[132:135]\nM4 a[136:139],v[56:59],v[16:19],a[136:139]\nDT v[126:127],v169 offset:16896\nM4 a[140:143],v[56:59],v[20:23],a[140:143]\nM4 a[144:147],v[56:59],v[24:27],a[144:147]\nM4 a[148:151],v[56:59],v[28:31],a[148:151]\nBS a[236:239],v201,s[44:47],s75 offen offset:2304 sc1\nM4 a[152:155],v[56:59],v[32:35],a[152:155]\n"
"M4 a[156:159],v[56:59],v[36:39],a[156:159]\nM4 a[192:195],v[56:59],a[200:203],a[192:195]\nM4 a[160:163],v[60:63],v[8:11],a[160:163]\nM4 a[164:167],v[60:63],v[12:15],a[164:167]\nM4 a[168:171],v[60:63],v[16:19],a[168:171]\nM4 a[172:175],v[60:63],v[20:23],a[172:175]\nM4 a[176:179],v[60:63],v[24:27],a[176:179]\nM4 a[180:183],v[60:63],v[28:31],a[180:183]\n"
"M4 a[184:187],v[60:63],v[32:35],a[184:187]\nM4 a[188:191],v[60:63],v[36:39],a[188:191]\nM4 a[196:199],v[60:63],a[200:203],a[196:199]\nSW vmcnt(7) lgkmcnt(0)\ns_barrier\nM4 a[0:3],v[104:107],v[72:75],a[0:3]\nVU v144,65536,v146\nM4 a[4:7],v[104:107],v[76:79],a[4:7]\n"
"VU v145,65536,v147\nSM s80,s60\nM4 a[8:11],v[104:107],v[80:83],a[8:11]\nDT v[40:41],v164 offset:32768\nM4 a[12:15],v[104:107],v[84:87],a[12:15]\nDT v[42:43],v164 offset:33792\nM4 a[16:19],v[104:107],v[88:91],a[16:19]\nSM s81,s61\n"
"M4 a[20:23],v[104:107],v[92:95],a[20:23]\nDR v[8:11],v144 offset:0\nM4 a[24:27],v[104:107],v[96:99],a[24:27]\nM4 a[28:31],v[104:107],v[100:103],a[28:31]\nDR v[12:15],v144 offset:2048\nSM s82,s62\nM4 a[32:35],v[108:111],v[72:75],a[32:35]\nDR v[16:19],v144 offset:4096\n"
"M4 a[36:39],v[108:111],v[76:79],a[36:39]\nM4 a[40:43],v[108:111],v[80:83],a[40:43]\nDR v[20:23],v144 offset:6144\nSM s83,s63\nM4 a[44:47],v[108:111],v[84:87],a[44:47]\nM4 a[48:51],v[108:111],v[88:91],a[48:51]\nDR v[24:27],v144 offset:8192\nM4 a[52:55],v[108:111],v[92:95],a[52:55]\n"
"DR v[28:31],v144 offset:10240\ns_load_dwordx2 s[84:85],s[94:95],0x38\nM4 a[56:59],v[108:111],v[96:99],a[56:59]\nM4 a[60:63],v[108:111],v[100:103],a[60:63]\nDR v[32:35],v144 offset:12288\nM4 a[64:67],v[112:115],v[72:75],a[64:67]\ns_sub_u32 s34,15,s83\ns_cmp_eq_u32 s80,0\n"
"s_cselect_b32 s34,s34,s83\ns_lshl_b32 s34,s34,10\nSA s34,s34,2621440\nSA s96,s92,s34\ns_addc_u32 s97,s93,0\ns_and_b32 s97,s97,0xffff\nSM s98,1024\nSM s99,0x20000\n"
"M4 a[68:71],v[112:115],v[76:79],a[68:71]\nDR v[36:39],v144 offset:14336\nM4 a[72:75],v[112:115],v[80:83],a[72:75]\nM4 a[76:79],v[112:115],v[84:87],a[76:79]\nDT v[44:45],v165 offset:32768\nVN v228,15,v200\nM4 a[80:83],v[112:115],v[88:91],a[80:83]\nDT v[46:47],v165 offset:33792\n"
"M4 a[84:87],v[112:115],v[92:95],a[84:87]\nM4 a[88:91],v[112:115],v[96:99],a[88:91]\nDT v[48:49],v166 offset:32768\nVL v228,3,v228\nM4 a[92:95],v[112:115],v[100:103],a[92:95]\nM4 a[96:99],v[116:119],v[72:75],a[96:99]\nDT v[50:51],v166 offset:33792\nM4 a[100:103],v[116:119],v[76:79],a[100:103]\n"
"DT v[52:53],v167 offset:32768\nbuffer_load_dwordx2 v[204:205],v228,s[96:99],0 offen offset:0 sc1\nM4 a[104:107],v[116:119],v[80:83],a[104:107]\nM4 a[108:111],v[116:119],v[84:87],a[108:111]\nDT v[54:55],v167 offset:33792\nM4 a[112:115],v[116:119],v[88:91],a[112:115]\nbuffer_load_dwordx2 v[206:207],v228,s[96:99],0 offen offset:128 sc1\nM4 a[116:119],v[116:119],v[92:95],a[116:119]\n"
"DT v[56:57],v168 offset:32768\nM4 a[120:123],v[116:119],v[96:99],a[120:123]\nDT v[58:59],v168 offset:33280\nM4 a[124:127],v[116:119],v[100:103],a[124:127]\nbuffer_load_dwordx2 v[208:209],v228,s[96:99],0 offen offset:256 sc1\nM4 a[128:131],v[120:123],v[72:75],a[128:131]\nDT v[60:61],v169 offset:32768\nM4 a[132:135],v[120:123],v[76:79],a[132:135]\n"
"M4 a[136:139],v[120:123],v[80:83],a[136:139]\nDT v[62:63],v169 offset:33280\nbuffer_load_dwordx2 v[210:211],v228,s[96:99],0 offen offset:384 sc1\nM4 a[140:143],v[120:123],v[84:87],a[140:143]\nM4 a[144:147],v[120:123],v[88:91],a[144:147]\nM4 a[148:151],v[120:123],v[92:95],a[148:151]\nbuffer_load_dwordx2 v[212:213],v228,s[96:99],0 offen offset:512 sc1\nM4 a[152:155],v[120:123],v[96:99],a[152:155]\n"
"M4 a[156:159],v[120:123],v[100:103],a[156:159]\nM4 a[192:195],v[120:123],a[200:203],a[192:195]\nbuffer_load_dwordx2 v[214:215],v228,s[96:99],0 offen offset:640 sc1\nM4 a[160:163],v[124:127],v[72:75],a[160:163]\nM4 a[164:167],v[124:127],v[76:79],a[164:167]\nM4 a[168:171],v[124:127],v[80:83],a[168:171]\nbuffer_load_dwordx2 v[216:217],v228,s[96:99],0 offen offset:768 sc1\nM4 a[172:175],v[124:127],v[84:87],a[172:175]\n"
"M4 a[176:179],v[124:127],v[88:91],a[176:179]\nM4 a[180:183],v[124:127],v[92:95],a[180:183]\nbuffer_load_dwordx2 v[218:219],v228,s[96:99],0 offen offset:896 sc1\nM4 a[184:187],v[124:127],v[96:99],a[184:187]\nM4 a[188:191],v[124:127],v[100:103],a[188:191]\nM4 a[196:199],v[124:127],a[200:203],a[196:199]\nSW vmcnt(11) lgkmcnt(0)\ns_barrier\n"
"SW vmcnt(8)\nM4 a[0:3],v[40:43],v[8:11],a[0:3]\nDT v[104:105],v164 offset:49152\nM4 a[4:7],v[40:43],v[12:15],a[4:7]\nDT v[106:107],v164 offset:50176\nSM s84,s84\ns_and_b32 s85,s85,0xffff\nSM s86,3456\n"
"SM s87,0x20000\ns_lshl_b32 s34,s82,7\ns_lshl_b32 s35,s81,6\nSA s34,s34,s35\nM4 a[8:11],v[40:43],v[16:19],a[8:11]\nM4 a[12:15],v[40:43],v[20:23],a[12:15]\nDR v[72:75],v145 offset:0\nM4 a[16:19],v[40:43],v[24:27],a[16:19]\n"
"M4 a[20:23],v[40:43],v[28:31],a[20:23]\nDR v[76:79],v145 offset:2048\nM4 a[24:27],v[40:43],v[32:35],a[24:27]\nM4 a[28:31],v[40:43],v[36:39],a[28:31]\nDR v[80:83],v145 offset:4096\nM4 a[32:35],v[44:47],v[8:11],a[32:35]\nM4 a[36:39],v[44:47],v[12:15],a[36:39]\nDR v[84:87],v145 offset:6144\n"
"M4 a[40:43],v[44:47],v[16:19],a[40:43]\nDR v[88:91],v145 offset:8192\nv_lshrrev_b32 v228,4,v200\nM4 a[44:47],v[44:47],v[20:23],a[44:47]\nM4 a[48:51],v[44:47],v[24:27],a[48:51]\nDR v[92:95],v145 offset:10240\nM4 a[52:55],v[44:47],v[28:31],a[52:55]\nM4 a[56:59],v[44:47],v[32:35],a[56:59]\n"
"DR v[96:99],v145 offset:12288\nM4 a[60:63],v[44:47],v[36:39],a[60:63]\nM4 a[64:67],v[48:51],v[8:11],a[64:67]\nDR v[100:103],v145 offset:14336\nM4 a[68:71],v[48:51],v[12:15],a[68:71]\nM4 a[72:75],v[48:51],v[16:19],a[72:75]\nDT v[108:109],v165 offset:49152\nM4 a[76:79],v[48:51],v[20:23],a[76:79]\n"
"DT v[110:111],v165 offset:50176\nM4 a[80:83],v[48:51],v[24:27],a[80:83]\nVL v228,3,v228\nM4 a[84:87],v[48:51],v[28:31],a[84:87]\nDT v[112:113],v166 offset:49152\nM4 a[88:91],v[48:51],v[32:35],a[88:91]\nM4 a[92:95],v[48:51],v[36:39],a[92:95]\nDT v[114:115],v166 offset:50176\n"
"M4 a[96:99],v[52:55],v[8:11],a[96:99]\nM4 a[100:103],v[52:55],v[12:15],a[100:103]\nDT v[116:117],v167 offset:49152\nM4 a[104:107],v[52:55],v[16:19],a[104:107]\nM4 a[108:111],v[52:55],v[20:23],a[108:111]\nDT v[118:119],v167 offset:50176\nM4 a[112:115],v[52:55],v[24:27],a[112:115]\nDT v[120:121],v168 offset:49152\n"
"M4 a[116:119],v[52:55],v[28:31],a[116:119]\nbuffer_load_dwordx2 v[220:221],v228,s[84:87],s34 offen offset:0\nM4 a[120:123],v[52:55],v[32:35],a[120:123]\nDT v[122:123],v168 offset:49664\nM4 a[124:127],v[52:55],v[36:39],a[124:127]\nM4 a[128:131],v[56:59],v[8:11],a[128:131]\nDT v[124:125],v169 offset:49152\nM4 a[132:135],v[56:59],v[12:15],a[132:135]\n"
"M4 a[136:139],v[56:59],v[16:19],a[136:139]\nDT v[126:127],v169 offset:49664\nM4 a[140:143],v[56:59],v[20:23],a[140:143]\nM4 a[144:147],v[56:59],v[24:27],a[144:147]\nM4 a[148:151],v[56:59],v[28:31],a[148:151]\nM4 a[152:155],v[56:59],v[32:35],a[152:155]\nM4 a[156:159],v[56:59],v[36:39],a[156:159]\nbuffer_load_dwordx2 v[222:223],v228,s[84:87],s34 offen offset:32\n"
"M4 a[192:195],v[56:59],a[200:203],a[192:195]\nM4 a[160:163],v[60:63],v[8:11],a[160:163]\nM4 a[164:167],v[60:63],v[12:15],a[164:167]\nM4 a[168:171],v[60:63],v[16:19],a[168:171]\nM4 a[172:175],v[60:63],v[20:23],a[172:175]\nM4 a[176:179],v[60:63],v[24:27],a[176:179]\nM4 a[180:183],v[60:63],v[28:31],a[180:183]\nM4 a[184:187],v[60:63],v[32:35],a[184:187]\n"
"M4 a[188:191],v[60:63],v[36:39],a[188:191]\nM4 a[196:199],v[60:63],a[200:203],a[196:199]\nSW lgkmcnt(0)\ns_barrier\ns_lshl_b32 s71,s88,3\nSA s71,s71,63971328\ns_cmp_eq_u32 s89,0\ns_cbranch_scc0 P1_kvsV\n"
"v_mov_b32 v170,s90\nv_mov_b32 v171,s91\nv_mov_b32 v172,0\nVU v172,s71,v172\nv_mov_b32 v173,s93\nv_add_co_u32 v172,vcc,s92,v172\ns_nop 1\nv_addc_co_u32 v173,vcc,0,v173,vcc\n"
"s_mov_b64 exec,1\nglobal_store_dwordx2 v[172:173],v[170:171],off sc1\ns_mov_b64 exec,-1\nP1_kvsV:\nM4 a[0:3],v[104:107],v[72:75],a[0:3]\nVU v144,0,v146\nM4 a[4:7],v[104:107],v[76:79],a[4:7]\nVU v145,0,v147\n"
"s_and_b32 s34,s88,7\ns_lshl_b32 s34,s34,3\ns_lshr_b32 s35,s88,6\ns_lshl_b32 s35,s35,9\nSA s34,s34,s35\nSA s34,s34,63971328\nVN v224,7,v200\nVL v224,6,v224\n"
"VU v224,s34,v224\nv_mov_b32 v225,s93\nv_add_co_u32 v224,vcc,s92,v224\ns_nop 1\nv_addc_co_u32 v225,vcc,0,v225,vcc\nM4 a[8:11],v[104:107],v[80:83],a[8:11]\nM4 a[12:15],v[104:107],v[84:87],a[12:15]\nM4 a[16:19],v[104:107],v[88:91],a[16:19]\n"
"M4 a[20:23],v[104:107],v[92:95],a[20:23]\nM4 a[24:27],v[104:107],v[96:99],a[24:27]\nM4 a[28:31],v[104:107],v[100:103],a[28:31]\nM4 a[32:35],v[108:111],v[72:75],a[32:35]\nM4 a[36:39],v[108:111],v[76:79],a[36:39]\nM4 a[40:43],v[108:111],v[80:83],a[40:43]\nM4 a[44:47],v[108:111],v[84:87],a[44:47]\nM4 a[48:51],v[108:111],v[88:91],a[48:51]\n"
"M4 a[52:55],v[108:111],v[92:95],a[52:55]\nM4 a[56:59],v[108:111],v[96:99],a[56:59]\nM4 a[60:63],v[108:111],v[100:103],a[60:63]\nM4 a[64:67],v[112:115],v[72:75],a[64:67]\nM4 a[68:71],v[112:115],v[76:79],a[68:71]\nM4 a[72:75],v[112:115],v[80:83],a[72:75]\nM4 a[76:79],v[112:115],v[84:87],a[76:79]\nM4 a[80:83],v[112:115],v[88:91],a[80:83]\n"
"M4 a[84:87],v[112:115],v[92:95],a[84:87]\nM4 a[88:91],v[112:115],v[96:99],a[88:91]\nM4 a[92:95],v[112:115],v[100:103],a[92:95]\nM4 a[96:99],v[116:119],v[72:75],a[96:99]\nM4 a[100:103],v[116:119],v[76:79],a[100:103]\nglobal_load_dwordx2 v[226:227],v[224:225],off sc1\nM4 a[104:107],v[116:119],v[80:83],a[104:107]\nM4 a[108:111],v[116:119],v[84:87],a[108:111]\n"
"M4 a[112:115],v[116:119],v[88:91],a[112:115]\nM4 a[116:119],v[116:119],v[92:95],a[116:119]\nM4 a[120:123],v[116:119],v[96:99],a[120:123]\nM4 a[124:127],v[116:119],v[100:103],a[124:127]\nM4 a[128:131],v[120:123],v[72:75],a[128:131]\nM4 a[132:135],v[120:123],v[76:79],a[132:135]\nM4 a[136:139],v[120:123],v[80:83],a[136:139]\nM4 a[140:143],v[120:123],v[84:87],a[140:143]\n"
"M4 a[144:147],v[120:123],v[88:91],a[144:147]\nM4 a[148:151],v[120:123],v[92:95],a[148:151]\nM4 a[152:155],v[120:123],v[96:99],a[152:155]\nM4 a[156:159],v[120:123],v[100:103],a[156:159]\nM4 a[192:195],v[120:123],a[200:203],a[192:195]\nM4 a[160:163],v[124:127],v[72:75],a[160:163]\nM4 a[164:167],v[124:127],v[76:79],a[164:167]\nM4 a[168:171],v[124:127],v[80:83],a[168:171]\n"
"M4 a[172:175],v[124:127],v[84:87],a[172:175]\nM4 a[176:179],v[124:127],v[88:91],a[176:179]\nM4 a[180:183],v[124:127],v[92:95],a[180:183]\nM4 a[184:187],v[124:127],v[96:99],a[184:187]\nM4 a[188:191],v[124:127],v[100:103],a[188:191]\nM4 a[196:199],v[124:127],a[200:203],a[196:199]\nSW lgkmcnt(0)\nP1_t2d:\n"
"VL v161,4,v200\ns_and_b32 s66,s88,7\ns_lshr_b32 s67,s88,3\ns_and_b32 s55,s67,7\ns_lshr_b32 s67,s67,3\ns_lshl_b32 s66,s66,2\nSA s54,s66,s67\ns_lshr_b32 s52,s89,1\n"
"s_and_b32 s53,s89,1\ns_sub_u32 s74,15,s55\nSA s57,s55,1\ns_lshl_b32 s57,s57,1\ns_sub_u32 s69,16,s55\ns_lshl_b32 s69,s69,1\ns_sub_u32 s56,s69,s57\ns_lshl_b32 s66,s54,19\n"
"s_lshl_b32 s68,s69,14\nSA s67,s66,6291456\nSA s36,s92,s67\ns_addc_u32 s37,s93,0\ns_and_b32 s37,s37,0xffff\nSM s38,s68\nSM s39,0x20000\nSA s67,s66,23068672\n"
"SA s40,s92,s67\ns_addc_u32 s41,s93,0\ns_and_b32 s41,s41,0xffff\nSM s42,s68\nSM s43,0x20000\ns_lshl_b32 s78,s89,12\nSM s59,s78\nSM s60,s78\n"
"SM s61,s78\ns_or_b32 s62,s78,0x4000\ns_barrier\nSW vmcnt(0)\nSM s66,0\nTR_kv:\nv_xor_b32 v226,s90,v226\nv_xor_b32 v227,s91,v227\n"
"v_or_b32 v226,v226,v227\nv_cmp_ne_u32 vcc,0,v226\ns_nop 4\ns_cbranch_vccz TR_kv_d\nSA s66,s66,1\ns_cmp_ge_u32 s66,200000\ns_cbranch_scc1 TR_kv_d\ns_sleep 2\n"
"global_load_dwordx2 v[226:227],v[224:225],off sc1\nSW vmcnt(0)\ns_branch TR_kv\nTR_kv_d:\ns_and_b32 m0,s60,0x1ffff\ns_nop 0\nBL v161,s[36:39],s59 offen offset:0 sc1 lds\nBL v161,s[36:39],s59 offen offset:1024 sc1 lds\n"
"BL v161,s[36:39],s59 offen offset:2048 sc1 lds\nBL v161,s[36:39],s59 offen offset:3072 sc1 lds\nSA s59,s59,16384\nSA s60,s60,0x8000\ns_and_b32 m0,s62,0x1ffff\ns_nop 0\nBL v161,s[40:43],s61 offen offset:0 sc1 lds\nBL v161,s[40:43],s61 offen offset:1024 sc1 lds\n"
"BL v161,s[40:43],s61 offen offset:2048 sc1 lds\nBL v161,s[40:43],s61 offen offset:3072 sc1 lds\nSA s61,s61,16384\nSA s62,s62,0x8000\ns_and_b32 m0,s60,0x1ffff\ns_nop 0\nBL v161,s[36:39],s59 offen offset:0 sc1 lds\nBL v161,s[36:39],s59 offen offset:1024 sc1 lds\n"
"BL v161,s[36:39],s59 offen offset:2048 sc1 lds\nBL v161,s[36:39],s59 offen offset:3072 sc1 lds\nSA s59,s59,16384\nSA s60,s60,0x8000\ns_and_b32 m0,s62,0x1ffff\ns_nop 0\nBL v161,s[40:43],s61 offen offset:0 sc1 lds\nBL v161,s[40:43],s61 offen offset:1024 sc1 lds\n"
"BL v161,s[40:43],s61 offen offset:2048 sc1 lds\nBL v161,s[40:43],s61 offen offset:3072 sc1 lds\nSA s61,s61,16384\nSA s62,s62,0x8000\ns_and_b32 m0,s60,0x1ffff\ns_nop 0\nBL v161,s[36:39],s59 offen offset:0 sc1 lds\nBL v161,s[36:39],s59 offen offset:1024 sc1 lds\n"
"BL v161,s[36:39],s59 offen offset:2048 sc1 lds\nBL v161,s[36:39],s59 offen offset:3072 sc1 lds\nSA s59,s59,16384\nSA s60,s60,0x8000\ns_load_dwordx2 s[96:97],s[94:95],0x28\ns_load_dwordx2 s[98:99],s[94:95],0x40\nSW lgkmcnt(0)\nSM s34,s98\n"
"SM s35,s99\nSM s96,s96\ns_and_b32 s97,s97,0xffff\nSM s98,5242880\nSM s99,0x20000\nbuffer_load_dword v14,v202,s[96:99],0 offen\nSM s96,s34\ns_and_b32 s97,s35,0xffff\n"
"SM s98,2211840\nSM s99,0x20000\nbuffer_load_dword v15,v203,s[96:99],0 offen\nv_lshrrev_b32 v170,4,v200\nVN v171,15,v200\nv_lshrrev_b32 v172,1,v170\nVL v201,10,v172\nVN v172,1,v170\n"
"v_lshl_add_u32 v201,v172,9,v201\nv_lshl_add_u32 v201,v171,4,v201\nSM s35,0x20000\nSM s34,0x4000\ns_cmp_eq_u32 s80,1\ns_cselect_b32 s35,0x14000,s35\ns_cselect_b32 s34,0x8000,s34\nVU v201,s35,v201\n"
"s_lshl_b32 s35,s81,12\nVU v201,s35,v201\nVU v203,s34,v201\nSM s34,0x3e0293ee\nSM s35,0x3e0293ee\nAR v104,a0\nAR v105,a1\nAR v106,a2\n"
"AR v107,a3\nAR v108,a32\nAR v109,a33\nAR v110,a34\nAR v111,a35\nPM v[104:105],v[104:105],s[34:35]\nPM v[106:107],v[106:107],s[34:35]\nPM v[108:109],v[108:109],s[34:35]\n"
"PM v[110:111],v[110:111],s[34:35]\nCV v72,v104,v105\nCV v73,v106,v107\nCV v74,v108,v109\nCV v75,v110,v111\nDW v201,v[72:75] offset:0\nAR v112,a64\nAR v113,a65\n"
"AR v114,a66\nAR v115,a67\nAR v116,a96\nAR v117,a97\nAR v118,a98\nAR v119,a99\nPM v[112:113],v[112:113],s[34:35]\nPM v[114:115],v[114:115],s[34:35]\n"
"PM v[116:117],v[116:117],s[34:35]\nPM v[118:119],v[118:119],s[34:35]\nCV v76,v112,v113\nCV v77,v114,v115\nCV v78,v116,v117\nCV v79,v118,v119\nDW v201,v[76:79] offset:2048\nAR v120,a4\n"
"AR v121,a5\nAR v122,a6\nAR v123,a7\nAR v124,a36\nAR v125,a37\nAR v126,a38\nAR v127,a39\nPM v[120:121],v[120:121],s[34:35]\n"
"PM v[122:123],v[122:123],s[34:35]\nPM v[124:125],v[124:125],s[34:35]\nPM v[126:127],v[126:127],s[34:35]\nCV v80,v120,v121\nCV v81,v122,v123\nCV v82,v124,v125\nCV v83,v126,v127\nDW v201,v[80:83] offset:256\n"
"AR v128,a68\nAR v129,a69\nAR v130,a70\nAR v131,a71\nAR v132,a100\nAR v133,a101\nAR v134,a102\nAR v135,a103\n"
"PM v[128:129],v[128:129],s[34:35]\nPM v[130:131],v[130:131],s[34:35]\nPM v[132:133],v[132:133],s[34:35]\nPM v[134:135],v[134:135],s[34:35]\nCV v84,v128,v129\nCV v85,v130,v131\nCV v86,v132,v133\nCV v87,v134,v135\n"
"DW v201,v[84:87] offset:2304\nAR v104,a8\nAR v105,a9\nAR v106,a10\nAR v107,a11\nAR v108,a40\nAR v109,a41\nAR v110,a42\n"
"AR v111,a43\nPM v[104:105],v[104:105],s[34:35]\nPM v[106:107],v[106:107],s[34:35]\nPM v[108:109],v[108:109],s[34:35]\nPM v[110:111],v[110:111],s[34:35]\nCV v88,v104,v105\nCV v89,v106,v107\nCV v90,v108,v109\n"
"CV v91,v110,v111\nDW v201,v[88:91] offset:8192\nAR v112,a72\nAR v113,a73\nAR v114,a74\nAR v115,a75\nAR v116,a104\nAR v117,a105\n"
"AR v118,a106\nAR v119,a107\nPM v[112:113],v[112:113],s[34:35]\nPM v[114:115],v[114:115],s[34:35]\nPM v[116:117],v[116:117],s[34:35]\nPM v[118:119],v[118:119],s[34:35]\nCV v92,v112,v113\nCV v93,v114,v115\n"
"CV v94,v116,v117\nCV v95,v118,v119\nDW v201,v[92:95] offset:10240\nAR v120,a12\nAR v121,a13\nAR v122,a14\nAR v123,a15\nAR v124,a44\n"
"AR v125,a45\nAR v126,a46\nAR v127,a47\nPM v[120:121],v[120:121],s[34:35]\nPM v[122:123],v[122:123],s[34:35]\nPM v[124:125],v[124:125],s[34:35]\nPM v[126:127],v[126:127],s[34:35]\nCV v96,v120,v121\n"
"CV v97,v122,v123\nCV v98,v124,v125\nCV v99,v126,v127\nDW v201,v[96:99] offset:8448\nAR v128,a76\nAR v129,a77\nAR v130,a78\nAR v131,a79\n"
"AR v132,a108\nAR v133,a109\nAR v134,a110\nAR v135,a111\nPM v[128:129],v[128:129],s[34:35]\nPM v[130:131],v[130:131],s[34:35]\nPM v[132:133],v[132:133],s[34:35]\nPM v[134:135],v[134:135],s[34:35]\n"
"CV v100,v128,v129\nCV v101,v130,v131\nCV v102,v132,v133\nCV v103,v134,v135\nDW v201,v[100:103] offset:10496\nAR v104,a16\nAR v105,a17\nAR v106,a18\n"
"AR v107,a19\nAR v108,a48\nAR v109,a49\nAR v110,a50\nAR v111,a51\nPM v[104:105],v[104:105],s[34:35]\nPM v[106:107],v[106:107],s[34:35]\nPM v[108:109],v[108:109],s[34:35]\n"
"PM v[110:111],v[110:111],s[34:35]\nCV v72,v104,v105\nCV v73,v106,v107\nCV v74,v108,v109\nCV v75,v110,v111\nDW v203,v[72:75] offset:0\nAR v112,a80\nAR v113,a81\n"
"AR v114,a82\nAR v115,a83\nAR v116,a112\nAR v117,a113\nAR v118,a114\nAR v119,a115\nPM v[112:113],v[112:113],s[34:35]\nPM v[114:115],v[114:115],s[34:35]\n"
"PM v[116:117],v[116:117],s[34:35]\nPM v[118:119],v[118:119],s[34:35]\nCV v76,v112,v113\nCV v77,v114,v115\nCV v78,v116,v117\nCV v79,v118,v119\nDW v203,v[76:79] offset:2048\nAR v120,a20\n"
"AR v121,a21\nAR v122,a22\nAR v123,a23\nAR v124,a52\nAR v125,a53\nAR v126,a54\nAR v127,a55\nPM v[120:121],v[120:121],s[34:35]\n"
"PM v[122:123],v[122:123],s[34:35]\nPM v[124:125],v[124:125],s[34:35]\nPM v[126:127],v[126:127],s[34:35]\nCV v80,v120,v121\nCV v81,v122,v123\nCV v82,v124,v125\nCV v83,v126,v127\nDW v203,v[80:83] offset:256\n"
"AR v128,a84\nAR v129,a85\nAR v130,a86\nAR v131,a87\nAR v132,a116\nAR v133,a117\nAR v134,a118\nAR v135,a119\n"
"PM v[128:129],v[128:129],s[34:35]\nPM v[130:131],v[130:131],s[34:35]\nPM v[132:133],v[132:133],s[34:35]\nPM v[134:135],v[134:135],s[34:35]\nCV v84,v128,v129\nCV v85,v130,v131\nCV v86,v132,v133\nCV v87,v134,v135\n"
"DW v203,v[84:87] offset:2304\nAR v104,a24\nAR v105,a25\nAR v106,a26\nAR v107,a27\nAR v108,a56\nAR v109,a57\nAR v110,a58\n"
"AR v111,a59\nPM v[104:105],v[104:105],s[34:35]\nPM v[106:107],v[106:107],s[34:35]\nPM v[108:109],v[108:109],s[34:35]\nPM v[110:111],v[110:111],s[34:35]\nCV v88,v104,v105\nCV v89,v106,v107\nCV v90,v108,v109\n"
"CV v91,v110,v111\nDW v203,v[88:91] offset:8192\nAR v112,a88\nAR v113,a89\nAR v114,a90\nAR v115,a91\nAR v116,a120\nAR v117,a121\n"
"AR v118,a122\nAR v119,a123\nPM v[112:113],v[112:113],s[34:35]\nPM v[114:115],v[114:115],s[34:35]\nPM v[116:117],v[116:117],s[34:35]\nPM v[118:119],v[118:119],s[34:35]\nCV v92,v112,v113\nCV v93,v114,v115\n"
"CV v94,v116,v117\nCV v95,v118,v119\nDW v203,v[92:95] offset:10240\nAR v120,a28\nAR v121,a29\nAR v122,a30\nAR v123,a31\nAR v124,a60\n"
"AR v125,a61\nAR v126,a62\nAR v127,a63\nPM v[120:121],v[120:121],s[34:35]\nPM v[122:123],v[122:123],s[34:35]\nPM v[124:125],v[124:125],s[34:35]\nPM v[126:127],v[126:127],s[34:35]\nCV v96,v120,v121\n"
"CV v97,v122,v123\nCV v98,v124,v125\nCV v99,v126,v127\nDW v203,v[96:99] offset:8448\nAR v128,a92\nAR v129,a93\nAR v130,a94\nAR v131,a95\n"
"AR v132,a124\nAR v133,a125\nAR v134,a126\nAR v135,a127\nPM v[128:129],v[128:129],s[34:35]\nPM v[130:131],v[130:131],s[34:35]\nPM v[132:133],v[132:133],s[34:35]\nPM v[134:135],v[134:135],s[34:35]\n"
"CV v100,v128,v129\nCV v101,v130,v131\nCV v102,v132,v133\nCV v103,v134,v135\nDW v203,v[100:103] offset:10496\nVL v36,16,v220\nVN v37,0xffff0000,v220\nVL v38,16,v221\n"
"VN v39,0xffff0000,v221\nVL v40,16,v222\nVN v41,0xffff0000,v222\nVL v42,16,v223\nVN v43,0xffff0000,v223\nAR v44,a192\nAR v45,a193\nAR v46,a194\n"
"AR v47,a195\nAR v48,a196\nAR v49,a197\nAR v50,a198\nAR v51,a199\nv_mov_b32 v52,0xc0135761\nv_mov_b32 v53,0xc0135761\nv_mov_b32 v54,0xbdd2d3e8\n"
"v_mov_b32 v55,0xbdd2d3e8\nv_mov_b32 v56,0x3f800000\nv_mov_b32 v57,0x3f800000\ns_sub_u32 s34,15,s83\ns_cmp_eq_u32 s80,0\ns_cselect_b32 s34,s34,s83\ns_mul_i32 s34,s34,1456\ns_lshl_b32 s35,s82,1\n"
"SA s34,s34,s35\nSA s34,s34,s81\nSA s34,s34,128\ns_lshl_b32 s34,s34,10\nSM s35,39845888\nSA s84,s92,s35\ns_addc_u32 s85,s93,0\ns_and_b32 s85,s85,0xffff\n"
"SM s86,23855104\nSM s87,0x20000\ns_cmp_ge_u32 s82,27\ns_cselect_b32 s86,0,s86\nVL v58,4,v200\nAR v72,a128\nAR v73,a129\nAR v74,a130\n"
"AR v75,a131\nAR v76,a160\nAR v77,a161\nAR v78,a162\nAR v79,a163\nPF v[80:81],v[204:205],v[44:45],v[36:37] op_sel:[1,0,0] op_sel_hi:[1,1,1] neg_lo:[1,0,0] neg_hi:[1,0,0]\nPF v[82:83],v[204:205],v[46:47],v[38:39] op_sel:[1,0,0] op_sel_hi:[1,1,1] neg_lo:[1,0,0] neg_hi:[1,0,0]\nPF v[84:85],v[204:205],v[48:49],v[40:41] op_sel:[1,0,0] op_sel_hi:[1,1,1] neg_lo:[1,0,0] neg_hi:[1,0,0]\n"
"PF v[86:87],v[204:205],v[50:51],v[42:43] op_sel:[1,0,0] op_sel_hi:[1,1,1] neg_lo:[1,0,0] neg_hi:[1,0,0]\nPF v[72:73],v[204:205],v[72:73],v[80:81] op_sel_hi:[0,1,1]\nPF v[74:75],v[204:205],v[74:75],v[82:83] op_sel_hi:[0,1,1]\nPF v[76:77],v[204:205],v[76:77],v[84:85] op_sel_hi:[0,1,1]\nPF v[78:79],v[204:205],v[78:79],v[86:87] op_sel_hi:[0,1,1]\nPM v[80:81],v[72:73],v[72:73]\nPM v[82:83],v[74:75],v[74:75]\nPM v[84:85],v[76:77],v[76:77]\n"
"PM v[86:87],v[78:79],v[78:79]\nPF v[80:81],v[80:81],v[54:55],v[52:53]\nPF v[82:83],v[82:83],v[54:55],v[52:53]\nPF v[84:85],v[84:85],v[54:55],v[52:53]\nPF v[86:87],v[86:87],v[54:55],v[52:53]\nPM v[80:81],v[80:81],v[72:73]\nPM v[82:83],v[82:83],v[74:75]\nPM v[84:85],v[84:85],v[76:77]\n"
"PM v[86:87],v[86:87],v[78:79]\nEX v80,v80\nEX v81,v81\nEX v82,v82\nEX v83,v83\nEX v84,v84\nEX v85,v85\nEX v86,v86\n"
"EX v87,v87\nv_pk_add_f32 v[80:81],v[80:81],v[56:57]\nv_pk_add_f32 v[82:83],v[82:83],v[56:57]\nv_pk_add_f32 v[84:85],v[84:85],v[56:57]\nv_pk_add_f32 v[86:87],v[86:87],v[56:57]\nRC v80,v80\nRC v81,v81\nRC v82,v82\n"
"RC v83,v83\nRC v84,v84\nRC v85,v85\nRC v86,v86\nRC v87,v87\nPM v[72:73],v[72:73],v[80:81]\nPM v[74:75],v[74:75],v[82:83]\nPM v[76:77],v[76:77],v[84:85]\n"
"PM v[78:79],v[78:79],v[86:87]\nCV v60,v72,v73\nCV v61,v74,v75\nCV v62,v76,v77\nCV v63,v78,v79\nBS v[60:63],v58,s[84:87],s34 offen sc1\nSA s34,s34,186368\nAR v88,a132\n"
"AR v89,a133\nAR v90,a134\nAR v91,a135\nAR v92,a164\nAR v93,a165\nAR v94,a166\nAR v95,a167\nPF v[96:97],v[206:207],v[44:45],v[36:37] op_sel:[1,0,0] op_sel_hi:[1,1,1] neg_lo:[1,0,0] neg_hi:[1,0,0]\n"
"PF v[98:99],v[206:207],v[46:47],v[38:39] op_sel:[1,0,0] op_sel_hi:[1,1,1] neg_lo:[1,0,0] neg_hi:[1,0,0]\nPF v[100:101],v[206:207],v[48:49],v[40:41] op_sel:[1,0,0] op_sel_hi:[1,1,1] neg_lo:[1,0,0] neg_hi:[1,0,0]\nPF v[102:103],v[206:207],v[50:51],v[42:43] op_sel:[1,0,0] op_sel_hi:[1,1,1] neg_lo:[1,0,0] neg_hi:[1,0,0]\nPF v[88:89],v[206:207],v[88:89],v[96:97] op_sel_hi:[0,1,1]\nPF v[90:91],v[206:207],v[90:91],v[98:99] op_sel_hi:[0,1,1]\nPF v[92:93],v[206:207],v[92:93],v[100:101] op_sel_hi:[0,1,1]\nPF v[94:95],v[206:207],v[94:95],v[102:103] op_sel_hi:[0,1,1]\nPM v[96:97],v[88:89],v[88:89]\n"
"PM v[98:99],v[90:91],v[90:91]\nPM v[100:101],v[92:93],v[92:93]\nPM v[102:103],v[94:95],v[94:95]\nPF v[96:97],v[96:97],v[54:55],v[52:53]\nPF v[98:99],v[98:99],v[54:55],v[52:53]\nPF v[100:101],v[100:101],v[54:55],v[52:53]\nPF v[102:103],v[102:103],v[54:55],v[52:53]\nPM v[96:97],v[96:97],v[88:89]\n"
"PM v[98:99],v[98:99],v[90:91]\nPM v[100:101],v[100:101],v[92:93]\nPM v[102:103],v[102:103],v[94:95]\nEX v96,v96\nEX v97,v97\nEX v98,v98\nEX v99,v99\nEX v100,v100\n"
"EX v101,v101\nEX v102,v102\nEX v103,v103\nv_pk_add_f32 v[96:97],v[96:97],v[56:57]\nv_pk_add_f32 v[98:99],v[98:99],v[56:57]\nv_pk_add_f32 v[100:101],v[100:101],v[56:57]\nv_pk_add_f32 v[102:103],v[102:103],v[56:57]\nRC v96,v96\n"
"RC v97,v97\nRC v98,v98\nRC v99,v99\nRC v100,v100\nRC v101,v101\nRC v102,v102\nRC v103,v103\nPM v[88:89],v[88:89],v[96:97]\n"
"PM v[90:91],v[90:91],v[98:99]\nPM v[92:93],v[92:93],v[100:101]\nPM v[94:95],v[94:95],v[102:103]\nCV v64,v88,v89\nCV v65,v90,v91\nCV v66,v92,v93\nCV v67,v94,v95\nBS v[64:67],v58,s[84:87],s34 offen sc1\n"
"SA s34,s34,186368\nAR v72,a136\nAR v73,a137\nAR v74,a138\nAR v75,a139\nAR v76,a168\nAR v77,a169\nAR v78,a170\n"
"AR v79,a171\nPF v[80:81],v[208:209],v[44:45],v[36:37] op_sel:[1,0,0] op_sel_hi:[1,1,1] neg_lo:[1,0,0] neg_hi:[1,0,0]\nPF v[82:83],v[208:209],v[46:47],v[38:39] op_sel:[1,0,0] op_sel_hi:[1,1,1] neg_lo:[1,0,0] neg_hi:[1,0,0]\nPF v[84:85],v[208:209],v[48:49],v[40:41] op_sel:[1,0,0] op_sel_hi:[1,1,1] neg_lo:[1,0,0] neg_hi:[1,0,0]\nPF v[86:87],v[208:209],v[50:51],v[42:43] op_sel:[1,0,0] op_sel_hi:[1,1,1] neg_lo:[1,0,0] neg_hi:[1,0,0]\nPF v[72:73],v[208:209],v[72:73],v[80:81] op_sel_hi:[0,1,1]\nPF v[74:75],v[208:209],v[74:75],v[82:83] op_sel_hi:[0,1,1]\nPF v[76:77],v[208:209],v[76:77],v[84:85] op_sel_hi:[0,1,1]\n"
"PF v[78:79],v[208:209],v[78:79],v[86:87] op_sel_hi:[0,1,1]\nPM v[80:81],v[72:73],v[72:73]\nPM v[82:83],v[74:75],v[74:75]\nPM v[84:85],v[76:77],v[76:77]\nPM v[86:87],v[78:79],v[78:79]\nPF v[80:81],v[80:81],v[54:55],v[52:53]\nPF v[82:83],v[82:83],v[54:55],v[52:53]\nPF v[84:85],v[84:85],v[54:55],v[52:53]\n"
"PF v[86:87],v[86:87],v[54:55],v[52:53]\nPM v[80:81],v[80:81],v[72:73]\nPM v[82:83],v[82:83],v[74:75]\nPM v[84:85],v[84:85],v[76:77]\nPM v[86:87],v[86:87],v[78:79]\nEX v80,v80\nEX v81,v81\nEX v82,v82\n"
"EX v83,v83\nEX v84,v84\nEX v85,v85\nEX v86,v86\nEX v87,v87\nv_pk_add_f32 v[80:81],v[80:81],v[56:57]\nv_pk_add_f32 v[82:83],v[82:83],v[56:57]\nv_pk_add_f32 v[84:85],v[84:85],v[56:57]\n"
"v_pk_add_f32 v[86:87],v[86:87],v[56:57]\nRC v80,v80\nRC v81,v81\nRC v82,v82\nRC v83,v83\nRC v84,v84\nRC v85,v85\nRC v86,v86\n"
"RC v87,v87\nPM v[72:73],v[72:73],v[80:81]\nPM v[74:75],v[74:75],v[82:83]\nPM v[76:77],v[76:77],v[84:85]\nPM v[78:79],v[78:79],v[86:87]\nCV v60,v72,v73\nCV v61,v74,v75\nCV v62,v76,v77\n"
"CV v63,v78,v79\nBS v[60:63],v58,s[84:87],s34 offen sc1\nSA s34,s34,186368\nAR v88,a140\nAR v89,a141\nAR v90,a142\nAR v91,a143\nAR v92,a172\n"
"AR v93,a173\nAR v94,a174\nAR v95,a175\nPF v[96:97],v[210:211],v[44:45],v[36:37] op_sel:[1,0,0] op_sel_hi:[1,1,1] neg_lo:[1,0,0] neg_hi:[1,0,0]\nPF v[98:99],v[210:211],v[46:47],v[38:39] op_sel:[1,0,0] op_sel_hi:[1,1,1] neg_lo:[1,0,0] neg_hi:[1,0,0]\nPF v[100:101],v[210:211],v[48:49],v[40:41] op_sel:[1,0,0] op_sel_hi:[1,1,1] neg_lo:[1,0,0] neg_hi:[1,0,0]\nPF v[102:103],v[210:211],v[50:51],v[42:43] op_sel:[1,0,0] op_sel_hi:[1,1,1] neg_lo:[1,0,0] neg_hi:[1,0,0]\nPF v[88:89],v[210:211],v[88:89],v[96:97] op_sel_hi:[0,1,1]\n"
"PF v[90:91],v[210:211],v[90:91],v[98:99] op_sel_hi:[0,1,1]\nPF v[92:93],v[210:211],v[92:93],v[100:101] op_sel_hi:[0,1,1]\nPF v[94:95],v[210:211],v[94:95],v[102:103] op_sel_hi:[0,1,1]\nPM v[96:97],v[88:89],v[88:89]\nPM v[98:99],v[90:91],v[90:91]\nPM v[100:101],v[92:93],v[92:93]\nPM v[102:103],v[94:95],v[94:95]\nPF v[96:97],v[96:97],v[54:55],v[52:53]\n"
"PF v[98:99],v[98:99],v[54:55],v[52:53]\nPF v[100:101],v[100:101],v[54:55],v[52:53]\nPF v[102:103],v[102:103],v[54:55],v[52:53]\nPM v[96:97],v[96:97],v[88:89]\nPM v[98:99],v[98:99],v[90:91]\nPM v[100:101],v[100:101],v[92:93]\nPM v[102:103],v[102:103],v[94:95]\nEX v96,v96\n"
"EX v97,v97\nEX v98,v98\nEX v99,v99\nEX v100,v100\nEX v101,v101\nEX v102,v102\nEX v103,v103\nv_pk_add_f32 v[96:97],v[96:97],v[56:57]\n"
"v_pk_add_f32 v[98:99],v[98:99],v[56:57]\nv_pk_add_f32 v[100:101],v[100:101],v[56:57]\nv_pk_add_f32 v[102:103],v[102:103],v[56:57]\nRC v96,v96\nRC v97,v97\nRC v98,v98\nRC v99,v99\nRC v100,v100\n"
"RC v101,v101\nRC v102,v102\nRC v103,v103\nPM v[88:89],v[88:89],v[96:97]\nPM v[90:91],v[90:91],v[98:99]\nPM v[92:93],v[92:93],v[100:101]\nPM v[94:95],v[94:95],v[102:103]\nCV v64,v88,v89\n"
"CV v65,v90,v91\nCV v66,v92,v93\nCV v67,v94,v95\nBS v[64:67],v58,s[84:87],s34 offen sc1\nSA s34,s34,186368\nAR v72,a144\nAR v73,a145\nAR v74,a146\n"
"AR v75,a147\nAR v76,a176\nAR v77,a177\nAR v78,a178\nAR v79,a179\nPF v[80:81],v[212:213],v[44:45],v[36:37] op_sel:[1,0,0] op_sel_hi:[1,1,1] neg_lo:[1,0,0] neg_hi:[1,0,0]\nPF v[82:83],v[212:213],v[46:47],v[38:39] op_sel:[1,0,0] op_sel_hi:[1,1,1] neg_lo:[1,0,0] neg_hi:[1,0,0]\nPF v[84:85],v[212:213],v[48:49],v[40:41] op_sel:[1,0,0] op_sel_hi:[1,1,1] neg_lo:[1,0,0] neg_hi:[1,0,0]\n"
"PF v[86:87],v[212:213],v[50:51],v[42:43] op_sel:[1,0,0] op_sel_hi:[1,1,1] neg_lo:[1,0,0] neg_hi:[1,0,0]\nPF v[72:73],v[212:213],v[72:73],v[80:81] op_sel_hi:[0,1,1]\nPF v[74:75],v[212:213],v[74:75],v[82:83] op_sel_hi:[0,1,1]\nPF v[76:77],v[212:213],v[76:77],v[84:85] op_sel_hi:[0,1,1]\nPF v[78:79],v[212:213],v[78:79],v[86:87] op_sel_hi:[0,1,1]\nPM v[80:81],v[72:73],v[72:73]\nPM v[82:83],v[74:75],v[74:75]\nPM v[84:85],v[76:77],v[76:77]\n"
"PM v[86:87],v[78:79],v[78:79]\nPF v[80:81],v[80:81],v[54:55],v[52:53]\nPF v[82:83],v[82:83],v[54:55],v[52:53]\nPF v[84:85],v[84:85],v[54:55],v[52:53]\nPF v[86:87],v[86:87],v[54:55],v[52:53]\nPM v[80:81],v[80:81],v[72:73]\nPM v[82:83],v[82:83],v[74:75]\nPM v[84:85],v[84:85],v[76:77]\n"
"PM v[86:87],v[86:87],v[78:79]\nEX v80,v80\nEX v81,v81\nEX v82,v82\nEX v83,v83\nEX v84,v84\nEX v85,v85\nEX v86,v86\n"
"EX v87,v87\nv_pk_add_f32 v[80:81],v[80:81],v[56:57]\nv_pk_add_f32 v[82:83],v[82:83],v[56:57]\nv_pk_add_f32 v[84:85],v[84:85],v[56:57]\nv_pk_add_f32 v[86:87],v[86:87],v[56:57]\nRC v80,v80\nRC v81,v81\nRC v82,v82\n"
"RC v83,v83\nRC v84,v84\nRC v85,v85\nRC v86,v86\nRC v87,v87\nPM v[72:73],v[72:73],v[80:81]\nPM v[74:75],v[74:75],v[82:83]\nPM v[76:77],v[76:77],v[84:85]\n"
"PM v[78:79],v[78:79],v[86:87]\nCV v60,v72,v73\nCV v61,v74,v75\nCV v62,v76,v77\nCV v63,v78,v79\nBS v[60:63],v58,s[84:87],s34 offen sc1\nSA s34,s34,186368\nAR v88,a148\n"
"AR v89,a149\nAR v90,a150\nAR v91,a151\nAR v92,a180\nAR v93,a181\nAR v94,a182\nAR v95,a183\nPF v[96:97],v[214:215],v[44:45],v[36:37] op_sel:[1,0,0] op_sel_hi:[1,1,1] neg_lo:[1,0,0] neg_hi:[1,0,0]\n"
"PF v[98:99],v[214:215],v[46:47],v[38:39] op_sel:[1,0,0] op_sel_hi:[1,1,1] neg_lo:[1,0,0] neg_hi:[1,0,0]\nPF v[100:101],v[214:215],v[48:49],v[40:41] op_sel:[1,0,0] op_sel_hi:[1,1,1] neg_lo:[1,0,0] neg_hi:[1,0,0]\nPF v[102:103],v[214:215],v[50:51],v[42:43] op_sel:[1,0,0] op_sel_hi:[1,1,1] neg_lo:[1,0,0] neg_hi:[1,0,0]\nPF v[88:89],v[214:215],v[88:89],v[96:97] op_sel_hi:[0,1,1]\nPF v[90:91],v[214:215],v[90:91],v[98:99] op_sel_hi:[0,1,1]\nPF v[92:93],v[214:215],v[92:93],v[100:101] op_sel_hi:[0,1,1]\nPF v[94:95],v[214:215],v[94:95],v[102:103] op_sel_hi:[0,1,1]\nPM v[96:97],v[88:89],v[88:89]\n"
"PM v[98:99],v[90:91],v[90:91]\nPM v[100:101],v[92:93],v[92:93]\nPM v[102:103],v[94:95],v[94:95]\nPF v[96:97],v[96:97],v[54:55],v[52:53]\nPF v[98:99],v[98:99],v[54:55],v[52:53]\nPF v[100:101],v[100:101],v[54:55],v[52:53]\nPF v[102:103],v[102:103],v[54:55],v[52:53]\nPM v[96:97],v[96:97],v[88:89]\n"
"PM v[98:99],v[98:99],v[90:91]\nPM v[100:101],v[100:101],v[92:93]\nPM v[102:103],v[102:103],v[94:95]\nEX v96,v96\nEX v97,v97\nEX v98,v98\nEX v99,v99\nEX v100,v100\n"
"EX v101,v101\nEX v102,v102\nEX v103,v103\nv_pk_add_f32 v[96:97],v[96:97],v[56:57]\nv_pk_add_f32 v[98:99],v[98:99],v[56:57]\nv_pk_add_f32 v[100:101],v[100:101],v[56:57]\nv_pk_add_f32 v[102:103],v[102:103],v[56:57]\nRC v96,v96\n"
"RC v97,v97\nRC v98,v98\nRC v99,v99\nRC v100,v100\nRC v101,v101\nRC v102,v102\nRC v103,v103\nPM v[88:89],v[88:89],v[96:97]\n"
"PM v[90:91],v[90:91],v[98:99]\nPM v[92:93],v[92:93],v[100:101]\nPM v[94:95],v[94:95],v[102:103]\nCV v64,v88,v89\nCV v65,v90,v91\nCV v66,v92,v93\nCV v67,v94,v95\nBS v[64:67],v58,s[84:87],s34 offen sc1\n"
"SA s34,s34,186368\nAR v72,a152\nAR v73,a153\nAR v74,a154\nAR v75,a155\nAR v76,a184\nAR v77,a185\nAR v78,a186\n"
"AR v79,a187\nPF v[80:81],v[216:217],v[44:45],v[36:37] op_sel:[1,0,0] op_sel_hi:[1,1,1] neg_lo:[1,0,0] neg_hi:[1,0,0]\nPF v[82:83],v[216:217],v[46:47],v[38:39] op_sel:[1,0,0] op_sel_hi:[1,1,1] neg_lo:[1,0,0] neg_hi:[1,0,0]\nPF v[84:85],v[216:217],v[48:49],v[40:41] op_sel:[1,0,0] op_sel_hi:[1,1,1] neg_lo:[1,0,0] neg_hi:[1,0,0]\nPF v[86:87],v[216:217],v[50:51],v[42:43] op_sel:[1,0,0] op_sel_hi:[1,1,1] neg_lo:[1,0,0] neg_hi:[1,0,0]\nPF v[72:73],v[216:217],v[72:73],v[80:81] op_sel_hi:[0,1,1]\nPF v[74:75],v[216:217],v[74:75],v[82:83] op_sel_hi:[0,1,1]\nPF v[76:77],v[216:217],v[76:77],v[84:85] op_sel_hi:[0,1,1]\n"
"PF v[78:79],v[216:217],v[78:79],v[86:87] op_sel_hi:[0,1,1]\nPM v[80:81],v[72:73],v[72:73]\nPM v[82:83],v[74:75],v[74:75]\nPM v[84:85],v[76:77],v[76:77]\nPM v[86:87],v[78:79],v[78:79]\nPF v[80:81],v[80:81],v[54:55],v[52:53]\nPF v[82:83],v[82:83],v[54:55],v[52:53]\nPF v[84:85],v[84:85],v[54:55],v[52:53]\n"
"PF v[86:87],v[86:87],v[54:55],v[52:53]\nPM v[80:81],v[80:81],v[72:73]\nPM v[82:83],v[82:83],v[74:75]\nPM v[84:85],v[84:85],v[76:77]\nPM v[86:87],v[86:87],v[78:79]\nEX v80,v80\nEX v81,v81\nEX v82,v82\n"
"EX v83,v83\nEX v84,v84\nEX v85,v85\nEX v86,v86\nEX v87,v87\nv_pk_add_f32 v[80:81],v[80:81],v[56:57]\nv_pk_add_f32 v[82:83],v[82:83],v[56:57]\nv_pk_add_f32 v[84:85],v[84:85],v[56:57]\n"
"v_pk_add_f32 v[86:87],v[86:87],v[56:57]\nRC v80,v80\nRC v81,v81\nRC v82,v82\nRC v83,v83\nRC v84,v84\nRC v85,v85\nRC v86,v86\n"
"RC v87,v87\nPM v[72:73],v[72:73],v[80:81]\nPM v[74:75],v[74:75],v[82:83]\nPM v[76:77],v[76:77],v[84:85]\nPM v[78:79],v[78:79],v[86:87]\nCV v60,v72,v73\nCV v61,v74,v75\nCV v62,v76,v77\n"
"CV v63,v78,v79\nBS v[60:63],v58,s[84:87],s34 offen sc1\nSA s34,s34,186368\nAR v88,a156\nAR v89,a157\nAR v90,a158\nAR v91,a159\nAR v92,a188\n"
"AR v93,a189\nAR v94,a190\nAR v95,a191\nPF v[96:97],v[218:219],v[44:45],v[36:37] op_sel:[1,0,0] op_sel_hi:[1,1,1] neg_lo:[1,0,0] neg_hi:[1,0,0]\nPF v[98:99],v[218:219],v[46:47],v[38:39] op_sel:[1,0,0] op_sel_hi:[1,1,1] neg_lo:[1,0,0] neg_hi:[1,0,0]\nPF v[100:101],v[218:219],v[48:49],v[40:41] op_sel:[1,0,0] op_sel_hi:[1,1,1] neg_lo:[1,0,0] neg_hi:[1,0,0]\nPF v[102:103],v[218:219],v[50:51],v[42:43] op_sel:[1,0,0] op_sel_hi:[1,1,1] neg_lo:[1,0,0] neg_hi:[1,0,0]\nPF v[88:89],v[218:219],v[88:89],v[96:97] op_sel_hi:[0,1,1]\n"
"PF v[90:91],v[218:219],v[90:91],v[98:99] op_sel_hi:[0,1,1]\nPF v[92:93],v[218:219],v[92:93],v[100:101] op_sel_hi:[0,1,1]\nPF v[94:95],v[218:219],v[94:95],v[102:103] op_sel_hi:[0,1,1]\nPM v[96:97],v[88:89],v[88:89]\nPM v[98:99],v[90:91],v[90:91]\nPM v[100:101],v[92:93],v[92:93]\nPM v[102:103],v[94:95],v[94:95]\nPF v[96:97],v[96:97],v[54:55],v[52:53]\n"
"PF v[98:99],v[98:99],v[54:55],v[52:53]\nPF v[100:101],v[100:101],v[54:55],v[52:53]\nPF v[102:103],v[102:103],v[54:55],v[52:53]\nPM v[96:97],v[96:97],v[88:89]\nPM v[98:99],v[98:99],v[90:91]\nPM v[100:101],v[100:101],v[92:93]\nPM v[102:103],v[102:103],v[94:95]\nEX v96,v96\n"
"EX v97,v97\nEX v98,v98\nEX v99,v99\nEX v100,v100\nEX v101,v101\nEX v102,v102\nEX v103,v103\nv_pk_add_f32 v[96:97],v[96:97],v[56:57]\n"
"v_pk_add_f32 v[98:99],v[98:99],v[56:57]\nv_pk_add_f32 v[100:101],v[100:101],v[56:57]\nv_pk_add_f32 v[102:103],v[102:103],v[56:57]\nRC v96,v96\nRC v97,v97\nRC v98,v98\nRC v99,v99\nRC v100,v100\n"
"RC v101,v101\nRC v102,v102\nRC v103,v103\nPM v[88:89],v[88:89],v[96:97]\nPM v[90:91],v[90:91],v[98:99]\nPM v[92:93],v[92:93],v[100:101]\nPM v[94:95],v[94:95],v[102:103]\nCV v64,v88,v89\n"
"CV v65,v90,v91\nCV v66,v92,v93\nCV v67,v94,v95\nBS v[64:67],v58,s[84:87],s34 offen sc1\nSM s66,39845888\nSA s48,s92,s66\ns_addc_u32 s49,s93,0\ns_and_b32 s49,s49,0xffff\n"
"SM s50,23855104\nSM s51,0x20000\ns_lshl_b32 s66,s53,13\nVU v165,s66,v161\nSA s66,s66,0x4000\nVU v166,s66,v161\nVU v2,0x4000,v161\nv_mov_b32 v164,0xff800000\n"
"VN v170,15,v200\nVL v168,4,v170\nv_lshrrev_b32 v170,5,v200\nv_lshl_add_u32 v168,v170,8,v168\nv_bfe_u32 v170,v200,4,1\nv_mul_u32_u24 v170,186368,v170\nVU v168,v168,v170\ns_lshl_b32 s66,s53,13\n"
"s_lshl_b32 s67,s52,15\nSA s66,s66,s67\nSA s66,s66,0x14000\nVU v202,s66,v161\ns_lshl_b32 s66,s89,13\nSA s66,s66,0x20000\nVU v203,s66,v161\ns_xor_b32 s66,s89,1\n"
"s_lshl_b32 s66,s66,13\nSA s66,s66,0x20000\nVU v3,s66,v161\nSW lgkmcnt(0)\ns_barrier\nDR a[128:131],v202 offset:0\nDR a[132:135],v202 offset:1024\nDR a[136:139],v202 offset:2048\n"
"DR a[140:143],v202 offset:3072\nDR a[144:147],v202 offset:4096\nDR a[148:151],v202 offset:5120\nDR a[152:155],v202 offset:6144\nDR a[156:159],v202 offset:7168\nDR a[160:163],v203 offset:0\nDR a[164:167],v203 offset:1024\nDR a[168:171],v203 offset:2048\n"
"DR a[172:175],v203 offset:3072\nDR a[176:179],v203 offset:4096\nDR a[180:183],v203 offset:5120\nDR a[184:187],v203 offset:6144\nDR a[188:191],v203 offset:7168\nv_mov_b32 v148,0\nv_mov_b32 v149,0\nv_mov_b32 v150,0\n"
"v_mov_b32 v151,0\ns_nop 1\nM8 a[0:15],v[148:151],v[148:151],0\nM8 a[16:31],v[148:151],v[148:151],0\nM8 a[32:47],v[148:151],v[148:151],0\nM8 a[48:63],v[148:151],v[148:151],0\nM8 a[64:79],v[148:151],v[148:151],0\nM8 a[80:95],v[148:151],v[148:151],0\n"
"M8 a[96:111],v[148:151],v[148:151],0\nM8 a[112:127],v[148:151],v[148:151],0\nSW lgkmcnt(0)\nv_mov_b32 v4,v161\nSW vmcnt(26)\ns_barrier\nDR a[192:195],v4 offset:0\nDR a[196:199],v4 offset:1024\n"
"DR a[200:203],v4 offset:2048\nDR a[204:207],v4 offset:3072\nDR a[208:211],v4 offset:4096\nDR a[212:215],v4 offset:5120\nDR a[216:219],v4 offset:6144\nDR a[220:223],v4 offset:7168\nVN v170,31,v200\nv_lshrrev_b32 v171,5,v200\n"
"VL v171,2,v171\nv_sub_u32 v170,v170,v171\ns_lshl_b32 s66,s55,7\ns_lshl_b32 s67,s89,5\nSA s66,s66,s67\nVU v162,s66,v170\nSW lgkmcnt(6)\nM8 v[16:31],a[192:195],a[128:131],0\n"
"M8 v[32:47],a[192:195],a[160:163],0\nM8 v[16:31],a[196:199],a[132:135],v[16:31]\nM8 v[32:47],a[196:199],a[164:167],v[32:47]\nSW lgkmcnt(4)\nM8 v[16:31],a[200:203],a[136:139],v[16:31]\nM8 v[32:47],a[200:203],a[168:171],v[32:47]\nM8 v[16:31],a[204:207],a[140:143],v[16:31]\nM8 v[32:47],a[204:207],a[172:175],v[32:47]\n"
"SW lgkmcnt(2)\nM8 v[16:31],a[208:211],a[144:147],v[16:31]\nM8 v[32:47],a[208:211],a[176:179],v[32:47]\nM8 v[16:31],a[212:215],a[148:151],v[16:31]\nM8 v[32:47],a[212:215],a[180:183],v[32:47]\nSW lgkmcnt(0)\nM8 v[16:31],a[216:219],a[152:155],v[16:31]\nM8 v[32:47],a[216:219],a[184:187],v[32:47]\n"
"M8 v[16:31],a[220:223],a[156:159],v[16:31]\nM8 v[32:47],a[220:223],a[188:191],v[32:47]\nv_mov_b32 v154,0\nv_mov_b32 v155,0\nSM s63,0x8000\nVU v5,s63,v161\nSM s64,0xffff8000\nDR a[192:195],v4 offset:8192\n"
"DR a[196:199],v4 offset:9216\nDR a[200:203],v4 offset:10240\nDR a[204:207],v4 offset:11264\nDR a[208:211],v4 offset:12288\nDR a[212:215],v4 offset:13312\nSW vmcnt(18)\ns_barrier\ns_cmp_eq_u32 s57,2\n"
"s_cbranch_scc0 AT_nomask0\ns_nop 7\nCG s[80:81],v162,0\nCG s[82:83],v162,1\nCG s[84:85],v162,2\nCG s[86:87],v162,3\nCM v16,v164,v16,s[80:81]\nCM v17,v164,v17,s[82:83]\n"
"CM v18,v164,v18,s[84:85]\nCM v19,v164,v19,s[86:87]\nCG s[80:81],v162,16\nCG s[82:83],v162,17\nCG s[84:85],v162,18\nCG s[86:87],v162,19\nCM v20,v164,v20,s[80:81]\nCM v21,v164,v21,s[82:83]\n"
"CM v22,v164,v22,s[84:85]\nCM v23,v164,v23,s[86:87]\nCG s[80:81],v162,8\nCG s[82:83],v162,9\nCG s[84:85],v162,10\nCG s[86:87],v162,11\nCM v24,v164,v24,s[80:81]\nCM v25,v164,v25,s[82:83]\n"
"CM v26,v164,v26,s[84:85]\nCM v27,v164,v27,s[86:87]\nCG s[80:81],v162,24\nCG s[82:83],v162,25\nCG s[84:85],v162,26\nCG s[86:87],v162,27\nCM v28,v164,v28,s[80:81]\nCM v29,v164,v29,s[82:83]\n"
"CM v30,v164,v30,s[84:85]\nCM v31,v164,v31,s[86:87]\nAT_nomask0:\nv_max3_f32 v156,v16,v17,v18\nv_max3_f32 v172,v19,v20,v21\nv_max3_f32 v156,v156,v22,v23\nv_max3_f32 v172,v172,v24,v25\nv_max3_f32 v156,v156,v26,v27\n"
"v_max3_f32 v172,v172,v28,v29\nv_max3_f32 v156,v156,v30,v31\nv_max_f32 v156,v156,v172\nv_mov_b32 v172,v156\ns_nop 1\nv_permlane32_swap_b32 v156,v172\ns_nop 1\nv_max_f32 v156,v156,v172\n"
"VA v156,0x41f00000,v156\nVS v112,0,v156\nVS v113,0,v156\nVS v114,0,v156\nVS v115,0,v156\nVS v116,0,v156\nVS v117,0,v156\nVS v118,0,v156\n"
"VS v119,0,v156\nVS v120,0,v156\nVS v121,0,v156\nVS v122,0,v156\nVS v123,0,v156\nVS v124,0,v156\nVS v125,0,v156\nVS v126,0,v156\n"
"VS v127,0,v156\nVS v16,v16,v156\nVS v17,v17,v156\nVS v18,v18,v156\nVS v19,v19,v156\nVS v20,v20,v156\nVS v21,v21,v156\nVS v22,v22,v156\n"
"VS v23,v23,v156\nVS v24,v24,v156\nVS v25,v25,v156\nVS v26,v26,v156\nVS v27,v27,v156\nVS v28,v28,v156\nVS v29,v29,v156\nVS v30,v30,v156\n"
"VS v31,v31,v156\nv_max3_f32 v157,v32,v33,v34\nv_max3_f32 v172,v35,v36,v37\nv_max3_f32 v157,v157,v38,v39\nv_max3_f32 v172,v172,v40,v41\nv_max3_f32 v157,v157,v42,v43\nv_max3_f32 v172,v172,v44,v45\nv_max3_f32 v157,v157,v46,v47\n"
"v_max_f32 v157,v157,v172\nv_mov_b32 v172,v157\ns_nop 1\nv_permlane32_swap_b32 v157,v172\ns_nop 1\nv_max_f32 v157,v157,v172\nVA v157,0x41f00000,v157\nVS v128,0,v157\n"
"VS v129,0,v157\nVS v130,0,v157\nVS v131,0,v157\nVS v132,0,v157\nVS v133,0,v157\nVS v134,0,v157\nVS v135,0,v157\nVS v136,0,v157\n"
"VS v137,0,v157\nVS v138,0,v157\nVS v139,0,v157\nVS v140,0,v157\nVS v141,0,v157\nVS v142,0,v157\nVS v143,0,v157\nVS v32,v32,v157\n"
"VS v33,v33,v157\nVS v34,v34,v157\nVS v35,v35,v157\nVS v36,v36,v157\nVS v37,v37,v157\nVS v38,v38,v157\nVS v39,v39,v157\nVS v40,v40,v157\n"
"VS v41,v41,v157\nVS v42,v42,v157\nVS v43,v43,v157\nVS v44,v44,v157\nVS v45,v45,v157\nVS v46,v46,v157\nVS v47,v47,v157\ns_lshl_b32 s66,s55,7\n"
"v_subrev_u32 v162,s66,v162\ns_cmp_eq_u32 s57,2\ns_cbranch_scc1 AT_d0am\nAT_d0a:\nSW lgkmcnt(2)\nM8 v[48:63],a[192:195],a[128:131],v[112:127]\nDR a[216:219],v4 offset:14336\nEX v144,v16\n"
"M8 v[64:79],a[192:195],a[160:163],v[128:143]\nEX v145,v17\ns_and_b32 m0,s62,0x1ffff\nM8 v[48:63],a[196:199],a[132:135],v[48:63]\nDR a[220:223],v4 offset:15360\nEX v146,v18\nVA v152,v144,v145\nCV v80,v144,v145\n"
"M8 v[64:79],a[196:199],a[164:167],v[64:79]\nEX v147,v19\nVA v152,v152,v146\nBL v161,s[40:43],s61 offen offset:0 sc1 lds\nM8 v[48:63],a[200:203],a[136:139],v[48:63]\nEX v144,v20\nVA v152,v152,v147\nCV v81,v146,v147\n"
"M8 v[64:79],a[200:203],a[168:171],v[64:79]\nEX v145,v21\nVA v152,v152,v144\nM8 v[48:63],a[204:207],a[140:143],v[48:63]\nEX v146,v22\nVA v152,v152,v145\nCV v82,v144,v145\nM8 v[64:79],a[204:207],a[172:175],v[64:79]\n"
"EX v147,v23\nVA v152,v152,v146\nBL v161,s[40:43],s61 offen offset:1024 sc1 lds\nSW lgkmcnt(0)\nM8 v[48:63],a[208:211],a[144:147],v[48:63]\nEX v144,v24\nVA v152,v152,v147\nCV v83,v146,v147\n"
"M8 v[64:79],a[208:211],a[176:179],v[64:79]\nEX v145,v25\nVA v152,v152,v144\nM8 v[48:63],a[212:215],a[148:151],v[48:63]\nEX v146,v26\nVA v152,v152,v145\nCV v84,v144,v145\nM8 v[64:79],a[212:215],a[180:183],v[64:79]\n"
"EX v147,v27\nVA v152,v152,v146\nEX v144,v28\nVA v152,v152,v147\nCV v85,v146,v147\nBL v161,s[40:43],s61 offen offset:2048 sc1 lds\nM8 v[48:63],a[216:219],a[152:155],v[48:63]\nEX v145,v29\n"
"VA v152,v152,v144\nM8 v[64:79],a[216:219],a[184:187],v[64:79]\nEX v146,v30\nVA v152,v152,v145\nCV v86,v144,v145\nM8 v[48:63],a[220:223],a[156:159],v[48:63]\nEX v147,v31\nVA v152,v152,v146\n"
"M8 v[64:79],a[220:223],a[188:191],v[64:79]\nEX v144,v32\nVA v152,v152,v147\nCV v87,v146,v147\nBL v161,s[40:43],s61 offen offset:3072 sc1 lds\nEX v145,v33\nEX v146,v34\nVA v153,v144,v145\n"
"CV v88,v144,v145\nSA s61,s61,16384\nSA s62,s62,0x8000\nEX v147,v35\nVA v153,v153,v146\nEX v144,v36\nVA v153,v153,v147\nCV v89,v146,v147\n"
"DR a[192:195],v5 offset:0\nEX v145,v37\nVA v153,v153,v144\nEX v146,v38\nVA v153,v153,v145\nCV v90,v144,v145\nEX v147,v39\nVA v153,v153,v146\n"
"SA s64,s64,0x8000\nDR a[196:199],v5 offset:1024\nEX v144,v40\nVA v153,v153,v147\nCV v91,v146,v147\nEX v145,v41\nVA v153,v153,v144\ns_and_b32 s67,s64,0x1ffff\n"
"DR a[200:203],v5 offset:2048\nEX v146,v42\nVA v153,v153,v145\nCV v92,v144,v145\nEX v147,v43\nVA v153,v153,v146\nVU v6,s67,v2\nDR a[204:207],v5 offset:3072\n"
"EX v144,v44\nVA v153,v153,v147\nCV v93,v146,v147\nEX v145,v45\nVA v153,v153,v144\nDR a[208:211],v5 offset:4096\nEX v146,v46\nVA v153,v153,v145\n"
"CV v94,v144,v145\nEX v147,v47\nVA v153,v153,v146\nVA v153,v153,v147\nCV v95,v146,v147\nDR a[212:215],v5 offset:5120\nv_max_f32 v170,v152,v153\nv_cmp_nge_f32 vcc,1.0,v170\n"
"s_cbranch_vccnz AT_d0a_fb\nAT_d0a_ret:\nAT_d1a:\nSW lgkmcnt(2)\nM8 v[16:31],a[192:195],a[128:131],v[112:127]\nDR a[216:219],v5 offset:6144\nVA v154,v154,v152\nEX v144,v48\n"
"M8 v[32:47],a[192:195],a[160:163],v[128:143]\nVA v155,v155,v153\nEX v145,v49\ns_and_b32 m0,s60,0x1ffff\nM8 v[16:31],a[196:199],a[132:135],v[16:31]\nDR a[220:223],v5 offset:7168\nEX v146,v50\nVA v152,v144,v145\n"
"CV v96,v144,v145\nM8 v[32:47],a[196:199],a[164:167],v[32:47]\nEX v147,v51\nVA v152,v152,v146\nBL v161,s[36:39],s59 offen offset:0 sc1 lds\nM8 v[16:31],a[200:203],a[136:139],v[16:31]\nDR a[224:227],v6 offset:0\nEX v144,v52\n"
"VA v152,v152,v147\nCV v97,v146,v147\nM8 v[32:47],a[200:203],a[168:171],v[32:47]\nEX v145,v53\nVA v152,v152,v144\nSA s63,s63,0x8000\nM8 v[16:31],a[204:207],a[140:143],v[16:31]\nDR a[228:231],v6 offset:2048\n"
"EX v146,v54\nVA v152,v152,v145\nCV v98,v144,v145\nM8 v[32:47],a[204:207],a[172:175],v[32:47]\nEX v147,v55\nVA v152,v152,v146\ns_and_b32 s66,s63,0x1ffff\nBL v161,s[36:39],s59 offen offset:1024 sc1 lds\n"
"SW lgkmcnt(2)\nM8 v[16:31],a[208:211],a[144:147],v[16:31]\nDR a[232:235],v6 offset:4096\nEX v144,v56\nVA v152,v152,v147\nCV v99,v146,v147\nM8 v[32:47],a[208:211],a[176:179],v[32:47]\nEX v145,v57\n"
"VA v152,v152,v144\nM8 v[16:31],a[212:215],a[148:151],v[16:31]\nDR a[236:239],v6 offset:6144\nEX v146,v58\nVA v152,v152,v145\nCV v100,v144,v145\nM8 v[32:47],a[212:215],a[180:183],v[32:47]\nEX v147,v59\n"
"VA v152,v152,v146\nEX v144,v60\nVA v152,v152,v147\nCV v101,v146,v147\nBL v161,s[36:39],s59 offen offset:2048 sc1 lds\nM8 v[16:31],a[216:219],a[152:155],v[16:31]\nDR a[240:243],v6 offset:1024\nEX v145,v61\n"
"VA v152,v152,v144\nM8 v[32:47],a[216:219],a[184:187],v[32:47]\nEX v146,v62\nVA v152,v152,v145\nCV v102,v144,v145\nM8 v[16:31],a[220:223],a[156:159],v[16:31]\nDR a[244:247],v6 offset:3072\nEX v147,v63\n"
"VA v152,v152,v146\nM8 v[32:47],a[220:223],a[188:191],v[32:47]\nEX v144,v64\nVA v152,v152,v147\nCV v103,v146,v147\nBL v161,s[36:39],s59 offen offset:3072 sc1 lds\nSW lgkmcnt(2)\nM8 a[0:15],a[224:227],v[80:83],a[0:15]\n"
"DR a[248:251],v6 offset:5120\nEX v145,v65\nM8 a[16:31],a[224:227],v[88:91],a[16:31]\nEX v146,v66\nVA v153,v144,v145\nCV v104,v144,v145\nSA s59,s59,16384\nSA s60,s60,0x8000\n"
"M8 a[32:47],a[228:231],v[80:83],a[32:47]\nDR a[252:255],v6 offset:7168\nEX v147,v67\nVA v153,v153,v146\nM8 a[48:63],a[228:231],v[88:91],a[48:63]\nEX v144,v68\nVA v153,v153,v147\nCV v105,v146,v147\n"
"M8 a[64:79],a[232:235],v[80:83],a[64:79]\nDR a[192:195],v5 offset:8192\nEX v145,v69\nVA v153,v153,v144\nM8 a[80:95],a[232:235],v[88:91],a[80:95]\nEX v146,v70\nVA v153,v153,v145\nCV v106,v144,v145\n"
"EX v147,v71\nVA v153,v153,v146\nM8 a[96:111],a[236:239],v[80:83],a[96:111]\nDR a[196:199],v5 offset:9216\nEX v144,v72\nVA v153,v153,v147\nCV v107,v146,v147\nM8 a[112:127],a[236:239],v[88:91],a[112:127]\n"
"EX v145,v73\nVA v153,v153,v144\nSW lgkmcnt(2)\nM8 a[0:15],a[240:243],v[84:87],a[0:15]\nDR a[200:203],v5 offset:10240\nEX v146,v74\nVA v153,v153,v145\nCV v108,v144,v145\n"
"M8 a[16:31],a[240:243],v[92:95],a[16:31]\nEX v147,v75\nVA v153,v153,v146\nM8 a[32:47],a[244:247],v[84:87],a[32:47]\nDR a[204:207],v5 offset:11264\nEX v144,v76\nVA v153,v153,v147\nCV v109,v146,v147\n"
"M8 a[48:63],a[244:247],v[92:95],a[48:63]\nEX v145,v77\nVA v153,v153,v144\nM8 a[64:79],a[248:251],v[84:87],a[64:79]\nDR a[208:211],v5 offset:12288\nEX v146,v78\nVA v153,v153,v145\nCV v110,v144,v145\n"
"M8 a[80:95],a[248:251],v[92:95],a[80:95]\nEX v147,v79\nVA v153,v153,v146\nM8 a[96:111],a[252:255],v[84:87],a[96:111]\nVA v153,v153,v147\nCV v111,v146,v147\nDR a[212:215],v5 offset:13312\nv_max_f32 v170,v152,v153\n"
"v_cmp_nge_f32 vcc,1.0,v170\nM8 a[112:127],a[252:255],v[92:95],a[112:127]\nSW vmcnt(8)\ns_barrier\nv_mov_b32 v4,v5\nVU v5,s66,v161\ns_cbranch_vccnz AT_d1a_fb\nAT_d1a_ret:\n"
"s_sub_u32 s58,s57,3\ns_cmp_eq_u32 s58,1\ns_cmp_le_u32 s57,3\ns_cbranch_scc1 AT_dtail\nAT_dloop:\nAT_d0:\nSW lgkmcnt(2)\nM8 v[48:63],a[192:195],a[128:131],v[112:127]\n"
"DR a[216:219],v4 offset:14336\nVA v154,v154,v152\nEX v144,v16\nM8 v[64:79],a[192:195],a[160:163],v[128:143]\nVA v155,v155,v153\nEX v145,v17\ns_and_b32 m0,s62,0x1ffff\nM8 v[48:63],a[196:199],a[132:135],v[48:63]\n"
"DR a[220:223],v4 offset:15360\nEX v146,v18\nVA v152,v144,v145\nCV v80,v144,v145\nM8 v[64:79],a[196:199],a[164:167],v[64:79]\nEX v147,v19\nVA v152,v152,v146\nBL v161,s[40:43],s61 offen offset:0 sc1 lds\n"
"M8 v[48:63],a[200:203],a[136:139],v[48:63]\nDR a[224:227],v6 offset:8192\nEX v144,v20\nVA v152,v152,v147\nCV v81,v146,v147\nM8 v[64:79],a[200:203],a[168:171],v[64:79]\nEX v145,v21\nVA v152,v152,v144\n"
"M8 v[48:63],a[204:207],a[140:143],v[48:63]\nDR a[228:231],v6 offset:10240\nEX v146,v22\nVA v152,v152,v145\nCV v82,v144,v145\nM8 v[64:79],a[204:207],a[172:175],v[64:79]\nEX v147,v23\nVA v152,v152,v146\n"
"BL v161,s[40:43],s61 offen offset:1024 sc1 lds\nSW lgkmcnt(2)\nM8 v[48:63],a[208:211],a[144:147],v[48:63]\nDR a[232:235],v6 offset:12288\nEX v144,v24\nVA v152,v152,v147\nCV v83,v146,v147\nM8 v[64:79],a[208:211],a[176:179],v[64:79]\n"
"EX v145,v25\nVA v152,v152,v144\nM8 v[48:63],a[212:215],a[148:151],v[48:63]\nDR a[236:239],v6 offset:14336\nEX v146,v26\nVA v152,v152,v145\nCV v84,v144,v145\nM8 v[64:79],a[212:215],a[180:183],v[64:79]\n"
"EX v147,v27\nVA v152,v152,v146\nEX v144,v28\nVA v152,v152,v147\nCV v85,v146,v147\nBL v161,s[40:43],s61 offen offset:2048 sc1 lds\nM8 v[48:63],a[216:219],a[152:155],v[48:63]\nDR a[240:243],v6 offset:9216\n"
"EX v145,v29\nVA v152,v152,v144\nM8 v[64:79],a[216:219],a[184:187],v[64:79]\nEX v146,v30\nVA v152,v152,v145\nCV v86,v144,v145\nM8 v[48:63],a[220:223],a[156:159],v[48:63]\nDR a[244:247],v6 offset:11264\n"
"EX v147,v31\nVA v152,v152,v146\nM8 v[64:79],a[220:223],a[188:191],v[64:79]\nEX v144,v32\nVA v152,v152,v147\nCV v87,v146,v147\nBL v161,s[40:43],s61 offen offset:3072 sc1 lds\nSW lgkmcnt(2)\n"
"M8 a[0:15],a[224:227],v[96:99],a[0:15]\nDR a[248:251],v6 offset:13312\nEX v145,v33\nM8 a[16:31],a[224:227],v[104:107],a[16:31]\nEX v146,v34\nVA v153,v144,v145\nCV v88,v144,v145\nSA s61,s61,16384\n"
"SA s62,s62,0x8000\nM8 a[32:47],a[228:231],v[96:99],a[32:47]\nDR a[252:255],v6 offset:15360\nEX v147,v35\nVA v153,v153,v146\nM8 a[48:63],a[228:231],v[104:107],a[48:63]\nEX v144,v36\nVA v153,v153,v147\n"
"CV v89,v146,v147\nM8 a[64:79],a[232:235],v[96:99],a[64:79]\nDR a[192:195],v5 offset:0\nEX v145,v37\nVA v153,v153,v144\nM8 a[80:95],a[232:235],v[104:107],a[80:95]\nEX v146,v38\nVA v153,v153,v145\n"
"CV v90,v144,v145\nEX v147,v39\nVA v153,v153,v146\nSA s64,s64,0x8000\nM8 a[96:111],a[236:239],v[96:99],a[96:111]\nDR a[196:199],v5 offset:1024\nEX v144,v40\nVA v153,v153,v147\n"
"CV v91,v146,v147\nM8 a[112:127],a[236:239],v[104:107],a[112:127]\nEX v145,v41\nVA v153,v153,v144\ns_and_b32 s67,s64,0x1ffff\nSW lgkmcnt(2)\nM8 a[0:15],a[240:243],v[100:103],a[0:15]\nDR a[200:203],v5 offset:2048\n"
"EX v146,v42\nVA v153,v153,v145\nCV v92,v144,v145\nM8 a[16:31],a[240:243],v[108:111],a[16:31]\nEX v147,v43\nVA v153,v153,v146\nVU v6,s67,v2\nM8 a[32:47],a[244:247],v[100:103],a[32:47]\n"
"DR a[204:207],v5 offset:3072\nEX v144,v44\nVA v153,v153,v147\nCV v93,v146,v147\nM8 a[48:63],a[244:247],v[108:111],a[48:63]\nEX v145,v45\nVA v153,v153,v144\nM8 a[64:79],a[248:251],v[100:103],a[64:79]\n"
"DR a[208:211],v5 offset:4096\nEX v146,v46\nVA v153,v153,v145\nCV v94,v144,v145\nM8 a[80:95],a[248:251],v[108:111],a[80:95]\nEX v147,v47\nVA v153,v153,v146\nM8 a[96:111],a[252:255],v[100:103],a[96:111]\n"
"VA v153,v153,v147\nCV v95,v146,v147\nDR a[212:215],v5 offset:5120\nv_max_f32 v170,v152,v153\nv_cmp_nge_f32 vcc,1.0,v170\nM8 a[112:127],a[252:255],v[108:111],a[112:127]\ns_cbranch_vccnz AT_d0_fb\nAT_d0_ret:\n"
"AT_d1:\nSW lgkmcnt(2)\nM8 v[16:31],a[192:195],a[128:131],v[112:127]\nDR a[216:219],v5 offset:6144\nVA v154,v154,v152\nEX v144,v48\nM8 v[32:47],a[192:195],a[160:163],v[128:143]\nVA v155,v155,v153\n"
"EX v145,v49\ns_and_b32 m0,s60,0x1ffff\nM8 v[16:31],a[196:199],a[132:135],v[16:31]\nDR a[220:223],v5 offset:7168\nEX v146,v50\nVA v152,v144,v145\nCV v96,v144,v145\nM8 v[32:47],a[196:199],a[164:167],v[32:47]\n"
"EX v147,v51\nVA v152,v152,v146\nBL v161,s[36:39],s59 offen offset:0 sc1 lds\nM8 v[16:31],a[200:203],a[136:139],v[16:31]\nDR a[224:227],v6 offset:0\nEX v144,v52\nVA v152,v152,v147\nCV v97,v146,v147\n"
"M8 v[32:47],a[200:203],a[168:171],v[32:47]\nEX v145,v53\nVA v152,v152,v144\nSA s63,s63,0x8000\nM8 v[16:31],a[204:207],a[140:143],v[16:31]\nDR a[228:231],v6 offset:2048\nEX v146,v54\nVA v152,v152,v145\n"
"CV v98,v144,v145\nM8 v[32:47],a[204:207],a[172:175],v[32:47]\nEX v147,v55\nVA v152,v152,v146\ns_and_b32 s66,s63,0x1ffff\nBL v161,s[36:39],s59 offen offset:1024 sc1 lds\nSW lgkmcnt(2)\nM8 v[16:31],a[208:211],a[144:147],v[16:31]\n"
"DR a[232:235],v6 offset:4096\nEX v144,v56\nVA v152,v152,v147\nCV v99,v146,v147\nM8 v[32:47],a[208:211],a[176:179],v[32:47]\nEX v145,v57\nVA v152,v152,v144\nM8 v[16:31],a[212:215],a[148:151],v[16:31]\n"
"DR a[236:239],v6 offset:6144\nEX v146,v58\nVA v152,v152,v145\nCV v100,v144,v145\nM8 v[32:47],a[212:215],a[180:183],v[32:47]\nEX v147,v59\nVA v152,v152,v146\nEX v144,v60\n"
"VA v152,v152,v147\nCV v101,v146,v147\nBL v161,s[36:39],s59 offen offset:2048 sc1 lds\nM8 v[16:31],a[216:219],a[152:155],v[16:31]\nDR a[240:243],v6 offset:1024\nEX v145,v61\nVA v152,v152,v144\nM8 v[32:47],a[216:219],a[184:187],v[32:47]\n"
"EX v146,v62\nVA v152,v152,v145\nCV v102,v144,v145\nM8 v[16:31],a[220:223],a[156:159],v[16:31]\nDR a[244:247],v6 offset:3072\nEX v147,v63\nVA v152,v152,v146\nM8 v[32:47],a[220:223],a[188:191],v[32:47]\n"
"EX v144,v64\nVA v152,v152,v147\nCV v103,v146,v147\nBL v161,s[36:39],s59 offen offset:3072 sc1 lds\nSW lgkmcnt(2)\nM8 a[0:15],a[224:227],v[80:83],a[0:15]\nDR a[248:251],v6 offset:5120\nEX v145,v65\n"
"M8 a[16:31],a[224:227],v[88:91],a[16:31]\nEX v146,v66\nVA v153,v144,v145\nCV v104,v144,v145\nSA s59,s59,16384\nSA s60,s60,0x8000\nM8 a[32:47],a[228:231],v[80:83],a[32:47]\nDR a[252:255],v6 offset:7168\n"
"EX v147,v67\nVA v153,v153,v146\nM8 a[48:63],a[228:231],v[88:91],a[48:63]\nEX v144,v68\nVA v153,v153,v147\nCV v105,v146,v147\nM8 a[64:79],a[232:235],v[80:83],a[64:79]\nDR a[192:195],v5 offset:8192\n"
"EX v145,v69\nVA v153,v153,v144\nM8 a[80:95],a[232:235],v[88:91],a[80:95]\nEX v146,v70\nVA v153,v153,v145\nCV v106,v144,v145\nEX v147,v71\nVA v153,v153,v146\n"
"M8 a[96:111],a[236:239],v[80:83],a[96:111]\nDR a[196:199],v5 offset:9216\nEX v144,v72\nVA v153,v153,v147\nCV v107,v146,v147\nM8 a[112:127],a[236:239],v[88:91],a[112:127]\nEX v145,v73\nVA v153,v153,v144\n"
"SW lgkmcnt(2)\nM8 a[0:15],a[240:243],v[84:87],a[0:15]\nDR a[200:203],v5 offset:10240\nEX v146,v74\nVA v153,v153,v145\nCV v108,v144,v145\nM8 a[16:31],a[240:243],v[92:95],a[16:31]\nEX v147,v75\n"
"VA v153,v153,v146\nM8 a[32:47],a[244:247],v[84:87],a[32:47]\nDR a[204:207],v5 offset:11264\nEX v144,v76\nVA v153,v153,v147\nCV v109,v146,v147\nM8 a[48:63],a[244:247],v[92:95],a[48:63]\nEX v145,v77\n"
"VA v153,v153,v144\nM8 a[64:79],a[248:251],v[84:87],a[64:79]\nDR a[208:211],v5 offset:12288\nEX v146,v78\nVA v153,v153,v145\nCV v110,v144,v145\nM8 a[80:95],a[248:251],v[92:95],a[80:95]\nEX v147,v79\n"
"VA v153,v153,v146\nM8 a[96:111],a[252:255],v[84:87],a[96:111]\nVA v153,v153,v147\nCV v111,v146,v147\nDR a[212:215],v5 offset:13312\nv_max_f32 v170,v152,v153\nv_cmp_nge_f32 vcc,1.0,v170\nM8 a[112:127],a[252:255],v[92:95],a[112:127]\n"
"SW vmcnt(8)\ns_barrier\nv_mov_b32 v4,v5\nVU v5,s66,v161\ns_cbranch_vccnz AT_d1_fb\nAT_d1_ret:\ns_sub_u32 s58,s58,1\ns_cmp_lg_u32 s58,0\n"
"s_cbranch_scc1 AT_dloop\nAT_dtail:\nAT_d0m:\nCG s[80:81],v162,0\nCG s[82:83],v162,1\nCG s[84:85],v162,2\nCG s[86:87],v162,3\nCM v16,v164,v16,s[80:81]\n"
"CM v17,v164,v17,s[82:83]\nCM v18,v164,v18,s[84:85]\nCM v19,v164,v19,s[86:87]\nSW lgkmcnt(2)\nM8 v[48:63],a[192:195],a[128:131],v[112:127]\nDR a[216:219],v4 offset:14336\nVA v154,v154,v152\nCG s[80:81],v162,16\n"
"EX v144,v16\ns_nop 0\nCM v20,v164,v20,s[80:81]\nM8 v[64:79],a[192:195],a[160:163],v[128:143]\nVA v155,v155,v153\nCG s[82:83],v162,17\nEX v145,v17\ns_nop 0\n"
"CM v21,v164,v21,s[82:83]\ns_and_b32 m0,s62,0x1ffff\nM8 v[48:63],a[196:199],a[132:135],v[48:63]\nDR a[220:223],v4 offset:15360\nCG s[84:85],v162,18\nEX v146,v18\nVA v152,v144,v145\nCV v80,v144,v145\n"
"CM v22,v164,v22,s[84:85]\nM8 v[64:79],a[196:199],a[164:167],v[64:79]\nCG s[86:87],v162,19\nEX v147,v19\nVA v152,v152,v146\nCM v23,v164,v23,s[86:87]\nBL v161,s[40:43],s61 offen offset:0 sc1 lds\nM8 v[48:63],a[200:203],a[136:139],v[48:63]\n"
"DR a[224:227],v6 offset:8192\nCG s[80:81],v162,8\nEX v144,v20\nVA v152,v152,v147\nCV v81,v146,v147\nCM v24,v164,v24,s[80:81]\nM8 v[64:79],a[200:203],a[168:171],v[64:79]\nCG s[82:83],v162,9\n"
"EX v145,v21\nVA v152,v152,v144\nCM v25,v164,v25,s[82:83]\nM8 v[48:63],a[204:207],a[140:143],v[48:63]\nDR a[228:231],v6 offset:10240\nCG s[84:85],v162,10\nEX v146,v22\nVA v152,v152,v145\n"
"CV v82,v144,v145\nCM v26,v164,v26,s[84:85]\nM8 v[64:79],a[204:207],a[172:175],v[64:79]\nCG s[86:87],v162,11\nEX v147,v23\nVA v152,v152,v146\nCM v27,v164,v27,s[86:87]\nBL v161,s[40:43],s61 offen offset:1024 sc1 lds\n"
"SW lgkmcnt(2)\nM8 v[48:63],a[208:211],a[144:147],v[48:63]\nDR a[232:235],v6 offset:12288\nCG s[80:81],v162,24\nEX v144,v24\nVA v152,v152,v147\nCV v83,v146,v147\nCM v28,v164,v28,s[80:81]\n"
"M8 v[64:79],a[208:211],a[176:179],v[64:79]\nCG s[82:83],v162,25\nEX v145,v25\nVA v152,v152,v144\nCM v29,v164,v29,s[82:83]\nM8 v[48:63],a[212:215],a[148:151],v[48:63]\nDR a[236:239],v6 offset:14336\nCG s[84:85],v162,26\n"
"EX v146,v26\nVA v152,v152,v145\nCV v84,v144,v145\nCM v30,v164,v30,s[84:85]\nM8 v[64:79],a[212:215],a[180:183],v[64:79]\nCG s[86:87],v162,27\nEX v147,v27\nVA v152,v152,v146\n"
"CM v31,v164,v31,s[86:87]\nEX v144,v28\nVA v152,v152,v147\nCV v85,v146,v147\nBL v161,s[40:43],s61 offen offset:2048 sc1 lds\nM8 v[48:63],a[216:219],a[152:155],v[48:63]\nDR a[240:243],v6 offset:9216\nEX v145,v29\n"
"VA v152,v152,v144\nM8 v[64:79],a[216:219],a[184:187],v[64:79]\nEX v146,v30\nVA v152,v152,v145\nCV v86,v144,v145\nM8 v[48:63],a[220:223],a[156:159],v[48:63]\nDR a[244:247],v6 offset:11264\nEX v147,v31\n"
"VA v152,v152,v146\nM8 v[64:79],a[220:223],a[188:191],v[64:79]\nEX v144,v32\nVA v152,v152,v147\nCV v87,v146,v147\nBL v161,s[40:43],s61 offen offset:3072 sc1 lds\nSW lgkmcnt(2)\nM8 a[0:15],a[224:227],v[96:99],a[0:15]\n"
"DR a[248:251],v6 offset:13312\nEX v145,v33\nM8 a[16:31],a[224:227],v[104:107],a[16:31]\nEX v146,v34\nVA v153,v144,v145\nCV v88,v144,v145\nSA s61,s61,16384\nSA s62,s62,0x8000\n"
"M8 a[32:47],a[228:231],v[96:99],a[32:47]\nDR a[252:255],v6 offset:15360\nEX v147,v35\nVA v153,v153,v146\nM8 a[48:63],a[228:231],v[104:107],a[48:63]\nEX v144,v36\nVA v153,v153,v147\nCV v89,v146,v147\n"
"M8 a[64:79],a[232:235],v[96:99],a[64:79]\nDR a[192:195],v5 offset:0\nEX v145,v37\nVA v153,v153,v144\nM8 a[80:95],a[232:235],v[104:107],a[80:95]\nEX v146,v38\nVA v153,v153,v145\nCV v90,v144,v145\n"
"EX v147,v39\nVA v153,v153,v146\nv_subrev_u32 v162,32,v162\nSA s64,s64,0x8000\nM8 a[96:111],a[236:239],v[96:99],a[96:111]\nDR a[196:199],v5 offset:1024\nEX v144,v40\nVA v153,v153,v147\n"
"CV v91,v146,v147\nM8 a[112:127],a[236:239],v[104:107],a[112:127]\nEX v145,v41\nVA v153,v153,v144\ns_and_b32 s67,s64,0x1ffff\nSW lgkmcnt(2)\nM8 a[0:15],a[240:243],v[100:103],a[0:15]\nDR a[200:203],v5 offset:2048\n"
"EX v146,v42\nVA v153,v153,v145\nCV v92,v144,v145\nM8 a[16:31],a[240:243],v[108:111],a[16:31]\nEX v147,v43\nVA v153,v153,v146\nVU v6,s67,v2\nM8 a[32:47],a[244:247],v[100:103],a[32:47]\n"
"DR a[204:207],v5 offset:3072\nEX v144,v44\nVA v153,v153,v147\nCV v93,v146,v147\nM8 a[48:63],a[244:247],v[108:111],a[48:63]\nEX v145,v45\nVA v153,v153,v144\nM8 a[64:79],a[248:251],v[100:103],a[64:79]\n"
"DR a[208:211],v5 offset:4096\nEX v146,v46\nVA v153,v153,v145\nCV v94,v144,v145\nM8 a[80:95],a[248:251],v[108:111],a[80:95]\nEX v147,v47\nVA v153,v153,v146\nM8 a[96:111],a[252:255],v[100:103],a[96:111]\n"
"VA v153,v153,v147\nCV v95,v146,v147\nDR a[212:215],v5 offset:5120\nv_max_f32 v170,v152,v153\nv_cmp_nge_f32 vcc,1.0,v170\nM8 a[112:127],a[252:255],v[108:111],a[112:127]\ns_cbranch_vccnz AT_d0m_fb\nAT_d0m_ret:\n"
"s_branch AT_dt2\nAT_d0am:\nCG s[80:81],v162,0\nCG s[82:83],v162,1\nCG s[84:85],v162,2\nCG s[86:87],v162,3\nCM v16,v164,v16,s[80:81]\nCM v17,v164,v17,s[82:83]\n"
"CM v18,v164,v18,s[84:85]\nCM v19,v164,v19,s[86:87]\nSW lgkmcnt(2)\nM8 v[48:63],a[192:195],a[128:131],v[112:127]\nDR a[216:219],v4 offset:14336\nCG s[80:81],v162,16\nEX v144,v16\ns_nop 0\n"
"CM v20,v164,v20,s[80:81]\nM8 v[64:79],a[192:195],a[160:163],v[128:143]\nCG s[82:83],v162,17\nEX v145,v17\ns_nop 0\nCM v21,v164,v21,s[82:83]\ns_and_b32 m0,s62,0x1ffff\nM8 v[48:63],a[196:199],a[132:135],v[48:63]\n"
"DR a[220:223],v4 offset:15360\nCG s[84:85],v162,18\nEX v146,v18\nVA v152,v144,v145\nCV v80,v144,v145\nCM v22,v164,v22,s[84:85]\nM8 v[64:79],a[196:199],a[164:167],v[64:79]\nCG s[86:87],v162,19\n"
"EX v147,v19\nVA v152,v152,v146\nCM v23,v164,v23,s[86:87]\nBL v161,s[40:43],s61 offen offset:0 sc1 lds\nM8 v[48:63],a[200:203],a[136:139],v[48:63]\nCG s[80:81],v162,8\nEX v144,v20\nVA v152,v152,v147\n"
"CV v81,v146,v147\nCM v24,v164,v24,s[80:81]\nM8 v[64:79],a[200:203],a[168:171],v[64:79]\nCG s[82:83],v162,9\nEX v145,v21\nVA v152,v152,v144\nCM v25,v164,v25,s[82:83]\nM8 v[48:63],a[204:207],a[140:143],v[48:63]\n"
"CG s[84:85],v162,10\nEX v146,v22\nVA v152,v152,v145\nCV v82,v144,v145\nCM v26,v164,v26,s[84:85]\nM8 v[64:79],a[204:207],a[172:175],v[64:79]\nCG s[86:87],v162,11\nEX v147,v23\n"
"VA v152,v152,v146\nCM v27,v164,v27,s[86:87]\nBL v161,s[40:43],s61 offen offset:1024 sc1 lds\nSW lgkmcnt(0)\nM8 v[48:63],a[208:211],a[144:147],v[48:63]\nCG s[80:81],v162,24\nEX v144,v24\nVA v152,v152,v147\n"
"CV v83,v146,v147\nCM v28,v164,v28,s[80:81]\nM8 v[64:79],a[208:211],a[176:179],v[64:79]\nCG s[82:83],v162,25\nEX v145,v25\nVA v152,v152,v144\nCM v29,v164,v29,s[82:83]\nM8 v[48:63],a[212:215],a[148:151],v[48:63]\n"
"CG s[84:85],v162,26\nEX v146,v26\nVA v152,v152,v145\nCV v84,v144,v145\nCM v30,v164,v30,s[84:85]\nM8 v[64:79],a[212:215],a[180:183],v[64:79]\nCG s[86:87],v162,27\nEX v147,v27\n"
"VA v152,v152,v146\nCM v31,v164,v31,s[86:87]\nEX v144,v28\nVA v152,v152,v147\nCV v85,v146,v147\nBL v161,s[40:43],s61 offen offset:2048 sc1 lds\nM8 v[48:63],a[216:219],a[152:155],v[48:63]\nEX v145,v29\n"
"VA v152,v152,v144\nM8 v[64:79],a[216:219],a[184:187],v[64:79]\nEX v146,v30\nVA v152,v152,v145\nCV v86,v144,v145\nM8 v[48:63],a[220:223],a[156:159],v[48:63]\nEX v147,v31\nVA v152,v152,v146\n"
"M8 v[64:79],a[220:223],a[188:191],v[64:79]\nEX v144,v32\nVA v152,v152,v147\nCV v87,v146,v147\nBL v161,s[40:43],s61 offen offset:3072 sc1 lds\nEX v145,v33\nEX v146,v34\nVA v153,v144,v145\n"
"CV v88,v144,v145\nSA s61,s61,16384\nSA s62,s62,0x8000\nEX v147,v35\nVA v153,v153,v146\nEX v144,v36\nVA v153,v153,v147\nCV v89,v146,v147\n"
"DR a[192:195],v5 offset:0\nEX v145,v37\nVA v153,v153,v144\nEX v146,v38\nVA v153,v153,v145\nCV v90,v144,v145\nEX v147,v39\nVA v153,v153,v146\n"
"v_subrev_u32 v162,32,v162\nSA s64,s64,0x8000\nDR a[196:199],v5 offset:1024\nEX v144,v40\nVA v153,v153,v147\nCV v91,v146,v147\nEX v145,v41\nVA v153,v153,v144\n"
"s_and_b32 s67,s64,0x1ffff\nDR a[200:203],v5 offset:2048\nEX v146,v42\nVA v153,v153,v145\nCV v92,v144,v145\nEX v147,v43\nVA v153,v153,v146\nVU v6,s67,v2\n"
"DR a[204:207],v5 offset:3072\nEX v144,v44\nVA v153,v153,v147\nCV v93,v146,v147\nEX v145,v45\nVA v153,v153,v144\nDR a[208:211],v5 offset:4096\nEX v146,v46\n"
"VA v153,v153,v145\nCV v94,v144,v145\nEX v147,v47\nVA v153,v153,v146\nVA v153,v153,v147\nCV v95,v146,v147\nDR a[212:215],v5 offset:5120\nv_max_f32 v170,v152,v153\n"
"v_cmp_nge_f32 vcc,1.0,v170\ns_cbranch_vccnz AT_d0am_fb\nAT_d0am_ret:\nAT_dt2:\nAT_d1m:\nCG s[80:81],v162,0\nCG s[82:83],v162,1\nCG s[84:85],v162,2\n"
"CG s[86:87],v162,3\nCM v48,v164,v48,s[80:81]\nCM v49,v164,v49,s[82:83]\nCM v50,v164,v50,s[84:85]\nCM v51,v164,v51,s[86:87]\nSW lgkmcnt(2)\nM8 v[16:31],a[192:195],a[128:131],v[112:127]\nDR a[216:219],v5 offset:6144\n"
"VA v154,v154,v152\nCG s[80:81],v162,16\nEX v144,v48\ns_nop 0\nCM v52,v164,v52,s[80:81]\nM8 v[32:47],a[192:195],a[160:163],v[128:143]\nVA v155,v155,v153\nCG s[82:83],v162,17\n"
"EX v145,v49\ns_nop 0\nCM v53,v164,v53,s[82:83]\ns_and_b32 m0,s60,0x1ffff\nM8 v[16:31],a[196:199],a[132:135],v[16:31]\nDR a[220:223],v5 offset:7168\nCG s[84:85],v162,18\nEX v146,v50\n"
"VA v152,v144,v145\nCV v96,v144,v145\nCM v54,v164,v54,s[84:85]\nM8 v[32:47],a[196:199],a[164:167],v[32:47]\nCG s[86:87],v162,19\nEX v147,v51\nVA v152,v152,v146\nCM v55,v164,v55,s[86:87]\n"
"BL v161,s[36:39],s59 offen offset:0 sc1 lds\nM8 v[16:31],a[200:203],a[136:139],v[16:31]\nDR a[224:227],v6 offset:0\nCG s[80:81],v162,8\nEX v144,v52\nVA v152,v152,v147\nCV v97,v146,v147\nCM v56,v164,v56,s[80:81]\n"
"M8 v[32:47],a[200:203],a[168:171],v[32:47]\nCG s[82:83],v162,9\nEX v145,v53\nVA v152,v152,v144\nCM v57,v164,v57,s[82:83]\nSA s63,s63,0x8000\nM8 v[16:31],a[204:207],a[140:143],v[16:31]\nDR a[228:231],v6 offset:2048\n"
"CG s[84:85],v162,10\nEX v146,v54\nVA v152,v152,v145\nCV v98,v144,v145\nCM v58,v164,v58,s[84:85]\nM8 v[32:47],a[204:207],a[172:175],v[32:47]\nCG s[86:87],v162,11\nEX v147,v55\n"
"VA v152,v152,v146\nCM v59,v164,v59,s[86:87]\ns_and_b32 s66,s63,0x1ffff\nBL v161,s[36:39],s59 offen offset:1024 sc1 lds\nSW lgkmcnt(2)\nM8 v[16:31],a[208:211],a[144:147],v[16:31]\nDR a[232:235],v6 offset:4096\nCG s[80:81],v162,24\n"
"EX v144,v56\nVA v152,v152,v147\nCV v99,v146,v147\nCM v60,v164,v60,s[80:81]\nM8 v[32:47],a[208:211],a[176:179],v[32:47]\nCG s[82:83],v162,25\nEX v145,v57\nVA v152,v152,v144\n"
"CM v61,v164,v61,s[82:83]\nM8 v[16:31],a[212:215],a[148:151],v[16:31]\nDR a[236:239],v6 offset:6144\nCG s[84:85],v162,26\nEX v146,v58\nVA v152,v152,v145\nCV v100,v144,v145\nCM v62,v164,v62,s[84:85]\n"
"M8 v[32:47],a[212:215],a[180:183],v[32:47]\nCG s[86:87],v162,27\nEX v147,v59\nVA v152,v152,v146\nCM v63,v164,v63,s[86:87]\nEX v144,v60\nVA v152,v152,v147\nCV v101,v146,v147\n"
"BL v161,s[36:39],s59 offen offset:2048 sc1 lds\nM8 v[16:31],a[216:219],a[152:155],v[16:31]\nDR a[240:243],v6 offset:1024\nEX v145,v61\nVA v152,v152,v144\nM8 v[32:47],a[216:219],a[184:187],v[32:47]\nEX v146,v62\nVA v152,v152,v145\n"
"CV v102,v144,v145\nM8 v[16:31],a[220:223],a[156:159],v[16:31]\nDR a[244:247],v6 offset:3072\nEX v147,v63\nVA v152,v152,v146\nM8 v[32:47],a[220:223],a[188:191],v[32:47]\nEX v144,v64\nVA v152,v152,v147\n"
"CV v103,v146,v147\nBL v161,s[36:39],s59 offen offset:3072 sc1 lds\nSW lgkmcnt(2)\nM8 a[0:15],a[224:227],v[80:83],a[0:15]\nDR a[248:251],v6 offset:5120\nEX v145,v65\nM8 a[16:31],a[224:227],v[88:91],a[16:31]\nEX v146,v66\n"
"VA v153,v144,v145\nCV v104,v144,v145\nSA s59,s59,16384\nSA s60,s60,0x8000\nM8 a[32:47],a[228:231],v[80:83],a[32:47]\nDR a[252:255],v6 offset:7168\nEX v147,v67\nVA v153,v153,v146\n"
"M8 a[48:63],a[228:231],v[88:91],a[48:63]\nEX v144,v68\nVA v153,v153,v147\nCV v105,v146,v147\nM8 a[64:79],a[232:235],v[80:83],a[64:79]\nDR a[192:195],v5 offset:8192\nEX v145,v69\nVA v153,v153,v144\n"
"M8 a[80:95],a[232:235],v[88:91],a[80:95]\nEX v146,v70\nVA v153,v153,v145\nCV v106,v144,v145\nEX v147,v71\nVA v153,v153,v146\nv_subrev_u32 v162,32,v162\nM8 a[96:111],a[236:239],v[80:83],a[96:111]\n"
"DR a[196:199],v5 offset:9216\nEX v144,v72\nVA v153,v153,v147\nCV v107,v146,v147\nM8 a[112:127],a[236:239],v[88:91],a[112:127]\nEX v145,v73\nVA v153,v153,v144\nSW lgkmcnt(2)\n"
"M8 a[0:15],a[240:243],v[84:87],a[0:15]\nDR a[200:203],v5 offset:10240\nEX v146,v74\nVA v153,v153,v145\nCV v108,v144,v145\nM8 a[16:31],a[240:243],v[92:95],a[16:31]\nEX v147,v75\nVA v153,v153,v146\n"
"M8 a[32:47],a[244:247],v[84:87],a[32:47]\nDR a[204:207],v5 offset:11264\nEX v144,v76\nVA v153,v153,v147\nCV v109,v146,v147\nM8 a[48:63],a[244:247],v[92:95],a[48:63]\nEX v145,v77\nVA v153,v153,v144\n"
"M8 a[64:79],a[248:251],v[84:87],a[64:79]\nDR a[208:211],v5 offset:12288\nEX v146,v78\nVA v153,v153,v145\nCV v110,v144,v145\nM8 a[80:95],a[248:251],v[92:95],a[80:95]\nEX v147,v79\nVA v153,v153,v146\n"
"M8 a[96:111],a[252:255],v[84:87],a[96:111]\nVA v153,v153,v147\nCV v111,v146,v147\nDR a[212:215],v5 offset:13312\nv_max_f32 v170,v152,v153\nv_cmp_nge_f32 vcc,1.0,v170\nM8 a[112:127],a[252:255],v[92:95],a[112:127]\nSW vmcnt(8)\n"
"s_barrier\nv_mov_b32 v4,v5\nVU v5,s66,v161\ns_cbranch_vccnz AT_d1m_fb\nAT_d1m_ret:\nAT_d0m2:\nCG s[80:81],v162,0\nCG s[82:83],v162,1\n"
"CG s[84:85],v162,2\nCG s[86:87],v162,3\nCM v16,v164,v16,s[80:81]\nCM v17,v164,v17,s[82:83]\nCM v18,v164,v18,s[84:85]\nCM v19,v164,v19,s[86:87]\nSW lgkmcnt(2)\nM8 v[48:63],a[192:195],a[128:131],v[112:127]\n"
"DR a[216:219],v4 offset:14336\nVA v154,v154,v152\nCG s[80:81],v162,16\nEX v144,v16\ns_nop 0\nCM v20,v164,v20,s[80:81]\nM8 v[64:79],a[192:195],a[160:163],v[128:143]\nVA v155,v155,v153\n"
"CG s[82:83],v162,17\nEX v145,v17\ns_nop 0\nCM v21,v164,v21,s[82:83]\ns_and_b32 m0,s62,0x1ffff\nM8 v[48:63],a[196:199],a[132:135],v[48:63]\nDR a[220:223],v4 offset:15360\nCG s[84:85],v162,18\n"
"EX v146,v18\nVA v152,v144,v145\nCV v80,v144,v145\nCM v22,v164,v22,s[84:85]\nM8 v[64:79],a[196:199],a[164:167],v[64:79]\nCG s[86:87],v162,19\nEX v147,v19\nVA v152,v152,v146\n"
"CM v23,v164,v23,s[86:87]\nBL v161,s[40:43],s61 offen offset:0 sc1 lds\nM8 v[48:63],a[200:203],a[136:139],v[48:63]\nDR a[224:227],v6 offset:8192\nCG s[80:81],v162,8\nEX v144,v20\nVA v152,v152,v147\nCV v81,v146,v147\n"
"CM v24,v164,v24,s[80:81]\nM8 v[64:79],a[200:203],a[168:171],v[64:79]\nCG s[82:83],v162,9\nEX v145,v21\nVA v152,v152,v144\nCM v25,v164,v25,s[82:83]\nM8 v[48:63],a[204:207],a[140:143],v[48:63]\nDR a[228:231],v6 offset:10240\n"
"CG s[84:85],v162,10\nEX v146,v22\nVA v152,v152,v145\nCV v82,v144,v145\nCM v26,v164,v26,s[84:85]\nM8 v[64:79],a[204:207],a[172:175],v[64:79]\nCG s[86:87],v162,11\nEX v147,v23\n"
"VA v152,v152,v146\nCM v27,v164,v27,s[86:87]\nBL v161,s[40:43],s61 offen offset:1024 sc1 lds\nSW lgkmcnt(2)\nM8 v[48:63],a[208:211],a[144:147],v[48:63]\nDR a[232:235],v6 offset:12288\nCG s[80:81],v162,24\nEX v144,v24\n"
"VA v152,v152,v147\nCV v83,v146,v147\nCM v28,v164,v28,s[80:81]\nM8 v[64:79],a[208:211],a[176:179],v[64:79]\nCG s[82:83],v162,25\nEX v145,v25\nVA v152,v152,v144\nCM v29,v164,v29,s[82:83]\n"
"M8 v[48:63],a[212:215],a[148:151],v[48:63]\nDR a[236:239],v6 offset:14336\nCG s[84:85],v162,26\nEX v146,v26\nVA v152,v152,v145\nCV v84,v144,v145\nCM v30,v164,v30,s[84:85]\nM8 v[64:79],a[212:215],a[180:183],v[64:79]\n"
"CG s[86:87],v162,27\nEX v147,v27\nVA v152,v152,v146\nCM v31,v164,v31,s[86:87]\nEX v144,v28\nVA v152,v152,v147\nCV v85,v146,v147\nBL v161,s[40:43],s61 offen offset:2048 sc1 lds\n"
"M8 v[48:63],a[216:219],a[152:155],v[48:63]\nDR a[240:243],v6 offset:9216\nEX v145,v29\nVA v152,v152,v144\nM8 v[64:79],a[216:219],a[184:187],v[64:79]\nEX v146,v30\nVA v152,v152,v145\nCV v86,v144,v145\n"
"M8 v[48:63],a[220:223],a[156:159],v[48:63]\nDR a[244:247],v6 offset:11264\nEX v147,v31\nVA v152,v152,v146\nM8 v[64:79],a[220:223],a[188:191],v[64:79]\nEX v144,v32\nVA v152,v152,v147\nCV v87,v146,v147\n"
"BL v161,s[40:43],s61 offen offset:3072 sc1 lds\nSW lgkmcnt(2)\nM8 a[0:15],a[224:227],v[96:99],a[0:15]\nDR a[248:251],v6 offset:13312\nEX v145,v33\nM8 a[16:31],a[224:227],v[104:107],a[16:31]\nEX v146,v34\nVA v153,v144,v145\n"
"CV v88,v144,v145\nSA s61,s61,16384\nSA s62,s62,0x8000\nM8 a[32:47],a[228:231],v[96:99],a[32:47]\nDR a[252:255],v6 offset:15360\nEX v147,v35\nVA v153,v153,v146\nM8 a[48:63],a[228:231],v[104:107],a[48:63]\n"
"EX v144,v36\nVA v153,v153,v147\nCV v89,v146,v147\nM8 a[64:79],a[232:235],v[96:99],a[64:79]\nDR a[192:195],v5 offset:0\nEX v145,v37\nVA v153,v153,v144\nM8 a[80:95],a[232:235],v[104:107],a[80:95]\n"
"EX v146,v38\nVA v153,v153,v145\nCV v90,v144,v145\nEX v147,v39\nVA v153,v153,v146\nv_subrev_u32 v162,32,v162\nSA s64,s64,0x8000\nM8 a[96:111],a[236:239],v[96:99],a[96:111]\n"
"DR a[196:199],v5 offset:1024\nEX v144,v40\nVA v153,v153,v147\nCV v91,v146,v147\nM8 a[112:127],a[236:239],v[104:107],a[112:127]\nEX v145,v41\nVA v153,v153,v144\ns_and_b32 s67,s64,0x1ffff\n"
"SW lgkmcnt(2)\nM8 a[0:15],a[240:243],v[100:103],a[0:15]\nDR a[200:203],v5 offset:2048\nEX v146,v42\nVA v153,v153,v145\nCV v92,v144,v145\nM8 a[16:31],a[240:243],v[108:111],a[16:31]\nEX v147,v43\n"
"VA v153,v153,v146\nVU v6,s67,v2\nM8 a[32:47],a[244:247],v[100:103],a[32:47]\nDR a[204:207],v5 offset:3072\nEX v144,v44\nVA v153,v153,v147\nCV v93,v146,v147\nM8 a[48:63],a[244:247],v[108:111],a[48:63]\n"
"EX v145,v45\nVA v153,v153,v144\nM8 a[64:79],a[248:251],v[100:103],a[64:79]\nDR a[208:211],v5 offset:4096\nEX v146,v46\nVA v153,v153,v145\nCV v94,v144,v145\nM8 a[80:95],a[248:251],v[108:111],a[80:95]\n"
"EX v147,v47\nVA v153,v153,v146\nM8 a[96:111],a[252:255],v[100:103],a[96:111]\nVA v153,v153,v147\nCV v95,v146,v147\nDR a[212:215],v5 offset:5120\nv_max_f32 v170,v152,v153\nv_cmp_nge_f32 vcc,1.0,v170\n"
"M8 a[112:127],a[252:255],v[108:111],a[112:127]\ns_cbranch_vccnz AT_d0m2_fb\nAT_d0m2_ret:\nAT_d1l:\nCG s[80:81],v162,0\nCG s[82:83],v162,1\nCG s[84:85],v162,2\nCG s[86:87],v162,3\n"
"CM v48,v164,v48,s[80:81]\nCM v49,v164,v49,s[82:83]\nCM v50,v164,v50,s[84:85]\nCM v51,v164,v51,s[86:87]\nVA v154,v154,v152\nCG s[80:81],v162,16\nEX v144,v48\ns_nop 0\n"
"CM v52,v164,v52,s[80:81]\nVA v155,v155,v153\nCG s[82:83],v162,17\nEX v145,v49\ns_nop 0\nCM v53,v164,v53,s[82:83]\ns_and_b32 m0,s60,0x1ffff\nCG s[84:85],v162,18\n"
"EX v146,v50\nVA v152,v144,v145\nCV v96,v144,v145\nCM v54,v164,v54,s[84:85]\nCG s[86:87],v162,19\nEX v147,v51\nVA v152,v152,v146\nCM v55,v164,v55,s[86:87]\n"
"BL v161,s[36:39],s59 offen offset:0 sc1 lds\nDR a[224:227],v6 offset:0\nCG s[80:81],v162,8\nEX v144,v52\nVA v152,v152,v147\nCV v97,v146,v147\nCM v56,v164,v56,s[80:81]\nCG s[82:83],v162,9\n"
"EX v145,v53\nVA v152,v152,v144\nCM v57,v164,v57,s[82:83]\nSA s63,s63,0x8000\nDR a[228:231],v6 offset:2048\nCG s[84:85],v162,10\nEX v146,v54\nVA v152,v152,v145\n"
"CV v98,v144,v145\nCM v58,v164,v58,s[84:85]\nCG s[86:87],v162,11\nEX v147,v55\nVA v152,v152,v146\nCM v59,v164,v59,s[86:87]\ns_and_b32 s66,s63,0x1ffff\nBL v161,s[36:39],s59 offen offset:1024 sc1 lds\n"
"DR a[232:235],v6 offset:4096\nCG s[80:81],v162,24\nEX v144,v56\nVA v152,v152,v147\nCV v99,v146,v147\nCM v60,v164,v60,s[80:81]\nCG s[82:83],v162,25\nEX v145,v57\n"
"VA v152,v152,v144\nCM v61,v164,v61,s[82:83]\nDR a[236:239],v6 offset:6144\nCG s[84:85],v162,26\nEX v146,v58\nVA v152,v152,v145\nCV v100,v144,v145\nCM v62,v164,v62,s[84:85]\n"
"CG s[86:87],v162,27\nEX v147,v59\nVA v152,v152,v146\nCM v63,v164,v63,s[86:87]\nEX v144,v60\nVA v152,v152,v147\nCV v101,v146,v147\nBL v161,s[36:39],s59 offen offset:2048 sc1 lds\n"
"DR a[240:243],v6 offset:1024\nEX v145,v61\nVA v152,v152,v144\nEX v146,v62\nVA v152,v152,v145\nCV v102,v144,v145\nDR a[244:247],v6 offset:3072\nEX v147,v63\n"
"VA v152,v152,v146\nEX v144,v64\nVA v152,v152,v147\nCV v103,v146,v147\nBL v161,s[36:39],s59 offen offset:3072 sc1 lds\nSW lgkmcnt(2)\nM8 a[0:15],a[224:227],v[80:83],a[0:15]\nDR a[248:251],v6 offset:5120\n"
"EX v145,v65\nM8 a[16:31],a[224:227],v[88:91],a[16:31]\nEX v146,v66\nVA v153,v144,v145\nCV v104,v144,v145\nSA s59,s59,16384\nSA s60,s60,0x8000\nM8 a[32:47],a[228:231],v[80:83],a[32:47]\n"
"DR a[252:255],v6 offset:7168\nEX v147,v67\nVA v153,v153,v146\nM8 a[48:63],a[228:231],v[88:91],a[48:63]\nEX v144,v68\nVA v153,v153,v147\nCV v105,v146,v147\nM8 a[64:79],a[232:235],v[80:83],a[64:79]\n"
"EX v145,v69\nVA v153,v153,v144\nM8 a[80:95],a[232:235],v[88:91],a[80:95]\nEX v146,v70\nVA v153,v153,v145\nCV v106,v144,v145\nEX v147,v71\nVA v153,v153,v146\n"
"v_subrev_u32 v162,32,v162\nM8 a[96:111],a[236:239],v[80:83],a[96:111]\nEX v144,v72\nVA v153,v153,v147\nCV v107,v146,v147\nM8 a[112:127],a[236:239],v[88:91],a[112:127]\nEX v145,v73\nVA v153,v153,v144\n"
"SW lgkmcnt(0)\nM8 a[0:15],a[240:243],v[84:87],a[0:15]\nEX v146,v74\nVA v153,v153,v145\nCV v108,v144,v145\nM8 a[16:31],a[240:243],v[92:95],a[16:31]\nEX v147,v75\nVA v153,v153,v146\n"
"M8 a[32:47],a[244:247],v[84:87],a[32:47]\nEX v144,v76\nVA v153,v153,v147\nCV v109,v146,v147\nM8 a[48:63],a[244:247],v[92:95],a[48:63]\nEX v145,v77\nVA v153,v153,v144\nM8 a[64:79],a[248:251],v[84:87],a[64:79]\n"
"EX v146,v78\nVA v153,v153,v145\nCV v110,v144,v145\nM8 a[80:95],a[248:251],v[92:95],a[80:95]\nEX v147,v79\nVA v153,v153,v146\nM8 a[96:111],a[252:255],v[84:87],a[96:111]\nVA v153,v153,v147\n"
"CV v111,v146,v147\nv_max_f32 v170,v152,v153\nv_cmp_nge_f32 vcc,1.0,v170\nM8 a[112:127],a[252:255],v[92:95],a[112:127]\nSW vmcnt(8)\ns_barrier\nv_mov_b32 v4,v5\nVU v5,s66,v161\n"
"s_cbranch_vccnz AT_d1l_fb\nAT_d1l_ret:\ns_and_b32 m0,s60,0x1ffff\ns_nop 0\nBL v161,s[36:39],s59 offen offset:0 sc1 lds\nBL v161,s[36:39],s59 offen offset:1024 sc1 lds\nBL v161,s[36:39],s59 offen offset:2048 sc1 lds\nBL v161,s[36:39],s59 offen offset:3072 sc1 lds\n"
"SA s59,s59,16384\nSA s60,s60,0x8000\nDR a[224:227],v6 offset:8192\nDR a[228:231],v6 offset:10240\nDR a[232:235],v6 offset:12288\nDR a[236:239],v6 offset:14336\nDR a[240:243],v6 offset:9216\nDR a[244:247],v6 offset:11264\n"
"DR a[248:251],v6 offset:13312\nDR a[252:255],v6 offset:15360\nVA v154,v154,v152\nVA v155,v155,v153\nSW lgkmcnt(6)\nM8 a[0:15],a[224:227],v[96:99],a[0:15]\nM8 a[16:31],a[224:227],v[104:107],a[16:31]\nM8 a[32:47],a[228:231],v[96:99],a[32:47]\n"
"M8 a[48:63],a[228:231],v[104:107],a[48:63]\nSW lgkmcnt(4)\nM8 a[64:79],a[232:235],v[96:99],a[64:79]\nM8 a[80:95],a[232:235],v[104:107],a[80:95]\nM8 a[96:111],a[236:239],v[96:99],a[96:111]\nM8 a[112:127],a[236:239],v[104:107],a[112:127]\nSW lgkmcnt(2)\nM8 a[0:15],a[240:243],v[100:103],a[0:15]\n"
"M8 a[16:31],a[240:243],v[108:111],a[16:31]\nM8 a[32:47],a[244:247],v[100:103],a[32:47]\nM8 a[48:63],a[244:247],v[108:111],a[48:63]\nSW lgkmcnt(0)\nM8 a[64:79],a[248:251],v[100:103],a[64:79]\nM8 a[80:95],a[248:251],v[108:111],a[80:95]\nM8 a[96:111],a[252:255],v[100:103],a[96:111]\nM8 a[112:127],a[252:255],v[108:111],a[112:127]\n"
"s_lshl_b32 s63,s57,15\ns_and_b32 s66,s63,0x1ffff\nVU v158,s66,v165\nDR a[192:195],v158 offset:0\nDR a[196:199],v158 offset:1024\nDR a[200:203],v158 offset:2048\nDR a[204:207],v158 offset:3072\nDR a[208:211],v158 offset:4096\n"
"DR a[212:215],v158 offset:5120\nDR a[216:219],v158 offset:6144\nDR a[220:223],v158 offset:7168\nDR a[128:131],v3 offset:0\nDR a[132:135],v3 offset:1024\nDR a[136:139],v3 offset:2048\nDR a[140:143],v3 offset:3072\nDR a[144:147],v3 offset:4096\n"
"DR a[148:151],v3 offset:5120\nDR a[152:155],v3 offset:6144\nDR a[156:159],v3 offset:7168\nv_mov_b32 v170,v154\ns_nop 1\nv_permlane32_swap_b32 v154,v170\ns_nop 1\nVA v154,v154,v170\n"
"RC v172,v154\ns_lshl_b32 s66,s55,3\ns_lshl_b32 s67,s89,1\nSA s66,s66,s67\ns_mul_i32 s66,s66,182\ns_lshl_b32 s67,s54,2\nSA s66,s66,s67\ns_lshl_b32 s69,s66,10\n"
"v_mov_b32 v173,v172\ns_nop 3\nAR v48,a0\nAR v49,a1\nAR v50,a2\nAR v51,a3\nAR v52,a4\nAR v53,a5\n"
"AR v54,a6\nAR v55,a7\nAR v56,a8\nAR v57,a9\nAR v58,a10\nAR v59,a11\nAR v60,a12\nAR v61,a13\n"
"AR v62,a14\nAR v63,a15\nPM v[48:49],v[48:49],v[172:173]\nPM v[50:51],v[50:51],v[172:173]\nPM v[52:53],v[52:53],v[172:173]\nPM v[54:55],v[54:55],v[172:173]\nPM v[56:57],v[56:57],v[172:173]\nPM v[58:59],v[58:59],v[172:173]\n"
"PM v[60:61],v[60:61],v[172:173]\nPM v[62:63],v[62:63],v[172:173]\nCV v80,v48,v49\nCV v81,v50,v51\nCV v82,v56,v57\nCV v83,v58,v59\nBS v[80:83],v168,s[48:51],s69 offen offset:0 sc1\nCV v84,v52,v53\n"
"CV v85,v54,v55\nCV v86,v60,v61\nCV v87,v62,v63\nBS v[84:87],v168,s[48:51],s69 offen offset:512 sc1\nAR v64,a32\nAR v65,a33\nAR v66,a34\nAR v67,a35\n"
"AR v68,a36\nAR v69,a37\nAR v70,a38\nAR v71,a39\nAR v72,a40\nAR v73,a41\nAR v74,a42\nAR v75,a43\n"
"AR v76,a44\nAR v77,a45\nAR v78,a46\nAR v79,a47\nPM v[64:65],v[64:65],v[172:173]\nPM v[66:67],v[66:67],v[172:173]\nPM v[68:69],v[68:69],v[172:173]\nPM v[70:71],v[70:71],v[172:173]\n"
"PM v[72:73],v[72:73],v[172:173]\nPM v[74:75],v[74:75],v[172:173]\nPM v[76:77],v[76:77],v[172:173]\nPM v[78:79],v[78:79],v[172:173]\nCV v88,v64,v65\nCV v89,v66,v67\nCV v90,v72,v73\nCV v91,v74,v75\n"
"BS v[88:91],v168,s[48:51],s69 offen offset:1024 sc1\nCV v92,v68,v69\nCV v93,v70,v71\nCV v94,v76,v77\nCV v95,v78,v79\nBS v[92:95],v168,s[48:51],s69 offen offset:1536 sc1\nAR v48,a64\nAR v49,a65\n"
"AR v50,a66\nAR v51,a67\nAR v52,a68\nAR v53,a69\nAR v54,a70\nAR v55,a71\nAR v56,a72\nAR v57,a73\n"
"AR v58,a74\nAR v59,a75\nAR v60,a76\nAR v61,a77\nAR v62,a78\nAR v63,a79\nPM v[48:49],v[48:49],v[172:173]\nPM v[50:51],v[50:51],v[172:173]\n"
"PM v[52:53],v[52:53],v[172:173]\nPM v[54:55],v[54:55],v[172:173]\nPM v[56:57],v[56:57],v[172:173]\nPM v[58:59],v[58:59],v[172:173]\nPM v[60:61],v[60:61],v[172:173]\nPM v[62:63],v[62:63],v[172:173]\nCV v80,v48,v49\nCV v81,v50,v51\n"
"CV v82,v56,v57\nCV v83,v58,v59\nBS v[80:83],v168,s[48:51],s69 offen offset:2048 sc1\nCV v84,v52,v53\nCV v85,v54,v55\nCV v86,v60,v61\nCV v87,v62,v63\nBS v[84:87],v168,s[48:51],s69 offen offset:2560 sc1\n"
"AR v64,a96\nAR v65,a97\nAR v66,a98\nAR v67,a99\nAR v68,a100\nAR v69,a101\nAR v70,a102\nAR v71,a103\n"
"AR v72,a104\nAR v73,a105\nAR v74,a106\nAR v75,a107\nAR v76,a108\nAR v77,a109\nAR v78,a110\nAR v79,a111\n"
"PM v[64:65],v[64:65],v[172:173]\nPM v[66:67],v[66:67],v[172:173]\nPM v[68:69],v[68:69],v[172:173]\nPM v[70:71],v[70:71],v[172:173]\nPM v[72:73],v[72:73],v[172:173]\nPM v[74:75],v[74:75],v[172:173]\nPM v[76:77],v[76:77],v[172:173]\nPM v[78:79],v[78:79],v[172:173]\n"
"CV v88,v64,v65\nCV v89,v66,v67\nCV v90,v72,v73\nCV v91,v74,v75\nBS v[88:91],v168,s[48:51],s69 offen offset:3072 sc1\nCV v92,v68,v69\nCV v93,v70,v71\nCV v94,v76,v77\n"
"CV v95,v78,v79\nBS v[92:95],v168,s[48:51],s69 offen offset:3584 sc1\ns_cmp_eq_u32 s89,0\ns_cbranch_scc0 SG_g\ns_lshl_b32 s34,s88,3\nSA s34,s34,63987712\nv_mov_b32 v8,s90\nv_mov_b32 v9,s91\n"
"VL v10,11,v200\nVU v10,s34,v10\nv_mov_b32 v11,s93\nv_add_co_u32 v10,vcc,s92,v10\ns_nop 1\nv_addc_co_u32 v11,vcc,0,v11,vcc\ns_mov_b64 exec,0xff\nglobal_store_dwordx2 v[10:11],v[8:9],off sc1\n"
"s_mov_b64 exec,-1\nSG_g:\nv_mov_b32 v148,0\nv_mov_b32 v149,0\nv_mov_b32 v150,0\nv_mov_b32 v151,0\ns_nop 1\nM8 a[0:15],v[148:151],v[148:151],0\n"
"M8 a[32:47],v[148:151],v[148:151],0\nM8 a[64:79],v[148:151],v[148:151],0\nM8 a[96:111],v[148:151],v[148:151],0\nVN v170,31,v200\nv_lshrrev_b32 v171,5,v200\nVL v171,2,v171\nv_sub_u32 v170,v170,v171\ns_lshl_b32 s66,s74,7\n"
"s_lshl_b32 s67,s57,6\ns_sub_u32 s66,s66,s67\ns_lshl_b32 s67,s53,5\ns_sub_u32 s66,s66,s67\ns_lshl_b32 s67,s89,5\nSA s67,s67,s66\nVU v163,s67,v170\ns_xor_b32 s67,s89,1\n"
"s_lshl_b32 s67,s67,5\nSA s67,s67,s66\nVU v162,s67,v170\nSW lgkmcnt(0)\nM8 v[16:31],a[192:195],a[128:131],0\nM8 v[32:47],a[192:195],a[160:163],v[128:143]\nM8 v[16:31],a[196:199],a[132:135],v[16:31]\nM8 v[32:47],a[196:199],a[164:167],v[32:47]\n"
"M8 v[16:31],a[200:203],a[136:139],v[16:31]\nM8 v[32:47],a[200:203],a[168:171],v[32:47]\nM8 v[16:31],a[204:207],a[140:143],v[16:31]\nM8 v[32:47],a[204:207],a[172:175],v[32:47]\nM8 v[16:31],a[208:211],a[144:147],v[16:31]\nM8 v[32:47],a[208:211],a[176:179],v[32:47]\nM8 v[16:31],a[212:215],a[148:151],v[16:31]\nM8 v[32:47],a[212:215],a[180:183],v[32:47]\n"
"M8 v[16:31],a[216:219],a[152:155],v[16:31]\nM8 v[32:47],a[216:219],a[184:187],v[32:47]\nM8 v[16:31],a[220:223],a[156:159],v[16:31]\nM8 v[32:47],a[220:223],a[188:191],v[32:47]\nv_mov_b32 v154,0\nSA s63,s63,0x8000\ns_and_b32 s66,s63,0x1ffff\nVU v159,s66,v165\n"
"s_lshl_b32 s64,s57,15\ns_and_b32 s66,s64,0x1ffff\nVU v160,s66,v166\nSW vmcnt(12)\ns_barrier\nDR a[192:195],v159 offset:0\nDR a[196:199],v159 offset:1024\nDR a[200:203],v159 offset:2048\n"
"DR a[204:207],v159 offset:3072\nDR a[208:211],v159 offset:4096\nDR a[212:215],v159 offset:5120\ns_cmp_eq_u32 s56,2\ns_cbranch_scc0 AT_nomask2\ns_nop 7\nCG s[80:81],v162,0\nCG s[82:83],v162,1\n"
"CG s[84:85],v162,2\nCG s[86:87],v162,3\nCM v16,v164,v16,s[80:81]\nCM v17,v164,v17,s[82:83]\nCM v18,v164,v18,s[84:85]\nCM v19,v164,v19,s[86:87]\nCG s[80:81],v162,16\nCG s[82:83],v162,17\n"
"CG s[84:85],v162,18\nCG s[86:87],v162,19\nCM v20,v164,v20,s[80:81]\nCM v21,v164,v21,s[82:83]\nCM v22,v164,v22,s[84:85]\nCM v23,v164,v23,s[86:87]\nCG s[80:81],v162,8\nCG s[82:83],v162,9\n"
"CG s[84:85],v162,10\nCG s[86:87],v162,11\nCM v24,v164,v24,s[80:81]\nCM v25,v164,v25,s[82:83]\nCM v26,v164,v26,s[84:85]\nCM v27,v164,v27,s[86:87]\nCG s[80:81],v162,24\nCG s[82:83],v162,25\n"
"CG s[84:85],v162,26\nCG s[86:87],v162,27\nCM v28,v164,v28,s[80:81]\nCM v29,v164,v29,s[82:83]\nCM v30,v164,v30,s[84:85]\nCM v31,v164,v31,s[86:87]\nCG s[80:81],v163,0\nCG s[82:83],v163,1\n"
"CG s[84:85],v163,2\nCG s[86:87],v163,3\nCM v32,v164,v32,s[80:81]\nCM v33,v164,v33,s[82:83]\nCM v34,v164,v34,s[84:85]\nCM v35,v164,v35,s[86:87]\nCG s[80:81],v163,16\nCG s[82:83],v163,17\n"
"CG s[84:85],v163,18\nCG s[86:87],v163,19\nCM v36,v164,v36,s[80:81]\nCM v37,v164,v37,s[82:83]\nCM v38,v164,v38,s[84:85]\nCM v39,v164,v39,s[86:87]\nCG s[80:81],v163,8\nCG s[82:83],v163,9\n"
"CG s[84:85],v163,10\nCG s[86:87],v163,11\nCM v40,v164,v40,s[80:81]\nCM v41,v164,v41,s[82:83]\nCM v42,v164,v42,s[84:85]\nCM v43,v164,v43,s[86:87]\nCG s[80:81],v163,24\nCG s[82:83],v163,25\n"
"CG s[84:85],v163,26\nCG s[86:87],v163,27\nCM v44,v164,v44,s[80:81]\nCM v45,v164,v45,s[82:83]\nCM v46,v164,v46,s[84:85]\nCM v47,v164,v47,s[86:87]\nAT_nomask2:\nv_max3_f32 v156,v16,v17,v18\n"
"v_max3_f32 v172,v19,v20,v21\nv_max3_f32 v156,v156,v22,v23\nv_max3_f32 v172,v172,v24,v25\nv_max3_f32 v156,v156,v26,v27\nv_max3_f32 v172,v172,v28,v29\nv_max3_f32 v156,v156,v30,v31\nv_max_f32 v156,v156,v172\nv_mov_b32 v172,v156\n"
"s_nop 1\nv_permlane32_swap_b32 v156,v172\ns_nop 1\nv_max_f32 v156,v156,v172\nv_max_f32 v156,0xf149f2ca,v156\nVA v156,0x41f00000,v156\nVS v112,0,v156\nVS v113,0,v156\n"
"VS v114,0,v156\nVS v115,0,v156\nVS v116,0,v156\nVS v117,0,v156\nVS v118,0,v156\nVS v119,0,v156\nVS v120,0,v156\nVS v121,0,v156\n"
"VS v122,0,v156\nVS v123,0,v156\nVS v124,0,v156\nVS v125,0,v156\nVS v126,0,v156\nVS v127,0,v156\nVS v16,v16,v156\nVS v17,v17,v156\n"
"VS v18,v18,v156\nVS v19,v19,v156\nVS v20,v20,v156\nVS v21,v21,v156\nVS v22,v22,v156\nVS v23,v23,v156\nVS v24,v24,v156\nVS v25,v25,v156\n"
"VS v26,v26,v156\nVS v27,v27,v156\nVS v28,v28,v156\nVS v29,v29,v156\nVS v30,v30,v156\nVS v31,v31,v156\ns_sub_u32 s66,s56,2\ns_lshl_b32 s66,s66,6\n"
"v_subrev_u32 v162,s66,v162\nv_subrev_u32 v163,s66,v163\ns_cmp_eq_u32 s56,2\ns_cbranch_scc1 AT_s0m\nAT_s0:\nSW lgkmcnt(2)\nM8 v[48:63],a[192:195],a[128:131],v[112:127]\nDR a[216:219],v159 offset:6144\n"
"EX v144,v16\nM8 v[64:79],a[192:195],a[160:163],v[128:143]\nEX v145,v17\ns_and_b32 m0,s60,0x1ffff\nM8 v[48:63],a[196:199],a[132:135],v[48:63]\nDR a[220:223],v159 offset:7168\nEX v146,v18\nVA v152,v144,v145\n"
"CV v80,v144,v145\nM8 v[64:79],a[196:199],a[164:167],v[64:79]\nEX v147,v19\nVA v152,v152,v146\nBL v161,s[36:39],s59 offen offset:0 sc1 lds\nM8 v[48:63],a[200:203],a[136:139],v[48:63]\nEX v144,v20\nVA v152,v152,v147\n"
"CV v81,v146,v147\nM8 v[64:79],a[200:203],a[168:171],v[64:79]\nEX v145,v21\nVA v152,v152,v144\nSA s63,s63,0x8000\nBL v161,s[36:39],s59 offen offset:1024 sc1 lds\nM8 v[48:63],a[204:207],a[140:143],v[48:63]\nEX v146,v22\n"
"VA v152,v152,v145\nCV v82,v144,v145\nM8 v[64:79],a[204:207],a[172:175],v[64:79]\nEX v147,v23\nVA v152,v152,v146\ns_and_b32 s66,s63,0x1ffff\nBL v161,s[36:39],s59 offen offset:2048 sc1 lds\nSW lgkmcnt(0)\n"
"M8 v[48:63],a[208:211],a[144:147],v[48:63]\nEX v144,v24\nVA v152,v152,v147\nCV v83,v146,v147\nM8 v[64:79],a[208:211],a[176:179],v[64:79]\nEX v145,v25\nVA v152,v152,v144\nVU v158,s66,v165\n"
"BL v161,s[36:39],s59 offen offset:3072 sc1 lds\nM8 v[48:63],a[212:215],a[148:151],v[48:63]\nEX v146,v26\nVA v152,v152,v145\nCV v84,v144,v145\nM8 v[64:79],a[212:215],a[180:183],v[64:79]\nEX v147,v27\nVA v152,v152,v146\n"
"EX v144,v28\nVA v152,v152,v147\nCV v85,v146,v147\nSA s59,s59,16384\nSA s60,s60,0x8000\nM8 v[48:63],a[216:219],a[152:155],v[48:63]\nEX v145,v29\nVA v152,v152,v144\n"
"M8 v[64:79],a[216:219],a[184:187],v[64:79]\nEX v146,v30\nVA v152,v152,v145\nCV v86,v144,v145\ns_and_b32 m0,s62,0x1ffff\nM8 v[48:63],a[220:223],a[156:159],v[48:63]\nEX v147,v31\nVA v152,v152,v146\n"
"M8 v[64:79],a[220:223],a[188:191],v[64:79]\nEX v144,v32\nVA v152,v152,v147\nCV v87,v146,v147\nBL v161,s[40:43],s61 offen offset:0 sc1 lds\nEX v145,v33\nEX v146,v34\nVA v153,v144,v145\n"
"CV v88,v144,v145\nBL v161,s[40:43],s61 offen offset:1024 sc1 lds\nEX v147,v35\nVA v153,v153,v146\nEX v144,v36\nVA v153,v153,v147\nCV v89,v146,v147\nBL v161,s[40:43],s61 offen offset:2048 sc1 lds\n"
"DR a[192:195],v158 offset:0\nEX v145,v37\nVA v153,v153,v144\nEX v146,v38\nVA v153,v153,v145\nCV v90,v144,v145\nEX v147,v39\nVA v153,v153,v146\n"
"BL v161,s[40:43],s61 offen offset:3072 sc1 lds\nDR a[196:199],v158 offset:1024\nEX v144,v40\nVA v153,v153,v147\nCV v91,v146,v147\nEX v145,v41\nVA v153,v153,v144\nDR a[200:203],v158 offset:2048\n"
"EX v146,v42\nVA v153,v153,v145\nCV v92,v144,v145\nEX v147,v43\nVA v153,v153,v146\nDR a[204:207],v158 offset:3072\nEX v144,v44\nVA v153,v153,v147\n"
"CV v93,v146,v147\nEX v145,v45\nVA v153,v153,v144\nSA s61,s61,16384\nSA s62,s62,0x8000\nDR a[208:211],v158 offset:4096\nEX v146,v46\nVA v153,v153,v145\n"
"CV v94,v144,v145\nEX v147,v47\nVA v153,v153,v146\nVA v153,v153,v147\nCV v95,v146,v147\nDR a[212:215],v158 offset:5120\nv_max_f32 v170,v152,v153\nv_cmp_nge_f32 vcc,1.0,v170\n"
"SW vmcnt(12)\ns_barrier\ns_cbranch_vccnz AT_s0_fb\nAT_s0_ret:\ns_sub_u32 s58,s56,4\ns_lshr_b32 s58,s58,1\ns_cmp_eq_u32 s58,0\ns_cbranch_scc1 AT_tail\n"
"AT_lo:\nSW lgkmcnt(2)\nM8 v[16:31],a[192:195],a[128:131],v[112:127]\nDR a[216:219],v158 offset:6144\nVA v154,v154,v152\nEX v144,v48\nM8 v[32:47],a[192:195],a[160:163],v[128:143]\nVA v155,v155,v153\n"
"EX v145,v49\ns_and_b32 m0,s60,0x1ffff\nM8 v[16:31],a[196:199],a[132:135],v[16:31]\nDR a[220:223],v158 offset:7168\nEX v146,v50\nVA v152,v144,v145\nCV v96,v144,v145\nM8 v[32:47],a[196:199],a[164:167],v[32:47]\n"
"EX v147,v51\nVA v152,v152,v146\nBL v161,s[36:39],s59 offen offset:0 sc1 lds\nM8 v[16:31],a[200:203],a[136:139],v[16:31]\nDR a[224:227],v160 offset:0\nEX v144,v52\nVA v152,v152,v147\nCV v97,v146,v147\n"
"M8 v[32:47],a[200:203],a[168:171],v[32:47]\nEX v145,v53\nVA v152,v152,v144\nSA s63,s63,0x8000\nBL v161,s[36:39],s59 offen offset:1024 sc1 lds\nM8 v[16:31],a[204:207],a[140:143],v[16:31]\nDR a[228:231],v160 offset:2048\nEX v146,v54\n"
"VA v152,v152,v145\nCV v98,v144,v145\nM8 v[32:47],a[204:207],a[172:175],v[32:47]\nEX v147,v55\nVA v152,v152,v146\ns_and_b32 s66,s63,0x1ffff\nBL v161,s[36:39],s59 offen offset:2048 sc1 lds\nSW lgkmcnt(2)\n"
"M8 v[16:31],a[208:211],a[144:147],v[16:31]\nDR a[232:235],v160 offset:4096\nEX v144,v56\nVA v152,v152,v147\nCV v99,v146,v147\nM8 v[32:47],a[208:211],a[176:179],v[32:47]\nEX v145,v57\nVA v152,v152,v144\n"
"VU v159,s66,v165\nBL v161,s[36:39],s59 offen offset:3072 sc1 lds\nM8 v[16:31],a[212:215],a[148:151],v[16:31]\nDR a[236:239],v160 offset:6144\nEX v146,v58\nVA v152,v152,v145\nCV v100,v144,v145\nM8 v[32:47],a[212:215],a[180:183],v[32:47]\n"
"EX v147,v59\nVA v152,v152,v146\nEX v144,v60\nVA v152,v152,v147\nCV v101,v146,v147\nSA s59,s59,16384\nSA s60,s60,0x8000\nM8 v[16:31],a[216:219],a[152:155],v[16:31]\n"
"DR a[240:243],v160 offset:1024\nEX v145,v61\nVA v152,v152,v144\nM8 v[32:47],a[216:219],a[184:187],v[32:47]\nEX v146,v62\nVA v152,v152,v145\nCV v102,v144,v145\ns_and_b32 m0,s62,0x1ffff\n"
"M8 v[16:31],a[220:223],a[156:159],v[16:31]\nDR a[244:247],v160 offset:3072\nEX v147,v63\nVA v152,v152,v146\nM8 v[32:47],a[220:223],a[188:191],v[32:47]\nEX v144,v64\nVA v152,v152,v147\nCV v103,v146,v147\n"
"BL v161,s[40:43],s61 offen offset:0 sc1 lds\nSW lgkmcnt(2)\nM8 a[0:15],a[224:227],v[80:83],a[0:15]\nDR a[248:251],v160 offset:5120\nEX v145,v65\nM8 a[16:31],a[224:227],v[88:91],a[16:31]\nEX v146,v66\nVA v153,v144,v145\n"
"CV v104,v144,v145\nBL v161,s[40:43],s61 offen offset:1024 sc1 lds\nM8 a[32:47],a[228:231],v[80:83],a[32:47]\nDR a[252:255],v160 offset:7168\nEX v147,v67\nVA v153,v153,v146\nM8 a[48:63],a[228:231],v[88:91],a[48:63]\nEX v144,v68\n"
"VA v153,v153,v147\nCV v105,v146,v147\nBL v161,s[40:43],s61 offen offset:2048 sc1 lds\nM8 a[64:79],a[232:235],v[80:83],a[64:79]\nDR a[192:195],v159 offset:0\nEX v145,v69\nVA v153,v153,v144\nM8 a[80:95],a[232:235],v[88:91],a[80:95]\n"
"EX v146,v70\nVA v153,v153,v145\nCV v106,v144,v145\nEX v147,v71\nVA v153,v153,v146\nSA s64,s64,0x8000\nBL v161,s[40:43],s61 offen offset:3072 sc1 lds\nM8 a[96:111],a[236:239],v[80:83],a[96:111]\n"
"DR a[196:199],v159 offset:1024\nEX v144,v72\nVA v153,v153,v147\nCV v107,v146,v147\nM8 a[112:127],a[236:239],v[88:91],a[112:127]\nEX v145,v73\nVA v153,v153,v144\ns_and_b32 s67,s64,0x1ffff\n"
"SW lgkmcnt(2)\nM8 a[0:15],a[240:243],v[84:87],a[0:15]\nDR a[200:203],v159 offset:2048\nEX v146,v74\nVA v153,v153,v145\nCV v108,v144,v145\nM8 a[16:31],a[240:243],v[92:95],a[16:31]\nEX v147,v75\n"
"VA v153,v153,v146\nVU v160,s67,v166\nM8 a[32:47],a[244:247],v[84:87],a[32:47]\nDR a[204:207],v159 offset:3072\nEX v144,v76\nVA v153,v153,v147\nCV v109,v146,v147\nM8 a[48:63],a[244:247],v[92:95],a[48:63]\n"
"EX v145,v77\nVA v153,v153,v144\nSA s61,s61,16384\nSA s62,s62,0x8000\nM8 a[64:79],a[248:251],v[84:87],a[64:79]\nDR a[208:211],v159 offset:4096\nEX v146,v78\nVA v153,v153,v145\n"
"CV v110,v144,v145\nM8 a[80:95],a[248:251],v[92:95],a[80:95]\nEX v147,v79\nVA v153,v153,v146\nM8 a[96:111],a[252:255],v[84:87],a[96:111]\nVA v153,v153,v147\nCV v111,v146,v147\nDR a[212:215],v159 offset:5120\n"
"v_max_f32 v170,v152,v153\nv_cmp_nge_f32 vcc,1.0,v170\nM8 a[112:127],a[252:255],v[92:95],a[112:127]\nSW vmcnt(12)\ns_barrier\ns_cbranch_vccnz AT_lo_fb\nAT_lo_ret:\nAT_le:\n"
"SW lgkmcnt(2)\nM8 v[48:63],a[192:195],a[128:131],v[112:127]\nDR a[216:219],v159 offset:6144\nVA v154,v154,v152\nEX v144,v16\nM8 v[64:79],a[192:195],a[160:163],v[128:143]\nVA v155,v155,v153\nEX v145,v17\n"
"s_and_b32 m0,s60,0x1ffff\nM8 v[48:63],a[196:199],a[132:135],v[48:63]\nDR a[220:223],v159 offset:7168\nEX v146,v18\nVA v152,v144,v145\nCV v80,v144,v145\nM8 v[64:79],a[196:199],a[164:167],v[64:79]\nEX v147,v19\n"
"VA v152,v152,v146\nBL v161,s[36:39],s59 offen offset:0 sc1 lds\nM8 v[48:63],a[200:203],a[136:139],v[48:63]\nDR a[224:227],v160 offset:0\nEX v144,v20\nVA v152,v152,v147\nCV v81,v146,v147\nM8 v[64:79],a[200:203],a[168:171],v[64:79]\n"
"EX v145,v21\nVA v152,v152,v144\nSA s63,s63,0x8000\nBL v161,s[36:39],s59 offen offset:1024 sc1 lds\nM8 v[48:63],a[204:207],a[140:143],v[48:63]\nDR a[228:231],v160 offset:2048\nEX v146,v22\nVA v152,v152,v145\n"
"CV v82,v144,v145\nM8 v[64:79],a[204:207],a[172:175],v[64:79]\nEX v147,v23\nVA v152,v152,v146\ns_and_b32 s66,s63,0x1ffff\nBL v161,s[36:39],s59 offen offset:2048 sc1 lds\nSW lgkmcnt(2)\nM8 v[48:63],a[208:211],a[144:147],v[48:63]\n"
"DR a[232:235],v160 offset:4096\nEX v144,v24\nVA v152,v152,v147\nCV v83,v146,v147\nM8 v[64:79],a[208:211],a[176:179],v[64:79]\nEX v145,v25\nVA v152,v152,v144\nVU v158,s66,v165\n"
"BL v161,s[36:39],s59 offen offset:3072 sc1 lds\nM8 v[48:63],a[212:215],a[148:151],v[48:63]\nDR a[236:239],v160 offset:6144\nEX v146,v26\nVA v152,v152,v145\nCV v84,v144,v145\nM8 v[64:79],a[212:215],a[180:183],v[64:79]\nEX v147,v27\n"
"VA v152,v152,v146\nEX v144,v28\nVA v152,v152,v147\nCV v85,v146,v147\nSA s59,s59,16384\nSA s60,s60,0x8000\nM8 v[48:63],a[216:219],a[152:155],v[48:63]\nDR a[240:243],v160 offset:1024\n"
"EX v145,v29\nVA v152,v152,v144\nM8 v[64:79],a[216:219],a[184:187],v[64:79]\nEX v146,v30\nVA v152,v152,v145\nCV v86,v144,v145\ns_and_b32 m0,s62,0x1ffff\nM8 v[48:63],a[220:223],a[156:159],v[48:63]\n"
"DR a[244:247],v160 offset:3072\nEX v147,v31\nVA v152,v152,v146\nM8 v[64:79],a[220:223],a[188:191],v[64:79]\nEX v144,v32\nVA v152,v152,v147\nCV v87,v146,v147\nBL v161,s[40:43],s61 offen offset:0 sc1 lds\n"
"SW lgkmcnt(2)\nM8 a[0:15],a[224:227],v[96:99],a[0:15]\nDR a[248:251],v160 offset:5120\nEX v145,v33\nM8 a[16:31],a[224:227],v[104:107],a[16:31]\nEX v146,v34\nVA v153,v144,v145\nCV v88,v144,v145\n"
"BL v161,s[40:43],s61 offen offset:1024 sc1 lds\nM8 a[32:47],a[228:231],v[96:99],a[32:47]\nDR a[252:255],v160 offset:7168\nEX v147,v35\nVA v153,v153,v146\nM8 a[48:63],a[228:231],v[104:107],a[48:63]\nEX v144,v36\nVA v153,v153,v147\n"
"CV v89,v146,v147\nBL v161,s[40:43],s61 offen offset:2048 sc1 lds\nM8 a[64:79],a[232:235],v[96:99],a[64:79]\nDR a[192:195],v158 offset:0\nEX v145,v37\nVA v153,v153,v144\nM8 a[80:95],a[232:235],v[104:107],a[80:95]\nEX v146,v38\n"
"VA v153,v153,v145\nCV v90,v144,v145\nEX v147,v39\nVA v153,v153,v146\nSA s64,s64,0x8000\nBL v161,s[40:43],s61 offen offset:3072 sc1 lds\nM8 a[96:111],a[236:239],v[96:99],a[96:111]\nDR a[196:199],v158 offset:1024\n"
"EX v144,v40\nVA v153,v153,v147\nCV v91,v146,v147\nM8 a[112:127],a[236:239],v[104:107],a[112:127]\nEX v145,v41\nVA v153,v153,v144\ns_and_b32 s67,s64,0x1ffff\nSW lgkmcnt(2)\n"
"M8 a[0:15],a[240:243],v[100:103],a[0:15]\nDR a[200:203],v158 offset:2048\nEX v146,v42\nVA v153,v153,v145\nCV v92,v144,v145\nM8 a[16:31],a[240:243],v[108:111],a[16:31]\nEX v147,v43\nVA v153,v153,v146\n"
"VU v160,s67,v166\nM8 a[32:47],a[244:247],v[100:103],a[32:47]\nDR a[204:207],v158 offset:3072\nEX v144,v44\nVA v153,v153,v147\nCV v93,v146,v147\nM8 a[48:63],a[244:247],v[108:111],a[48:63]\nEX v145,v45\n"
"VA v153,v153,v144\nSA s61,s61,16384\nSA s62,s62,0x8000\nM8 a[64:79],a[248:251],v[100:103],a[64:79]\nDR a[208:211],v158 offset:4096\nEX v146,v46\nVA v153,v153,v145\nCV v94,v144,v145\n"
"M8 a[80:95],a[248:251],v[108:111],a[80:95]\nEX v147,v47\nVA v153,v153,v146\nM8 a[96:111],a[252:255],v[100:103],a[96:111]\nVA v153,v153,v147\nCV v95,v146,v147\nDR a[212:215],v158 offset:5120\nv_max_f32 v170,v152,v153\n"
"v_cmp_nge_f32 vcc,1.0,v170\nM8 a[112:127],a[252:255],v[108:111],a[112:127]\nSW vmcnt(12)\ns_barrier\ns_cbranch_vccnz AT_le_fb\nAT_le_ret:\ns_sub_u32 s58,s58,1\ns_cmp_lg_u32 s58,0\n"
"s_cbranch_scc1 AT_lo\nAT_tail:\nAT_to:\nSW lgkmcnt(2)\nM8 v[16:31],a[192:195],a[128:131],v[112:127]\nDR a[216:219],v158 offset:6144\nVA v154,v154,v152\nEX v144,v48\n"
"M8 v[32:47],a[192:195],a[160:163],v[128:143]\nVA v155,v155,v153\nEX v145,v49\ns_and_b32 m0,s60,0x1ffff\nM8 v[16:31],a[196:199],a[132:135],v[16:31]\nDR a[220:223],v158 offset:7168\nEX v146,v50\nVA v152,v144,v145\n"
"CV v96,v144,v145\nM8 v[32:47],a[196:199],a[164:167],v[32:47]\nEX v147,v51\nVA v152,v152,v146\nBL v161,s[36:39],s59 offen offset:0 sc1 lds\nM8 v[16:31],a[200:203],a[136:139],v[16:31]\nDR a[224:227],v160 offset:0\nEX v144,v52\n"
"VA v152,v152,v147\nCV v97,v146,v147\nM8 v[32:47],a[200:203],a[168:171],v[32:47]\nEX v145,v53\nVA v152,v152,v144\nSA s63,s63,0x8000\nBL v161,s[36:39],s59 offen offset:1024 sc1 lds\nM8 v[16:31],a[204:207],a[140:143],v[16:31]\n"
"DR a[228:231],v160 offset:2048\nEX v146,v54\nVA v152,v152,v145\nCV v98,v144,v145\nM8 v[32:47],a[204:207],a[172:175],v[32:47]\nEX v147,v55\nVA v152,v152,v146\ns_and_b32 s66,s63,0x1ffff\n"
"BL v161,s[36:39],s59 offen offset:2048 sc1 lds\nSW lgkmcnt(2)\nM8 v[16:31],a[208:211],a[144:147],v[16:31]\nDR a[232:235],v160 offset:4096\nEX v144,v56\nVA v152,v152,v147\nCV v99,v146,v147\nM8 v[32:47],a[208:211],a[176:179],v[32:47]\n"
"EX v145,v57\nVA v152,v152,v144\nVU v159,s66,v165\nBL v161,s[36:39],s59 offen offset:3072 sc1 lds\nM8 v[16:31],a[212:215],a[148:151],v[16:31]\nDR a[236:239],v160 offset:6144\nEX v146,v58\nVA v152,v152,v145\n"
"CV v100,v144,v145\nM8 v[32:47],a[212:215],a[180:183],v[32:47]\nEX v147,v59\nVA v152,v152,v146\nEX v144,v60\nVA v152,v152,v147\nCV v101,v146,v147\nSA s59,s59,16384\n"
"SA s60,s60,0x8000\nM8 v[16:31],a[216:219],a[152:155],v[16:31]\nDR a[240:243],v160 offset:1024\nEX v145,v61\nVA v152,v152,v144\nM8 v[32:47],a[216:219],a[184:187],v[32:47]\nEX v146,v62\nVA v152,v152,v145\n"
"CV v102,v144,v145\ns_and_b32 m0,s62,0x1ffff\nM8 v[16:31],a[220:223],a[156:159],v[16:31]\nDR a[244:247],v160 offset:3072\nEX v147,v63\nVA v152,v152,v146\nM8 v[32:47],a[220:223],a[188:191],v[32:47]\nEX v144,v64\n"
"VA v152,v152,v147\nCV v103,v146,v147\nBL v161,s[40:43],s61 offen offset:0 sc1 lds\nSW lgkmcnt(2)\nM8 a[0:15],a[224:227],v[80:83],a[0:15]\nDR a[248:251],v160 offset:5120\nEX v145,v65\nM8 a[16:31],a[224:227],v[88:91],a[16:31]\n"
"EX v146,v66\nVA v153,v144,v145\nCV v104,v144,v145\nBL v161,s[40:43],s61 offen offset:1024 sc1 lds\nM8 a[32:47],a[228:231],v[80:83],a[32:47]\nDR a[252:255],v160 offset:7168\nEX v147,v67\nVA v153,v153,v146\n"
"M8 a[48:63],a[228:231],v[88:91],a[48:63]\nEX v144,v68\nVA v153,v153,v147\nCV v105,v146,v147\nBL v161,s[40:43],s61 offen offset:2048 sc1 lds\nM8 a[64:79],a[232:235],v[80:83],a[64:79]\nDR a[192:195],v159 offset:0\nEX v145,v69\n"
"VA v153,v153,v144\nM8 a[80:95],a[232:235],v[88:91],a[80:95]\nEX v146,v70\nVA v153,v153,v145\nCV v106,v144,v145\nEX v147,v71\nVA v153,v153,v146\nSA s64,s64,0x8000\n"
"BL v161,s[40:43],s61 offen offset:3072 sc1 lds\nM8 a[96:111],a[236:239],v[80:83],a[96:111]\nDR a[196:199],v159 offset:1024\nEX v144,v72\nVA v153,v153,v147\nCV v107,v146,v147\nM8 a[112:127],a[236:239],v[88:91],a[112:127]\nEX v145,v73\n"
"VA v153,v153,v144\ns_and_b32 s67,s64,0x1ffff\nSW lgkmcnt(2)\nM8 a[0:15],a[240:243],v[84:87],a[0:15]\nDR a[200:203],v159 offset:2048\nEX v146,v74\nVA v153,v153,v145\nCV v108,v144,v145\n"
"M8 a[16:31],a[240:243],v[92:95],a[16:31]\nEX v147,v75\nVA v153,v153,v146\nVU v160,s67,v166\nM8 a[32:47],a[244:247],v[84:87],a[32:47]\nDR a[204:207],v159 offset:3072\nEX v144,v76\nVA v153,v153,v147\n"
"CV v109,v146,v147\nM8 a[48:63],a[244:247],v[92:95],a[48:63]\nEX v145,v77\nVA v153,v153,v144\nSA s61,s61,16384\nSA s62,s62,0x8000\nM8 a[64:79],a[248:251],v[84:87],a[64:79]\nDR a[208:211],v159 offset:4096\n"
"EX v146,v78\nVA v153,v153,v145\nCV v110,v144,v145\nM8 a[80:95],a[248:251],v[92:95],a[80:95]\nEX v147,v79\nVA v153,v153,v146\nM8 a[96:111],a[252:255],v[84:87],a[96:111]\nVA v153,v153,v147\n"
"CV v111,v146,v147\nDR a[212:215],v159 offset:5120\nv_max_f32 v170,v152,v153\nv_cmp_nge_f32 vcc,1.0,v170\nM8 a[112:127],a[252:255],v[92:95],a[112:127]\nSW vmcnt(12)\ns_barrier\ns_cbranch_vccnz AT_to_fb\n"
"AT_to_ret:\nAT_tem:\nCG s[80:81],v162,0\nCG s[82:83],v162,1\nCG s[84:85],v162,2\nCG s[86:87],v162,3\nCM v16,v164,v16,s[80:81]\nCM v17,v164,v17,s[82:83]\n"
"CM v18,v164,v18,s[84:85]\nCM v19,v164,v19,s[86:87]\nSW lgkmcnt(2)\nM8 v[48:63],a[192:195],a[128:131],v[112:127]\nDR a[216:219],v159 offset:6144\nVA v154,v154,v152\nCG s[80:81],v162,16\nEX v144,v16\n"
"s_nop 0\nCM v20,v164,v20,s[80:81]\nM8 v[64:79],a[192:195],a[160:163],v[128:143]\nVA v155,v155,v153\nCG s[82:83],v162,17\nEX v145,v17\ns_nop 0\nCM v21,v164,v21,s[82:83]\n"
"M8 v[48:63],a[196:199],a[132:135],v[48:63]\nDR a[220:223],v159 offset:7168\nCG s[84:85],v162,18\nEX v146,v18\nVA v152,v144,v145\nCV v80,v144,v145\nCM v22,v164,v22,s[84:85]\nM8 v[64:79],a[196:199],a[164:167],v[64:79]\n"
"CG s[86:87],v162,19\nEX v147,v19\nVA v152,v152,v146\nCM v23,v164,v23,s[86:87]\nM8 v[48:63],a[200:203],a[136:139],v[48:63]\nDR a[224:227],v160 offset:0\nCG s[80:81],v162,8\nEX v144,v20\n"
"VA v152,v152,v147\nCV v81,v146,v147\nCM v24,v164,v24,s[80:81]\nM8 v[64:79],a[200:203],a[168:171],v[64:79]\nCG s[82:83],v162,9\nEX v145,v21\nVA v152,v152,v144\nCM v25,v164,v25,s[82:83]\n"
"SA s63,s63,0x8000\nM8 v[48:63],a[204:207],a[140:143],v[48:63]\nDR a[228:231],v160 offset:2048\nCG s[84:85],v162,10\nEX v146,v22\nVA v152,v152,v145\nCV v82,v144,v145\nCM v26,v164,v26,s[84:85]\n"
"M8 v[64:79],a[204:207],a[172:175],v[64:79]\nCG s[86:87],v162,11\nEX v147,v23\nVA v152,v152,v146\nCM v27,v164,v27,s[86:87]\ns_and_b32 s66,s63,0x1ffff\nSW lgkmcnt(2)\nM8 v[48:63],a[208:211],a[144:147],v[48:63]\n"
"DR a[232:235],v160 offset:4096\nCG s[80:81],v162,24\nEX v144,v24\nVA v152,v152,v147\nCV v83,v146,v147\nCM v28,v164,v28,s[80:81]\nM8 v[64:79],a[208:211],a[176:179],v[64:79]\nCG s[82:83],v162,25\n"
"EX v145,v25\nVA v152,v152,v144\nCM v29,v164,v29,s[82:83]\nVU v158,s66,v165\nM8 v[48:63],a[212:215],a[148:151],v[48:63]\nDR a[236:239],v160 offset:6144\nCG s[84:85],v162,26\nEX v146,v26\n"
"VA v152,v152,v145\nCV v84,v144,v145\nCM v30,v164,v30,s[84:85]\nM8 v[64:79],a[212:215],a[180:183],v[64:79]\nCG s[86:87],v162,27\nEX v147,v27\nVA v152,v152,v146\nCM v31,v164,v31,s[86:87]\n"
"CG s[80:81],v163,0\nEX v144,v28\nVA v152,v152,v147\nCV v85,v146,v147\nCM v32,v164,v32,s[80:81]\nM8 v[48:63],a[216:219],a[152:155],v[48:63]\nDR a[240:243],v160 offset:1024\nCG s[82:83],v163,1\n"
"EX v145,v29\nVA v152,v152,v144\nCM v33,v164,v33,s[82:83]\nM8 v[64:79],a[216:219],a[184:187],v[64:79]\nCG s[84:85],v163,2\nEX v146,v30\nVA v152,v152,v145\nCV v86,v144,v145\n"
"CM v34,v164,v34,s[84:85]\nM8 v[48:63],a[220:223],a[156:159],v[48:63]\nDR a[244:247],v160 offset:3072\nCG s[86:87],v163,3\nEX v147,v31\nVA v152,v152,v146\nCM v35,v164,v35,s[86:87]\nM8 v[64:79],a[220:223],a[188:191],v[64:79]\n"
"CG s[80:81],v163,16\nEX v144,v32\nVA v152,v152,v147\nCV v87,v146,v147\nCM v36,v164,v36,s[80:81]\nSW lgkmcnt(2)\nM8 a[0:15],a[224:227],v[96:99],a[0:15]\nDR a[248:251],v160 offset:5120\n"
"CG s[82:83],v163,17\nEX v145,v33\ns_nop 0\nCM v37,v164,v37,s[82:83]\nM8 a[16:31],a[224:227],v[104:107],a[16:31]\nCG s[84:85],v163,18\nEX v146,v34\nVA v153,v144,v145\n"
"CV v88,v144,v145\nCM v38,v164,v38,s[84:85]\nM8 a[32:47],a[228:231],v[96:99],a[32:47]\nDR a[252:255],v160 offset:7168\nCG s[86:87],v163,19\nEX v147,v35\nVA v153,v153,v146\nCM v39,v164,v39,s[86:87]\n"
"M8 a[48:63],a[228:231],v[104:107],a[48:63]\nCG s[80:81],v163,8\nEX v144,v36\nVA v153,v153,v147\nCV v89,v146,v147\nCM v40,v164,v40,s[80:81]\nM8 a[64:79],a[232:235],v[96:99],a[64:79]\nCG s[82:83],v163,9\n"
"EX v145,v37\nVA v153,v153,v144\nCM v41,v164,v41,s[82:83]\nM8 a[80:95],a[232:235],v[104:107],a[80:95]\nCG s[84:85],v163,10\nEX v146,v38\nVA v153,v153,v145\nCV v90,v144,v145\n"
"CM v42,v164,v42,s[84:85]\nCG s[86:87],v163,11\nEX v147,v39\nVA v153,v153,v146\nCM v43,v164,v43,s[86:87]\nSA s64,s64,0x8000\nM8 a[96:111],a[236:239],v[96:99],a[96:111]\nCG s[80:81],v163,24\n"
"EX v144,v40\nVA v153,v153,v147\nCV v91,v146,v147\nCM v44,v164,v44,s[80:81]\nM8 a[112:127],a[236:239],v[104:107],a[112:127]\nCG s[82:83],v163,25\nEX v145,v41\nVA v153,v153,v144\n"
"CM v45,v164,v45,s[82:83]\ns_and_b32 s67,s64,0x1ffff\nSW lgkmcnt(0)\nM8 a[0:15],a[240:243],v[100:103],a[0:15]\nCG s[84:85],v163,26\nEX v146,v42\nVA v153,v153,v145\nCV v92,v144,v145\n"
"CM v46,v164,v46,s[84:85]\nM8 a[16:31],a[240:243],v[108:111],a[16:31]\nCG s[86:87],v163,27\nEX v147,v43\nVA v153,v153,v146\nCM v47,v164,v47,s[86:87]\nVU v160,s67,v166\nM8 a[32:47],a[244:247],v[100:103],a[32:47]\n"
"EX v144,v44\nVA v153,v153,v147\nCV v93,v146,v147\nM8 a[48:63],a[244:247],v[108:111],a[48:63]\nEX v145,v45\nVA v153,v153,v144\nM8 a[64:79],a[248:251],v[100:103],a[64:79]\nEX v146,v46\n"
"VA v153,v153,v145\nCV v94,v144,v145\nM8 a[80:95],a[248:251],v[108:111],a[80:95]\nEX v147,v47\nVA v153,v153,v146\nv_subrev_u32 v162,64,v162\nM8 a[96:111],a[252:255],v[100:103],a[96:111]\nVA v153,v153,v147\n"
"CV v95,v146,v147\nv_max_f32 v170,v152,v153\nv_cmp_nge_f32 vcc,1.0,v170\nM8 a[112:127],a[252:255],v[108:111],a[112:127]\nSW vmcnt(4)\ns_barrier\nv_subrev_u32 v163,64,v163\ns_cbranch_vccnz AT_tem_fb\n"
"AT_tem_ret:\ns_branch AT_last\nAT_s0m:\nCG s[80:81],v162,0\nCG s[82:83],v162,1\nCG s[84:85],v162,2\nCG s[86:87],v162,3\nCM v16,v164,v16,s[80:81]\n"
"CM v17,v164,v17,s[82:83]\nCM v18,v164,v18,s[84:85]\nCM v19,v164,v19,s[86:87]\nSW lgkmcnt(2)\nM8 v[48:63],a[192:195],a[128:131],v[112:127]\nDR a[216:219],v159 offset:6144\nCG s[80:81],v162,16\nEX v144,v16\n"
"s_nop 0\nCM v20,v164,v20,s[80:81]\nM8 v[64:79],a[192:195],a[160:163],v[128:143]\nCG s[82:83],v162,17\nEX v145,v17\ns_nop 0\nCM v21,v164,v21,s[82:83]\nM8 v[48:63],a[196:199],a[132:135],v[48:63]\n"
"DR a[220:223],v159 offset:7168\nCG s[84:85],v162,18\nEX v146,v18\nVA v152,v144,v145\nCV v80,v144,v145\nCM v22,v164,v22,s[84:85]\nM8 v[64:79],a[196:199],a[164:167],v[64:79]\nCG s[86:87],v162,19\n"
"EX v147,v19\nVA v152,v152,v146\nCM v23,v164,v23,s[86:87]\nM8 v[48:63],a[200:203],a[136:139],v[48:63]\nCG s[80:81],v162,8\nEX v144,v20\nVA v152,v152,v147\nCV v81,v146,v147\n"
"CM v24,v164,v24,s[80:81]\nM8 v[64:79],a[200:203],a[168:171],v[64:79]\nCG s[82:83],v162,9\nEX v145,v21\nVA v152,v152,v144\nCM v25,v164,v25,s[82:83]\nSA s63,s63,0x8000\nM8 v[48:63],a[204:207],a[140:143],v[48:63]\n"
"CG s[84:85],v162,10\nEX v146,v22\nVA v152,v152,v145\nCV v82,v144,v145\nCM v26,v164,v26,s[84:85]\nM8 v[64:79],a[204:207],a[172:175],v[64:79]\nCG s[86:87],v162,11\nEX v147,v23\n"
"VA v152,v152,v146\nCM v27,v164,v27,s[86:87]\ns_and_b32 s66,s63,0x1ffff\nSW lgkmcnt(0)\nM8 v[48:63],a[208:211],a[144:147],v[48:63]\nCG s[80:81],v162,24\nEX v144,v24\nVA v152,v152,v147\n"
"CV v83,v146,v147\nCM v28,v164,v28,s[80:81]\nM8 v[64:79],a[208:211],a[176:179],v[64:79]\nCG s[82:83],v162,25\nEX v145,v25\nVA v152,v152,v144\nCM v29,v164,v29,s[82:83]\nVU v158,s66,v165\n"
"M8 v[48:63],a[212:215],a[148:151],v[48:63]\nCG s[84:85],v162,26\nEX v146,v26\nVA v152,v152,v145\nCV v84,v144,v145\nCM v30,v164,v30,s[84:85]\nM8 v[64:79],a[212:215],a[180:183],v[64:79]\nCG s[86:87],v162,27\n"
"EX v147,v27\nVA v152,v152,v146\nCM v31,v164,v31,s[86:87]\nCG s[80:81],v163,0\nEX v144,v28\nVA v152,v152,v147\nCV v85,v146,v147\nCM v32,v164,v32,s[80:81]\n"
"M8 v[48:63],a[216:219],a[152:155],v[48:63]\nCG s[82:83],v163,1\nEX v145,v29\nVA v152,v152,v144\nCM v33,v164,v33,s[82:83]\nM8 v[64:79],a[216:219],a[184:187],v[64:79]\nCG s[84:85],v163,2\nEX v146,v30\n"
"VA v152,v152,v145\nCV v86,v144,v145\nCM v34,v164,v34,s[84:85]\nM8 v[48:63],a[220:223],a[156:159],v[48:63]\nCG s[86:87],v163,3\nEX v147,v31\nVA v152,v152,v146\nCM v35,v164,v35,s[86:87]\n"
"M8 v[64:79],a[220:223],a[188:191],v[64:79]\nCG s[80:81],v163,16\nEX v144,v32\nVA v152,v152,v147\nCV v87,v146,v147\nCM v36,v164,v36,s[80:81]\nCG s[82:83],v163,17\nEX v145,v33\n"
"s_nop 0\nCM v37,v164,v37,s[82:83]\nCG s[84:85],v163,18\nEX v146,v34\nVA v153,v144,v145\nCV v88,v144,v145\nCM v38,v164,v38,s[84:85]\nCG s[86:87],v163,19\n"
"EX v147,v35\nVA v153,v153,v146\nCM v39,v164,v39,s[86:87]\nCG s[80:81],v163,8\nEX v144,v36\nVA v153,v153,v147\nCV v89,v146,v147\nCM v40,v164,v40,s[80:81]\n"
"CG s[82:83],v163,9\nEX v145,v37\nVA v153,v153,v144\nCM v41,v164,v41,s[82:83]\nCG s[84:85],v163,10\nEX v146,v38\nVA v153,v153,v145\nCV v90,v144,v145\n"
"CM v42,v164,v42,s[84:85]\nCG s[86:87],v163,11\nEX v147,v39\nVA v153,v153,v146\nCM v43,v164,v43,s[86:87]\nCG s[80:81],v163,24\nEX v144,v40\nVA v153,v153,v147\n"
"CV v91,v146,v147\nCM v44,v164,v44,s[80:81]\nCG s[82:83],v163,25\nEX v145,v41\nVA v153,v153,v144\nCM v45,v164,v45,s[82:83]\nCG s[84:85],v163,26\nEX v146,v42\n"
"VA v153,v153,v145\nCV v92,v144,v145\nCM v46,v164,v46,s[84:85]\nCG s[86:87],v163,27\nEX v147,v43\nVA v153,v153,v146\nCM v47,v164,v47,s[86:87]\nEX v144,v44\n"
"VA v153,v153,v147\nCV v93,v146,v147\nEX v145,v45\nVA v153,v153,v144\nEX v146,v46\nVA v153,v153,v145\nCV v94,v144,v145\nEX v147,v47\n"
"VA v153,v153,v146\nv_subrev_u32 v162,64,v162\nVA v153,v153,v147\nCV v95,v146,v147\nv_max_f32 v170,v152,v153\nv_cmp_nge_f32 vcc,1.0,v170\nSW vmcnt(4)\ns_barrier\n"
"v_subrev_u32 v163,64,v163\ns_cbranch_vccnz AT_s0m_fb\nAT_s0m_ret:\nAT_last:\nAT_lm:\nCG s[80:81],v162,0\nCG s[82:83],v162,1\nCG s[84:85],v162,2\n"
"CG s[86:87],v162,3\nCM v48,v164,v48,s[80:81]\nCM v49,v164,v49,s[82:83]\nCM v50,v164,v50,s[84:85]\nCM v51,v164,v51,s[86:87]\nVA v154,v154,v152\nCG s[80:81],v162,16\nEX v144,v48\n"
"s_nop 0\nCM v52,v164,v52,s[80:81]\nVA v155,v155,v153\nCG s[82:83],v162,17\nEX v145,v49\ns_nop 0\nCM v53,v164,v53,s[82:83]\nCG s[84:85],v162,18\n"
"EX v146,v50\nVA v152,v144,v145\nCV v96,v144,v145\nCM v54,v164,v54,s[84:85]\nCG s[86:87],v162,19\nEX v147,v51\nVA v152,v152,v146\nCM v55,v164,v55,s[86:87]\n"
"DR a[224:227],v160 offset:0\nCG s[80:81],v162,8\nEX v144,v52\nVA v152,v152,v147\nCV v97,v146,v147\nCM v56,v164,v56,s[80:81]\nCG s[82:83],v162,9\nEX v145,v53\n"
"VA v152,v152,v144\nCM v57,v164,v57,s[82:83]\nSA s63,s63,0x8000\nDR a[228:231],v160 offset:2048\nCG s[84:85],v162,10\nEX v146,v54\nVA v152,v152,v145\nCV v98,v144,v145\n"
"CM v58,v164,v58,s[84:85]\nCG s[86:87],v162,11\nEX v147,v55\nVA v152,v152,v146\nCM v59,v164,v59,s[86:87]\ns_and_b32 s66,s63,0x1ffff\nDR a[232:235],v160 offset:4096\nCG s[80:81],v162,24\n"
"EX v144,v56\nVA v152,v152,v147\nCV v99,v146,v147\nCM v60,v164,v60,s[80:81]\nCG s[82:83],v162,25\nEX v145,v57\nVA v152,v152,v144\nCM v61,v164,v61,s[82:83]\n"
"VU v159,s66,v165\nDR a[236:239],v160 offset:6144\nCG s[84:85],v162,26\nEX v146,v58\nVA v152,v152,v145\nCV v100,v144,v145\nCM v62,v164,v62,s[84:85]\nCG s[86:87],v162,27\n"
"EX v147,v59\nVA v152,v152,v146\nCM v63,v164,v63,s[86:87]\nCG s[80:81],v163,0\nEX v144,v60\nVA v152,v152,v147\nCV v101,v146,v147\nCM v64,v164,v64,s[80:81]\n"
"DR a[240:243],v160 offset:1024\nCG s[82:83],v163,1\nEX v145,v61\nVA v152,v152,v144\nCM v65,v164,v65,s[82:83]\nCG s[84:85],v163,2\nEX v146,v62\nVA v152,v152,v145\n"
"CV v102,v144,v145\nCM v66,v164,v66,s[84:85]\nDR a[244:247],v160 offset:3072\nCG s[86:87],v163,3\nEX v147,v63\nVA v152,v152,v146\nCM v67,v164,v67,s[86:87]\nCG s[80:81],v163,16\n"
"EX v144,v64\nVA v152,v152,v147\nCV v103,v146,v147\nCM v68,v164,v68,s[80:81]\nSW lgkmcnt(2)\nM8 a[0:15],a[224:227],v[80:83],a[0:15]\nDR a[248:251],v160 offset:5120\nCG s[82:83],v163,17\n"
"EX v145,v65\ns_nop 0\nCM v69,v164,v69,s[82:83]\nM8 a[16:31],a[224:227],v[88:91],a[16:31]\nCG s[84:85],v163,18\nEX v146,v66\nVA v153,v144,v145\nCV v104,v144,v145\n"
"CM v70,v164,v70,s[84:85]\nM8 a[32:47],a[228:231],v[80:83],a[32:47]\nDR a[252:255],v160 offset:7168\nCG s[86:87],v163,19\nEX v147,v67\nVA v153,v153,v146\nCM v71,v164,v71,s[86:87]\nM8 a[48:63],a[228:231],v[88:91],a[48:63]\n"
"CG s[80:81],v163,8\nEX v144,v68\nVA v153,v153,v147\nCV v105,v146,v147\nCM v72,v164,v72,s[80:81]\nM8 a[64:79],a[232:235],v[80:83],a[64:79]\nCG s[82:83],v163,9\nEX v145,v69\n"
"VA v153,v153,v144\nCM v73,v164,v73,s[82:83]\nM8 a[80:95],a[232:235],v[88:91],a[80:95]\nCG s[84:85],v163,10\nEX v146,v70\nVA v153,v153,v145\nCV v106,v144,v145\nCM v74,v164,v74,s[84:85]\n"
"CG s[86:87],v163,11\nEX v147,v71\nVA v153,v153,v146\nCM v75,v164,v75,s[86:87]\nSA s64,s64,0x8000\nM8 a[96:111],a[236:239],v[80:83],a[96:111]\nCG s[80:81],v163,24\nEX v144,v72\n"
"VA v153,v153,v147\nCV v107,v146,v147\nCM v76,v164,v76,s[80:81]\nM8 a[112:127],a[236:239],v[88:91],a[112:127]\nCG s[82:83],v163,25\nEX v145,v73\nVA v153,v153,v144\nCM v77,v164,v77,s[82:83]\n"
"s_and_b32 s67,s64,0x1ffff\nSW lgkmcnt(0)\nM8 a[0:15],a[240:243],v[84:87],a[0:15]\nCG s[84:85],v163,26\nEX v146,v74\nVA v153,v153,v145\nCV v108,v144,v145\nCM v78,v164,v78,s[84:85]\n"
"M8 a[16:31],a[240:243],v[92:95],a[16:31]\nCG s[86:87],v163,27\nEX v147,v75\nVA v153,v153,v146\nCM v79,v164,v79,s[86:87]\nVU v160,s67,v166\nM8 a[32:47],a[244:247],v[84:87],a[32:47]\nEX v144,v76\n"
"VA v153,v153,v147\nCV v109,v146,v147\nM8 a[48:63],a[244:247],v[92:95],a[48:63]\nEX v145,v77\nVA v153,v153,v144\nM8 a[64:79],a[248:251],v[84:87],a[64:79]\nEX v146,v78\nVA v153,v153,v145\n"
"CV v110,v144,v145\nM8 a[80:95],a[248:251],v[92:95],a[80:95]\nEX v147,v79\nVA v153,v153,v146\nv_subrev_u32 v162,64,v162\nM8 a[96:111],a[252:255],v[84:87],a[96:111]\nVA v153,v153,v147\nCV v111,v146,v147\n"
"v_max_f32 v170,v152,v153\nv_cmp_nge_f32 vcc,1.0,v170\nM8 a[112:127],a[252:255],v[92:95],a[112:127]\nSW vmcnt(0)\ns_barrier\nv_subrev_u32 v163,64,v163\ns_cbranch_vccnz AT_lm_fb\nAT_lm_ret:\n"
"DR a[224:227],v160 offset:0\nDR a[228:231],v160 offset:2048\nDR a[232:235],v160 offset:4096\nDR a[236:239],v160 offset:6144\nDR a[240:243],v160 offset:1024\nDR a[244:247],v160 offset:3072\nDR a[248:251],v160 offset:5120\nDR a[252:255],v160 offset:7168\n"
"VA v154,v154,v152\nVA v155,v155,v153\nSW lgkmcnt(6)\nM8 a[0:15],a[224:227],v[96:99],a[0:15]\ns_and_b32 s70,s88,7\ns_lshr_b32 s71,s88,3\ns_and_b32 s58,s71,3\ns_bfe_u32 s57,s71,0x20002\n"
"s_lshr_b32 s71,s71,4\ns_lshl_b32 s70,s70,1\nSA s56,s70,s71\ns_lshl_b32 s81,s56,2\nSA s81,s81,s57\ns_lshr_b32 s59,s89,1\nM8 a[16:31],a[224:227],v[104:107],a[16:31]\ns_and_b32 s60,s89,1\n"
"SM s63,s59\nSM s64,7\ns_cmp_eq_u32 s58,1\ns_cselect_b32 s64,7,s64\ns_cmp_eq_u32 s58,2\ns_cselect_b32 s64,8,s64\ns_cmp_eq_u32 s58,3\ns_cselect_b32 s64,7,s64\n"
"SM s65,24\nM8 a[32:47],a[228:231],v[96:99],a[32:47]\ns_cmp_eq_u32 s58,1\ns_cselect_b32 s65,24,s65\ns_cmp_eq_u32 s58,2\ns_cselect_b32 s65,24,s65\ns_cmp_eq_u32 s58,3\ns_cselect_b32 s65,23,s65\n"
"SM s70,0\ns_cmp_eq_u32 s58,1\ns_cselect_b32 s70,13,s70\ns_cmp_eq_u32 s58,2\nM8 a[48:63],a[228:231],v[104:107],a[48:63]\ns_cselect_b32 s70,26,s70\ns_cmp_eq_u32 s58,3\ns_cselect_b32 s70,41,s70\n"
"SM s73,0\ns_cmp_eq_u32 s58,1\ns_cselect_b32 s73,33,s73\ns_cmp_eq_u32 s58,2\ns_cselect_b32 s73,66,s73\ns_cmp_eq_u32 s58,3\ns_cselect_b32 s73,97,s73\nSW lgkmcnt(4)\n"
"M8 a[64:79],a[232:235],v[96:99],a[64:79]\ns_mul_i32 s71,s56,1490944\nSA s71,s71,39845888\nSA s36,s92,s71\ns_addc_u32 s37,s93,0\ns_and_b32 s37,s37,0xffff\nSM s38,1490944\nSM s39,0x20000\n"
"SA s61,s70,127\ns_lshl_b32 s61,s61,10\ns_lshl_b32 s75,s73,10\nM8 a[80:95],a[232:235],v[104:107],a[80:95]\nVL v234,4,v200\ns_mul_i32 s71,s89,372736\nVU v224,s71,v234\nVU v225,185344,v224\n"
"VU v226,1024,v224\nVU v227,1024,v225\ns_lshl_b32 s82,s89,11\ns_mul_i32 s83,s59,18432\ns_mul_i32 s71,s60,5120\nSA s83,s83,s71\nM8 a[96:111],a[236:239],v[96:99],a[96:111]\nSA s83,s83,8192\n"
"s_mul_i32 s71,s60,5\nSA s71,s71,0\ns_lshl_b32 s71,s71,6\nVU v234,s71,v200\nv_mul_u32_u24 v235,3277,v234\nv_lshrrev_b32 v235,16,v235\nv_mul_u32_u24 v236,20,v235\nv_sub_u32 v236,v234,v236\n"
"v_bfe_u32 v237,v235,2,1\nM8 a[112:127],a[236:239],v[104:107],a[112:127]\nVL v237,1,v237\nv_xor_b32 v236,v236,v237\nv_mul_u32_u24 v238,1280,v235\nv_lshl_add_u32 v238,v236,4,v238\nv_subrev_u32 v228,0,v238\ns_mul_i32 s71,s60,5\n"
"SA s71,s71,1\ns_lshl_b32 s71,s71,6\nVU v234,s71,v200\nv_mul_u32_u24 v235,3277,v234\nSW lgkmcnt(2)\nM8 a[0:15],a[240:243],v[100:103],a[0:15]\nv_lshrrev_b32 v235,16,v235\nv_mul_u32_u24 v236,20,v235\n"
"v_sub_u32 v236,v234,v236\nv_bfe_u32 v237,v235,2,1\nVL v237,1,v237\nv_xor_b32 v236,v236,v237\nv_mul_u32_u24 v238,1280,v235\nv_lshl_add_u32 v238,v236,4,v238\nv_subrev_u32 v229,1024,v238\ns_mul_i32 s71,s60,5\n"
"M8 a[16:31],a[240:243],v[108:111],a[16:31]\nSA s71,s71,2\ns_lshl_b32 s71,s71,6\nVU v234,s71,v200\nv_mul_u32_u24 v235,3277,v234\nv_lshrrev_b32 v235,16,v235\nv_mul_u32_u24 v236,20,v235\nv_sub_u32 v236,v234,v236\n"
"v_bfe_u32 v237,v235,2,1\nVL v237,1,v237\nv_xor_b32 v236,v236,v237\nM8 a[32:47],a[244:247],v[100:103],a[32:47]\nv_mul_u32_u24 v238,1280,v235\nv_lshl_add_u32 v238,v236,4,v238\nv_subrev_u32 v230,2048,v238\ns_mul_i32 s71,s60,5\n"
"SA s71,s71,3\ns_lshl_b32 s71,s71,6\nVU v234,s71,v200\nv_mul_u32_u24 v235,3277,v234\nv_lshrrev_b32 v235,16,v235\nv_mul_u32_u24 v236,20,v235\nM8 a[48:63],a[244:247],v[108:111],a[48:63]\nv_sub_u32 v236,v234,v236\n"
"v_bfe_u32 v237,v235,2,1\nVL v237,1,v237\nv_xor_b32 v236,v236,v237\nv_mul_u32_u24 v238,1280,v235\nv_lshl_add_u32 v238,v236,4,v238\nv_subrev_u32 v231,3072,v238\ns_mul_i32 s71,s60,5\nSA s71,s71,4\n"
"s_lshl_b32 s71,s71,6\nSW lgkmcnt(0)\nM8 a[64:79],a[248:251],v[100:103],a[64:79]\ns_load_dwordx2 s[76:77],s[94:95],0x40\ns_load_dwordx2 s[78:79],s[94:95],0x28\nVU v234,s71,v200\nv_mul_u32_u24 v235,3277,v234\nv_lshrrev_b32 v235,16,v235\n"
"v_mul_u32_u24 v236,20,v235\nv_sub_u32 v236,v234,v236\nv_bfe_u32 v237,v235,2,1\nVL v237,1,v237\nv_xor_b32 v236,v236,v237\nv_mul_u32_u24 v238,1280,v235\nv_lshl_add_u32 v238,v236,4,v238\nM8 a[80:95],a[248:251],v[108:111],a[80:95]\n"
"v_subrev_u32 v232,0,v238\nM8 a[96:111],a[252:255],v[100:103],a[96:111]\nM8 a[112:127],a[252:255],v[108:111],a[112:127]\nv_mov_b32 v170,v154\nv_mov_b32 v171,v155\ns_lshl_b32 s66,s89,15\nSA s66,s66,0x4000\nv_permlane32_swap_b32 v154,v170\n"
"v_permlane32_swap_b32 v155,v171\nVU v178,s66,v161\ns_xor_b32 s67,s89,1\ns_lshl_b32 s66,s67,15\nSA s66,s66,0x4000\nVA v154,v154,v170\nVA v155,v155,v171\nVU v179,s66,v161\n"
"s_lshl_b32 s66,s89,10\nSA s66,s66,0x20000\nVU v180,s66,v161\ns_lshl_b32 s66,s67,10\nSA s66,s66,0x20000\nVU v181,s66,v161\nv_mov_b32 v174,v156\nv_mov_b32 v175,v154\n"
"SW lgkmcnt(0)\ns_mul_i32 s71,s57,320\ns_sub_u32 s72,2211840,s71\nSA s40,s76,s71\ns_addc_u32 s41,s77,0\ns_and_b32 s41,s41,0xffff\nSM s42,s72\nSM s43,0x20000\n"
"s_mul_i32 s62,s70,40960\ns_mul_i32 s73,s73,40960\nSA s73,s73,s71\ns_sub_u32 s72,5242880,s73\nSA s84,s78,s73\ns_addc_u32 s85,s79,0\ns_and_b32 s85,s85,0xffff\nSM s86,s72\n"
"SM s87,0x20000\nVL v234,4,v200\ns_lshl_b32 s70,s59,12\nSA s71,s70,0\nVU v204,s71,v234\nSA s71,s70,55296\nVU v205,s71,v234\nSA s71,s70,110592\n"
"VU v206,s71,v234\nv_lshrrev_b32 v234,4,v200\nv_bfe_u32 v235,v200,2,2\nv_lshl_add_u32 v235,v234,2,v235\nv_mul_u32_u24 v235,320,v235\nVN v236,3,v200\nv_lshl_add_u32 v235,v236,3,v235\nVU v235,8192,v235\n"
"VN v234,1,v234\ns_mul_i32 s70,s60,5\nSA s71,s70,0\nv_xor_b32 v236,s71,v234\nv_lshl_add_u32 v208,v236,5,v235\nVU v213,55296,v208\nVU v218,110592,v208\nSA s71,s70,1\n"
"v_xor_b32 v236,s71,v234\nv_lshl_add_u32 v209,v236,5,v235\nVU v214,55296,v209\nVU v219,110592,v209\nSA s71,s70,2\nv_xor_b32 v236,s71,v234\nv_lshl_add_u32 v210,v236,5,v235\nVU v215,55296,v210\n"
"VU v220,110592,v210\nSA s71,s70,3\nv_xor_b32 v236,s71,v234\nv_lshl_add_u32 v211,v236,5,v235\nVU v216,55296,v211\nVU v221,110592,v211\nSA s71,s70,4\nv_xor_b32 v236,s71,v234\n"
"v_lshl_add_u32 v212,v236,5,v235\nVU v217,55296,v212\nVU v222,110592,v212\ns_and_b32 s34,s88,7\ns_lshl_b32 s34,s34,11\nSA s34,s34,63987712\nVL v240,5,v200\nVU v240,s34,v240\n"
"v_mov_b32 v241,s93\nv_add_co_u32 v240,vcc,s92,v240\ns_nop 1\nv_addc_co_u32 v241,vcc,0,v241,vcc\nglobal_load_dwordx4 v[242:245],v[240:241],off sc1\nglobal_load_dwordx4 v[246:249],v[240:241],off offset:16 sc1\ns_barrier\nds_write_b64 v180,v[174:175]\n"
"s_nop 5\nDW v178,a[0:3] offset:0\nDW v178,a[4:7] offset:1024\nDW v178,a[8:11] offset:2048\nDW v178,a[12:15] offset:3072\nDW v178,a[32:35] offset:4096\nDW v178,a[36:39] offset:5120\nDW v178,a[40:43] offset:6144\n"
"DW v178,a[44:47] offset:7168\nDW v178,a[64:67] offset:8192\nDW v178,a[68:71] offset:9216\nDW v178,a[72:75] offset:10240\nDW v178,a[76:79] offset:11264\nDW v178,a[96:99] offset:12288\nDW v178,a[100:103] offset:13312\nDW v178,a[104:107] offset:14336\n"
"DW v178,a[108:111] offset:15360\ns_lshl_b32 s66,s74,3\ns_lshl_b32 s67,s89,1\nSA s66,s66,s67\ns_mul_i32 s66,s66,182\ns_lshl_b32 s67,s54,2\nSA s66,s66,s67\ns_lshl_b32 s69,s66,10\n"
"SW lgkmcnt(0)\ns_barrier\nds_read_b64 v[182:183],v181\nDR v[16:19],v179 offset:0\nDR v[20:23],v179 offset:1024\nDR v[24:27],v179 offset:2048\nDR v[28:31],v179 offset:3072\nDR v[32:35],v179 offset:4096\n"
"DR v[36:39],v179 offset:5120\nDR v[40:43],v179 offset:6144\nDR v[44:47],v179 offset:7168\nDR v[48:51],v179 offset:8192\nDR v[52:55],v179 offset:9216\nDR v[56:59],v179 offset:10240\nDR v[60:63],v179 offset:11264\nDR v[64:67],v179 offset:12288\n"
"DR v[68:71],v179 offset:13312\nDR v[72:75],v179 offset:14336\nDR v[76:79],v179 offset:15360\nSW lgkmcnt(15)\nv_max_f32 v190,v157,v182\nVS v191,v157,v190\nVS v192,v182,v190\nEX v191,v191\n"
"EX v192,v192\ns_nop 0\nVM v193,v155,v191\nv_fma_f32 v193,v183,v192,v193\nRC v193,v193\ns_nop 0\nVM v186,v191,v193\nVM v188,v192,v193\n"
"v_mov_b32 v187,v186\nv_mov_b32 v189,v188\nAR v112,a16\nAR v113,a17\nAR v114,a18\nAR v115,a19\nAR v116,a20\nAR v117,a21\n"
"AR v118,a22\nAR v119,a23\nAR v120,a24\nAR v121,a25\nAR v122,a26\nAR v123,a27\nAR v124,a28\nAR v125,a29\n"
"AR v126,a30\nAR v127,a31\nPM v[112:113],v[112:113],v[186:187]\nPM v[114:115],v[114:115],v[186:187]\nPM v[116:117],v[116:117],v[186:187]\nPM v[118:119],v[118:119],v[186:187]\nPM v[120:121],v[120:121],v[186:187]\nPM v[122:123],v[122:123],v[186:187]\n"
"PM v[124:125],v[124:125],v[186:187]\nPM v[126:127],v[126:127],v[186:187]\nSW lgkmcnt(12)\nPF v[112:113],v[16:17],v[188:189],v[112:113]\nPF v[114:115],v[18:19],v[188:189],v[114:115]\nPF v[116:117],v[20:21],v[188:189],v[116:117]\nPF v[118:119],v[22:23],v[188:189],v[118:119]\nPF v[120:121],v[24:25],v[188:189],v[120:121]\n"
"PF v[122:123],v[26:27],v[188:189],v[122:123]\nPF v[124:125],v[28:29],v[188:189],v[124:125]\nPF v[126:127],v[30:31],v[188:189],v[126:127]\nCV v80,v112,v113\nCV v81,v114,v115\nCV v82,v120,v121\nCV v83,v122,v123\nBS v[80:83],v168,s[48:51],s69 offen offset:0 sc1\n"
"CV v84,v116,v117\nCV v85,v118,v119\nCV v86,v124,v125\nCV v87,v126,v127\nBS v[84:87],v168,s[48:51],s69 offen offset:512 sc1\nAR v128,a48\nAR v129,a49\nAR v130,a50\n"
"AR v131,a51\nAR v132,a52\nAR v133,a53\nAR v134,a54\nAR v135,a55\nAR v136,a56\nAR v137,a57\nAR v138,a58\n"
"AR v139,a59\nAR v140,a60\nAR v141,a61\nAR v142,a62\nAR v143,a63\nPM v[128:129],v[128:129],v[186:187]\nPM v[130:131],v[130:131],v[186:187]\nPM v[132:133],v[132:133],v[186:187]\n"
"PM v[134:135],v[134:135],v[186:187]\nPM v[136:137],v[136:137],v[186:187]\nPM v[138:139],v[138:139],v[186:187]\nPM v[140:141],v[140:141],v[186:187]\nPM v[142:143],v[142:143],v[186:187]\nSW lgkmcnt(8)\nPF v[128:129],v[32:33],v[188:189],v[128:129]\nPF v[130:131],v[34:35],v[188:189],v[130:131]\n"
"PF v[132:133],v[36:37],v[188:189],v[132:133]\nPF v[134:135],v[38:39],v[188:189],v[134:135]\nPF v[136:137],v[40:41],v[188:189],v[136:137]\nPF v[138:139],v[42:43],v[188:189],v[138:139]\nPF v[140:141],v[44:45],v[188:189],v[140:141]\nPF v[142:143],v[46:47],v[188:189],v[142:143]\nCV v88,v128,v129\nCV v89,v130,v131\n"
"CV v90,v136,v137\nCV v91,v138,v139\nBS v[88:91],v168,s[48:51],s69 offen offset:1024 sc1\nCV v92,v132,v133\nCV v93,v134,v135\nCV v94,v140,v141\nCV v95,v142,v143\nBS v[92:95],v168,s[48:51],s69 offen offset:1536 sc1\n"
"AR v112,a80\nAR v113,a81\nAR v114,a82\nAR v115,a83\nAR v116,a84\nAR v117,a85\nAR v118,a86\nAR v119,a87\n"
"AR v120,a88\nAR v121,a89\nAR v122,a90\nAR v123,a91\nAR v124,a92\nAR v125,a93\nAR v126,a94\nAR v127,a95\n"
"PM v[112:113],v[112:113],v[186:187]\nPM v[114:115],v[114:115],v[186:187]\nPM v[116:117],v[116:117],v[186:187]\nPM v[118:119],v[118:119],v[186:187]\nPM v[120:121],v[120:121],v[186:187]\nPM v[122:123],v[122:123],v[186:187]\nPM v[124:125],v[124:125],v[186:187]\nPM v[126:127],v[126:127],v[186:187]\n"
"SW lgkmcnt(4)\nPF v[112:113],v[48:49],v[188:189],v[112:113]\nPF v[114:115],v[50:51],v[188:189],v[114:115]\nPF v[116:117],v[52:53],v[188:189],v[116:117]\nPF v[118:119],v[54:55],v[188:189],v[118:119]\nPF v[120:121],v[56:57],v[188:189],v[120:121]\nPF v[122:123],v[58:59],v[188:189],v[122:123]\nPF v[124:125],v[60:61],v[188:189],v[124:125]\n"
"PF v[126:127],v[62:63],v[188:189],v[126:127]\nCV v80,v112,v113\nCV v81,v114,v115\nCV v82,v120,v121\nCV v83,v122,v123\nBS v[80:83],v168,s[48:51],s69 offen offset:2048 sc1\nCV v84,v116,v117\nCV v85,v118,v119\n"
"CV v86,v124,v125\nCV v87,v126,v127\nBS v[84:87],v168,s[48:51],s69 offen offset:2560 sc1\nAR v128,a112\nAR v129,a113\nAR v130,a114\nAR v131,a115\nAR v132,a116\n"
"AR v133,a117\nAR v134,a118\nAR v135,a119\nAR v136,a120\nAR v137,a121\nAR v138,a122\nAR v139,a123\nAR v140,a124\n"
"AR v141,a125\nAR v142,a126\nAR v143,a127\nPM v[128:129],v[128:129],v[186:187]\nPM v[130:131],v[130:131],v[186:187]\nPM v[132:133],v[132:133],v[186:187]\nPM v[134:135],v[134:135],v[186:187]\nPM v[136:137],v[136:137],v[186:187]\n"
"PM v[138:139],v[138:139],v[186:187]\nPM v[140:141],v[140:141],v[186:187]\nPM v[142:143],v[142:143],v[186:187]\nSW lgkmcnt(0)\nPF v[128:129],v[64:65],v[188:189],v[128:129]\nPF v[130:131],v[66:67],v[188:189],v[130:131]\nPF v[132:133],v[68:69],v[188:189],v[132:133]\nPF v[134:135],v[70:71],v[188:189],v[134:135]\n"
"PF v[136:137],v[72:73],v[188:189],v[136:137]\nPF v[138:139],v[74:75],v[188:189],v[138:139]\nPF v[140:141],v[76:77],v[188:189],v[140:141]\nPF v[142:143],v[78:79],v[188:189],v[142:143]\nCV v88,v128,v129\nCV v89,v130,v131\nCV v90,v136,v137\nCV v91,v138,v139\n"
"BS v[88:91],v168,s[48:51],s69 offen offset:3072 sc1\nCV v92,v132,v133\nCV v93,v134,v135\nCV v94,v140,v141\nCV v95,v142,v143\nBS v[92:95],v168,s[48:51],s69 offen offset:3584 sc1\ns_branch AT_end\nAT_d0a_fb:\n"
"s_getpc_b64 s[72:73]\nret_AT_fb0_13521_pc:\nSA s72,s72,AT_fb0-ret_AT_fb0_13521_pc\ns_addc_u32 s73,s73,0\ns_swappc_b64 s[70:71],s[72:73]\ns_branch AT_d0a_ret\nAT_d1a_fb:\ns_getpc_b64 s[72:73]\n"
"ret_AT_fb1_13528_pc:\nSA s72,s72,AT_fb1-ret_AT_fb1_13528_pc\ns_addc_u32 s73,s73,0\ns_swappc_b64 s[70:71],s[72:73]\ns_branch AT_d1a_ret\nAT_d0_fb:\ns_getpc_b64 s[72:73]\nret_AT_fb0_13535_pc:\n"
"SA s72,s72,AT_fb0-ret_AT_fb0_13535_pc\ns_addc_u32 s73,s73,0\ns_swappc_b64 s[70:71],s[72:73]\ns_branch AT_d0_ret\nAT_d1_fb:\ns_getpc_b64 s[72:73]\nret_AT_fb1_13542_pc:\nSA s72,s72,AT_fb1-ret_AT_fb1_13542_pc\n"
"s_addc_u32 s73,s73,0\ns_swappc_b64 s[70:71],s[72:73]\ns_branch AT_d1_ret\nAT_d0m_fb:\ns_getpc_b64 s[72:73]\nret_AT_fb0_13549_pc:\nSA s72,s72,AT_fb0-ret_AT_fb0_13549_pc\ns_addc_u32 s73,s73,0\n"
"s_swappc_b64 s[70:71],s[72:73]\ns_branch AT_d0m_ret\nAT_d0am_fb:\ns_getpc_b64 s[72:73]\nret_AT_fb0_13556_pc:\nSA s72,s72,AT_fb0-ret_AT_fb0_13556_pc\ns_addc_u32 s73,s73,0\ns_swappc_b64 s[70:71],s[72:73]\n"
"s_branch AT_d0am_ret\nAT_d1m_fb:\ns_getpc_b64 s[72:73]\nret_AT_fb1_13563_pc:\nSA s72,s72,AT_fb1-ret_AT_fb1_13563_pc\ns_addc_u32 s73,s73,0\ns_swappc_b64 s[70:71],s[72:73]\ns_branch AT_d1m_ret\n"
"AT_d0m2_fb:\ns_getpc_b64 s[72:73]\nret_AT_fb0_13570_pc:\nSA s72,s72,AT_fb0-ret_AT_fb0_13570_pc\ns_addc_u32 s73,s73,0\ns_swappc_b64 s[70:71],s[72:73]\ns_branch AT_d0m2_ret\nAT_d1l_fb:\n"
"s_getpc_b64 s[72:73]\nret_AT_fb1_13577_pc:\nSA s72,s72,AT_fb1-ret_AT_fb1_13577_pc\ns_addc_u32 s73,s73,0\ns_swappc_b64 s[70:71],s[72:73]\ns_branch AT_d1l_ret\nAT_s0_fb:\ns_getpc_b64 s[72:73]\n"
"ret_AT_fb0_13584_pc:\nSA s72,s72,AT_fb0-ret_AT_fb0_13584_pc\ns_addc_u32 s73,s73,0\ns_swappc_b64 s[70:71],s[72:73]\ns_branch AT_s0_ret\nAT_lo_fb:\ns_getpc_b64 s[72:73]\nret_AT_fb1_13591_pc:\n"
"SA s72,s72,AT_fb1-ret_AT_fb1_13591_pc\ns_addc_u32 s73,s73,0\ns_swappc_b64 s[70:71],s[72:73]\ns_branch AT_lo_ret\nAT_le_fb:\ns_getpc_b64 s[72:73]\nret_AT_fb0_13598_pc:\nSA s72,s72,AT_fb0-ret_AT_fb0_13598_pc\n"
"s_addc_u32 s73,s73,0\ns_swappc_b64 s[70:71],s[72:73]\ns_branch AT_le_ret\nAT_to_fb:\ns_getpc_b64 s[72:73]\nret_AT_fb1_13605_pc:\nSA s72,s72,AT_fb1-ret_AT_fb1_13605_pc\ns_addc_u32 s73,s73,0\n"
"s_swappc_b64 s[70:71],s[72:73]\ns_branch AT_to_ret\nAT_tem_fb:\ns_getpc_b64 s[72:73]\nret_AT_fb0_13612_pc:\nSA s72,s72,AT_fb0-ret_AT_fb0_13612_pc\ns_addc_u32 s73,s73,0\ns_swappc_b64 s[70:71],s[72:73]\n"
"s_branch AT_tem_ret\nAT_s0m_fb:\ns_getpc_b64 s[72:73]\nret_AT_fb0_13619_pc:\nSA s72,s72,AT_fb0-ret_AT_fb0_13619_pc\ns_addc_u32 s73,s73,0\ns_swappc_b64 s[70:71],s[72:73]\ns_branch AT_s0m_ret\n"
"AT_lm_fb:\ns_getpc_b64 s[72:73]\nret_AT_fb1_13626_pc:\nSA s72,s72,AT_fb1-ret_AT_fb1_13626_pc\ns_addc_u32 s73,s73,0\ns_swappc_b64 s[70:71],s[72:73]\ns_branch AT_lm_ret\nAT_fb0:\n"
"s_nop 15\nv_max3_f32 v170,v16,v17,v18\nv_max3_f32 v174,v19,v20,v21\nv_max3_f32 v170,v170,v22,v23\nv_max3_f32 v174,v174,v24,v25\nv_max3_f32 v170,v170,v26,v27\nv_max3_f32 v174,v174,v28,v29\nv_max3_f32 v170,v170,v30,v31\n"
"v_max_f32 v170,v170,v174\nv_mov_b32 v174,v170\ns_nop 1\nv_permlane32_swap_b32 v170,v174\ns_nop 1\nv_max_f32 v170,v170,v174\nVA v170,0x41f00000,v170\nv_max_f32 v170,0,v170\n"
"EX v172,-v170\nVA v156,v156,v170\nVM v154,v154,v172\nVS v112,0,v156\nVS v113,0,v156\nVS v114,0,v156\nVS v115,0,v156\nVS v116,0,v156\n"
"VS v117,0,v156\nVS v118,0,v156\nVS v119,0,v156\nVS v120,0,v156\nVS v121,0,v156\nVS v122,0,v156\nVS v123,0,v156\nVS v124,0,v156\n"
"VS v125,0,v156\nVS v126,0,v156\nVS v127,0,v156\nVS v16,v16,v170\nVS v17,v17,v170\nVS v18,v18,v170\nVS v19,v19,v170\nVS v20,v20,v170\n"
"VS v21,v21,v170\nVS v22,v22,v170\nVS v23,v23,v170\nVS v24,v24,v170\nVS v25,v25,v170\nVS v26,v26,v170\nVS v27,v27,v170\nVS v28,v28,v170\n"
"VS v29,v29,v170\nVS v30,v30,v170\nVS v31,v31,v170\nVS v48,v48,v170\nVS v49,v49,v170\nVS v50,v50,v170\nVS v51,v51,v170\nVS v52,v52,v170\n"
"VS v53,v53,v170\nVS v54,v54,v170\nVS v55,v55,v170\nVS v56,v56,v170\nVS v57,v57,v170\nVS v58,v58,v170\nVS v59,v59,v170\nVS v60,v60,v170\n"
"VS v61,v61,v170\nVS v62,v62,v170\nVS v63,v63,v170\nAR v176,a0\nAR v177,a1\nAR v178,a2\nAR v179,a3\nVM v176,v176,v172\n"
"VM v177,v177,v172\nVM v178,v178,v172\nVM v179,v179,v172\nAW a0,v176\nAW a1,v177\nAW a2,v178\nAW a3,v179\nAR v176,a4\n"
"AR v177,a5\nAR v178,a6\nAR v179,a7\nVM v176,v176,v172\nVM v177,v177,v172\nVM v178,v178,v172\nVM v179,v179,v172\nAW a4,v176\n"
"AW a5,v177\nAW a6,v178\nAW a7,v179\nAR v176,a8\nAR v177,a9\nAR v178,a10\nAR v179,a11\nVM v176,v176,v172\n"
"VM v177,v177,v172\nVM v178,v178,v172\nVM v179,v179,v172\nAW a8,v176\nAW a9,v177\nAW a10,v178\nAW a11,v179\nAR v176,a12\n"
"AR v177,a13\nAR v178,a14\nAR v179,a15\nVM v176,v176,v172\nVM v177,v177,v172\nVM v178,v178,v172\nVM v179,v179,v172\nAW a12,v176\n"
"AW a13,v177\nAW a14,v178\nAW a15,v179\nAR v176,a32\nAR v177,a33\nAR v178,a34\nAR v179,a35\nVM v176,v176,v172\n"
"VM v177,v177,v172\nVM v178,v178,v172\nVM v179,v179,v172\nAW a32,v176\nAW a33,v177\nAW a34,v178\nAW a35,v179\nAR v176,a36\n"
"AR v177,a37\nAR v178,a38\nAR v179,a39\nVM v176,v176,v172\nVM v177,v177,v172\nVM v178,v178,v172\nVM v179,v179,v172\nAW a36,v176\n"
"AW a37,v177\nAW a38,v178\nAW a39,v179\nAR v176,a40\nAR v177,a41\nAR v178,a42\nAR v179,a43\nVM v176,v176,v172\n"
"VM v177,v177,v172\nVM v178,v178,v172\nVM v179,v179,v172\nAW a40,v176\nAW a41,v177\nAW a42,v178\nAW a43,v179\nAR v176,a44\n"
"AR v177,a45\nAR v178,a46\nAR v179,a47\nVM v176,v176,v172\nVM v177,v177,v172\nVM v178,v178,v172\nVM v179,v179,v172\nAW a44,v176\n"
"AW a45,v177\nAW a46,v178\nAW a47,v179\nAR v176,a64\nAR v177,a65\nAR v178,a66\nAR v179,a67\nVM v176,v176,v172\n"
"VM v177,v177,v172\nVM v178,v178,v172\nVM v179,v179,v172\nAW a64,v176\nAW a65,v177\nAW a66,v178\nAW a67,v179\nAR v176,a68\n"
"AR v177,a69\nAR v178,a70\nAR v179,a71\nVM v176,v176,v172\nVM v177,v177,v172\nVM v178,v178,v172\nVM v179,v179,v172\nAW a68,v176\n"
"AW a69,v177\nAW a70,v178\nAW a71,v179\nAR v176,a72\nAR v177,a73\nAR v178,a74\nAR v179,a75\nVM v176,v176,v172\n"
"VM v177,v177,v172\nVM v178,v178,v172\nVM v179,v179,v172\nAW a72,v176\nAW a73,v177\nAW a74,v178\nAW a75,v179\nAR v176,a76\n"
"AR v177,a77\nAR v178,a78\nAR v179,a79\nVM v176,v176,v172\nVM v177,v177,v172\nVM v178,v178,v172\nVM v179,v179,v172\nAW a76,v176\n"
"AW a77,v177\nAW a78,v178\nAW a79,v179\nAR v176,a96\nAR v177,a97\nAR v178,a98\nAR v179,a99\nVM v176,v176,v172\n"
"VM v177,v177,v172\nVM v178,v178,v172\nVM v179,v179,v172\nAW a96,v176\nAW a97,v177\nAW a98,v178\nAW a99,v179\nAR v176,a100\n"
"AR v177,a101\nAR v178,a102\nAR v179,a103\nVM v176,v176,v172\nVM v177,v177,v172\nVM v178,v178,v172\nVM v179,v179,v172\nAW a100,v176\n"
"AW a101,v177\nAW a102,v178\nAW a103,v179\nAR v176,a104\nAR v177,a105\nAR v178,a106\nAR v179,a107\nVM v176,v176,v172\n"
"VM v177,v177,v172\nVM v178,v178,v172\nVM v179,v179,v172\nAW a104,v176\nAW a105,v177\nAW a106,v178\nAW a107,v179\nAR v176,a108\n"
"AR v177,a109\nAR v178,a110\nAR v179,a111\nVM v176,v176,v172\nVM v177,v177,v172\nVM v178,v178,v172\nVM v179,v179,v172\nAW a108,v176\n"
"AW a109,v177\nAW a110,v178\nAW a111,v179\nv_max3_f32 v171,v32,v33,v34\nv_max3_f32 v174,v35,v36,v37\nv_max3_f32 v171,v171,v38,v39\nv_max3_f32 v174,v174,v40,v41\nv_max3_f32 v171,v171,v42,v43\n"
"v_max3_f32 v174,v174,v44,v45\nv_max3_f32 v171,v171,v46,v47\nv_max_f32 v171,v171,v174\nv_mov_b32 v174,v171\ns_nop 1\nv_permlane32_swap_b32 v171,v174\ns_nop 1\nv_max_f32 v171,v171,v174\n"
"VA v171,0x41f00000,v171\nv_max_f32 v171,0,v171\nEX v173,-v171\nVA v157,v157,v171\nVM v155,v155,v173\nVS v128,0,v157\nVS v129,0,v157\nVS v130,0,v157\n"
"VS v131,0,v157\nVS v132,0,v157\nVS v133,0,v157\nVS v134,0,v157\nVS v135,0,v157\nVS v136,0,v157\nVS v137,0,v157\nVS v138,0,v157\n"
"VS v139,0,v157\nVS v140,0,v157\nVS v141,0,v157\nVS v142,0,v157\nVS v143,0,v157\nVS v32,v32,v171\nVS v33,v33,v171\nVS v34,v34,v171\n"
"VS v35,v35,v171\nVS v36,v36,v171\nVS v37,v37,v171\nVS v38,v38,v171\nVS v39,v39,v171\nVS v40,v40,v171\nVS v41,v41,v171\nVS v42,v42,v171\n"
"VS v43,v43,v171\nVS v44,v44,v171\nVS v45,v45,v171\nVS v46,v46,v171\nVS v47,v47,v171\nVS v64,v64,v171\nVS v65,v65,v171\nVS v66,v66,v171\n"
"VS v67,v67,v171\nVS v68,v68,v171\nVS v69,v69,v171\nVS v70,v70,v171\nVS v71,v71,v171\nVS v72,v72,v171\nVS v73,v73,v171\nVS v74,v74,v171\n"
"VS v75,v75,v171\nVS v76,v76,v171\nVS v77,v77,v171\nVS v78,v78,v171\nVS v79,v79,v171\nAR v176,a16\nAR v177,a17\nAR v178,a18\n"
"AR v179,a19\nVM v176,v176,v173\nVM v177,v177,v173\nVM v178,v178,v173\nVM v179,v179,v173\nAW a16,v176\nAW a17,v177\nAW a18,v178\n"
"AW a19,v179\nAR v176,a20\nAR v177,a21\nAR v178,a22\nAR v179,a23\nVM v176,v176,v173\nVM v177,v177,v173\nVM v178,v178,v173\n"
"VM v179,v179,v173\nAW a20,v176\nAW a21,v177\nAW a22,v178\nAW a23,v179\nAR v176,a24\nAR v177,a25\nAR v178,a26\n"
"AR v179,a27\nVM v176,v176,v173\nVM v177,v177,v173\nVM v178,v178,v173\nVM v179,v179,v173\nAW a24,v176\nAW a25,v177\nAW a26,v178\n"
"AW a27,v179\nAR v176,a28\nAR v177,a29\nAR v178,a30\nAR v179,a31\nVM v176,v176,v173\nVM v177,v177,v173\nVM v178,v178,v173\n"
"VM v179,v179,v173\nAW a28,v176\nAW a29,v177\nAW a30,v178\nAW a31,v179\nAR v176,a48\nAR v177,a49\nAR v178,a50\n"
"AR v179,a51\nVM v176,v176,v173\nVM v177,v177,v173\nVM v178,v178,v173\nVM v179,v179,v173\nAW a48,v176\nAW a49,v177\nAW a50,v178\n"
"AW a51,v179\nAR v176,a52\nAR v177,a53\nAR v178,a54\nAR v179,a55\nVM v176,v176,v173\nVM v177,v177,v173\nVM v178,v178,v173\n"
"VM v179,v179,v173\nAW a52,v176\nAW a53,v177\nAW a54,v178\nAW a55,v179\nAR v176,a56\nAR v177,a57\nAR v178,a58\n"
"AR v179,a59\nVM v176,v176,v173\nVM v177,v177,v173\nVM v178,v178,v173\nVM v179,v179,v173\nAW a56,v176\nAW a57,v177\nAW a58,v178\n"
"AW a59,v179\nAR v176,a60\nAR v177,a61\nAR v178,a62\nAR v179,a63\nVM v176,v176,v173\nVM v177,v177,v173\nVM v178,v178,v173\n"
"VM v179,v179,v173\nAW a60,v176\nAW a61,v177\nAW a62,v178\nAW a63,v179\nAR v176,a80\nAR v177,a81\nAR v178,a82\n"
"AR v179,a83\nVM v176,v176,v173\nVM v177,v177,v173\nVM v178,v178,v173\nVM v179,v179,v173\nAW a80,v176\nAW a81,v177\nAW a82,v178\n"
"AW a83,v179\nAR v176,a84\nAR v177,a85\nAR v178,a86\nAR v179,a87\nVM v176,v176,v173\nVM v177,v177,v173\nVM v178,v178,v173\n"
"VM v179,v179,v173\nAW a84,v176\nAW a85,v177\nAW a86,v178\nAW a87,v179\nAR v176,a88\nAR v177,a89\nAR v178,a90\n"
"AR v179,a91\nVM v176,v176,v173\nVM v177,v177,v173\nVM v178,v178,v173\nVM v179,v179,v173\nAW a88,v176\nAW a89,v177\nAW a90,v178\n"
"AW a91,v179\nAR v176,a92\nAR v177,a93\nAR v178,a94\nAR v179,a95\nVM v176,v176,v173\nVM v177,v177,v173\nVM v178,v178,v173\n"
"VM v179,v179,v173\nAW a92,v176\nAW a93,v177\nAW a94,v178\nAW a95,v179\nAR v176,a112\nAR v177,a113\nAR v178,a114\n"
"AR v179,a115\nVM v176,v176,v173\nVM v177,v177,v173\nVM v178,v178,v173\nVM v179,v179,v173\nAW a112,v176\nAW a113,v177\nAW a114,v178\n"
"AW a115,v179\nAR v176,a116\nAR v177,a117\nAR v178,a118\nAR v179,a119\nVM v176,v176,v173\nVM v177,v177,v173\nVM v178,v178,v173\n"
"VM v179,v179,v173\nAW a116,v176\nAW a117,v177\nAW a118,v178\nAW a119,v179\nAR v176,a120\nAR v177,a121\nAR v178,a122\n"
"AR v179,a123\nVM v176,v176,v173\nVM v177,v177,v173\nVM v178,v178,v173\nVM v179,v179,v173\nAW a120,v176\nAW a121,v177\nAW a122,v178\n"
"AW a123,v179\nAR v176,a124\nAR v177,a125\nAR v178,a126\nAR v179,a127\nVM v176,v176,v173\nVM v177,v177,v173\nVM v178,v178,v173\n"
"VM v179,v179,v173\nAW a124,v176\nAW a125,v177\nAW a126,v178\nAW a127,v179\nEX v144,v16\nEX v145,v17\nEX v146,v18\n"
"VA v152,v144,v145\nCV v80,v144,v145\nEX v147,v19\nVA v152,v152,v146\nEX v144,v20\nVA v152,v152,v147\nCV v81,v146,v147\nEX v145,v21\n"
"VA v152,v152,v144\nEX v146,v22\nVA v152,v152,v145\nCV v82,v144,v145\nEX v147,v23\nVA v152,v152,v146\nEX v144,v24\nVA v152,v152,v147\n"
"CV v83,v146,v147\nEX v145,v25\nVA v152,v152,v144\nEX v146,v26\nVA v152,v152,v145\nCV v84,v144,v145\nEX v147,v27\nVA v152,v152,v146\n"
"EX v144,v28\nVA v152,v152,v147\nCV v85,v146,v147\nEX v145,v29\nVA v152,v152,v144\nEX v146,v30\nVA v152,v152,v145\nCV v86,v144,v145\n"
"EX v147,v31\nVA v152,v152,v146\nEX v144,v32\nVA v152,v152,v147\nCV v87,v146,v147\nEX v145,v33\nEX v146,v34\nVA v153,v144,v145\n"
"CV v88,v144,v145\nEX v147,v35\nVA v153,v153,v146\nEX v144,v36\nVA v153,v153,v147\nCV v89,v146,v147\nEX v145,v37\nVA v153,v153,v144\n"
"EX v146,v38\nVA v153,v153,v145\nCV v90,v144,v145\nEX v147,v39\nVA v153,v153,v146\nEX v144,v40\nVA v153,v153,v147\nCV v91,v146,v147\n"
"EX v145,v41\nVA v153,v153,v144\nEX v146,v42\nVA v153,v153,v145\nCV v92,v144,v145\nEX v147,v43\nVA v153,v153,v146\nEX v144,v44\n"
"VA v153,v153,v147\nCV v93,v146,v147\nEX v145,v45\nVA v153,v153,v144\nEX v146,v46\nVA v153,v153,v145\nCV v94,v144,v145\nEX v147,v47\n"
"VA v153,v153,v146\ns_nop 1\nVA v153,v153,v147\nCV v95,v146,v147\ns_nop 3\ns_setpc_b64 s[70:71]\nAT_fb1:\ns_nop 15\n"
"v_max3_f32 v170,v48,v49,v50\nv_max3_f32 v174,v51,v52,v53\nv_max3_f32 v170,v170,v54,v55\nv_max3_f32 v174,v174,v56,v57\nv_max3_f32 v170,v170,v58,v59\nv_max3_f32 v174,v174,v60,v61\nv_max3_f32 v170,v170,v62,v63\nv_max_f32 v170,v170,v174\n"
"v_mov_b32 v174,v170\ns_nop 1\nv_permlane32_swap_b32 v170,v174\ns_nop 1\nv_max_f32 v170,v170,v174\nVA v170,0x41f00000,v170\nv_max_f32 v170,0,v170\nEX v172,-v170\n"
"VA v156,v156,v170\nVM v154,v154,v172\nVS v112,0,v156\nVS v113,0,v156\nVS v114,0,v156\nVS v115,0,v156\nVS v116,0,v156\nVS v117,0,v156\n"
"VS v118,0,v156\nVS v119,0,v156\nVS v120,0,v156\nVS v121,0,v156\nVS v122,0,v156\nVS v123,0,v156\nVS v124,0,v156\nVS v125,0,v156\n"
"VS v126,0,v156\nVS v127,0,v156\nVS v48,v48,v170\nVS v49,v49,v170\nVS v50,v50,v170\nVS v51,v51,v170\nVS v52,v52,v170\nVS v53,v53,v170\n"
"VS v54,v54,v170\nVS v55,v55,v170\nVS v56,v56,v170\nVS v57,v57,v170\nVS v58,v58,v170\nVS v59,v59,v170\nVS v60,v60,v170\nVS v61,v61,v170\n"
"VS v62,v62,v170\nVS v63,v63,v170\nVS v16,v16,v170\nVS v17,v17,v170\nVS v18,v18,v170\nVS v19,v19,v170\nVS v20,v20,v170\nVS v21,v21,v170\n"
"VS v22,v22,v170\nVS v23,v23,v170\nVS v24,v24,v170\nVS v25,v25,v170\nVS v26,v26,v170\nVS v27,v27,v170\nVS v28,v28,v170\nVS v29,v29,v170\n"
"VS v30,v30,v170\nVS v31,v31,v170\nAR v176,a0\nAR v177,a1\nAR v178,a2\nAR v179,a3\nVM v176,v176,v172\nVM v177,v177,v172\n"
"VM v178,v178,v172\nVM v179,v179,v172\nAW a0,v176\nAW a1,v177\nAW a2,v178\nAW a3,v179\nAR v176,a4\nAR v177,a5\n"
"AR v178,a6\nAR v179,a7\nVM v176,v176,v172\nVM v177,v177,v172\nVM v178,v178,v172\nVM v179,v179,v172\nAW a4,v176\nAW a5,v177\n"
"AW a6,v178\nAW a7,v179\nAR v176,a8\nAR v177,a9\nAR v178,a10\nAR v179,a11\nVM v176,v176,v172\nVM v177,v177,v172\n"
"VM v178,v178,v172\nVM v179,v179,v172\nAW a8,v176\nAW a9,v177\nAW a10,v178\nAW a11,v179\nAR v176,a12\nAR v177,a13\n"
"AR v178,a14\nAR v179,a15\nVM v176,v176,v172\nVM v177,v177,v172\nVM v178,v178,v172\nVM v179,v179,v172\nAW a12,v176\nAW a13,v177\n"
"AW a14,v178\nAW a15,v179\nAR v176,a32\nAR v177,a33\nAR v178,a34\nAR v179,a35\nVM v176,v176,v172\nVM v177,v177,v172\n"
"VM v178,v178,v172\nVM v179,v179,v172\nAW a32,v176\nAW a33,v177\nAW a34,v178\nAW a35,v179\nAR v176,a36\nAR v177,a37\n"
"AR v178,a38\nAR v179,a39\nVM v176,v176,v172\nVM v177,v177,v172\nVM v178,v178,v172\nVM v179,v179,v172\nAW a36,v176\nAW a37,v177\n"
"AW a38,v178\nAW a39,v179\nAR v176,a40\nAR v177,a41\nAR v178,a42\nAR v179,a43\nVM v176,v176,v172\nVM v177,v177,v172\n"
"VM v178,v178,v172\nVM v179,v179,v172\nAW a40,v176\nAW a41,v177\nAW a42,v178\nAW a43,v179\nAR v176,a44\nAR v177,a45\n"
"AR v178,a46\nAR v179,a47\nVM v176,v176,v172\nVM v177,v177,v172\nVM v178,v178,v172\nVM v179,v179,v172\nAW a44,v176\nAW a45,v177\n"
"AW a46,v178\nAW a47,v179\nAR v176,a64\nAR v177,a65\nAR v178,a66\nAR v179,a67\nVM v176,v176,v172\nVM v177,v177,v172\n"
"VM v178,v178,v172\nVM v179,v179,v172\nAW a64,v176\nAW a65,v177\nAW a66,v178\nAW a67,v179\nAR v176,a68\nAR v177,a69\n"
"AR v178,a70\nAR v179,a71\nVM v176,v176,v172\nVM v177,v177,v172\nVM v178,v178,v172\nVM v179,v179,v172\nAW a68,v176\nAW a69,v177\n"
"AW a70,v178\nAW a71,v179\nAR v176,a72\nAR v177,a73\nAR v178,a74\nAR v179,a75\nVM v176,v176,v172\nVM v177,v177,v172\n"
"VM v178,v178,v172\nVM v179,v179,v172\nAW a72,v176\nAW a73,v177\nAW a74,v178\nAW a75,v179\nAR v176,a76\nAR v177,a77\n"
"AR v178,a78\nAR v179,a79\nVM v176,v176,v172\nVM v177,v177,v172\nVM v178,v178,v172\nVM v179,v179,v172\nAW a76,v176\nAW a77,v177\n"
"AW a78,v178\nAW a79,v179\nAR v176,a96\nAR v177,a97\nAR v178,a98\nAR v179,a99\nVM v176,v176,v172\nVM v177,v177,v172\n"
"VM v178,v178,v172\nVM v179,v179,v172\nAW a96,v176\nAW a97,v177\nAW a98,v178\nAW a99,v179\nAR v176,a100\nAR v177,a101\n"
"AR v178,a102\nAR v179,a103\nVM v176,v176,v172\nVM v177,v177,v172\nVM v178,v178,v172\nVM v179,v179,v172\nAW a100,v176\nAW a101,v177\n"
"AW a102,v178\nAW a103,v179\nAR v176,a104\nAR v177,a105\nAR v178,a106\nAR v179,a107\nVM v176,v176,v172\nVM v177,v177,v172\n"
"VM v178,v178,v172\nVM v179,v179,v172\nAW a104,v176\nAW a105,v177\nAW a106,v178\nAW a107,v179\nAR v176,a108\nAR v177,a109\n"
"AR v178,a110\nAR v179,a111\nVM v176,v176,v172\nVM v177,v177,v172\nVM v178,v178,v172\nVM v179,v179,v172\nAW a108,v176\nAW a109,v177\n"
"AW a110,v178\nAW a111,v179\nv_max3_f32 v171,v64,v65,v66\nv_max3_f32 v174,v67,v68,v69\nv_max3_f32 v171,v171,v70,v71\nv_max3_f32 v174,v174,v72,v73\nv_max3_f32 v171,v171,v74,v75\nv_max3_f32 v174,v174,v76,v77\n"
"v_max3_f32 v171,v171,v78,v79\nv_max_f32 v171,v171,v174\nv_mov_b32 v174,v171\ns_nop 1\nv_permlane32_swap_b32 v171,v174\ns_nop 1\nv_max_f32 v171,v171,v174\nVA v171,0x41f00000,v171\n"
"v_max_f32 v171,0,v171\nEX v173,-v171\nVA v157,v157,v171\nVM v155,v155,v173\nVS v128,0,v157\nVS v129,0,v157\nVS v130,0,v157\nVS v131,0,v157\n"
"VS v132,0,v157\nVS v133,0,v157\nVS v134,0,v157\nVS v135,0,v157\nVS v136,0,v157\nVS v137,0,v157\nVS v138,0,v157\nVS v139,0,v157\n"
"VS v140,0,v157\nVS v141,0,v157\nVS v142,0,v157\nVS v143,0,v157\nVS v64,v64,v171\nVS v65,v65,v171\nVS v66,v66,v171\nVS v67,v67,v171\n"
"VS v68,v68,v171\nVS v69,v69,v171\nVS v70,v70,v171\nVS v71,v71,v171\nVS v72,v72,v171\nVS v73,v73,v171\nVS v74,v74,v171\nVS v75,v75,v171\n"
"VS v76,v76,v171\nVS v77,v77,v171\nVS v78,v78,v171\nVS v79,v79,v171\nVS v32,v32,v171\nVS v33,v33,v171\nVS v34,v34,v171\nVS v35,v35,v171\n"
"VS v36,v36,v171\nVS v37,v37,v171\nVS v38,v38,v171\nVS v39,v39,v171\nVS v40,v40,v171\nVS v41,v41,v171\nVS v42,v42,v171\nVS v43,v43,v171\n"
"VS v44,v44,v171\nVS v45,v45,v171\nVS v46,v46,v171\nVS v47,v47,v171\nAR v176,a16\nAR v177,a17\nAR v178,a18\nAR v179,a19\n"
"VM v176,v176,v173\nVM v177,v177,v173\nVM v178,v178,v173\nVM v179,v179,v173\nAW a16,v176\nAW a17,v177\nAW a18,v178\nAW a19,v179\n"
"AR v176,a20\nAR v177,a21\nAR v178,a22\nAR v179,a23\nVM v176,v176,v173\nVM v177,v177,v173\nVM v178,v178,v173\nVM v179,v179,v173\n"
"AW a20,v176\nAW a21,v177\nAW a22,v178\nAW a23,v179\nAR v176,a24\nAR v177,a25\nAR v178,a26\nAR v179,a27\n"
"VM v176,v176,v173\nVM v177,v177,v173\nVM v178,v178,v173\nVM v179,v179,v173\nAW a24,v176\nAW a25,v177\nAW a26,v178\nAW a27,v179\n"
"AR v176,a28\nAR v177,a29\nAR v178,a30\nAR v179,a31\nVM v176,v176,v173\nVM v177,v177,v173\nVM v178,v178,v173\nVM v179,v179,v173\n"
"AW a28,v176\nAW a29,v177\nAW a30,v178\nAW a31,v179\nAR v176,a48\nAR v177,a49\nAR v178,a50\nAR v179,a51\n"
"VM v176,v176,v173\nVM v177,v177,v173\nVM v178,v178,v173\nVM v179,v179,v173\nAW a48,v176\nAW a49,v177\nAW a50,v178\nAW a51,v179\n"
"AR v176,a52\nAR v177,a53\nAR v178,a54\nAR v179,a55\nVM v176,v176,v173\nVM v177,v177,v173\nVM v178,v178,v173\nVM v179,v179,v173\n"
"AW a52,v176\nAW a53,v177\nAW a54,v178\nAW a55,v179\nAR v176,a56\nAR v177,a57\nAR v178,a58\nAR v179,a59\n"
"VM v176,v176,v173\nVM v177,v177,v173\nVM v178,v178,v173\nVM v179,v179,v173\nAW a56,v176\nAW a57,v177\nAW a58,v178\nAW a59,v179\n"
"AR v176,a60\nAR v177,a61\nAR v178,a62\nAR v179,a63\nVM v176,v176,v173\nVM v177,v177,v173\nVM v178,v178,v173\nVM v179,v179,v173\n"
"AW a60,v176\nAW a61,v177\nAW a62,v178\nAW a63,v179\nAR v176,a80\nAR v177,a81\nAR v178,a82\nAR v179,a83\n"
"VM v176,v176,v173\nVM v177,v177,v173\nVM v178,v178,v173\nVM v179,v179,v173\nAW a80,v176\nAW a81,v177\nAW a82,v178\nAW a83,v179\n"
"AR v176,a84\nAR v177,a85\nAR v178,a86\nAR v179,a87\nVM v176,v176,v173\nVM v177,v177,v173\nVM v178,v178,v173\nVM v179,v179,v173\n"
"AW a84,v176\nAW a85,v177\nAW a86,v178\nAW a87,v179\nAR v176,a88\nAR v177,a89\nAR v178,a90\nAR v179,a91\n"
"VM v176,v176,v173\nVM v177,v177,v173\nVM v178,v178,v173\nVM v179,v179,v173\nAW a88,v176\nAW a89,v177\nAW a90,v178\nAW a91,v179\n"
"AR v176,a92\nAR v177,a93\nAR v178,a94\nAR v179,a95\nVM v176,v176,v173\nVM v177,v177,v173\nVM v178,v178,v173\nVM v179,v179,v173\n"
"AW a92,v176\nAW a93,v177\nAW a94,v178\nAW a95,v179\nAR v176,a112\nAR v177,a113\nAR v178,a114\nAR v179,a115\n"
"VM v176,v176,v173\nVM v177,v177,v173\nVM v178,v178,v173\nVM v179,v179,v173\nAW a112,v176\nAW a113,v177\nAW a114,v178\nAW a115,v179\n"
"AR v176,a116\nAR v177,a117\nAR v178,a118\nAR v179,a119\nVM v176,v176,v173\nVM v177,v177,v173\nVM v178,v178,v173\nVM v179,v179,v173\n"
"AW a116,v176\nAW a117,v177\nAW a118,v178\nAW a119,v179\nAR v176,a120\nAR v177,a121\nAR v178,a122\nAR v179,a123\n"
"VM v176,v176,v173\nVM v177,v177,v173\nVM v178,v178,v173\nVM v179,v179,v173\nAW a120,v176\nAW a121,v177\nAW a122,v178\nAW a123,v179\n"
"AR v176,a124\nAR v177,a125\nAR v178,a126\nAR v179,a127\nVM v176,v176,v173\nVM v177,v177,v173\nVM v178,v178,v173\nVM v179,v179,v173\n"
"AW a124,v176\nAW a125,v177\nAW a126,v178\nAW a127,v179\nEX v144,v48\nEX v145,v49\nEX v146,v50\nVA v152,v144,v145\n"
"CV v96,v144,v145\nEX v147,v51\nVA v152,v152,v146\nEX v144,v52\nVA v152,v152,v147\nCV v97,v146,v147\nEX v145,v53\nVA v152,v152,v144\n"
"EX v146,v54\nVA v152,v152,v145\nCV v98,v144,v145\nEX v147,v55\nVA v152,v152,v146\nEX v144,v56\nVA v152,v152,v147\nCV v99,v146,v147\n"
"EX v145,v57\nVA v152,v152,v144\nEX v146,v58\nVA v152,v152,v145\nCV v100,v144,v145\nEX v147,v59\nVA v152,v152,v146\nEX v144,v60\n"
"VA v152,v152,v147\nCV v101,v146,v147\nEX v145,v61\nVA v152,v152,v144\nEX v146,v62\nVA v152,v152,v145\nCV v102,v144,v145\nEX v147,v63\n"
"VA v152,v152,v146\nEX v144,v64\nVA v152,v152,v147\nCV v103,v146,v147\nEX v145,v65\nEX v146,v66\nVA v153,v144,v145\nCV v104,v144,v145\n"
"EX v147,v67\nVA v153,v153,v146\nEX v144,v68\nVA v153,v153,v147\nCV v105,v146,v147\nEX v145,v69\nVA v153,v153,v144\nEX v146,v70\n"
"VA v153,v153,v145\nCV v106,v144,v145\nEX v147,v71\nVA v153,v153,v146\nEX v144,v72\nVA v153,v153,v147\nCV v107,v146,v147\nEX v145,v73\n"
"VA v153,v153,v144\nEX v146,v74\nVA v153,v153,v145\nCV v108,v144,v145\nEX v147,v75\nVA v153,v153,v146\nEX v144,v76\nVA v153,v153,v147\n"
"CV v109,v146,v147\nEX v145,v77\nVA v153,v153,v144\nEX v146,v78\nVA v153,v153,v145\nCV v110,v144,v145\nEX v147,v79\nVA v153,v153,v146\n"
"s_nop 1\nVA v153,v153,v147\nCV v111,v146,v147\ns_nop 3\ns_setpc_b64 s[70:71]\nAT_end:\nSW vmcnt(0)\ns_barrier\n"
"s_cmp_eq_u32 s89,0\ns_cbranch_scc0 SG_at\ns_lshl_b32 s34,s88,3\nSA s34,s34,64004096\nv_mov_b32 v2,s90\nv_mov_b32 v3,s91\nVL v4,11,v200\nVU v4,s34,v4\n"
"v_mov_b32 v5,s93\nv_add_co_u32 v4,vcc,s92,v4\ns_nop 1\nv_addc_co_u32 v5,vcc,0,v5,vcc\ns_mov_b64 exec,0xff\nglobal_store_dwordx2 v[4:5],v[2:3],off sc1\ns_mov_b64 exec,-1\nSG_at:\n"
"SM s35,0\nPF_g:\nv_xor_b32 v242,s90,v242\nv_xor_b32 v243,s91,v243\nv_xor_b32 v244,s90,v244\nv_xor_b32 v245,s91,v245\nv_xor_b32 v246,s90,v246\nv_xor_b32 v247,s91,v247\n"
"v_xor_b32 v248,s90,v248\nv_xor_b32 v249,s91,v249\nv_or3_b32 v242,v242,v243,v244\nv_or3_b32 v245,v245,v246,v247\nv_or3_b32 v242,v242,v248,v249\nv_or_b32 v242,v242,v245\nv_cmp_ne_u32 vcc,0,v242\ns_nop 4\n"
"s_cbranch_vccz PF_g_d\nSA s35,s35,1\ns_cmp_ge_u32 s35,200000\ns_cbranch_scc1 PF_g_d\ns_sleep 1\nglobal_load_dwordx4 v[242:245],v[240:241],off sc1\nglobal_load_dwordx4 v[246:249],v[240:241],off offset:16 sc1\nSW vmcnt(0)\n"
"s_branch PF_g\nPF_g_d:\nSA m0,s82,0\ns_nop 0\nBL v224,s[36:39],s61 offen sc1 lds\nBL v225,s[36:39],s61 offen offset:1024 sc1 lds\nSA m0,s82,18432\ns_nop 0\n"
"BL v226,s[36:39],s61 offen sc1 lds\nBL v227,s[36:39],s61 offen offset:1024 sc1 lds\nSA s61,s61,2048\nSA m0,s83,0\ns_nop 0\nBL v228,s[40:43],s62 offen offset:0 lds\nBL v229,s[40:43],s62 offen offset:1024 lds\nBL v230,s[40:43],s62 offen offset:2048 lds\n"
"BL v231,s[40:43],s62 offen offset:3072 lds\nSA m0,m0,4096\ns_nop 0\nBL v232,s[40:43],s62 offen lds\ns_mul_i32 s70,s63,40960\nSA s62,s62,s70\nSA s62,s62,40960\nSA m0,s82,36864\n"
"s_nop 0\nBL v224,s[36:39],s61 offen sc1 lds\nBL v225,s[36:39],s61 offen offset:1024 sc1 lds\nSA m0,s82,55296\ns_nop 0\nBL v226,s[36:39],s61 offen sc1 lds\nBL v227,s[36:39],s61 offen offset:1024 sc1 lds\nSA s61,s61,2048\n"
"SA m0,s83,36864\ns_nop 0\nBL v228,s[40:43],s62 offen offset:0 lds\nBL v229,s[40:43],s62 offen offset:1024 lds\nBL v230,s[40:43],s62 offen offset:2048 lds\nBL v231,s[40:43],s62 offen offset:3072 lds\nSA m0,m0,4096\ns_nop 0\n"
"BL v232,s[40:43],s62 offen lds\nSA s62,s62,81920\nSA m0,s82,73728\ns_nop 0\nBL v224,s[36:39],s61 offen sc1 lds\nBL v225,s[36:39],s61 offen offset:1024 sc1 lds\nSA m0,s82,92160\ns_nop 0\n"
"BL v226,s[36:39],s61 offen sc1 lds\nBL v227,s[36:39],s61 offen offset:1024 sc1 lds\nSA s61,s61,2048\nSA m0,s83,73728\ns_nop 0\nBL v228,s[40:43],s62 offen offset:0 lds\nBL v229,s[40:43],s62 offen offset:1024 lds\nBL v230,s[40:43],s62 offen offset:2048 lds\n"
"BL v231,s[40:43],s62 offen offset:3072 lds\nSA m0,m0,4096\ns_nop 0\nBL v232,s[40:43],s62 offen lds\nSA s62,s62,81920\nSA m0,s82,110592\ns_nop 0\nBL v224,s[36:39],s61 offen sc1 lds\n"
"BL v225,s[36:39],s61 offen offset:1024 sc1 lds\nSA m0,s82,129024\ns_nop 0\nBL v226,s[36:39],s61 offen sc1 lds\nBL v227,s[36:39],s61 offen offset:1024 sc1 lds\nSA s61,s61,2048\nSA m0,s83,110592\ns_nop 0\n"
"BL v228,s[40:43],s62 offen offset:0 lds\nBL v229,s[40:43],s62 offen offset:1024 lds\nBL v230,s[40:43],s62 offen offset:2048 lds\nBL v231,s[40:43],s62 offen offset:3072 lds\nSA m0,m0,4096\ns_nop 0\nBL v232,s[40:43],s62 offen lds\nSA s62,s62,81920\n"
"SW vmcnt(27)\ns_barrier\nDT v[24:25],v208 offset:18432\nDT v[26:27],v208 offset:23552\nDR v[8:11],v204 offset:18432\nDR v[12:15],v204 offset:19456\nDR v[16:19],v204 offset:20480\nDR v[20:23],v204 offset:21504\n"
"DT v[28:29],v209 offset:18432\nDT v[30:31],v209 offset:23552\nDT v[32:33],v210 offset:18432\nDT v[34:35],v210 offset:23552\nDT v[36:37],v211 offset:18432\nDT v[38:39],v211 offset:23552\nDT v[40:41],v212 offset:18432\nDT v[42:43],v212 offset:23552\n"
"SW vmcnt(18) lgkmcnt(0)\ns_barrier\nM4 a[0:3],v[24:27],v[8:11],0\nDT v[60:61],v208 offset:36864\nSA m0,s82,0\nM4 a[4:7],v[24:27],v[12:15],0\nDT v[62:63],v208 offset:41984\nM4 a[8:11],v[24:27],v[16:19],0\n"
"DR v[44:47],v204 offset:36864\nBL v224,s[36:39],s61 offen sc1 lds\nM4 a[12:15],v[24:27],v[20:23],0\nDR v[48:51],v204 offset:37888\nM4 a[16:19],v[28:31],v[8:11],0\nDR v[52:55],v204 offset:38912\nM4 a[20:23],v[28:31],v[12:15],0\nDR v[56:59],v204 offset:39936\n"
"BL v225,s[36:39],s61 offen offset:1024 sc1 lds\nM4 a[24:27],v[28:31],v[16:19],0\nDT v[64:65],v209 offset:36864\nM4 a[28:31],v[28:31],v[20:23],0\nDT v[66:67],v209 offset:41984\nSA m0,s82,18432\nM4 a[32:35],v[32:35],v[8:11],0\nDT v[68:69],v210 offset:36864\n"
"M4 a[36:39],v[32:35],v[12:15],0\nDT v[70:71],v210 offset:41984\nM4 a[40:43],v[32:35],v[16:19],0\nDT v[72:73],v211 offset:36864\nBL v226,s[36:39],s61 offen sc1 lds\nM4 a[44:47],v[32:35],v[20:23],0\nDT v[74:75],v211 offset:41984\nM4 a[48:51],v[36:39],v[8:11],0\n"
"DT v[76:77],v212 offset:36864\nBL v227,s[36:39],s61 offen offset:1024 sc1 lds\nM4 a[52:55],v[36:39],v[12:15],0\nDT v[78:79],v212 offset:41984\nM4 a[56:59],v[36:39],v[16:19],0\nM4 a[60:63],v[36:39],v[20:23],0\nSA s61,s61,2048\nM4 a[64:67],v[40:43],v[8:11],0\n"
"M4 a[68:71],v[40:43],v[12:15],0\nSA m0,s83,0\nM4 a[72:75],v[40:43],v[16:19],0\nM4 a[76:79],v[40:43],v[20:23],0\nSW lgkmcnt(0)\nM4 a[0:3],v[60:63],v[44:47],a[0:3]\nDT v[24:25],v213 offset:0\nBL v228,s[40:43],s62 offen offset:0 lds\n"
"M4 a[4:7],v[60:63],v[48:51],a[4:7]\nDT v[26:27],v213 offset:5120\nM4 a[8:11],v[60:63],v[52:55],a[8:11]\nDR v[8:11],v205 offset:0\nBL v229,s[40:43],s62 offen offset:1024 lds\nM4 a[12:15],v[60:63],v[56:59],a[12:15]\nDR v[12:15],v205 offset:1024\nM4 a[16:19],v[64:67],v[44:47],a[16:19]\n"
"DR v[16:19],v205 offset:2048\nM4 a[20:23],v[64:67],v[48:51],a[20:23]\nDR v[20:23],v205 offset:3072\nBL v230,s[40:43],s62 offen offset:2048 lds\nM4 a[24:27],v[64:67],v[52:55],a[24:27]\nDT v[28:29],v214 offset:0\nM4 a[28:31],v[64:67],v[56:59],a[28:31]\nDT v[30:31],v214 offset:5120\n"
"M4 a[32:35],v[68:71],v[44:47],a[32:35]\nDT v[32:33],v215 offset:0\nBL v231,s[40:43],s62 offen offset:3072 lds\nM4 a[36:39],v[68:71],v[48:51],a[36:39]\nDT v[34:35],v215 offset:5120\nM4 a[40:43],v[68:71],v[52:55],a[40:43]\nDT v[36:37],v216 offset:0\nM4 a[44:47],v[68:71],v[56:59],a[44:47]\n"
"DT v[38:39],v216 offset:5120\nSA m0,m0,4096\nM4 a[48:51],v[72:75],v[44:47],a[48:51]\nDT v[40:41],v217 offset:0\nM4 a[52:55],v[72:75],v[48:51],a[52:55]\nDT v[42:43],v217 offset:5120\nM4 a[56:59],v[72:75],v[52:55],a[56:59]\nBL v232,s[40:43],s62 offen lds\n"
"M4 a[60:63],v[72:75],v[56:59],a[60:63]\nM4 a[64:67],v[76:79],v[44:47],a[64:67]\nM4 a[68:71],v[76:79],v[48:51],a[68:71]\nSA s62,s62,81920\nM4 a[72:75],v[76:79],v[52:55],a[72:75]\nM4 a[76:79],v[76:79],v[56:59],a[76:79]\nSW lgkmcnt(0)\nSW vmcnt(18)\n"
"s_barrier\nM4 a[0:3],v[24:27],v[8:11],a[0:3]\nDT v[60:61],v213 offset:18432\nSA m0,s82,36864\nM4 a[4:7],v[24:27],v[12:15],a[4:7]\nDT v[62:63],v213 offset:23552\nM4 a[8:11],v[24:27],v[16:19],a[8:11]\nDR v[44:47],v205 offset:18432\n"
"BL v224,s[36:39],s61 offen sc1 lds\nM4 a[12:15],v[24:27],v[20:23],a[12:15]\nDR v[48:51],v205 offset:19456\nM4 a[16:19],v[28:31],v[8:11],a[16:19]\nDR v[52:55],v205 offset:20480\nM4 a[20:23],v[28:31],v[12:15],a[20:23]\nDR v[56:59],v205 offset:21504\nBL v225,s[36:39],s61 offen offset:1024 sc1 lds\n"
"M4 a[24:27],v[28:31],v[16:19],a[24:27]\nDT v[64:65],v214 offset:18432\nM4 a[28:31],v[28:31],v[20:23],a[28:31]\nDT v[66:67],v214 offset:23552\nSA m0,s82,55296\nM4 a[32:35],v[32:35],v[8:11],a[32:35]\nDT v[68:69],v215 offset:18432\nM4 a[36:39],v[32:35],v[12:15],a[36:39]\n"
"DT v[70:71],v215 offset:23552\nM4 a[40:43],v[32:35],v[16:19],a[40:43]\nDT v[72:73],v216 offset:18432\nBL v226,s[36:39],s61 offen sc1 lds\nM4 a[44:47],v[32:35],v[20:23],a[44:47]\nDT v[74:75],v216 offset:23552\nM4 a[48:51],v[36:39],v[8:11],a[48:51]\nDT v[76:77],v217 offset:18432\n"
"BL v227,s[36:39],s61 offen offset:1024 sc1 lds\nM4 a[52:55],v[36:39],v[12:15],a[52:55]\nDT v[78:79],v217 offset:23552\nM4 a[56:59],v[36:39],v[16:19],a[56:59]\nM4 a[60:63],v[36:39],v[20:23],a[60:63]\nSA s61,s61,2048\nM4 a[64:67],v[40:43],v[8:11],a[64:67]\nM4 a[68:71],v[40:43],v[12:15],a[68:71]\n"
"SA m0,s83,36864\nM4 a[72:75],v[40:43],v[16:19],a[72:75]\nM4 a[76:79],v[40:43],v[20:23],a[76:79]\nSW lgkmcnt(0)\nM4 a[0:3],v[60:63],v[44:47],a[0:3]\nDT v[24:25],v213 offset:36864\nBL v228,s[40:43],s62 offen offset:0 lds\nM4 a[4:7],v[60:63],v[48:51],a[4:7]\n"
"DT v[26:27],v213 offset:41984\nM4 a[8:11],v[60:63],v[52:55],a[8:11]\nDR v[8:11],v205 offset:36864\nBL v229,s[40:43],s62 offen offset:1024 lds\nM4 a[12:15],v[60:63],v[56:59],a[12:15]\nDR v[12:15],v205 offset:37888\nM4 a[16:19],v[64:67],v[44:47],a[16:19]\nDR v[16:19],v205 offset:38912\n"
"M4 a[20:23],v[64:67],v[48:51],a[20:23]\nDR v[20:23],v205 offset:39936\nBL v230,s[40:43],s62 offen offset:2048 lds\nM4 a[24:27],v[64:67],v[52:55],a[24:27]\nDT v[28:29],v214 offset:36864\nM4 a[28:31],v[64:67],v[56:59],a[28:31]\nDT v[30:31],v214 offset:41984\nM4 a[32:35],v[68:71],v[44:47],a[32:35]\n"
"DT v[32:33],v215 offset:36864\nBL v231,s[40:43],s62 offen offset:3072 lds\nM4 a[36:39],v[68:71],v[48:51],a[36:39]\nDT v[34:35],v215 offset:41984\nM4 a[40:43],v[68:71],v[52:55],a[40:43]\nDT v[36:37],v216 offset:36864\nM4 a[44:47],v[68:71],v[56:59],a[44:47]\nDT v[38:39],v216 offset:41984\n"
"SA m0,m0,4096\nM4 a[48:51],v[72:75],v[44:47],a[48:51]\nDT v[40:41],v217 offset:36864\nM4 a[52:55],v[72:75],v[48:51],a[52:55]\nDT v[42:43],v217 offset:41984\nM4 a[56:59],v[72:75],v[52:55],a[56:59]\nBL v232,s[40:43],s62 offen lds\nM4 a[60:63],v[72:75],v[56:59],a[60:63]\n"
"M4 a[64:67],v[76:79],v[44:47],a[64:67]\nM4 a[68:71],v[76:79],v[48:51],a[68:71]\nSA s62,s62,81920\nM4 a[72:75],v[76:79],v[52:55],a[72:75]\nM4 a[76:79],v[76:79],v[56:59],a[76:79]\nSW lgkmcnt(0)\nSW vmcnt(18)\ns_barrier\n"
"M4 a[0:3],v[24:27],v[8:11],a[0:3]\nDT v[60:61],v218 offset:0\nSA m0,s82,73728\nM4 a[4:7],v[24:27],v[12:15],a[4:7]\nDT v[62:63],v218 offset:5120\nM4 a[8:11],v[24:27],v[16:19],a[8:11]\nDR v[44:47],v206 offset:0\nBL v224,s[36:39],s61 offen sc1 lds\n"
"M4 a[12:15],v[24:27],v[20:23],a[12:15]\nDR v[48:51],v206 offset:1024\nM4 a[16:19],v[28:31],v[8:11],a[16:19]\nDR v[52:55],v206 offset:2048\nM4 a[20:23],v[28:31],v[12:15],a[20:23]\nDR v[56:59],v206 offset:3072\nBL v225,s[36:39],s61 offen offset:1024 sc1 lds\nM4 a[24:27],v[28:31],v[16:19],a[24:27]\n"
"DT v[64:65],v219 offset:0\nM4 a[28:31],v[28:31],v[20:23],a[28:31]\nDT v[66:67],v219 offset:5120\nSA m0,s82,92160\nM4 a[32:35],v[32:35],v[8:11],a[32:35]\nDT v[68:69],v220 offset:0\nM4 a[36:39],v[32:35],v[12:15],a[36:39]\nDT v[70:71],v220 offset:5120\n"
"M4 a[40:43],v[32:35],v[16:19],a[40:43]\nDT v[72:73],v221 offset:0\nBL v226,s[36:39],s61 offen sc1 lds\nM4 a[44:47],v[32:35],v[20:23],a[44:47]\nDT v[74:75],v221 offset:5120\nM4 a[48:51],v[36:39],v[8:11],a[48:51]\nDT v[76:77],v222 offset:0\nBL v227,s[36:39],s61 offen offset:1024 sc1 lds\n"
"M4 a[52:55],v[36:39],v[12:15],a[52:55]\nDT v[78:79],v222 offset:5120\nM4 a[56:59],v[36:39],v[16:19],a[56:59]\nM4 a[60:63],v[36:39],v[20:23],a[60:63]\nSA s61,s61,2048\nM4 a[64:67],v[40:43],v[8:11],a[64:67]\nM4 a[68:71],v[40:43],v[12:15],a[68:71]\nSA m0,s83,73728\n"
"M4 a[72:75],v[40:43],v[16:19],a[72:75]\nM4 a[76:79],v[40:43],v[20:23],a[76:79]\nSW lgkmcnt(0)\nM4 a[0:3],v[60:63],v[44:47],a[0:3]\nDT v[24:25],v218 offset:18432\nBL v228,s[40:43],s62 offen offset:0 lds\nM4 a[4:7],v[60:63],v[48:51],a[4:7]\nDT v[26:27],v218 offset:23552\n"
"M4 a[8:11],v[60:63],v[52:55],a[8:11]\nDR v[8:11],v206 offset:18432\nBL v229,s[40:43],s62 offen offset:1024 lds\nM4 a[12:15],v[60:63],v[56:59],a[12:15]\nDR v[12:15],v206 offset:19456\nM4 a[16:19],v[64:67],v[44:47],a[16:19]\nDR v[16:19],v206 offset:20480\nM4 a[20:23],v[64:67],v[48:51],a[20:23]\n"
"DR v[20:23],v206 offset:21504\nBL v230,s[40:43],s62 offen offset:2048 lds\nM4 a[24:27],v[64:67],v[52:55],a[24:27]\nDT v[28:29],v219 offset:18432\nM4 a[28:31],v[64:67],v[56:59],a[28:31]\nDT v[30:31],v219 offset:23552\nM4 a[32:35],v[68:71],v[44:47],a[32:35]\nDT v[32:33],v220 offset:18432\n"
"BL v231,s[40:43],s62 offen offset:3072 lds\nM4 a[36:39],v[68:71],v[48:51],a[36:39]\nDT v[34:35],v220 offset:23552\nM4 a[40:43],v[68:71],v[52:55],a[40:43]\nDT v[36:37],v221 offset:18432\nM4 a[44:47],v[68:71],v[56:59],a[44:47]\nDT v[38:39],v221 offset:23552\nSA m0,m0,4096\n"
"M4 a[48:51],v[72:75],v[44:47],a[48:51]\nDT v[40:41],v222 offset:18432\nM4 a[52:55],v[72:75],v[48:51],a[52:55]\nDT v[42:43],v222 offset:23552\nM4 a[56:59],v[72:75],v[52:55],a[56:59]\nBL v232,s[40:43],s62 offen lds\nM4 a[60:63],v[72:75],v[56:59],a[60:63]\nM4 a[64:67],v[76:79],v[44:47],a[64:67]\n"
"M4 a[68:71],v[76:79],v[48:51],a[68:71]\nSA s62,s62,81920\nM4 a[72:75],v[76:79],v[52:55],a[72:75]\nM4 a[76:79],v[76:79],v[56:59],a[76:79]\nSW lgkmcnt(0)\nSW vmcnt(18)\ns_barrier\nM4 a[0:3],v[24:27],v[8:11],a[0:3]\n"
"DT v[60:61],v208 offset:0\ns_cmp_eq_u32 s64,7\ns_cbranch_scc0 P2_ns7\ns_and_b32 s34,s88,7\ns_lshl_b32 s34,s34,1\ns_lshr_b32 s35,s88,7\nSA s34,s34,s35\ns_sub_u32 s35,15,s34\n"
"s_min_u32 s34,s34,s35\ns_lshl_b32 s34,s34,3\ns_bfe_u32 s35,s88,0x20003\ns_lshl_b32 s35,s35,3\nVN v234,15,v200\nv_min_u32 v234,8,v234\nVU v234,s35,v234\nv_min_u32 v234,31,v234\n"
"VN v235,3,v234\nVL v235,6,v235\nv_lshrrev_b32 v234,2,v234\nVU v234,v234,v235\nVU v234,s34,v234\nVL v234,3,v234\nVU v234,64004096,v234\nv_mov_b32 v235,s93\n"
"v_add_co_u32 v234,vcc,s92,v234\ns_nop 1\nv_addc_co_u32 v235,vcc,0,v235,vcc\nglobal_load_dwordx2 v[236:237],v[234:235],off sc1\nSW vmcnt(0)\nSM s35,0\nP2_aw7:\nv_xor_b32 v236,s90,v236\n"
"v_xor_b32 v237,s91,v237\nv_or_b32 v236,v236,v237\nv_cmp_ne_u32 vcc,0,v236\ns_nop 4\ns_cbranch_vccz P2_awd7\nSA s35,s35,1\ns_cmp_ge_u32 s35,200000\ns_cbranch_scc1 P2_awd7\n"
"global_load_dwordx2 v[236:237],v[234:235],off sc1\nSW vmcnt(0)\ns_branch P2_aw7\nP2_awd7:\nSM s40,s84\nSM s41,s85\nSM s42,s86\nSM s43,s87\n"
"SM s61,s75\ns_mul_i32 s62,s63,40960\nP2_ns7:\nM4 a[4:7],v[24:27],v[12:15],a[4:7]\nDT v[62:63],v208 offset:5120\nM4 a[8:11],v[24:27],v[16:19],a[8:11]\nDR v[44:47],v204 offset:0\nSA m0,s82,110592\n"
"M4 a[12:15],v[24:27],v[20:23],a[12:15]\nDR v[48:51],v204 offset:1024\nM4 a[16:19],v[28:31],v[8:11],a[16:19]\nDR v[52:55],v204 offset:2048\nM4 a[20:23],v[28:31],v[12:15],a[20:23]\nDR v[56:59],v204 offset:3072\nBL v224,s[36:39],s61 offen sc1 lds\nM4 a[24:27],v[28:31],v[16:19],a[24:27]\n"
"DT v[64:65],v209 offset:0\nM4 a[28:31],v[28:31],v[20:23],a[28:31]\nDT v[66:67],v209 offset:5120\nBL v225,s[36:39],s61 offen offset:1024 sc1 lds\nM4 a[32:35],v[32:35],v[8:11],a[32:35]\nDT v[68:69],v210 offset:0\nM4 a[36:39],v[32:35],v[12:15],a[36:39]\nDT v[70:71],v210 offset:5120\n"
"M4 a[40:43],v[32:35],v[16:19],a[40:43]\nDT v[72:73],v211 offset:0\nSA m0,s82,129024\nM4 a[44:47],v[32:35],v[20:23],a[44:47]\nDT v[74:75],v211 offset:5120\nM4 a[48:51],v[36:39],v[8:11],a[48:51]\nDT v[76:77],v212 offset:0\nBL v226,s[36:39],s61 offen sc1 lds\n"
"M4 a[52:55],v[36:39],v[12:15],a[52:55]\nDT v[78:79],v212 offset:5120\nM4 a[56:59],v[36:39],v[16:19],a[56:59]\nM4 a[60:63],v[36:39],v[20:23],a[60:63]\nBL v227,s[36:39],s61 offen offset:1024 sc1 lds\nM4 a[64:67],v[40:43],v[8:11],a[64:67]\nM4 a[68:71],v[40:43],v[12:15],a[68:71]\nSA s61,s61,2048\n"
"M4 a[72:75],v[40:43],v[16:19],a[72:75]\nM4 a[76:79],v[40:43],v[20:23],a[76:79]\nSW lgkmcnt(0)\nM4 a[0:3],v[60:63],v[44:47],a[0:3]\nDT v[24:25],v208 offset:18432\nSA m0,s83,110592\nM4 a[4:7],v[60:63],v[48:51],a[4:7]\nDT v[26:27],v208 offset:23552\n"
"M4 a[8:11],v[60:63],v[52:55],a[8:11]\nDR v[8:11],v204 offset:18432\nBL v228,s[40:43],s62 offen offset:0 lds\nM4 a[12:15],v[60:63],v[56:59],a[12:15]\nDR v[12:15],v204 offset:19456\nM4 a[16:19],v[64:67],v[44:47],a[16:19]\nDR v[16:19],v204 offset:20480\nM4 a[20:23],v[64:67],v[48:51],a[20:23]\n"
"DR v[20:23],v204 offset:21504\nBL v229,s[40:43],s62 offen offset:1024 lds\nM4 a[24:27],v[64:67],v[52:55],a[24:27]\nDT v[28:29],v209 offset:18432\nM4 a[28:31],v[64:67],v[56:59],a[28:31]\nDT v[30:31],v209 offset:23552\nBL v230,s[40:43],s62 offen offset:2048 lds\nM4 a[32:35],v[68:71],v[44:47],a[32:35]\n"
"DT v[32:33],v210 offset:18432\nM4 a[36:39],v[68:71],v[48:51],a[36:39]\nDT v[34:35],v210 offset:23552\nM4 a[40:43],v[68:71],v[52:55],a[40:43]\nDT v[36:37],v211 offset:18432\nBL v231,s[40:43],s62 offen offset:3072 lds\nM4 a[44:47],v[68:71],v[56:59],a[44:47]\nDT v[38:39],v211 offset:23552\n"
"M4 a[48:51],v[72:75],v[44:47],a[48:51]\nDT v[40:41],v212 offset:18432\nSA m0,m0,4096\nM4 a[52:55],v[72:75],v[48:51],a[52:55]\nDT v[42:43],v212 offset:23552\nM4 a[56:59],v[72:75],v[52:55],a[56:59]\nM4 a[60:63],v[72:75],v[56:59],a[60:63]\nBL v232,s[40:43],s62 offen lds\n"
"M4 a[64:67],v[76:79],v[44:47],a[64:67]\nM4 a[68:71],v[76:79],v[48:51],a[68:71]\nSA s62,s62,81920\nM4 a[72:75],v[76:79],v[52:55],a[72:75]\nM4 a[76:79],v[76:79],v[56:59],a[76:79]\nSW lgkmcnt(0)\nSW vmcnt(20)\ns_barrier\n"
"M4 a[0:3],v[24:27],v[8:11],a[0:3]\nDT v[60:61],v208 offset:36864\ns_cmp_eq_u32 s64,8\ns_cbranch_scc0 P2_ns8\ns_and_b32 s34,s88,7\ns_lshl_b32 s34,s34,1\ns_lshr_b32 s35,s88,7\nSA s34,s34,s35\n"
"s_sub_u32 s35,15,s34\ns_min_u32 s34,s34,s35\ns_lshl_b32 s34,s34,3\ns_bfe_u32 s35,s88,0x20003\ns_lshl_b32 s35,s35,3\nVN v234,15,v200\nv_min_u32 v234,8,v234\nVU v234,s35,v234\n"
"v_min_u32 v234,31,v234\nVN v235,3,v234\nVL v235,6,v235\nv_lshrrev_b32 v234,2,v234\nVU v234,v234,v235\nVU v234,s34,v234\nVL v234,3,v234\nVU v234,64004096,v234\n"
"v_mov_b32 v235,s93\nv_add_co_u32 v234,vcc,s92,v234\ns_nop 1\nv_addc_co_u32 v235,vcc,0,v235,vcc\nglobal_load_dwordx2 v[236:237],v[234:235],off sc1\nSW vmcnt(0)\nSM s35,0\nP2_aw8:\n"
"v_xor_b32 v236,s90,v236\nv_xor_b32 v237,s91,v237\nv_or_b32 v236,v236,v237\nv_cmp_ne_u32 vcc,0,v236\ns_nop 4\ns_cbranch_vccz P2_awd8\nSA s35,s35,1\ns_cmp_ge_u32 s35,200000\n"
"s_cbranch_scc1 P2_awd8\nglobal_load_dwordx2 v[236:237],v[234:235],off sc1\nSW vmcnt(0)\ns_branch P2_aw8\nP2_awd8:\nSM s40,s84\nSM s41,s85\nSM s42,s86\n"
"SM s43,s87\nSM s61,s75\ns_mul_i32 s62,s63,40960\nP2_ns8:\nM4 a[4:7],v[24:27],v[12:15],a[4:7]\nDT v[62:63],v208 offset:41984\nM4 a[8:11],v[24:27],v[16:19],a[8:11]\nDR v[44:47],v204 offset:36864\n"
"SA m0,s82,0\nM4 a[12:15],v[24:27],v[20:23],a[12:15]\nDR v[48:51],v204 offset:37888\nM4 a[16:19],v[28:31],v[8:11],a[16:19]\nDR v[52:55],v204 offset:38912\nM4 a[20:23],v[28:31],v[12:15],a[20:23]\nDR v[56:59],v204 offset:39936\nBL v224,s[36:39],s61 offen sc1 lds\n"
"M4 a[24:27],v[28:31],v[16:19],a[24:27]\nDT v[64:65],v209 offset:36864\nM4 a[28:31],v[28:31],v[20:23],a[28:31]\nDT v[66:67],v209 offset:41984\nBL v225,s[36:39],s61 offen offset:1024 sc1 lds\nM4 a[32:35],v[32:35],v[8:11],a[32:35]\nDT v[68:69],v210 offset:36864\nM4 a[36:39],v[32:35],v[12:15],a[36:39]\n"
"DT v[70:71],v210 offset:41984\nM4 a[40:43],v[32:35],v[16:19],a[40:43]\nDT v[72:73],v211 offset:36864\nSA m0,s82,18432\nM4 a[44:47],v[32:35],v[20:23],a[44:47]\nDT v[74:75],v211 offset:41984\nM4 a[48:51],v[36:39],v[8:11],a[48:51]\nDT v[76:77],v212 offset:36864\n"
"BL v226,s[36:39],s61 offen sc1 lds\nM4 a[52:55],v[36:39],v[12:15],a[52:55]\nDT v[78:79],v212 offset:41984\nM4 a[56:59],v[36:39],v[16:19],a[56:59]\nM4 a[60:63],v[36:39],v[20:23],a[60:63]\nBL v227,s[36:39],s61 offen offset:1024 sc1 lds\nM4 a[64:67],v[40:43],v[8:11],a[64:67]\nM4 a[68:71],v[40:43],v[12:15],a[68:71]\n"
"SA s61,s61,2048\nM4 a[72:75],v[40:43],v[16:19],a[72:75]\nM4 a[76:79],v[40:43],v[20:23],a[76:79]\nSW lgkmcnt(0)\nM4 a[0:3],v[60:63],v[44:47],a[0:3]\nDT v[24:25],v213 offset:0\nSA m0,s83,0\nM4 a[4:7],v[60:63],v[48:51],a[4:7]\n"
"DT v[26:27],v213 offset:5120\nM4 a[8:11],v[60:63],v[52:55],a[8:11]\nDR v[8:11],v205 offset:0\nBL v228,s[40:43],s62 offen offset:0 lds\nM4 a[12:15],v[60:63],v[56:59],a[12:15]\nDR v[12:15],v205 offset:1024\nM4 a[16:19],v[64:67],v[44:47],a[16:19]\nDR v[16:19],v205 offset:2048\n"
"M4 a[20:23],v[64:67],v[48:51],a[20:23]\nDR v[20:23],v205 offset:3072\nBL v229,s[40:43],s62 offen offset:1024 lds\nM4 a[24:27],v[64:67],v[52:55],a[24:27]\nDT v[28:29],v214 offset:0\nM4 a[28:31],v[64:67],v[56:59],a[28:31]\nDT v[30:31],v214 offset:5120\nBL v230,s[40:43],s62 offen offset:2048 lds\n"
"M4 a[32:35],v[68:71],v[44:47],a[32:35]\nDT v[32:33],v215 offset:0\nM4 a[36:39],v[68:71],v[48:51],a[36:39]\nDT v[34:35],v215 offset:5120\nM4 a[40:43],v[68:71],v[52:55],a[40:43]\nDT v[36:37],v216 offset:0\nBL v231,s[40:43],s62 offen offset:3072 lds\nM4 a[44:47],v[68:71],v[56:59],a[44:47]\n"
"DT v[38:39],v216 offset:5120\nM4 a[48:51],v[72:75],v[44:47],a[48:51]\nDT v[40:41],v217 offset:0\nSA m0,m0,4096\nM4 a[52:55],v[72:75],v[48:51],a[52:55]\nDT v[42:43],v217 offset:5120\nM4 a[56:59],v[72:75],v[52:55],a[56:59]\nM4 a[60:63],v[72:75],v[56:59],a[60:63]\n"
"BL v232,s[40:43],s62 offen lds\nM4 a[64:67],v[76:79],v[44:47],a[64:67]\nM4 a[68:71],v[76:79],v[48:51],a[68:71]\nSA s62,s62,81920\nM4 a[72:75],v[76:79],v[52:55],a[72:75]\nM4 a[76:79],v[76:79],v[56:59],a[76:79]\nSW lgkmcnt(0)\nSW vmcnt(22)\n"
"s_barrier\nM4 a[0:3],v[24:27],v[8:11],a[0:3]\nDT v[60:61],v213 offset:18432\nSA m0,s82,36864\nM4 a[4:7],v[24:27],v[12:15],a[4:7]\nDT v[62:63],v213 offset:23552\nM4 a[8:11],v[24:27],v[16:19],a[8:11]\nDR v[44:47],v205 offset:18432\n"
"BL v224,s[36:39],s61 offen sc1 lds\nM4 a[12:15],v[24:27],v[20:23],a[12:15]\nDR v[48:51],v205 offset:19456\nM4 a[16:19],v[28:31],v[8:11],a[16:19]\nDR v[52:55],v205 offset:20480\nM4 a[20:23],v[28:31],v[12:15],a[20:23]\nDR v[56:59],v205 offset:21504\nBL v225,s[36:39],s61 offen offset:1024 sc1 lds\n"
"M4 a[24:27],v[28:31],v[16:19],a[24:27]\nDT v[64:65],v214 offset:18432\nM4 a[28:31],v[28:31],v[20:23],a[28:31]\nDT v[66:67],v214 offset:23552\nSA m0,s82,55296\nM4 a[32:35],v[32:35],v[8:11],a[32:35]\nDT v[68:69],v215 offset:18432\nM4 a[36:39],v[32:35],v[12:15],a[36:39]\n"
"DT v[70:71],v215 offset:23552\nM4 a[40:43],v[32:35],v[16:19],a[40:43]\nDT v[72:73],v216 offset:18432\nBL v226,s[36:39],s61 offen sc1 lds\nM4 a[44:47],v[32:35],v[20:23],a[44:47]\nDT v[74:75],v216 offset:23552\nM4 a[48:51],v[36:39],v[8:11],a[48:51]\nDT v[76:77],v217 offset:18432\n"
"BL v227,s[36:39],s61 offen offset:1024 sc1 lds\nM4 a[52:55],v[36:39],v[12:15],a[52:55]\nDT v[78:79],v217 offset:23552\nM4 a[56:59],v[36:39],v[16:19],a[56:59]\nM4 a[60:63],v[36:39],v[20:23],a[60:63]\nSA s61,s61,2048\nM4 a[64:67],v[40:43],v[8:11],a[64:67]\nM4 a[68:71],v[40:43],v[12:15],a[68:71]\n"
"SA m0,s83,36864\nM4 a[72:75],v[40:43],v[16:19],a[72:75]\nM4 a[76:79],v[40:43],v[20:23],a[76:79]\nSW lgkmcnt(0)\nM4 a[0:3],v[60:63],v[44:47],a[0:3]\nDT v[24:25],v213 offset:36864\nBL v228,s[40:43],s62 offen offset:0 lds\nM4 a[4:7],v[60:63],v[48:51],a[4:7]\n"
"DT v[26:27],v213 offset:41984\nM4 a[8:11],v[60:63],v[52:55],a[8:11]\nDR v[8:11],v205 offset:36864\nBL v229,s[40:43],s62 offen offset:1024 lds\nM4 a[12:15],v[60:63],v[56:59],a[12:15]\nDR v[12:15],v205 offset:37888\nM4 a[16:19],v[64:67],v[44:47],a[16:19]\nDR v[16:19],v205 offset:38912\n"
"M4 a[20:23],v[64:67],v[48:51],a[20:23]\nDR v[20:23],v205 offset:39936\nBL v230,s[40:43],s62 offen offset:2048 lds\nM4 a[24:27],v[64:67],v[52:55],a[24:27]\nDT v[28:29],v214 offset:36864\nM4 a[28:31],v[64:67],v[56:59],a[28:31]\nDT v[30:31],v214 offset:41984\nM4 a[32:35],v[68:71],v[44:47],a[32:35]\n"
"DT v[32:33],v215 offset:36864\nBL v231,s[40:43],s62 offen offset:3072 lds\nM4 a[36:39],v[68:71],v[48:51],a[36:39]\nDT v[34:35],v215 offset:41984\nM4 a[40:43],v[68:71],v[52:55],a[40:43]\nDT v[36:37],v216 offset:36864\nM4 a[44:47],v[68:71],v[56:59],a[44:47]\nDT v[38:39],v216 offset:41984\n"
"SA m0,m0,4096\nM4 a[48:51],v[72:75],v[44:47],a[48:51]\nDT v[40:41],v217 offset:36864\nM4 a[52:55],v[72:75],v[48:51],a[52:55]\nDT v[42:43],v217 offset:41984\nM4 a[56:59],v[72:75],v[52:55],a[56:59]\nBL v232,s[40:43],s62 offen lds\nM4 a[60:63],v[72:75],v[56:59],a[60:63]\n"
"M4 a[64:67],v[76:79],v[44:47],a[64:67]\nM4 a[68:71],v[76:79],v[48:51],a[68:71]\nSA s62,s62,81920\nM4 a[72:75],v[76:79],v[52:55],a[72:75]\nM4 a[76:79],v[76:79],v[56:59],a[76:79]\nSW lgkmcnt(0)\nSW vmcnt(20)\ns_barrier\n"
"M4 a[0:3],v[24:27],v[8:11],a[0:3]\nDT v[60:61],v218 offset:0\nSA m0,s82,73728\nM4 a[4:7],v[24:27],v[12:15],a[4:7]\nDT v[62:63],v218 offset:5120\nM4 a[8:11],v[24:27],v[16:19],a[8:11]\nDR v[44:47],v206 offset:0\nBL v224,s[36:39],s61 offen sc1 lds\n"
"M4 a[12:15],v[24:27],v[20:23],a[12:15]\nDR v[48:51],v206 offset:1024\nM4 a[16:19],v[28:31],v[8:11],a[16:19]\nDR v[52:55],v206 offset:2048\nM4 a[20:23],v[28:31],v[12:15],a[20:23]\nDR v[56:59],v206 offset:3072\nBL v225,s[36:39],s61 offen offset:1024 sc1 lds\nM4 a[24:27],v[28:31],v[16:19],a[24:27]\n"
"DT v[64:65],v219 offset:0\nM4 a[28:31],v[28:31],v[20:23],a[28:31]\nDT v[66:67],v219 offset:5120\nSA m0,s82,92160\nM4 a[32:35],v[32:35],v[8:11],a[32:35]\nDT v[68:69],v220 offset:0\nM4 a[36:39],v[32:35],v[12:15],a[36:39]\nDT v[70:71],v220 offset:5120\n"
"M4 a[40:43],v[32:35],v[16:19],a[40:43]\nDT v[72:73],v221 offset:0\nBL v226,s[36:39],s61 offen sc1 lds\nM4 a[44:47],v[32:35],v[20:23],a[44:47]\nDT v[74:75],v221 offset:5120\nM4 a[48:51],v[36:39],v[8:11],a[48:51]\nDT v[76:77],v222 offset:0\nBL v227,s[36:39],s61 offen offset:1024 sc1 lds\n"
"M4 a[52:55],v[36:39],v[12:15],a[52:55]\nDT v[78:79],v222 offset:5120\nM4 a[56:59],v[36:39],v[16:19],a[56:59]\nM4 a[60:63],v[36:39],v[20:23],a[60:63]\nSA s61,s61,2048\nM4 a[64:67],v[40:43],v[8:11],a[64:67]\nM4 a[68:71],v[40:43],v[12:15],a[68:71]\nSA m0,s83,73728\n"
"M4 a[72:75],v[40:43],v[16:19],a[72:75]\nM4 a[76:79],v[40:43],v[20:23],a[76:79]\nSW lgkmcnt(0)\nM4 a[0:3],v[60:63],v[44:47],a[0:3]\nDT v[24:25],v218 offset:18432\nBL v228,s[40:43],s62 offen offset:0 lds\nM4 a[4:7],v[60:63],v[48:51],a[4:7]\nDT v[26:27],v218 offset:23552\n"
"M4 a[8:11],v[60:63],v[52:55],a[8:11]\nDR v[8:11],v206 offset:18432\nBL v229,s[40:43],s62 offen offset:1024 lds\nM4 a[12:15],v[60:63],v[56:59],a[12:15]\nDR v[12:15],v206 offset:19456\nM4 a[16:19],v[64:67],v[44:47],a[16:19]\nDR v[16:19],v206 offset:20480\nM4 a[20:23],v[64:67],v[48:51],a[20:23]\n"
"DR v[20:23],v206 offset:21504\nBL v230,s[40:43],s62 offen offset:2048 lds\nM4 a[24:27],v[64:67],v[52:55],a[24:27]\nDT v[28:29],v219 offset:18432\nM4 a[28:31],v[64:67],v[56:59],a[28:31]\nDT v[30:31],v219 offset:23552\nM4 a[32:35],v[68:71],v[44:47],a[32:35]\nDT v[32:33],v220 offset:18432\n"
"BL v231,s[40:43],s62 offen offset:3072 lds\nM4 a[36:39],v[68:71],v[48:51],a[36:39]\nDT v[34:35],v220 offset:23552\nM4 a[40:43],v[68:71],v[52:55],a[40:43]\nDT v[36:37],v221 offset:18432\nM4 a[44:47],v[68:71],v[56:59],a[44:47]\nDT v[38:39],v221 offset:23552\nSA m0,m0,4096\n"
"M4 a[48:51],v[72:75],v[44:47],a[48:51]\nDT v[40:41],v222 offset:18432\nM4 a[52:55],v[72:75],v[48:51],a[52:55]\nDT v[42:43],v222 offset:23552\nM4 a[56:59],v[72:75],v[52:55],a[56:59]\nBL v232,s[40:43],s62 offen lds\nM4 a[60:63],v[72:75],v[56:59],a[60:63]\nM4 a[64:67],v[76:79],v[44:47],a[64:67]\n"
"M4 a[68:71],v[76:79],v[48:51],a[68:71]\nSA s62,s62,81920\nM4 a[72:75],v[76:79],v[52:55],a[72:75]\nM4 a[76:79],v[76:79],v[56:59],a[76:79]\nSW lgkmcnt(0)\nSW vmcnt(18)\ns_barrier\nM4 a[0:3],v[24:27],v[8:11],a[0:3]\n"
"DT v[60:61],v208 offset:0\nSA m0,s82,110592\nM4 a[4:7],v[24:27],v[12:15],a[4:7]\nDT v[62:63],v208 offset:5120\nM4 a[8:11],v[24:27],v[16:19],a[8:11]\nDR v[44:47],v204 offset:0\nBL v224,s[36:39],s61 offen sc1 lds\nM4 a[12:15],v[24:27],v[20:23],a[12:15]\n"
"DR v[48:51],v204 offset:1024\nM4 a[16:19],v[28:31],v[8:11],a[16:19]\nDR v[52:55],v204 offset:2048\nM4 a[20:23],v[28:31],v[12:15],a[20:23]\nDR v[56:59],v204 offset:3072\nBL v225,s[36:39],s61 offen offset:1024 sc1 lds\nM4 a[24:27],v[28:31],v[16:19],a[24:27]\nDT v[64:65],v209 offset:0\n"
"M4 a[28:31],v[28:31],v[20:23],a[28:31]\nDT v[66:67],v209 offset:5120\nSA m0,s82,129024\nM4 a[32:35],v[32:35],v[8:11],a[32:35]\nDT v[68:69],v210 offset:0\nM4 a[36:39],v[32:35],v[12:15],a[36:39]\nDT v[70:71],v210 offset:5120\nM4 a[40:43],v[32:35],v[16:19],a[40:43]\n"
"DT v[72:73],v211 offset:0\nBL v226,s[36:39],s61 offen sc1 lds\nM4 a[44:47],v[32:35],v[20:23],a[44:47]\nDT v[74:75],v211 offset:5120\nM4 a[48:51],v[36:39],v[8:11],a[48:51]\nDT v[76:77],v212 offset:0\nBL v227,s[36:39],s61 offen offset:1024 sc1 lds\nM4 a[52:55],v[36:39],v[12:15],a[52:55]\n"
"DT v[78:79],v212 offset:5120\nM4 a[56:59],v[36:39],v[16:19],a[56:59]\nM4 a[60:63],v[36:39],v[20:23],a[60:63]\nSA s61,s61,2048\nM4 a[64:67],v[40:43],v[8:11],a[64:67]\nM4 a[68:71],v[40:43],v[12:15],a[68:71]\nSA m0,s83,110592\nM4 a[72:75],v[40:43],v[16:19],a[72:75]\n"
"M4 a[76:79],v[40:43],v[20:23],a[76:79]\nSW lgkmcnt(0)\nM4 a[0:3],v[60:63],v[44:47],a[0:3]\nDT v[24:25],v208 offset:18432\nBL v228,s[40:43],s62 offen offset:0 lds\nM4 a[4:7],v[60:63],v[48:51],a[4:7]\nDT v[26:27],v208 offset:23552\nM4 a[8:11],v[60:63],v[52:55],a[8:11]\n"
"DR v[8:11],v204 offset:18432\nBL v229,s[40:43],s62 offen offset:1024 lds\nM4 a[12:15],v[60:63],v[56:59],a[12:15]\nDR v[12:15],v204 offset:19456\nM4 a[16:19],v[64:67],v[44:47],a[16:19]\nDR v[16:19],v204 offset:20480\nM4 a[20:23],v[64:67],v[48:51],a[20:23]\nDR v[20:23],v204 offset:21504\n"
"BL v230,s[40:43],s62 offen offset:2048 lds\nM4 a[24:27],v[64:67],v[52:55],a[24:27]\nDT v[28:29],v209 offset:18432\nM4 a[28:31],v[64:67],v[56:59],a[28:31]\nDT v[30:31],v209 offset:23552\nM4 a[32:35],v[68:71],v[44:47],a[32:35]\nDT v[32:33],v210 offset:18432\nBL v231,s[40:43],s62 offen offset:3072 lds\n"
"M4 a[36:39],v[68:71],v[48:51],a[36:39]\nDT v[34:35],v210 offset:23552\nM4 a[40:43],v[68:71],v[52:55],a[40:43]\nDT v[36:37],v211 offset:18432\nM4 a[44:47],v[68:71],v[56:59],a[44:47]\nDT v[38:39],v211 offset:23552\nSA m0,m0,4096\nM4 a[48:51],v[72:75],v[44:47],a[48:51]\n"
"DT v[40:41],v212 offset:18432\nM4 a[52:55],v[72:75],v[48:51],a[52:55]\nDT v[42:43],v212 offset:23552\nM4 a[56:59],v[72:75],v[52:55],a[56:59]\nBL v232,s[40:43],s62 offen lds\nM4 a[60:63],v[72:75],v[56:59],a[60:63]\nM4 a[64:67],v[76:79],v[44:47],a[64:67]\nM4 a[68:71],v[76:79],v[48:51],a[68:71]\n"
"SA s62,s62,81920\nM4 a[72:75],v[76:79],v[52:55],a[72:75]\nM4 a[76:79],v[76:79],v[56:59],a[76:79]\nSW lgkmcnt(0)\nSW vmcnt(18)\ns_barrier\nM4 a[0:3],v[24:27],v[8:11],a[0:3]\nDT v[60:61],v208 offset:36864\n"
"SA m0,s82,0\nM4 a[4:7],v[24:27],v[12:15],a[4:7]\nDT v[62:63],v208 offset:41984\nM4 a[8:11],v[24:27],v[16:19],a[8:11]\nDR v[44:47],v204 offset:36864\nBL v224,s[36:39],s61 offen sc1 lds\nM4 a[12:15],v[24:27],v[20:23],a[12:15]\nDR v[48:51],v204 offset:37888\n"
"M4 a[16:19],v[28:31],v[8:11],a[16:19]\nDR v[52:55],v204 offset:38912\nM4 a[20:23],v[28:31],v[12:15],a[20:23]\nDR v[56:59],v204 offset:39936\nBL v225,s[36:39],s61 offen offset:1024 sc1 lds\nM4 a[24:27],v[28:31],v[16:19],a[24:27]\nDT v[64:65],v209 offset:36864\nM4 a[28:31],v[28:31],v[20:23],a[28:31]\n"
"DT v[66:67],v209 offset:41984\nSA m0,s82,18432\nM4 a[32:35],v[32:35],v[8:11],a[32:35]\nDT v[68:69],v210 offset:36864\nM4 a[36:39],v[32:35],v[12:15],a[36:39]\nDT v[70:71],v210 offset:41984\nM4 a[40:43],v[32:35],v[16:19],a[40:43]\nDT v[72:73],v211 offset:36864\n"
"BL v226,s[36:39],s61 offen sc1 lds\nM4 a[44:47],v[32:35],v[20:23],a[44:47]\nDT v[74:75],v211 offset:41984\nM4 a[48:51],v[36:39],v[8:11],a[48:51]\nDT v[76:77],v212 offset:36864\nBL v227,s[36:39],s61 offen offset:1024 sc1 lds\nM4 a[52:55],v[36:39],v[12:15],a[52:55]\nDT v[78:79],v212 offset:41984\n"
"M4 a[56:59],v[36:39],v[16:19],a[56:59]\nM4 a[60:63],v[36:39],v[20:23],a[60:63]\nSA s61,s61,2048\nM4 a[64:67],v[40:43],v[8:11],a[64:67]\nM4 a[68:71],v[40:43],v[12:15],a[68:71]\nSA m0,s83,0\nM4 a[72:75],v[40:43],v[16:19],a[72:75]\nM4 a[76:79],v[40:43],v[20:23],a[76:79]\n"
"SW lgkmcnt(0)\nM4 a[0:3],v[60:63],v[44:47],a[0:3]\nDT v[24:25],v213 offset:0\nBL v228,s[40:43],s62 offen offset:0 lds\nM4 a[4:7],v[60:63],v[48:51],a[4:7]\nDT v[26:27],v213 offset:5120\nM4 a[8:11],v[60:63],v[52:55],a[8:11]\nDR v[8:11],v205 offset:0\n"
"BL v229,s[40:43],s62 offen offset:1024 lds\nM4 a[12:15],v[60:63],v[56:59],a[12:15]\nDR v[12:15],v205 offset:1024\nM4 a[16:19],v[64:67],v[44:47],a[16:19]\nDR v[16:19],v205 offset:2048\nM4 a[20:23],v[64:67],v[48:51],a[20:23]\nDR v[20:23],v205 offset:3072\nBL v230,s[40:43],s62 offen offset:2048 lds\n"
"M4 a[24:27],v[64:67],v[52:55],a[24:27]\nDT v[28:29],v214 offset:0\nM4 a[28:31],v[64:67],v[56:59],a[28:31]\nDT v[30:31],v214 offset:5120\nM4 a[32:35],v[68:71],v[44:47],a[32:35]\nDT v[32:33],v215 offset:0\nBL v231,s[40:43],s62 offen offset:3072 lds\nM4 a[36:39],v[68:71],v[48:51],a[36:39]\n"
"DT v[34:35],v215 offset:5120\nM4 a[40:43],v[68:71],v[52:55],a[40:43]\nDT v[36:37],v216 offset:0\nM4 a[44:47],v[68:71],v[56:59],a[44:47]\nDT v[38:39],v216 offset:5120\nSA m0,m0,4096\nM4 a[48:51],v[72:75],v[44:47],a[48:51]\nDT v[40:41],v217 offset:0\n"
"M4 a[52:55],v[72:75],v[48:51],a[52:55]\nDT v[42:43],v217 offset:5120\nM4 a[56:59],v[72:75],v[52:55],a[56:59]\nBL v232,s[40:43],s62 offen lds\nM4 a[60:63],v[72:75],v[56:59],a[60:63]\nM4 a[64:67],v[76:79],v[44:47],a[64:67]\nM4 a[68:71],v[76:79],v[48:51],a[68:71]\nSA s62,s62,81920\n"
"M4 a[72:75],v[76:79],v[52:55],a[72:75]\nM4 a[76:79],v[76:79],v[56:59],a[76:79]\nSW lgkmcnt(0)\nSW vmcnt(18)\ns_barrier\nM4 a[0:3],v[24:27],v[8:11],a[0:3]\nDT v[60:61],v213 offset:18432\nSA m0,s82,36864\n"
"M4 a[4:7],v[24:27],v[12:15],a[4:7]\nDT v[62:63],v213 offset:23552\nM4 a[8:11],v[24:27],v[16:19],a[8:11]\nDR v[44:47],v205 offset:18432\nBL v224,s[36:39],s61 offen sc1 lds\nM4 a[12:15],v[24:27],v[20:23],a[12:15]\nDR v[48:51],v205 offset:19456\nM4 a[16:19],v[28:31],v[8:11],a[16:19]\n"
"DR v[52:55],v205 offset:20480\nM4 a[20:23],v[28:31],v[12:15],a[20:23]\nDR v[56:59],v205 offset:21504\nBL v225,s[36:39],s61 offen offset:1024 sc1 lds\nM4 a[24:27],v[28:31],v[16:19],a[24:27]\nDT v[64:65],v214 offset:18432\nM4 a[28:31],v[28:31],v[20:23],a[28:31]\nDT v[66:67],v214 offset:23552\n"
"SA m0,s82,55296\nM4 a[32:35],v[32:35],v[8:11],a[32:35]\nDT v[68:69],v215 offset:18432\nM4 a[36:39],v[32:35],v[12:15],a[36:39]\nDT v[70:71],v215 offset:23552\nM4 a[40:43],v[32:35],v[16:19],a[40:43]\nDT v[72:73],v216 offset:18432\nBL v226,s[36:39],s61 offen sc1 lds\n"
"M4 a[44:47],v[32:35],v[20:23],a[44:47]\nDT v[74:75],v216 offset:23552\nM4 a[48:51],v[36:39],v[8:11],a[48:51]\nDT v[76:77],v217 offset:18432\nBL v227,s[36:39],s61 offen offset:1024 sc1 lds\nM4 a[52:55],v[36:39],v[12:15],a[52:55]\nDT v[78:79],v217 offset:23552\nM4 a[56:59],v[36:39],v[16:19],a[56:59]\n"
"M4 a[60:63],v[36:39],v[20:23],a[60:63]\nSA s61,s61,2048\nM4 a[64:67],v[40:43],v[8:11],a[64:67]\nM4 a[68:71],v[40:43],v[12:15],a[68:71]\nSA m0,s83,36864\nM4 a[72:75],v[40:43],v[16:19],a[72:75]\nM4 a[76:79],v[40:43],v[20:23],a[76:79]\nSW lgkmcnt(0)\n"
"M4 a[0:3],v[60:63],v[44:47],a[0:3]\nDT v[24:25],v213 offset:36864\nBL v228,s[40:43],s62 offen offset:0 lds\nM4 a[4:7],v[60:63],v[48:51],a[4:7]\nDT v[26:27],v213 offset:41984\nM4 a[8:11],v[60:63],v[52:55],a[8:11]\nDR v[8:11],v205 offset:36864\nBL v229,s[40:43],s62 offen offset:1024 lds\n"
"M4 a[12:15],v[60:63],v[56:59],a[12:15]\nDR v[12:15],v205 offset:37888\nM4 a[16:19],v[64:67],v[44:47],a[16:19]\nDR v[16:19],v205 offset:38912\nM4 a[20:23],v[64:67],v[48:51],a[20:23]\nDR v[20:23],v205 offset:39936\nBL v230,s[40:43],s62 offen offset:2048 lds\nM4 a[24:27],v[64:67],v[52:55],a[24:27]\n"
"DT v[28:29],v214 offset:36864\nM4 a[28:31],v[64:67],v[56:59],a[28:31]\nDT v[30:31],v214 offset:41984\nM4 a[32:35],v[68:71],v[44:47],a[32:35]\nDT v[32:33],v215 offset:36864\nBL v231,s[40:43],s62 offen offset:3072 lds\nM4 a[36:39],v[68:71],v[48:51],a[36:39]\nDT v[34:35],v215 offset:41984\n"
"M4 a[40:43],v[68:71],v[52:55],a[40:43]\nDT v[36:37],v216 offset:36864\nM4 a[44:47],v[68:71],v[56:59],a[44:47]\nDT v[38:39],v216 offset:41984\nSA m0,m0,4096\nM4 a[48:51],v[72:75],v[44:47],a[48:51]\nDT v[40:41],v217 offset:36864\nM4 a[52:55],v[72:75],v[48:51],a[52:55]\n"
"DT v[42:43],v217 offset:41984\nM4 a[56:59],v[72:75],v[52:55],a[56:59]\nBL v232,s[40:43],s62 offen lds\nM4 a[60:63],v[72:75],v[56:59],a[60:63]\nM4 a[64:67],v[76:79],v[44:47],a[64:67]\nM4 a[68:71],v[76:79],v[48:51],a[68:71]\nSA s62,s62,81920\nM4 a[72:75],v[76:79],v[52:55],a[72:75]\n"
"M4 a[76:79],v[76:79],v[56:59],a[76:79]\nSW lgkmcnt(0)\nSW vmcnt(18)\ns_barrier\nM4 a[0:3],v[24:27],v[8:11],a[0:3]\nDT v[60:61],v218 offset:0\nSA m0,s82,73728\nM4 a[4:7],v[24:27],v[12:15],a[4:7]\n"
"DT v[62:63],v218 offset:5120\nM4 a[8:11],v[24:27],v[16:19],a[8:11]\nDR v[44:47],v206 offset:0\nBL v224,s[36:39],s61 offen sc1 lds\nM4 a[12:15],v[24:27],v[20:23],a[12:15]\nDR v[48:51],v206 offset:1024\nM4 a[16:19],v[28:31],v[8:11],a[16:19]\nDR v[52:55],v206 offset:2048\n"
"M4 a[20:23],v[28:31],v[12:15],a[20:23]\nDR v[56:59],v206 offset:3072\nBL v225,s[36:39],s61 offen offset:1024 sc1 lds\nM4 a[24:27],v[28:31],v[16:19],a[24:27]\nDT v[64:65],v219 offset:0\nM4 a[28:31],v[28:31],v[20:23],a[28:31]\nDT v[66:67],v219 offset:5120\nSA m0,s82,92160\n"
"M4 a[32:35],v[32:35],v[8:11],a[32:35]\nDT v[68:69],v220 offset:0\nM4 a[36:39],v[32:35],v[12:15],a[36:39]\nDT v[70:71],v220 offset:5120\nM4 a[40:43],v[32:35],v[16:19],a[40:43]\nDT v[72:73],v221 offset:0\nBL v226,s[36:39],s61 offen sc1 lds\nM4 a[44:47],v[32:35],v[20:23],a[44:47]\n"
"DT v[74:75],v221 offset:5120\nM4 a[48:51],v[36:39],v[8:11],a[48:51]\nDT v[76:77],v222 offset:0\nBL v227,s[36:39],s61 offen offset:1024 sc1 lds\nM4 a[52:55],v[36:39],v[12:15],a[52:55]\nDT v[78:79],v222 offset:5120\nM4 a[56:59],v[36:39],v[16:19],a[56:59]\nM4 a[60:63],v[36:39],v[20:23],a[60:63]\n"
"SA s61,s61,2048\nM4 a[64:67],v[40:43],v[8:11],a[64:67]\nM4 a[68:71],v[40:43],v[12:15],a[68:71]\nSA m0,s83,73728\nM4 a[72:75],v[40:43],v[16:19],a[72:75]\nM4 a[76:79],v[40:43],v[20:23],a[76:79]\nSW lgkmcnt(0)\nM4 a[0:3],v[60:63],v[44:47],a[0:3]\n"
"DT v[24:25],v218 offset:18432\nBL v228,s[40:43],s62 offen offset:0 lds\nM4 a[4:7],v[60:63],v[48:51],a[4:7]\nDT v[26:27],v218 offset:23552\nM4 a[8:11],v[60:63],v[52:55],a[8:11]\nDR v[8:11],v206 offset:18432\nBL v229,s[40:43],s62 offen offset:1024 lds\nM4 a[12:15],v[60:63],v[56:59],a[12:15]\n"
"DR v[12:15],v206 offset:19456\nM4 a[16:19],v[64:67],v[44:47],a[16:19]\nDR v[16:19],v206 offset:20480\nM4 a[20:23],v[64:67],v[48:51],a[20:23]\nDR v[20:23],v206 offset:21504\nBL v230,s[40:43],s62 offen offset:2048 lds\nM4 a[24:27],v[64:67],v[52:55],a[24:27]\nDT v[28:29],v219 offset:18432\n"
"M4 a[28:31],v[64:67],v[56:59],a[28:31]\nDT v[30:31],v219 offset:23552\nM4 a[32:35],v[68:71],v[44:47],a[32:35]\nDT v[32:33],v220 offset:18432\nBL v231,s[40:43],s62 offen offset:3072 lds\nM4 a[36:39],v[68:71],v[48:51],a[36:39]\nDT v[34:35],v220 offset:23552\nM4 a[40:43],v[68:71],v[52:55],a[40:43]\n"
"DT v[36:37],v221 offset:18432\nM4 a[44:47],v[68:71],v[56:59],a[44:47]\nDT v[38:39],v221 offset:23552\nSA m0,m0,4096\nM4 a[48:51],v[72:75],v[44:47],a[48:51]\nDT v[40:41],v222 offset:18432\nM4 a[52:55],v[72:75],v[48:51],a[52:55]\nDT v[42:43],v222 offset:23552\n"
"M4 a[56:59],v[72:75],v[52:55],a[56:59]\nBL v232,s[40:43],s62 offen lds\nM4 a[60:63],v[72:75],v[56:59],a[60:63]\nM4 a[64:67],v[76:79],v[44:47],a[64:67]\nM4 a[68:71],v[76:79],v[48:51],a[68:71]\nSA s62,s62,81920\nM4 a[72:75],v[76:79],v[52:55],a[72:75]\nM4 a[76:79],v[76:79],v[56:59],a[76:79]\n"
"SW lgkmcnt(0)\nSW vmcnt(18)\ns_barrier\nM4 a[0:3],v[24:27],v[8:11],a[0:3]\nDT v[60:61],v208 offset:0\nSA m0,s82,110592\nM4 a[4:7],v[24:27],v[12:15],a[4:7]\nDT v[62:63],v208 offset:5120\n"
"M4 a[8:11],v[24:27],v[16:19],a[8:11]\nDR v[44:47],v204 offset:0\nBL v224,s[36:39],s61 offen sc1 lds\nM4 a[12:15],v[24:27],v[20:23],a[12:15]\nDR v[48:51],v204 offset:1024\nM4 a[16:19],v[28:31],v[8:11],a[16:19]\nDR v[52:55],v204 offset:2048\nM4 a[20:23],v[28:31],v[12:15],a[20:23]\n"
"DR v[56:59],v204 offset:3072\nBL v225,s[36:39],s61 offen offset:1024 sc1 lds\nM4 a[24:27],v[28:31],v[16:19],a[24:27]\nDT v[64:65],v209 offset:0\nM4 a[28:31],v[28:31],v[20:23],a[28:31]\nDT v[66:67],v209 offset:5120\nSA m0,s82,129024\nM4 a[32:35],v[32:35],v[8:11],a[32:35]\n"
"DT v[68:69],v210 offset:0\nM4 a[36:39],v[32:35],v[12:15],a[36:39]\nDT v[70:71],v210 offset:5120\nM4 a[40:43],v[32:35],v[16:19],a[40:43]\nDT v[72:73],v211 offset:0\nBL v226,s[36:39],s61 offen sc1 lds\nM4 a[44:47],v[32:35],v[20:23],a[44:47]\nDT v[74:75],v211 offset:5120\n"
"M4 a[48:51],v[36:39],v[8:11],a[48:51]\nDT v[76:77],v212 offset:0\nBL v227,s[36:39],s61 offen offset:1024 sc1 lds\nM4 a[52:55],v[36:39],v[12:15],a[52:55]\nDT v[78:79],v212 offset:5120\nM4 a[56:59],v[36:39],v[16:19],a[56:59]\nM4 a[60:63],v[36:39],v[20:23],a[60:63]\nSA s61,s61,2048\n"
"M4 a[64:67],v[40:43],v[8:11],a[64:67]\nM4 a[68:71],v[40:43],v[12:15],a[68:71]\nSA m0,s83,110592\nM4 a[72:75],v[40:43],v[16:19],a[72:75]\nM4 a[76:79],v[40:43],v[20:23],a[76:79]\nSW lgkmcnt(0)\nM4 a[0:3],v[60:63],v[44:47],a[0:3]\nDT v[24:25],v208 offset:18432\n"
"BL v228,s[40:43],s62 offen offset:0 lds\nM4 a[4:7],v[60:63],v[48:51],a[4:7]\nDT v[26:27],v208 offset:23552\nM4 a[8:11],v[60:63],v[52:55],a[8:11]\nDR v[8:11],v204 offset:18432\nBL v229,s[40:43],s62 offen offset:1024 lds\nM4 a[12:15],v[60:63],v[56:59],a[12:15]\nDR v[12:15],v204 offset:19456\n"
"M4 a[16:19],v[64:67],v[44:47],a[16:19]\nDR v[16:19],v204 offset:20480\nM4 a[20:23],v[64:67],v[48:51],a[20:23]\nDR v[20:23],v204 offset:21504\nBL v230,s[40:43],s62 offen offset:2048 lds\nM4 a[24:27],v[64:67],v[52:55],a[24:27]\nDT v[28:29],v209 offset:18432\nM4 a[28:31],v[64:67],v[56:59],a[28:31]\n"
"DT v[30:31],v209 offset:23552\nM4 a[32:35],v[68:71],v[44:47],a[32:35]\nDT v[32:33],v210 offset:18432\nBL v231,s[40:43],s62 offen offset:3072 lds\nM4 a[36:39],v[68:71],v[48:51],a[36:39]\nDT v[34:35],v210 offset:23552\nM4 a[40:43],v[68:71],v[52:55],a[40:43]\nDT v[36:37],v211 offset:18432\n"
"M4 a[44:47],v[68:71],v[56:59],a[44:47]\nDT v[38:39],v211 offset:23552\nSA m0,m0,4096\nM4 a[48:51],v[72:75],v[44:47],a[48:51]\nDT v[40:41],v212 offset:18432\nM4 a[52:55],v[72:75],v[48:51],a[52:55]\nDT v[42:43],v212 offset:23552\nM4 a[56:59],v[72:75],v[52:55],a[56:59]\n"
"BL v232,s[40:43],s62 offen lds\nM4 a[60:63],v[72:75],v[56:59],a[60:63]\nM4 a[64:67],v[76:79],v[44:47],a[64:67]\nM4 a[68:71],v[76:79],v[48:51],a[68:71]\nSA s62,s62,81920\nM4 a[72:75],v[76:79],v[52:55],a[72:75]\nM4 a[76:79],v[76:79],v[56:59],a[76:79]\nSW lgkmcnt(0)\n"
"SW vmcnt(18)\ns_barrier\nM4 a[0:3],v[24:27],v[8:11],a[0:3]\nDT v[60:61],v208 offset:36864\nSA m0,s82,0\nM4 a[4:7],v[24:27],v[12:15],a[4:7]\nDT v[62:63],v208 offset:41984\nM4 a[8:11],v[24:27],v[16:19],a[8:11]\n"
"DR v[44:47],v204 offset:36864\nBL v224,s[36:39],s61 offen sc1 lds\nM4 a[12:15],v[24:27],v[20:23],a[12:15]\nDR v[48:51],v204 offset:37888\nM4 a[16:19],v[28:31],v[8:11],a[16:19]\nDR v[52:55],v204 offset:38912\nM4 a[20:23],v[28:31],v[12:15],a[20:23]\nDR v[56:59],v204 offset:39936\n"
"BL v225,s[36:39],s61 offen offset:1024 sc1 lds\nM4 a[24:27],v[28:31],v[16:19],a[24:27]\nDT v[64:65],v209 offset:36864\nM4 a[28:31],v[28:31],v[20:23],a[28:31]\nDT v[66:67],v209 offset:41984\nSA m0,s82,18432\nM4 a[32:35],v[32:35],v[8:11],a[32:35]\nDT v[68:69],v210 offset:36864\n"
"M4 a[36:39],v[32:35],v[12:15],a[36:39]\nDT v[70:71],v210 offset:41984\nM4 a[40:43],v[32:35],v[16:19],a[40:43]\nDT v[72:73],v211 offset:36864\nBL v226,s[36:39],s61 offen sc1 lds\nM4 a[44:47],v[32:35],v[20:23],a[44:47]\nDT v[74:75],v211 offset:41984\nM4 a[48:51],v[36:39],v[8:11],a[48:51]\n"
"DT v[76:77],v212 offset:36864\nBL v227,s[36:39],s61 offen offset:1024 sc1 lds\nM4 a[52:55],v[36:39],v[12:15],a[52:55]\nDT v[78:79],v212 offset:41984\nM4 a[56:59],v[36:39],v[16:19],a[56:59]\nM4 a[60:63],v[36:39],v[20:23],a[60:63]\nSA s61,s61,2048\nM4 a[64:67],v[40:43],v[8:11],a[64:67]\n"
"M4 a[68:71],v[40:43],v[12:15],a[68:71]\nSA m0,s83,0\nM4 a[72:75],v[40:43],v[16:19],a[72:75]\nM4 a[76:79],v[40:43],v[20:23],a[76:79]\nSW lgkmcnt(0)\nM4 a[0:3],v[60:63],v[44:47],a[0:3]\nDT v[24:25],v213 offset:0\nBL v228,s[40:43],s62 offen offset:0 lds\n"
"M4 a[4:7],v[60:63],v[48:51],a[4:7]\nDT v[26:27],v213 offset:5120\nM4 a[8:11],v[60:63],v[52:55],a[8:11]\nDR v[8:11],v205 offset:0\nBL v229,s[40:43],s62 offen offset:1024 lds\nM4 a[12:15],v[60:63],v[56:59],a[12:15]\nDR v[12:15],v205 offset:1024\nM4 a[16:19],v[64:67],v[44:47],a[16:19]\n"
"DR v[16:19],v205 offset:2048\nM4 a[20:23],v[64:67],v[48:51],a[20:23]\nDR v[20:23],v205 offset:3072\nBL v230,s[40:43],s62 offen offset:2048 lds\nM4 a[24:27],v[64:67],v[52:55],a[24:27]\nDT v[28:29],v214 offset:0\nM4 a[28:31],v[64:67],v[56:59],a[28:31]\nDT v[30:31],v214 offset:5120\n"
"M4 a[32:35],v[68:71],v[44:47],a[32:35]\nDT v[32:33],v215 offset:0\nBL v231,s[40:43],s62 offen offset:3072 lds\nM4 a[36:39],v[68:71],v[48:51],a[36:39]\nDT v[34:35],v215 offset:5120\nM4 a[40:43],v[68:71],v[52:55],a[40:43]\nDT v[36:37],v216 offset:0\nM4 a[44:47],v[68:71],v[56:59],a[44:47]\n"
"DT v[38:39],v216 offset:5120\nSA m0,m0,4096\nM4 a[48:51],v[72:75],v[44:47],a[48:51]\nDT v[40:41],v217 offset:0\nM4 a[52:55],v[72:75],v[48:51],a[52:55]\nDT v[42:43],v217 offset:5120\nM4 a[56:59],v[72:75],v[52:55],a[56:59]\nBL v232,s[40:43],s62 offen lds\n"
"M4 a[60:63],v[72:75],v[56:59],a[60:63]\nM4 a[64:67],v[76:79],v[44:47],a[64:67]\nM4 a[68:71],v[76:79],v[48:51],a[68:71]\nSA s62,s62,81920\nM4 a[72:75],v[76:79],v[52:55],a[72:75]\nM4 a[76:79],v[76:79],v[56:59],a[76:79]\nSW lgkmcnt(0)\nSW vmcnt(18)\n"
"s_barrier\nM4 a[0:3],v[24:27],v[8:11],a[0:3]\nDT v[60:61],v213 offset:18432\nSA m0,s82,36864\nM4 a[4:7],v[24:27],v[12:15],a[4:7]\nDT v[62:63],v213 offset:23552\nM4 a[8:11],v[24:27],v[16:19],a[8:11]\nDR v[44:47],v205 offset:18432\n"
"BL v224,s[36:39],s61 offen sc1 lds\nM4 a[12:15],v[24:27],v[20:23],a[12:15]\nDR v[48:51],v205 offset:19456\nM4 a[16:19],v[28:31],v[8:11],a[16:19]\nDR v[52:55],v205 offset:20480\nM4 a[20:23],v[28:31],v[12:15],a[20:23]\nDR v[56:59],v205 offset:21504\nBL v225,s[36:39],s61 offen offset:1024 sc1 lds\n"
"M4 a[24:27],v[28:31],v[16:19],a[24:27]\nDT v[64:65],v214 offset:18432\nM4 a[28:31],v[28:31],v[20:23],a[28:31]\nDT v[66:67],v214 offset:23552\nSA m0,s82,55296\nM4 a[32:35],v[32:35],v[8:11],a[32:35]\nDT v[68:69],v215 offset:18432\nM4 a[36:39],v[32:35],v[12:15],a[36:39]\n"
"DT v[70:71],v215 offset:23552\nM4 a[40:43],v[32:35],v[16:19],a[40:43]\nDT v[72:73],v216 offset:18432\nBL v226,s[36:39],s61 offen sc1 lds\nM4 a[44:47],v[32:35],v[20:23],a[44:47]\nDT v[74:75],v216 offset:23552\nM4 a[48:51],v[36:39],v[8:11],a[48:51]\nDT v[76:77],v217 offset:18432\n"
"BL v227,s[36:39],s61 offen offset:1024 sc1 lds\nM4 a[52:55],v[36:39],v[12:15],a[52:55]\nDT v[78:79],v217 offset:23552\nM4 a[56:59],v[36:39],v[16:19],a[56:59]\nM4 a[60:63],v[36:39],v[20:23],a[60:63]\nSA s61,s61,2048\nM4 a[64:67],v[40:43],v[8:11],a[64:67]\nM4 a[68:71],v[40:43],v[12:15],a[68:71]\n"
"SA m0,s83,36864\nM4 a[72:75],v[40:43],v[16:19],a[72:75]\nM4 a[76:79],v[40:43],v[20:23],a[76:79]\nSW lgkmcnt(0)\nM4 a[0:3],v[60:63],v[44:47],a[0:3]\nDT v[24:25],v213 offset:36864\nBL v228,s[40:43],s62 offen offset:0 lds\nM4 a[4:7],v[60:63],v[48:51],a[4:7]\n"
"DT v[26:27],v213 offset:41984\nM4 a[8:11],v[60:63],v[52:55],a[8:11]\nDR v[8:11],v205 offset:36864\nBL v229,s[40:43],s62 offen offset:1024 lds\nM4 a[12:15],v[60:63],v[56:59],a[12:15]\nDR v[12:15],v205 offset:37888\nM4 a[16:19],v[64:67],v[44:47],a[16:19]\nDR v[16:19],v205 offset:38912\n"
"M4 a[20:23],v[64:67],v[48:51],a[20:23]\nDR v[20:23],v205 offset:39936\nBL v230,s[40:43],s62 offen offset:2048 lds\nM4 a[24:27],v[64:67],v[52:55],a[24:27]\nDT v[28:29],v214 offset:36864\nM4 a[28:31],v[64:67],v[56:59],a[28:31]\nDT v[30:31],v214 offset:41984\nM4 a[32:35],v[68:71],v[44:47],a[32:35]\n"
"DT v[32:33],v215 offset:36864\nBL v231,s[40:43],s62 offen offset:3072 lds\nM4 a[36:39],v[68:71],v[48:51],a[36:39]\nDT v[34:35],v215 offset:41984\nM4 a[40:43],v[68:71],v[52:55],a[40:43]\nDT v[36:37],v216 offset:36864\nM4 a[44:47],v[68:71],v[56:59],a[44:47]\nDT v[38:39],v216 offset:41984\n"
"SA m0,m0,4096\nM4 a[48:51],v[72:75],v[44:47],a[48:51]\nDT v[40:41],v217 offset:36864\nM4 a[52:55],v[72:75],v[48:51],a[52:55]\nDT v[42:43],v217 offset:41984\nM4 a[56:59],v[72:75],v[52:55],a[56:59]\nBL v232,s[40:43],s62 offen lds\nM4 a[60:63],v[72:75],v[56:59],a[60:63]\n"
"M4 a[64:67],v[76:79],v[44:47],a[64:67]\nM4 a[68:71],v[76:79],v[48:51],a[68:71]\nSA s62,s62,81920\nM4 a[72:75],v[76:79],v[52:55],a[72:75]\nM4 a[76:79],v[76:79],v[56:59],a[76:79]\nSW lgkmcnt(0)\nSW vmcnt(18)\ns_barrier\n"
"M4 a[0:3],v[24:27],v[8:11],a[0:3]\nDT v[60:61],v218 offset:0\nSA m0,s82,73728\nM4 a[4:7],v[24:27],v[12:15],a[4:7]\nDT v[62:63],v218 offset:5120\nM4 a[8:11],v[24:27],v[16:19],a[8:11]\nDR v[44:47],v206 offset:0\nBL v224,s[36:39],s61 offen sc1 lds\n"
"M4 a[12:15],v[24:27],v[20:23],a[12:15]\nDR v[48:51],v206 offset:1024\nM4 a[16:19],v[28:31],v[8:11],a[16:19]\nDR v[52:55],v206 offset:2048\nM4 a[20:23],v[28:31],v[12:15],a[20:23]\nDR v[56:59],v206 offset:3072\nBL v225,s[36:39],s61 offen offset:1024 sc1 lds\nM4 a[24:27],v[28:31],v[16:19],a[24:27]\n"
"DT v[64:65],v219 offset:0\nM4 a[28:31],v[28:31],v[20:23],a[28:31]\nDT v[66:67],v219 offset:5120\nSA m0,s82,92160\nM4 a[32:35],v[32:35],v[8:11],a[32:35]\nDT v[68:69],v220 offset:0\nM4 a[36:39],v[32:35],v[12:15],a[36:39]\nDT v[70:71],v220 offset:5120\n"
"M4 a[40:43],v[32:35],v[16:19],a[40:43]\nDT v[72:73],v221 offset:0\nBL v226,s[36:39],s61 offen sc1 lds\nM4 a[44:47],v[32:35],v[20:23],a[44:47]\nDT v[74:75],v221 offset:5120\nM4 a[48:51],v[36:39],v[8:11],a[48:51]\nDT v[76:77],v222 offset:0\nBL v227,s[36:39],s61 offen offset:1024 sc1 lds\n"
"M4 a[52:55],v[36:39],v[12:15],a[52:55]\nDT v[78:79],v222 offset:5120\nM4 a[56:59],v[36:39],v[16:19],a[56:59]\nM4 a[60:63],v[36:39],v[20:23],a[60:63]\nSA s61,s61,2048\nM4 a[64:67],v[40:43],v[8:11],a[64:67]\nM4 a[68:71],v[40:43],v[12:15],a[68:71]\nSA m0,s83,73728\n"
"M4 a[72:75],v[40:43],v[16:19],a[72:75]\nM4 a[76:79],v[40:43],v[20:23],a[76:79]\nSW lgkmcnt(0)\nM4 a[0:3],v[60:63],v[44:47],a[0:3]\nDT v[24:25],v218 offset:18432\nBL v228,s[40:43],s62 offen offset:0 lds\nM4 a[4:7],v[60:63],v[48:51],a[4:7]\nDT v[26:27],v218 offset:23552\n"
"M4 a[8:11],v[60:63],v[52:55],a[8:11]\nDR v[8:11],v206 offset:18432\nBL v229,s[40:43],s62 offen offset:1024 lds\nM4 a[12:15],v[60:63],v[56:59],a[12:15]\nDR v[12:15],v206 offset:19456\nM4 a[16:19],v[64:67],v[44:47],a[16:19]\nDR v[16:19],v206 offset:20480\nM4 a[20:23],v[64:67],v[48:51],a[20:23]\n"
"DR v[20:23],v206 offset:21504\nBL v230,s[40:43],s62 offen offset:2048 lds\nM4 a[24:27],v[64:67],v[52:55],a[24:27]\nDT v[28:29],v219 offset:18432\nM4 a[28:31],v[64:67],v[56:59],a[28:31]\nDT v[30:31],v219 offset:23552\nM4 a[32:35],v[68:71],v[44:47],a[32:35]\nDT v[32:33],v220 offset:18432\n"
"BL v231,s[40:43],s62 offen offset:3072 lds\nM4 a[36:39],v[68:71],v[48:51],a[36:39]\nDT v[34:35],v220 offset:23552\nM4 a[40:43],v[68:71],v[52:55],a[40:43]\nDT v[36:37],v221 offset:18432\nM4 a[44:47],v[68:71],v[56:59],a[44:47]\nDT v[38:39],v221 offset:23552\nSA m0,m0,4096\n"
"M4 a[48:51],v[72:75],v[44:47],a[48:51]\nDT v[40:41],v222 offset:18432\nM4 a[52:55],v[72:75],v[48:51],a[52:55]\nDT v[42:43],v222 offset:23552\nM4 a[56:59],v[72:75],v[52:55],a[56:59]\nBL v232,s[40:43],s62 offen lds\nM4 a[60:63],v[72:75],v[56:59],a[60:63]\nM4 a[64:67],v[76:79],v[44:47],a[64:67]\n"
"M4 a[68:71],v[76:79],v[48:51],a[68:71]\nSA s62,s62,81920\nM4 a[72:75],v[76:79],v[52:55],a[72:75]\nM4 a[76:79],v[76:79],v[56:59],a[76:79]\nSW lgkmcnt(0)\nSW vmcnt(18)\ns_barrier\nM4 a[0:3],v[24:27],v[8:11],a[0:3]\n"
"DT v[60:61],v208 offset:0\nSA m0,s82,110592\nM4 a[4:7],v[24:27],v[12:15],a[4:7]\nDT v[62:63],v208 offset:5120\nM4 a[8:11],v[24:27],v[16:19],a[8:11]\nDR v[44:47],v204 offset:0\nBL v224,s[36:39],s61 offen sc1 lds\nM4 a[12:15],v[24:27],v[20:23],a[12:15]\n"
"DR v[48:51],v204 offset:1024\nM4 a[16:19],v[28:31],v[8:11],a[16:19]\nDR v[52:55],v204 offset:2048\nM4 a[20:23],v[28:31],v[12:15],a[20:23]\nDR v[56:59],v204 offset:3072\nBL v225,s[36:39],s61 offen offset:1024 sc1 lds\nM4 a[24:27],v[28:31],v[16:19],a[24:27]\nDT v[64:65],v209 offset:0\n"
"M4 a[28:31],v[28:31],v[20:23],a[28:31]\nDT v[66:67],v209 offset:5120\nSA m0,s82,129024\nM4 a[32:35],v[32:35],v[8:11],a[32:35]\nDT v[68:69],v210 offset:0\nM4 a[36:39],v[32:35],v[12:15],a[36:39]\nDT v[70:71],v210 offset:5120\nM4 a[40:43],v[32:35],v[16:19],a[40:43]\n"
"DT v[72:73],v211 offset:0\nBL v226,s[36:39],s61 offen sc1 lds\nM4 a[44:47],v[32:35],v[20:23],a[44:47]\nDT v[74:75],v211 offset:5120\nM4 a[48:51],v[36:39],v[8:11],a[48:51]\nDT v[76:77],v212 offset:0\nBL v227,s[36:39],s61 offen offset:1024 sc1 lds\nM4 a[52:55],v[36:39],v[12:15],a[52:55]\n"
"DT v[78:79],v212 offset:5120\nM4 a[56:59],v[36:39],v[16:19],a[56:59]\nM4 a[60:63],v[36:39],v[20:23],a[60:63]\nSA s61,s61,2048\nM4 a[64:67],v[40:43],v[8:11],a[64:67]\nM4 a[68:71],v[40:43],v[12:15],a[68:71]\nSA m0,s83,110592\nM4 a[72:75],v[40:43],v[16:19],a[72:75]\n"
"M4 a[76:79],v[40:43],v[20:23],a[76:79]\nSW lgkmcnt(0)\nM4 a[0:3],v[60:63],v[44:47],a[0:3]\nDT v[24:25],v208 offset:18432\nBL v228,s[40:43],s62 offen offset:0 lds\nM4 a[4:7],v[60:63],v[48:51],a[4:7]\nDT v[26:27],v208 offset:23552\nM4 a[8:11],v[60:63],v[52:55],a[8:11]\n"
"DR v[8:11],v204 offset:18432\nBL v229,s[40:43],s62 offen offset:1024 lds\nM4 a[12:15],v[60:63],v[56:59],a[12:15]\nDR v[12:15],v204 offset:19456\nM4 a[16:19],v[64:67],v[44:47],a[16:19]\nDR v[16:19],v204 offset:20480\nM4 a[20:23],v[64:67],v[48:51],a[20:23]\nDR v[20:23],v204 offset:21504\n"
"BL v230,s[40:43],s62 offen offset:2048 lds\nM4 a[24:27],v[64:67],v[52:55],a[24:27]\nDT v[28:29],v209 offset:18432\nM4 a[28:31],v[64:67],v[56:59],a[28:31]\nDT v[30:31],v209 offset:23552\nM4 a[32:35],v[68:71],v[44:47],a[32:35]\nDT v[32:33],v210 offset:18432\nBL v231,s[40:43],s62 offen offset:3072 lds\n"
"M4 a[36:39],v[68:71],v[48:51],a[36:39]\nDT v[34:35],v210 offset:23552\nM4 a[40:43],v[68:71],v[52:55],a[40:43]\nDT v[36:37],v211 offset:18432\nM4 a[44:47],v[68:71],v[56:59],a[44:47]\nDT v[38:39],v211 offset:23552\nSA m0,m0,4096\nM4 a[48:51],v[72:75],v[44:47],a[48:51]\n"
"DT v[40:41],v212 offset:18432\nM4 a[52:55],v[72:75],v[48:51],a[52:55]\nDT v[42:43],v212 offset:23552\nM4 a[56:59],v[72:75],v[52:55],a[56:59]\nBL v232,s[40:43],s62 offen lds\nM4 a[60:63],v[72:75],v[56:59],a[60:63]\nM4 a[64:67],v[76:79],v[44:47],a[64:67]\nM4 a[68:71],v[76:79],v[48:51],a[68:71]\n"
"SA s62,s62,81920\nM4 a[72:75],v[76:79],v[52:55],a[72:75]\nM4 a[76:79],v[76:79],v[56:59],a[76:79]\nSW lgkmcnt(0)\nSW vmcnt(18)\ns_barrier\nM4 a[0:3],v[24:27],v[8:11],a[0:3]\nDT v[60:61],v208 offset:36864\n"
"SA m0,s82,0\nM4 a[4:7],v[24:27],v[12:15],a[4:7]\nDT v[62:63],v208 offset:41984\nM4 a[8:11],v[24:27],v[16:19],a[8:11]\nDR v[44:47],v204 offset:36864\nBL v224,s[36:39],s61 offen sc1 lds\nM4 a[12:15],v[24:27],v[20:23],a[12:15]\nDR v[48:51],v204 offset:37888\n"
"M4 a[16:19],v[28:31],v[8:11],a[16:19]\nDR v[52:55],v204 offset:38912\nM4 a[20:23],v[28:31],v[12:15],a[20:23]\nDR v[56:59],v204 offset:39936\nBL v225,s[36:39],s61 offen offset:1024 sc1 lds\nM4 a[24:27],v[28:31],v[16:19],a[24:27]\nDT v[64:65],v209 offset:36864\nM4 a[28:31],v[28:31],v[20:23],a[28:31]\n"
"DT v[66:67],v209 offset:41984\nSA m0,s82,18432\nM4 a[32:35],v[32:35],v[8:11],a[32:35]\nDT v[68:69],v210 offset:36864\nM4 a[36:39],v[32:35],v[12:15],a[36:39]\nDT v[70:71],v210 offset:41984\nM4 a[40:43],v[32:35],v[16:19],a[40:43]\nDT v[72:73],v211 offset:36864\n"
"BL v226,s[36:39],s61 offen sc1 lds\nM4 a[44:47],v[32:35],v[20:23],a[44:47]\nDT v[74:75],v211 offset:41984\nM4 a[48:51],v[36:39],v[8:11],a[48:51]\nDT v[76:77],v212 offset:36864\nBL v227,s[36:39],s61 offen offset:1024 sc1 lds\nM4 a[52:55],v[36:39],v[12:15],a[52:55]\nDT v[78:79],v212 offset:41984\n"
"M4 a[56:59],v[36:39],v[16:19],a[56:59]\nM4 a[60:63],v[36:39],v[20:23],a[60:63]\nSA s61,s61,2048\nM4 a[64:67],v[40:43],v[8:11],a[64:67]\nM4 a[68:71],v[40:43],v[12:15],a[68:71]\nSA m0,s83,0\nM4 a[72:75],v[40:43],v[16:19],a[72:75]\nM4 a[76:79],v[40:43],v[20:23],a[76:79]\n"
"SW lgkmcnt(0)\nM4 a[0:3],v[60:63],v[44:47],a[0:3]\nDT v[24:25],v213 offset:0\nBL v228,s[40:43],s62 offen offset:0 lds\nM4 a[4:7],v[60:63],v[48:51],a[4:7]\nDT v[26:27],v213 offset:5120\nM4 a[8:11],v[60:63],v[52:55],a[8:11]\nDR v[8:11],v205 offset:0\n"
"BL v229,s[40:43],s62 offen offset:1024 lds\nM4 a[12:15],v[60:63],v[56:59],a[12:15]\nDR v[12:15],v205 offset:1024\nM4 a[16:19],v[64:67],v[44:47],a[16:19]\nDR v[16:19],v205 offset:2048\nM4 a[20:23],v[64:67],v[48:51],a[20:23]\nDR v[20:23],v205 offset:3072\nBL v230,s[40:43],s62 offen offset:2048 lds\n"
"M4 a[24:27],v[64:67],v[52:55],a[24:27]\nDT v[28:29],v214 offset:0\nM4 a[28:31],v[64:67],v[56:59],a[28:31]\nDT v[30:31],v214 offset:5120\nM4 a[32:35],v[68:71],v[44:47],a[32:35]\nDT v[32:33],v215 offset:0\nBL v231,s[40:43],s62 offen offset:3072 lds\nM4 a[36:39],v[68:71],v[48:51],a[36:39]\n"
"DT v[34:35],v215 offset:5120\nM4 a[40:43],v[68:71],v[52:55],a[40:43]\nDT v[36:37],v216 offset:0\nM4 a[44:47],v[68:71],v[56:59],a[44:47]\nDT v[38:39],v216 offset:5120\nSA m0,m0,4096\nM4 a[48:51],v[72:75],v[44:47],a[48:51]\nDT v[40:41],v217 offset:0\n"
"M4 a[52:55],v[72:75],v[48:51],a[52:55]\nDT v[42:43],v217 offset:5120\nM4 a[56:59],v[72:75],v[52:55],a[56:59]\nBL v232,s[40:43],s62 offen lds\nM4 a[60:63],v[72:75],v[56:59],a[60:63]\nM4 a[64:67],v[76:79],v[44:47],a[64:67]\nM4 a[68:71],v[76:79],v[48:51],a[68:71]\nSA s62,s62,81920\n"
"M4 a[72:75],v[76:79],v[52:55],a[72:75]\nM4 a[76:79],v[76:79],v[56:59],a[76:79]\nSW lgkmcnt(0)\nSW vmcnt(18)\ns_barrier\nM4 a[0:3],v[24:27],v[8:11],a[0:3]\nDT v[60:61],v213 offset:18432\nSA m0,s82,36864\n"
"M4 a[4:7],v[24:27],v[12:15],a[4:7]\nDT v[62:63],v213 offset:23552\nM4 a[8:11],v[24:27],v[16:19],a[8:11]\nDR v[44:47],v205 offset:18432\nBL v224,s[36:39],s61 offen sc1 lds\nM4 a[12:15],v[24:27],v[20:23],a[12:15]\nDR v[48:51],v205 offset:19456\nM4 a[16:19],v[28:31],v[8:11],a[16:19]\n"
"DR v[52:55],v205 offset:20480\nM4 a[20:23],v[28:31],v[12:15],a[20:23]\nDR v[56:59],v205 offset:21504\nBL v225,s[36:39],s61 offen offset:1024 sc1 lds\nM4 a[24:27],v[28:31],v[16:19],a[24:27]\nDT v[64:65],v214 offset:18432\nM4 a[28:31],v[28:31],v[20:23],a[28:31]\nDT v[66:67],v214 offset:23552\n"
"SA m0,s82,55296\nM4 a[32:35],v[32:35],v[8:11],a[32:35]\nDT v[68:69],v215 offset:18432\nM4 a[36:39],v[32:35],v[12:15],a[36:39]\nDT v[70:71],v215 offset:23552\nM4 a[40:43],v[32:35],v[16:19],a[40:43]\nDT v[72:73],v216 offset:18432\nBL v226,s[36:39],s61 offen sc1 lds\n"
"M4 a[44:47],v[32:35],v[20:23],a[44:47]\nDT v[74:75],v216 offset:23552\nM4 a[48:51],v[36:39],v[8:11],a[48:51]\nDT v[76:77],v217 offset:18432\nBL v227,s[36:39],s61 offen offset:1024 sc1 lds\nM4 a[52:55],v[36:39],v[12:15],a[52:55]\nDT v[78:79],v217 offset:23552\nM4 a[56:59],v[36:39],v[16:19],a[56:59]\n"
"M4 a[60:63],v[36:39],v[20:23],a[60:63]\nSA s61,s61,2048\nM4 a[64:67],v[40:43],v[8:11],a[64:67]\nM4 a[68:71],v[40:43],v[12:15],a[68:71]\nSA m0,s83,36864\nM4 a[72:75],v[40:43],v[16:19],a[72:75]\nM4 a[76:79],v[40:43],v[20:23],a[76:79]\nSW lgkmcnt(0)\n"
"M4 a[0:3],v[60:63],v[44:47],a[0:3]\nDT v[24:25],v213 offset:36864\nBL v228,s[40:43],s62 offen offset:0 lds\nM4 a[4:7],v[60:63],v[48:51],a[4:7]\nDT v[26:27],v213 offset:41984\nM4 a[8:11],v[60:63],v[52:55],a[8:11]\nDR v[8:11],v205 offset:36864\nBL v229,s[40:43],s62 offen offset:1024 lds\n"
"M4 a[12:15],v[60:63],v[56:59],a[12:15]\nDR v[12:15],v205 offset:37888\nM4 a[16:19],v[64:67],v[44:47],a[16:19]\nDR v[16:19],v205 offset:38912\nM4 a[20:23],v[64:67],v[48:51],a[20:23]\nDR v[20:23],v205 offset:39936\nBL v230,s[40:43],s62 offen offset:2048 lds\nM4 a[24:27],v[64:67],v[52:55],a[24:27]\n"
"DT v[28:29],v214 offset:36864\nM4 a[28:31],v[64:67],v[56:59],a[28:31]\nDT v[30:31],v214 offset:41984\nM4 a[32:35],v[68:71],v[44:47],a[32:35]\nDT v[32:33],v215 offset:36864\nBL v231,s[40:43],s62 offen offset:3072 lds\nM4 a[36:39],v[68:71],v[48:51],a[36:39]\nDT v[34:35],v215 offset:41984\n"
"M4 a[40:43],v[68:71],v[52:55],a[40:43]\nDT v[36:37],v216 offset:36864\nM4 a[44:47],v[68:71],v[56:59],a[44:47]\nDT v[38:39],v216 offset:41984\nSA m0,m0,4096\nM4 a[48:51],v[72:75],v[44:47],a[48:51]\nDT v[40:41],v217 offset:36864\nM4 a[52:55],v[72:75],v[48:51],a[52:55]\n"
"DT v[42:43],v217 offset:41984\nM4 a[56:59],v[72:75],v[52:55],a[56:59]\nBL v232,s[40:43],s62 offen lds\nM4 a[60:63],v[72:75],v[56:59],a[60:63]\nM4 a[64:67],v[76:79],v[44:47],a[64:67]\nM4 a[68:71],v[76:79],v[48:51],a[68:71]\nSA s62,s62,81920\nM4 a[72:75],v[76:79],v[52:55],a[72:75]\n"
"M4 a[76:79],v[76:79],v[56:59],a[76:79]\nSW lgkmcnt(0)\nSW vmcnt(18)\ns_barrier\nM4 a[0:3],v[24:27],v[8:11],a[0:3]\nDT v[60:61],v218 offset:0\nSA m0,s82,73728\nM4 a[4:7],v[24:27],v[12:15],a[4:7]\n"
"DT v[62:63],v218 offset:5120\nM4 a[8:11],v[24:27],v[16:19],a[8:11]\nDR v[44:47],v206 offset:0\nBL v224,s[36:39],s61 offen sc1 lds\nM4 a[12:15],v[24:27],v[20:23],a[12:15]\nDR v[48:51],v206 offset:1024\nM4 a[16:19],v[28:31],v[8:11],a[16:19]\nDR v[52:55],v206 offset:2048\n"
"M4 a[20:23],v[28:31],v[12:15],a[20:23]\nDR v[56:59],v206 offset:3072\nBL v225,s[36:39],s61 offen offset:1024 sc1 lds\nM4 a[24:27],v[28:31],v[16:19],a[24:27]\nDT v[64:65],v219 offset:0\nM4 a[28:31],v[28:31],v[20:23],a[28:31]\nDT v[66:67],v219 offset:5120\nSA m0,s82,92160\n"
"M4 a[32:35],v[32:35],v[8:11],a[32:35]\nDT v[68:69],v220 offset:0\nM4 a[36:39],v[32:35],v[12:15],a[36:39]\nDT v[70:71],v220 offset:5120\nM4 a[40:43],v[32:35],v[16:19],a[40:43]\nDT v[72:73],v221 offset:0\nBL v226,s[36:39],s61 offen sc1 lds\nM4 a[44:47],v[32:35],v[20:23],a[44:47]\n"
"DT v[74:75],v221 offset:5120\nM4 a[48:51],v[36:39],v[8:11],a[48:51]\nDT v[76:77],v222 offset:0\nBL v227,s[36:39],s61 offen offset:1024 sc1 lds\nM4 a[52:55],v[36:39],v[12:15],a[52:55]\nDT v[78:79],v222 offset:5120\nM4 a[56:59],v[36:39],v[16:19],a[56:59]\nM4 a[60:63],v[36:39],v[20:23],a[60:63]\n"
"SA s61,s61,2048\nM4 a[64:67],v[40:43],v[8:11],a[64:67]\nM4 a[68:71],v[40:43],v[12:15],a[68:71]\nSA m0,s83,73728\nM4 a[72:75],v[40:43],v[16:19],a[72:75]\nM4 a[76:79],v[40:43],v[20:23],a[76:79]\nSW lgkmcnt(0)\nM4 a[0:3],v[60:63],v[44:47],a[0:3]\n"
"DT v[24:25],v218 offset:18432\nBL v228,s[40:43],s62 offen offset:0 lds\nM4 a[4:7],v[60:63],v[48:51],a[4:7]\nDT v[26:27],v218 offset:23552\nM4 a[8:11],v[60:63],v[52:55],a[8:11]\nDR v[8:11],v206 offset:18432\nBL v229,s[40:43],s62 offen offset:1024 lds\nM4 a[12:15],v[60:63],v[56:59],a[12:15]\n"
"DR v[12:15],v206 offset:19456\nM4 a[16:19],v[64:67],v[44:47],a[16:19]\nDR v[16:19],v206 offset:20480\nM4 a[20:23],v[64:67],v[48:51],a[20:23]\nDR v[20:23],v206 offset:21504\nBL v230,s[40:43],s62 offen offset:2048 lds\nM4 a[24:27],v[64:67],v[52:55],a[24:27]\nDT v[28:29],v219 offset:18432\n"
"M4 a[28:31],v[64:67],v[56:59],a[28:31]\nDT v[30:31],v219 offset:23552\nM4 a[32:35],v[68:71],v[44:47],a[32:35]\nDT v[32:33],v220 offset:18432\nBL v231,s[40:43],s62 offen offset:3072 lds\nM4 a[36:39],v[68:71],v[48:51],a[36:39]\nDT v[34:35],v220 offset:23552\nM4 a[40:43],v[68:71],v[52:55],a[40:43]\n"
"DT v[36:37],v221 offset:18432\nM4 a[44:47],v[68:71],v[56:59],a[44:47]\nDT v[38:39],v221 offset:23552\nSA m0,m0,4096\nM4 a[48:51],v[72:75],v[44:47],a[48:51]\nDT v[40:41],v222 offset:18432\nM4 a[52:55],v[72:75],v[48:51],a[52:55]\nDT v[42:43],v222 offset:23552\n"
"M4 a[56:59],v[72:75],v[52:55],a[56:59]\nBL v232,s[40:43],s62 offen lds\nM4 a[60:63],v[72:75],v[56:59],a[60:63]\nM4 a[64:67],v[76:79],v[44:47],a[64:67]\nM4 a[68:71],v[76:79],v[48:51],a[68:71]\nSA s62,s62,81920\nM4 a[72:75],v[76:79],v[52:55],a[72:75]\nM4 a[76:79],v[76:79],v[56:59],a[76:79]\n"
"SW lgkmcnt(0)\nSW vmcnt(18)\ns_barrier\nM4 a[0:3],v[24:27],v[8:11],a[0:3]\nDT v[60:61],v208 offset:0\ns_cmp_eq_u32 s65,23\ns_cselect_b32 s38,0,s38\ns_cselect_b32 s42,0,s42\n"
"M4 a[4:7],v[24:27],v[12:15],a[4:7]\nDT v[62:63],v208 offset:5120\nM4 a[8:11],v[24:27],v[16:19],a[8:11]\nDR v[44:47],v204 offset:0\nSA m0,s82,110592\nM4 a[12:15],v[24:27],v[20:23],a[12:15]\nDR v[48:51],v204 offset:1024\nM4 a[16:19],v[28:31],v[8:11],a[16:19]\n"
"DR v[52:55],v204 offset:2048\nM4 a[20:23],v[28:31],v[12:15],a[20:23]\nDR v[56:59],v204 offset:3072\nBL v224,s[36:39],s61 offen sc1 lds\nM4 a[24:27],v[28:31],v[16:19],a[24:27]\nDT v[64:65],v209 offset:0\nM4 a[28:31],v[28:31],v[20:23],a[28:31]\nDT v[66:67],v209 offset:5120\n"
"BL v225,s[36:39],s61 offen offset:1024 sc1 lds\nM4 a[32:35],v[32:35],v[8:11],a[32:35]\nDT v[68:69],v210 offset:0\nM4 a[36:39],v[32:35],v[12:15],a[36:39]\nDT v[70:71],v210 offset:5120\nM4 a[40:43],v[32:35],v[16:19],a[40:43]\nDT v[72:73],v211 offset:0\nSA m0,s82,129024\n"
"M4 a[44:47],v[32:35],v[20:23],a[44:47]\nDT v[74:75],v211 offset:5120\nM4 a[48:51],v[36:39],v[8:11],a[48:51]\nDT v[76:77],v212 offset:0\nBL v226,s[36:39],s61 offen sc1 lds\nM4 a[52:55],v[36:39],v[12:15],a[52:55]\nDT v[78:79],v212 offset:5120\nM4 a[56:59],v[36:39],v[16:19],a[56:59]\n"
"M4 a[60:63],v[36:39],v[20:23],a[60:63]\nBL v227,s[36:39],s61 offen offset:1024 sc1 lds\nM4 a[64:67],v[40:43],v[8:11],a[64:67]\nM4 a[68:71],v[40:43],v[12:15],a[68:71]\nSA s61,s61,2048\nM4 a[72:75],v[40:43],v[16:19],a[72:75]\nM4 a[76:79],v[40:43],v[20:23],a[76:79]\nSW lgkmcnt(0)\n"
"M4 a[0:3],v[60:63],v[44:47],a[0:3]\nDT v[24:25],v208 offset:18432\nSA m0,s83,110592\nM4 a[4:7],v[60:63],v[48:51],a[4:7]\nDT v[26:27],v208 offset:23552\nM4 a[8:11],v[60:63],v[52:55],a[8:11]\nDR v[8:11],v204 offset:18432\nBL v228,s[40:43],s62 offen offset:0 lds\n"
"M4 a[12:15],v[60:63],v[56:59],a[12:15]\nDR v[12:15],v204 offset:19456\nM4 a[16:19],v[64:67],v[44:47],a[16:19]\nDR v[16:19],v204 offset:20480\nM4 a[20:23],v[64:67],v[48:51],a[20:23]\nDR v[20:23],v204 offset:21504\nBL v229,s[40:43],s62 offen offset:1024 lds\nM4 a[24:27],v[64:67],v[52:55],a[24:27]\n"
"DT v[28:29],v209 offset:18432\nM4 a[28:31],v[64:67],v[56:59],a[28:31]\nDT v[30:31],v209 offset:23552\nBL v230,s[40:43],s62 offen offset:2048 lds\nM4 a[32:35],v[68:71],v[44:47],a[32:35]\nDT v[32:33],v210 offset:18432\nM4 a[36:39],v[68:71],v[48:51],a[36:39]\nDT v[34:35],v210 offset:23552\n"
"M4 a[40:43],v[68:71],v[52:55],a[40:43]\nDT v[36:37],v211 offset:18432\nBL v231,s[40:43],s62 offen offset:3072 lds\nM4 a[44:47],v[68:71],v[56:59],a[44:47]\nDT v[38:39],v211 offset:23552\nM4 a[48:51],v[72:75],v[44:47],a[48:51]\nDT v[40:41],v212 offset:18432\nSA m0,m0,4096\n"
"M4 a[52:55],v[72:75],v[48:51],a[52:55]\nDT v[42:43],v212 offset:23552\nM4 a[56:59],v[72:75],v[52:55],a[56:59]\nM4 a[60:63],v[72:75],v[56:59],a[60:63]\nBL v232,s[40:43],s62 offen lds\nM4 a[64:67],v[76:79],v[44:47],a[64:67]\nM4 a[68:71],v[76:79],v[48:51],a[68:71]\nSA s62,s62,81920\n"
"M4 a[72:75],v[76:79],v[52:55],a[72:75]\nM4 a[76:79],v[76:79],v[56:59],a[76:79]\nSW lgkmcnt(0)\nSW vmcnt(18)\ns_barrier\nM4 a[0:3],v[24:27],v[8:11],a[0:3]\nDT v[60:61],v208 offset:36864\ns_load_dwordx2 s[76:77],s[94:95],0x0\n"
"M4 a[4:7],v[24:27],v[12:15],a[4:7]\nDT v[62:63],v208 offset:41984\nM4 a[8:11],v[24:27],v[16:19],a[8:11]\nDR v[44:47],v204 offset:36864\ns_load_dwordx2 s[78:79],s[94:95],0x48\nM4 a[12:15],v[24:27],v[20:23],a[12:15]\nDR v[48:51],v204 offset:37888\nM4 a[16:19],v[28:31],v[8:11],a[16:19]\n"
"DR v[52:55],v204 offset:38912\ns_load_dwordx2 s[66:67],s[94:95],0x50\nM4 a[20:23],v[28:31],v[12:15],a[20:23]\nDR v[56:59],v204 offset:39936\nM4 a[24:27],v[28:31],v[16:19],a[24:27]\nDT v[64:65],v209 offset:36864\nVL v184,4,v200\nM4 a[28:31],v[28:31],v[20:23],a[28:31]\n"
"DT v[66:67],v209 offset:41984\nM4 a[32:35],v[32:35],v[8:11],a[32:35]\nDT v[68:69],v210 offset:36864\nVN v186,15,v200\nM4 a[36:39],v[32:35],v[12:15],a[36:39]\nDT v[70:71],v210 offset:41984\nM4 a[40:43],v[32:35],v[16:19],a[40:43]\nDT v[72:73],v211 offset:36864\n"
"M4 a[44:47],v[32:35],v[20:23],a[44:47]\nDT v[74:75],v211 offset:41984\nv_lshrrev_b32 v187,4,v200\nM4 a[48:51],v[36:39],v[8:11],a[48:51]\nDT v[76:77],v212 offset:36864\nM4 a[52:55],v[36:39],v[12:15],a[52:55]\nDT v[78:79],v212 offset:41984\nv_mul_u32_u24 v186,1280,v186\n"
"M4 a[56:59],v[36:39],v[16:19],a[56:59]\nM4 a[60:63],v[36:39],v[20:23],a[60:63]\nv_lshl_add_u32 v186,v187,3,v186\nM4 a[64:67],v[40:43],v[8:11],a[64:67]\nM4 a[68:71],v[40:43],v[12:15],a[68:71]\nVL v187,3,v187\nM4 a[72:75],v[40:43],v[16:19],a[72:75]\nM4 a[76:79],v[40:43],v[20:23],a[76:79]\n"
"SW lgkmcnt(0)\nM4 a[0:3],v[60:63],v[44:47],a[0:3]\nDT v[24:25],v213 offset:0\nSM s48,s76\nM4 a[4:7],v[60:63],v[48:51],a[4:7]\nDT v[26:27],v213 offset:5120\ns_and_b32 s49,s77,0xffff\nM4 a[8:11],v[60:63],v[52:55],a[8:11]\n"
"DR v[8:11],v205 offset:0\nM4 a[12:15],v[60:63],v[56:59],a[12:15]\nDR v[12:15],v205 offset:1024\nSM s50,2621440\nM4 a[16:19],v[64:67],v[44:47],a[16:19]\nDR v[16:19],v205 offset:2048\nM4 a[20:23],v[64:67],v[48:51],a[20:23]\nDR v[20:23],v205 offset:3072\n"
"SM s51,0x20000\nM4 a[24:27],v[64:67],v[52:55],a[24:27]\nDT v[28:29],v214 offset:0\nSM s84,s78\nM4 a[28:31],v[64:67],v[56:59],a[28:31]\nDT v[30:31],v214 offset:5120\nM4 a[32:35],v[68:71],v[44:47],a[32:35]\nDT v[32:33],v215 offset:0\n"
"s_and_b32 s85,s79,0xffff\nM4 a[36:39],v[68:71],v[48:51],a[36:39]\nDT v[34:35],v215 offset:5120\nM4 a[40:43],v[68:71],v[52:55],a[40:43]\nDT v[36:37],v216 offset:0\nSM s86,1280\nM4 a[44:47],v[68:71],v[56:59],a[44:47]\nDT v[38:39],v216 offset:5120\n"
"SM s87,0x20000\nM4 a[48:51],v[72:75],v[44:47],a[48:51]\nDT v[40:41],v217 offset:0\nM4 a[52:55],v[72:75],v[48:51],a[52:55]\nDT v[42:43],v217 offset:5120\nSM s96,s66\nM4 a[56:59],v[72:75],v[52:55],a[56:59]\nM4 a[60:63],v[72:75],v[56:59],a[60:63]\n"
"s_and_b32 s97,s67,0xffff\nM4 a[64:67],v[76:79],v[44:47],a[64:67]\nSM s98,2621440\nM4 a[68:71],v[76:79],v[48:51],a[68:71]\nM4 a[72:75],v[76:79],v[52:55],a[72:75]\nSM s99,0x20000\nM4 a[76:79],v[76:79],v[56:59],a[76:79]\nSW lgkmcnt(0)\n"
"SW vmcnt(9)\ns_barrier\nM4 a[0:3],v[24:27],v[8:11],a[0:3]\nDT v[60:61],v213 offset:18432\ns_lshl_b32 s71,s56,3\nM4 a[4:7],v[24:27],v[12:15],a[4:7]\nDT v[62:63],v213 offset:23552\nM4 a[8:11],v[24:27],v[16:19],a[8:11]\n"
"DR v[44:47],v205 offset:18432\ns_lshl_b32 s72,s59,2\nM4 a[12:15],v[24:27],v[20:23],a[12:15]\nDR v[48:51],v205 offset:19456\nM4 a[16:19],v[28:31],v[8:11],a[16:19]\nDR v[52:55],v205 offset:20480\nSA s71,s71,s72\nM4 a[20:23],v[28:31],v[12:15],a[20:23]\n"
"DR v[56:59],v205 offset:21504\nM4 a[24:27],v[28:31],v[16:19],a[24:27]\nDT v[64:65],v214 offset:18432\nSA s71,s71,s58\nM4 a[28:31],v[28:31],v[20:23],a[28:31]\nDT v[66:67],v214 offset:23552\nM4 a[32:35],v[32:35],v[8:11],a[32:35]\nDT v[68:69],v215 offset:18432\n"
"s_mul_i32 s71,s71,20480\nM4 a[36:39],v[32:35],v[12:15],a[36:39]\nDT v[70:71],v215 offset:23552\nM4 a[40:43],v[32:35],v[16:19],a[40:43]\nDT v[72:73],v216 offset:18432\nM4 a[44:47],v[32:35],v[20:23],a[44:47]\nDT v[74:75],v216 offset:23552\ns_mul_i32 s72,s57,320\n"
"M4 a[48:51],v[36:39],v[8:11],a[48:51]\nDT v[76:77],v217 offset:18432\nM4 a[52:55],v[36:39],v[12:15],a[52:55]\nDT v[78:79],v217 offset:23552\ns_mul_i32 s73,s60,160\nM4 a[56:59],v[36:39],v[16:19],a[56:59]\nM4 a[60:63],v[36:39],v[20:23],a[60:63]\nSA s72,s72,s73\n"
"M4 a[64:67],v[40:43],v[8:11],a[64:67]\nM4 a[68:71],v[40:43],v[12:15],a[68:71]\nSA s75,s71,s72\nM4 a[72:75],v[40:43],v[16:19],a[72:75]\nM4 a[76:79],v[40:43],v[20:23],a[76:79]\nSW lgkmcnt(0)\nM4 a[0:3],v[60:63],v[44:47],a[0:3]\nDT v[24:25],v213 offset:36864\n"
"buffer_load_dwordx2 v[128:129],v186,s[48:51],s75 offen offset:0\nM4 a[4:7],v[60:63],v[48:51],a[4:7]\nDT v[26:27],v213 offset:41984\nM4 a[8:11],v[60:63],v[52:55],a[8:11]\nDR v[8:11],v205 offset:36864\nbuffer_load_dwordx2 v[138:139],v187,s[84:87],s72 offen offset:0\nM4 a[12:15],v[60:63],v[56:59],a[12:15]\nDR v[12:15],v205 offset:37888\n"
"M4 a[16:19],v[64:67],v[44:47],a[16:19]\nDR v[16:19],v205 offset:38912\nbuffer_load_dwordx2 v[130:131],v186,s[48:51],s75 offen offset:32\nM4 a[20:23],v[64:67],v[48:51],a[20:23]\nDR v[20:23],v205 offset:39936\nM4 a[24:27],v[64:67],v[52:55],a[24:27]\nDT v[28:29],v214 offset:36864\nbuffer_load_dwordx2 v[140:141],v187,s[84:87],s72 offen offset:32\n"
"M4 a[28:31],v[64:67],v[56:59],a[28:31]\nDT v[30:31],v214 offset:41984\nM4 a[32:35],v[68:71],v[44:47],a[32:35]\nDT v[32:33],v215 offset:36864\nbuffer_load_dwordx2 v[132:133],v186,s[48:51],s75 offen offset:64\nM4 a[36:39],v[68:71],v[48:51],a[36:39]\nDT v[34:35],v215 offset:41984\nM4 a[40:43],v[68:71],v[52:55],a[40:43]\n"
"DT v[36:37],v216 offset:36864\nbuffer_load_dwordx2 v[142:143],v187,s[84:87],s72 offen offset:64\nM4 a[44:47],v[68:71],v[56:59],a[44:47]\nDT v[38:39],v216 offset:41984\nM4 a[48:51],v[72:75],v[44:47],a[48:51]\nDT v[40:41],v217 offset:36864\nbuffer_load_dwordx2 v[134:135],v186,s[48:51],s75 offen offset:96\nM4 a[52:55],v[72:75],v[48:51],a[52:55]\n"
"DT v[42:43],v217 offset:41984\nM4 a[56:59],v[72:75],v[52:55],a[56:59]\nbuffer_load_dwordx2 v[144:145],v187,s[84:87],s72 offen offset:96\nM4 a[60:63],v[72:75],v[56:59],a[60:63]\nM4 a[64:67],v[76:79],v[44:47],a[64:67]\nbuffer_load_dwordx2 v[136:137],v186,s[48:51],s75 offen offset:128\nM4 a[68:71],v[76:79],v[48:51],a[68:71]\nM4 a[72:75],v[76:79],v[52:55],a[72:75]\n"
"buffer_load_dwordx2 v[146:147],v187,s[84:87],s72 offen offset:128\nM4 a[76:79],v[76:79],v[56:59],a[76:79]\nSW lgkmcnt(0)\nSW vmcnt(10)\ns_barrier\nM4 a[0:3],v[24:27],v[8:11],a[0:3]\nDT v[60:61],v218 offset:0\nM4 a[4:7],v[24:27],v[12:15],a[4:7]\n"
"DT v[62:63],v218 offset:5120\nM4 a[8:11],v[24:27],v[16:19],a[8:11]\nDR v[44:47],v206 offset:0\nM4 a[12:15],v[24:27],v[20:23],a[12:15]\nDR v[48:51],v206 offset:1024\nM4 a[16:19],v[28:31],v[8:11],a[16:19]\nDR v[52:55],v206 offset:2048\nM4 a[20:23],v[28:31],v[12:15],a[20:23]\n"
"DR v[56:59],v206 offset:3072\nM4 a[24:27],v[28:31],v[16:19],a[24:27]\nDT v[64:65],v219 offset:0\nM4 a[28:31],v[28:31],v[20:23],a[28:31]\nDT v[66:67],v219 offset:5120\nM4 a[32:35],v[32:35],v[8:11],a[32:35]\nDT v[68:69],v220 offset:0\nM4 a[36:39],v[32:35],v[12:15],a[36:39]\n"
"DT v[70:71],v220 offset:5120\nM4 a[40:43],v[32:35],v[16:19],a[40:43]\nDT v[72:73],v221 offset:0\nM4 a[44:47],v[32:35],v[20:23],a[44:47]\nDT v[74:75],v221 offset:5120\nM4 a[48:51],v[36:39],v[8:11],a[48:51]\nDT v[76:77],v222 offset:0\nM4 a[52:55],v[36:39],v[12:15],a[52:55]\n"
"DT v[78:79],v222 offset:5120\nM4 a[56:59],v[36:39],v[16:19],a[56:59]\nM4 a[60:63],v[36:39],v[20:23],a[60:63]\nM4 a[64:67],v[40:43],v[8:11],a[64:67]\nM4 a[68:71],v[40:43],v[12:15],a[68:71]\nM4 a[72:75],v[40:43],v[16:19],a[72:75]\nM4 a[76:79],v[40:43],v[20:23],a[76:79]\nSW lgkmcnt(0)\n"
"M4 a[0:3],v[60:63],v[44:47],a[0:3]\nM4 a[4:7],v[60:63],v[48:51],a[4:7]\nM4 a[8:11],v[60:63],v[52:55],a[8:11]\nM4 a[12:15],v[60:63],v[56:59],a[12:15]\nM4 a[16:19],v[64:67],v[44:47],a[16:19]\nM4 a[20:23],v[64:67],v[48:51],a[20:23]\nM4 a[24:27],v[64:67],v[52:55],a[24:27]\nM4 a[28:31],v[64:67],v[56:59],a[28:31]\n"
"M4 a[32:35],v[68:71],v[44:47],a[32:35]\nM4 a[36:39],v[68:71],v[48:51],a[36:39]\nM4 a[40:43],v[68:71],v[52:55],a[40:43]\nM4 a[44:47],v[68:71],v[56:59],a[44:47]\nM4 a[48:51],v[72:75],v[44:47],a[48:51]\nM4 a[52:55],v[72:75],v[48:51],a[52:55]\nM4 a[56:59],v[72:75],v[52:55],a[56:59]\nM4 a[60:63],v[72:75],v[56:59],a[60:63]\n"
"M4 a[64:67],v[76:79],v[44:47],a[64:67]\nM4 a[68:71],v[76:79],v[48:51],a[68:71]\nM4 a[72:75],v[76:79],v[52:55],a[72:75]\nM4 a[76:79],v[76:79],v[56:59],a[76:79]\nSW lgkmcnt(0)\nSM s61,s75\nSM s70,65011712\nSA s44,s92,s70\n"
"s_addc_u32 s45,s93,0\ns_and_b32 s45,s45,0xffff\nSM s46,12582912\nSM s47,0x20000\ns_lshl_b32 s70,s81,2\nSA s70,s70,s58\ns_mul_i32 s70,s70,49152\ns_mul_i32 s71,s89,3072\n"
"SA s70,s70,s71\ns_nop 3\ns_cmp_eq_u32 s58,0\ns_cbranch_scc1 P2_sk0\nSA s71,s70,0\nAR v8,a0\nAR v9,a1\nAR v10,a2\n"
"AR v11,a3\nCV v48,v8,v9\nCV v49,v10,v11\nAR v12,a16\nAR v13,a17\nAR v14,a18\nAR v15,a19\nCV v50,v12,v13\n"
"CV v51,v14,v15\nBS v[48:51],v184,s[44:47],s71 offen offset:0 sc1\nAR v16,a32\nAR v17,a33\nAR v18,a34\nAR v19,a35\nCV v52,v16,v17\nCV v53,v18,v19\n"
"AR v20,a48\nAR v21,a49\nAR v22,a50\nAR v23,a51\nCV v54,v20,v21\nCV v55,v22,v23\nBS v[52:55],v184,s[44:47],s71 offen offset:1024 sc1\nAR v24,a64\n"
"AR v25,a65\nAR v26,a66\nAR v27,a67\nCV v56,v24,v25\nCV v57,v26,v27\nBS v[56:59],v184,s[44:47],s71 offen offset:2048 sc1\nP2_sk0:\ns_cmp_eq_u32 s58,1\n"
"s_cbranch_scc1 P2_sk1\nSA s71,s70,12288\nAR v28,a4\nAR v29,a5\nAR v30,a6\nAR v31,a7\nCV v60,v28,v29\nCV v61,v30,v31\n"
"AR v32,a20\nAR v33,a21\nAR v34,a22\nAR v35,a23\nCV v62,v32,v33\nCV v63,v34,v35\nBS v[60:63],v184,s[44:47],s71 offen offset:0 sc1\nAR v36,a36\n"
"AR v37,a37\nAR v38,a38\nAR v39,a39\nCV v64,v36,v37\nCV v65,v38,v39\nAR v40,a52\nAR v41,a53\nAR v42,a54\n"
"AR v43,a55\nCV v66,v40,v41\nCV v67,v42,v43\nBS v[64:67],v184,s[44:47],s71 offen offset:1024 sc1\nAR v44,a68\nAR v45,a69\nAR v46,a70\nAR v47,a71\n"
"CV v68,v44,v45\nCV v69,v46,v47\nBS v[68:71],v184,s[44:47],s71 offen offset:2048 sc1\nP2_sk1:\ns_cmp_eq_u32 s58,2\ns_cbranch_scc1 P2_sk2\nSA s71,s70,24576\nAR v8,a8\n"
"AR v9,a9\nAR v10,a10\nAR v11,a11\nCV v48,v8,v9\nCV v49,v10,v11\nAR v12,a24\nAR v13,a25\nAR v14,a26\n"
"AR v15,a27\nCV v50,v12,v13\nCV v51,v14,v15\nBS v[48:51],v184,s[44:47],s71 offen offset:0 sc1\nAR v16,a40\nAR v17,a41\nAR v18,a42\nAR v19,a43\n"
"CV v52,v16,v17\nCV v53,v18,v19\nAR v20,a56\nAR v21,a57\nAR v22,a58\nAR v23,a59\nCV v54,v20,v21\nCV v55,v22,v23\n"
"BS v[52:55],v184,s[44:47],s71 offen offset:1024 sc1\nAR v24,a72\nAR v25,a73\nAR v26,a74\nAR v27,a75\nCV v56,v24,v25\nCV v57,v26,v27\nBS v[56:59],v184,s[44:47],s71 offen offset:2048 sc1\n"
"P2_sk2:\ns_cmp_eq_u32 s58,3\ns_cbranch_scc1 P2_sk3\nSA s71,s70,36864\nAR v28,a12\nAR v29,a13\nAR v30,a14\nAR v31,a15\n"
"CV v60,v28,v29\nCV v61,v30,v31\nAR v32,a28\nAR v33,a29\nAR v34,a30\nAR v35,a31\nCV v62,v32,v33\nCV v63,v34,v35\n"
"BS v[60:63],v184,s[44:47],s71 offen offset:0 sc1\nAR v36,a44\nAR v37,a45\nAR v38,a46\nAR v39,a47\nCV v64,v36,v37\nCV v65,v38,v39\nAR v40,a60\n"
"AR v41,a61\nAR v42,a62\nAR v43,a63\nCV v66,v40,v41\nCV v67,v42,v43\nBS v[64:67],v184,s[44:47],s71 offen offset:1024 sc1\nAR v44,a76\nAR v45,a77\n"
"AR v46,a78\nAR v47,a79\nCV v68,v44,v45\nCV v69,v46,v47\nBS v[68:71],v184,s[44:47],s71 offen offset:2048 sc1\nP2_sk3:\ns_cmp_eq_u32 s58,0\ns_cbranch_scc0 P2_ow0\n"
"AR v160,a0\nAR v161,a1\nAR v162,a2\nAR v163,a3\nAR v164,a16\nAR v165,a17\nAR v166,a18\nAR v167,a19\n"
"AR v168,a32\nAR v169,a33\nAR v170,a34\nAR v171,a35\nAR v172,a48\nAR v173,a49\nAR v174,a50\nAR v175,a51\n"
"AR v176,a64\nAR v177,a65\nAR v178,a66\nAR v179,a67\nP2_ow0:\ns_cmp_eq_u32 s58,1\ns_cbranch_scc0 P2_ow1\nAR v160,a4\n"
"AR v161,a5\nAR v162,a6\nAR v163,a7\nAR v164,a20\nAR v165,a21\nAR v166,a22\nAR v167,a23\nAR v168,a36\n"
"AR v169,a37\nAR v170,a38\nAR v171,a39\nAR v172,a52\nAR v173,a53\nAR v174,a54\nAR v175,a55\nAR v176,a68\n"
"AR v177,a69\nAR v178,a70\nAR v179,a71\nP2_ow1:\ns_cmp_eq_u32 s58,2\ns_cbranch_scc0 P2_ow2\nAR v160,a8\nAR v161,a9\n"
"AR v162,a10\nAR v163,a11\nAR v164,a24\nAR v165,a25\nAR v166,a26\nAR v167,a27\nAR v168,a40\nAR v169,a41\n"
"AR v170,a42\nAR v171,a43\nAR v172,a56\nAR v173,a57\nAR v174,a58\nAR v175,a59\nAR v176,a72\nAR v177,a73\n"
"AR v178,a74\nAR v179,a75\nP2_ow2:\ns_cmp_eq_u32 s58,3\ns_cbranch_scc0 P2_ow3\nAR v160,a12\nAR v161,a13\nAR v162,a14\n"
"AR v163,a15\nAR v164,a28\nAR v165,a29\nAR v166,a30\nAR v167,a31\nAR v168,a44\nAR v169,a45\nAR v170,a46\n"
"AR v171,a47\nAR v172,a60\nAR v173,a61\nAR v174,a62\nAR v175,a63\nAR v176,a76\nAR v177,a77\nAR v178,a78\n"
"AR v179,a79\nP2_ow3:\nSW vmcnt(9)\nVL v192,16,v128\nVN v193,0xffff0000,v128\nv_pk_add_f32 v[160:161],v[160:161],v[192:193]\nVL v192,16,v138\nVN v193,0xffff0000,v138\n"
"v_pk_add_f32 v[160:161],v[160:161],v[192:193]\nVL v192,16,v129\nVN v193,0xffff0000,v129\nv_pk_add_f32 v[162:163],v[162:163],v[192:193]\nVL v192,16,v139\nVN v193,0xffff0000,v139\nv_pk_add_f32 v[162:163],v[162:163],v[192:193]\nVL v192,16,v130\n"
"VN v193,0xffff0000,v130\nv_pk_add_f32 v[164:165],v[164:165],v[192:193]\nVL v192,16,v140\nVN v193,0xffff0000,v140\nv_pk_add_f32 v[164:165],v[164:165],v[192:193]\nVL v192,16,v131\nVN v193,0xffff0000,v131\nv_pk_add_f32 v[166:167],v[166:167],v[192:193]\n"
"VL v192,16,v141\nVN v193,0xffff0000,v141\nv_pk_add_f32 v[166:167],v[166:167],v[192:193]\nVL v192,16,v132\nVN v193,0xffff0000,v132\nv_pk_add_f32 v[168:169],v[168:169],v[192:193]\nVL v192,16,v142\nVN v193,0xffff0000,v142\n"
"v_pk_add_f32 v[168:169],v[168:169],v[192:193]\nVL v192,16,v133\nVN v193,0xffff0000,v133\nv_pk_add_f32 v[170:171],v[170:171],v[192:193]\nVL v192,16,v143\nVN v193,0xffff0000,v143\nv_pk_add_f32 v[170:171],v[170:171],v[192:193]\nVL v192,16,v134\n"
"VN v193,0xffff0000,v134\nv_pk_add_f32 v[172:173],v[172:173],v[192:193]\nVL v192,16,v144\nVN v193,0xffff0000,v144\nv_pk_add_f32 v[172:173],v[172:173],v[192:193]\nVL v192,16,v135\nVN v193,0xffff0000,v135\nv_pk_add_f32 v[174:175],v[174:175],v[192:193]\n"
"VL v192,16,v145\nVN v193,0xffff0000,v145\nv_pk_add_f32 v[174:175],v[174:175],v[192:193]\nVL v192,16,v136\nVN v193,0xffff0000,v136\nv_pk_add_f32 v[176:177],v[176:177],v[192:193]\nVL v192,16,v146\nVN v193,0xffff0000,v146\n"
"v_pk_add_f32 v[176:177],v[176:177],v[192:193]\nVL v192,16,v137\nVN v193,0xffff0000,v137\nv_pk_add_f32 v[178:179],v[178:179],v[192:193]\nVL v192,16,v147\nVN v193,0xffff0000,v147\nv_pk_add_f32 v[178:179],v[178:179],v[192:193]\nSW vmcnt(0)\n"
"s_barrier\ns_cmp_eq_u32 s89,0\ns_cbranch_scc0 P2_sg\ns_lshl_b32 s71,s81,2\nSA s71,s71,s58\ns_lshl_b32 s71,s71,6\nSA s71,s71,64020480\nv_mov_b32 v188,s90\n"
"v_mov_b32 v189,s91\nv_mov_b32 v190,s71\nv_mov_b32 v191,s93\nv_add_co_u32 v190,vcc,s92,v190\ns_nop 1\nv_addc_co_u32 v191,vcc,0,v191,vcc\ns_mov_b64 exec,1\nglobal_store_dwordx2 v[190:191],v[188:189],off sc1\n"
"s_mov_b64 exec,-1\nP2_sg:\ns_cmp_eq_u32 s89,1\ns_cbranch_scc0 P2_xwd\ns_lshl_b32 s71,s81,8\nSA s71,s71,64020480\nVN v188,3,v200\nVL v188,6,v188\n"
"VU v188,s71,v188\nv_mov_b32 v189,s93\nv_add_co_u32 v188,vcc,s92,v188\ns_nop 1\nv_addc_co_u32 v189,vcc,0,v189,vcc\nSM s71,0\nP2_xw:\nglobal_load_dwordx2 v[190:191],v[188:189],off sc1\n"
"SW vmcnt(0)\nv_xor_b32 v190,s90,v190\nv_xor_b32 v191,s91,v191\nv_or_b32 v190,v190,v191\nv_cmp_ne_u32 vcc,0,v190\ns_nop 4\ns_cbranch_vccz P2_xwd\nSA s71,s71,1\n"
"s_cmp_ge_u32 s71,200000\ns_cbranch_scc1 P2_xwd\ns_branch P2_xw\nP2_xwd:\ns_barrier\ns_mul_i32 s70,s81,196608\ns_mul_i32 s71,s58,12288\nSA s70,s70,s71\n"
"s_mul_i32 s71,s89,3072\nSA s70,s70,s71\nSA s71,s58,1\ns_and_b32 s71,s71,3\ns_mul_i32 s71,s71,49152\nSA s71,s71,s70\nBL v[8:11],v184,s[44:47],s71 offen sc1\nBL v[12:15],v184,s[44:47],s71 offen offset:1024 sc1\n"
"BL v[16:19],v184,s[44:47],s71 offen offset:2048 sc1\nSA s71,s58,2\ns_and_b32 s71,s71,3\ns_mul_i32 s71,s71,49152\nSA s71,s71,s70\nBL v[20:23],v184,s[44:47],s71 offen sc1\nBL v[24:27],v184,s[44:47],s71 offen offset:1024 sc1\nBL v[28:31],v184,s[44:47],s71 offen offset:2048 sc1\n"
"SA s71,s58,3\ns_and_b32 s71,s71,3\ns_mul_i32 s71,s71,49152\nSA s71,s71,s70\nBL v[32:35],v184,s[44:47],s71 offen sc1\nBL v[36:39],v184,s[44:47],s71 offen offset:1024 sc1\nBL v[40:43],v184,s[44:47],s71 offen offset:2048 sc1\nSW vmcnt(6)\n"
"VL v192,16,v8\nVN v193,0xffff0000,v8\nv_pk_add_f32 v[160:161],v[160:161],v[192:193]\nVL v194,16,v9\nVN v195,0xffff0000,v9\nv_pk_add_f32 v[162:163],v[162:163],v[194:195]\nVL v192,16,v10\nVN v193,0xffff0000,v10\n"
"v_pk_add_f32 v[164:165],v[164:165],v[192:193]\nVL v194,16,v11\nVN v195,0xffff0000,v11\nv_pk_add_f32 v[166:167],v[166:167],v[194:195]\nVL v192,16,v12\nVN v193,0xffff0000,v12\nv_pk_add_f32 v[168:169],v[168:169],v[192:193]\nVL v194,16,v13\n"
"VN v195,0xffff0000,v13\nv_pk_add_f32 v[170:171],v[170:171],v[194:195]\nVL v192,16,v14\nVN v193,0xffff0000,v14\nv_pk_add_f32 v[172:173],v[172:173],v[192:193]\nVL v194,16,v15\nVN v195,0xffff0000,v15\nv_pk_add_f32 v[174:175],v[174:175],v[194:195]\n"
"VL v192,16,v16\nVN v193,0xffff0000,v16\nv_pk_add_f32 v[176:177],v[176:177],v[192:193]\nVL v194,16,v17\nVN v195,0xffff0000,v17\nv_pk_add_f32 v[178:179],v[178:179],v[194:195]\nSW vmcnt(3)\nVL v192,16,v20\n"
"VN v193,0xffff0000,v20\nv_pk_add_f32 v[160:161],v[160:161],v[192:193]\nVL v194,16,v21\nVN v195,0xffff0000,v21\nv_pk_add_f32 v[162:163],v[162:163],v[194:195]\nVL v192,16,v22\nVN v193,0xffff0000,v22\nv_pk_add_f32 v[164:165],v[164:165],v[192:193]\n"
"VL v194,16,v23\nVN v195,0xffff0000,v23\nv_pk_add_f32 v[166:167],v[166:167],v[194:195]\nVL v192,16,v24\nVN v193,0xffff0000,v24\nv_pk_add_f32 v[168:169],v[168:169],v[192:193]\nVL v194,16,v25\nVN v195,0xffff0000,v25\n"
"v_pk_add_f32 v[170:171],v[170:171],v[194:195]\nVL v192,16,v26\nVN v193,0xffff0000,v26\nv_pk_add_f32 v[172:173],v[172:173],v[192:193]\nVL v194,16,v27\nVN v195,0xffff0000,v27\nv_pk_add_f32 v[174:175],v[174:175],v[194:195]\nVL v192,16,v28\n"
"VN v193,0xffff0000,v28\nv_pk_add_f32 v[176:177],v[176:177],v[192:193]\nVL v194,16,v29\nVN v195,0xffff0000,v29\nv_pk_add_f32 v[178:179],v[178:179],v[194:195]\nSW vmcnt(0)\nVL v192,16,v32\nVN v193,0xffff0000,v32\n"
"v_pk_add_f32 v[160:161],v[160:161],v[192:193]\nVL v194,16,v33\nVN v195,0xffff0000,v33\nv_pk_add_f32 v[162:163],v[162:163],v[194:195]\nVL v192,16,v34\nVN v193,0xffff0000,v34\nv_pk_add_f32 v[164:165],v[164:165],v[192:193]\nVL v194,16,v35\n"
"VN v195,0xffff0000,v35\nv_pk_add_f32 v[166:167],v[166:167],v[194:195]\nVL v192,16,v36\nVN v193,0xffff0000,v36\nv_pk_add_f32 v[168:169],v[168:169],v[192:193]\nVL v194,16,v37\nVN v195,0xffff0000,v37\nv_pk_add_f32 v[170:171],v[170:171],v[194:195]\n"
"VL v192,16,v38\nVN v193,0xffff0000,v38\nv_pk_add_f32 v[172:173],v[172:173],v[192:193]\nVL v194,16,v39\nVN v195,0xffff0000,v39\nv_pk_add_f32 v[174:175],v[174:175],v[194:195]\nVL v192,16,v40\nVN v193,0xffff0000,v40\n"
"v_pk_add_f32 v[176:177],v[176:177],v[192:193]\nVL v194,16,v41\nVN v195,0xffff0000,v41\nv_pk_add_f32 v[178:179],v[178:179],v[194:195]\nCV v48,v160,v161\nCV v49,v162,v163\nbuffer_store_dwordx2 v[48:49],v186,s[96:99],s61 offen offset:0\nCV v50,v164,v165\n"
"CV v51,v166,v167\nbuffer_store_dwordx2 v[50:51],v186,s[96:99],s61 offen offset:32\nCV v52,v168,v169\nCV v53,v170,v171\nbuffer_store_dwordx2 v[52:53],v186,s[96:99],s61 offen offset:64\nCV v54,v172,v173\nCV v55,v174,v175\nbuffer_store_dwordx2 v[54:55],v186,s[96:99],s61 offen offset:96\n"
"CV v56,v176,v177\nCV v57,v178,v179\nbuffer_store_dwordx2 v[56:57],v186,s[96:99],s61 offen offset:128\ns_or_b32 s34,s88,s89\ns_cmp_eq_u32 s34,0\ns_cbranch_scc0 BG_d\nv_mov_b32 v2,s90\nv_mov_b32 v3,s91\n"
"v_mov_b32 v4,63963136\nv_mov_b32 v5,s93\nv_add_co_u32 v4,vcc,s92,v4\ns_nop 1\nv_addc_co_u32 v5,vcc,0,v5,vcc\ns_mov_b64 exec,1\nglobal_store_dwordx2 v[4:5],v[2:3],off sc1\ns_mov_b64 exec,-1\n"
"BG_d:\nSW vmcnt(0)\nMEGA_END:\n.purgem M4\n.purgem M8\n.purgem AR\n.purgem AW\n.purgem CV\n"
".purgem DR\n.purgem DT\n.purgem BL\n.purgem BS\n.purgem EX\n.purgem VA\n.purgem SA\n.purgem SW\n"
".purgem PM\n.purgem PF\n.purgem CM\n.purgem CG\n.purgem VS\n.purgem VU\n.purgem VL\n.purgem DW\n"
".purgem VN\n.purgem VM\n.purgem SM\n.purgem RC\n"
  :: "v"(threadIdx.x), "s"(blockIdx.x), "s"(__builtin_amdgcn_kernarg_segment_ptr())
  : "v1", "v2", "v3", "v4", "v5", "v6", "v7", "v8", "v9", "v10", "v11", "v12", "v13", "v14", "v15", "v16", "v17", "v18", "v19", "v20", "v21", "v22", "v23", "v24", "v25", "v26", "v27", "v28", "v29", "v30", "v31", "v32", "v33", "v34", "v35", "v36", "v37", "v38", "v39", "v40", "v41", "v42", "v43", "v44", "v45", "v46", "v47", "v48", "v49", "v50", "v51", "v52", "v53", "v54", "v55", "v56", "v57", "v58", "v59", "v60", "v61", "v62", "v63", "v64", "v65", "v66", "v67", "v68", "v69", "v70", "v71", "v72", "v73", "v74", "v75", "v76", "v77", "v78", "v79", "v80", "v81", "v82", "v83", "v84", "v85", "v86", "v87", "v88", "v89", "v90", "v91", "v92", "v93", "v94", "v95", "v96", "v97", "v98", "v99", "v100", "v101", "v102", "v103", "v104", "v105", "v106", "v107", "v108", "v109", "v110", "v111", "v112", "v113", "v114", "v115", "v116", "v117", "v118", "v119", "v120", "v121", "v122", "v123", "v124", "v125", "v126", "v127", "v128", "v129", "v130", "v131", "v132", "v133", "v134", "v135", "v136", "v137", "v138", "v139", "v140", "v141", "v142", "v143", "v144", "v145", "v146", "v147", "v148", "v149", "v150", "v151", "v152", "v153", "v154", "v155", "v156", "v157", "v158", "v159", "v160", "v161", "v162", "v163", "v164", "v165", "v166", "v167", "v168", "v169", "v170", "v171", "v172", "v173", "v174", "v175", "v176", "v177", "v178", "v179", "v180", "v181", "v182", "v183", "v184", "v185", "v186", "v187", "v188", "v189", "v190", "v191", "v192", "v193", "v194", "v195", "v196", "v197", "v198", "v199", "v200", "v201", "v202", "v203", "v204", "v205", "v206", "v207", "v208", "v209", "v210", "v211", "v212", "v213", "v214", "v215", "v216", "v217", "v218", "v219", "v220", "v221", "v222", "v223", "v224", "v225", "v226", "v227", "v228", "v229", "v230", "v231", "v232", "v233", "v234", "v235", "v236", "v237", "v238", "v239", "v240", "v241", "v242", "v243", "v244", "v245", "v246", "v247", "v248", "v249", "v250", "v251", "v252", "v253", "v254", "v255", "a0", "a1", "a2", "a3", "a4", "a5", "a6", "a7", "a8", "a9", "a10", "a11", "a12", "a13", "a14", "a15", "a16", "a17", "a18", "a19", "a20", "a21", "a22", "a23", "a24", "a25", "a26", "a27", "a28", "a29", "a30", "a31", "a32", "a33", "a34", "a35", "a36", "a37", "a38", "a39", "a40", "a41", "a42", "a43", "a44", "a45", "a46", "a47", "a48", "a49", "a50", "a51", "a52", "a53", "a54", "a55", "a56", "a57", "a58", "a59", "a60", "a61", "a62", "a63", "a64", "a65", "a66", "a67", "a68", "a69", "a70", "a71", "a72", "a73", "a74", "a75", "a76", "a77", "a78", "a79", "a80", "a81", "a82", "a83", "a84", "a85", "a86", "a87", "a88", "a89", "a90", "a91", "a92", "a93", "a94", "a95", "a96", "a97", "a98", "a99", "a100", "a101", "a102", "a103", "a104", "a105", "a106", "a107", "a108", "a109", "a110", "a111", "a112", "a113", "a114", "a115", "a116", "a117", "a118", "a119", "a120", "a121", "a122", "a123", "a124", "a125", "a126", "a127", "a128", "a129", "a130", "a131", "a132", "a133", "a134", "a135", "a136", "a137", "a138", "a139", "a140", "a141", "a142", "a143", "a144", "a145", "a146", "a147", "a148", "a149", "a150", "a151", "a152", "a153", "a154", "a155", "a156", "a157", "a158", "a159", "a160", "a161", "a162", "a163", "a164", "a165", "a166", "a167", "a168", "a169", "a170", "a171", "a172", "a173", "a174", "a175", "a176", "a177", "a178", "a179", "a180", "a181", "a182", "a183", "a184", "a185", "a186", "a187", "a188", "a189", "a190", "a191", "a192", "a193", "a194", "a195", "a196", "a197", "a198", "a199", "a200", "a201", "a202", "a203", "a204", "a205", "a206", "a207", "a208", "a209", "a210", "a211", "a212", "a213", "a214", "a215", "a216", "a217", "a218", "a219", "a220", "a221", "a222", "a223", "a224", "a225", "a226", "a227", "a228", "a229", "a230", "a231", "a232", "a233", "a234", "a235", "a236", "a237", "a238", "a239", "a240", "a241", "a242", "a243", "a244", "a245", "a246", "a247", "a248", "a249", "a250", "a251", "a252", "a253", "a254", "a255", "s34", "s35", "s36", "s37", "s38", "s39", "s40", "s41", "s42", "s43", "s44", "s45", "s46", "s47", "s48", "s49", "s50", "s51", "s52", "s53", "s54", "s55", "s56", "s57", "s58", "s59", "s60", "s61", "s62", "s63", "s64", "s65", "s66", "s67", "s68", "s69", "s70", "s71", "s72", "s73", "s74", "s75", "s76", "s77", "s78", "s79", "s80", "s81", "s82", "s83", "s84", "s85", "s86", "s87", "s88", "s89", "s90", "s91", "s92", "s93", "s94", "s95", "s96", "s97", "s98", "s99", "vcc", "scc", "memory");
}

extern "C" void kernel_launch(void* const* d_in, const int* in_sizes, int n_in, void* d_out, int out_size, void* d_ws, size_t ws_size, hipStream_t stream){
  mega_kernel<<<256,256,0,stream>>>((const u16*)d_in[0],(const u16*)d_in[1],(const u16*)d_in[2],(const u16*)d_in[3],(const u16*)d_in[4],(const u16*)d_in[5],
     (const u16*)d_in[7],(const u16*)d_in[8],(const u16*)d_in[9],(const u16*)d_in[10],(u16*)d_out,(char*)d_ws);
}
```
